```python
import torch

def _softmax(x, axis=-1):
    return torch.softmax(x, dim=axis)
def _gelu(x):
    return torch.nn.functional.gelu(x, approximate="tanh")
def _silu(x):
    return torch.nn.functional.silu(x)
def _sigmoid(x):
    return torch.sigmoid(x)
def _layernorm(x, axis=-1, eps=1e-5):
    m = x.mean(dim=axis, keepdim=True)
    v = x.var(dim=axis, keepdim=True, unbiased=False)
    return (x - m) / torch.sqrt(v + eps)
def _scatter_add(tgt, idx, src, axis=0):
    return tgt.clone().index_add_(axis, idx, src)
def _segment_sum(src, seg, n):
    out = torch.zeros((n,) + src.shape[1:], dtype=src.dtype)
    return out.index_add_(0, seg, src)
def _pad(x, pw):
    flat = tuple(v for pair in reversed(pw) for v in pair)
    return torch.nn.functional.pad(x, flat)

kernel_name = 'recurrent_scan__compressed_kv_cla__swiglu_ffn__prompt'

def setup_inputs():

    _w = (lambda a: (lambda t: t.to(torch.bfloat16) if t.is_floating_point() else t)(torch.as_tensor(a)))
    def _A_setup():
        B, H, Sq, Sk, D, M = 1, 16, 2048, 2048, 256, 448
        Dc = 4 * D
        return {
            "x": _w((lambda *s: torch.randn(*s, dtype=torch.bfloat16) * 0.05)(B, Sq, M)),
            "c_kv": _w((lambda *s: torch.randn(*s, dtype=torch.bfloat16) * 0.2)(B, Sk, Dc)),
            "w_q": _w((lambda *s: torch.randn(*s, dtype=torch.bfloat16) * 3.2)(M, H, D) * (M ** -0.5)),
            "w_uk": _w((lambda *s: torch.randn(*s, dtype=torch.bfloat16) * 3.2)(Dc, H, D) * (Dc ** -0.5)),
            "w_uv": _w((lambda *s: torch.randn(*s, dtype=torch.bfloat16) * 0.2)(Dc, H, D) * (Dc ** -0.5)),
            "w_o": _w((lambda *s: torch.randn(*s, dtype=torch.bfloat16) * 0.2)(H, D, M) * ((H * D) ** -0.5)),
        }
    _A_in = _A_setup()

    def _B_setup():
        B, S, D, F = 1, 2048, 448, 1792
        return {
            "x": _w((lambda *s: torch.randn(*s, dtype=torch.bfloat16) * 0.05)(B, S, D)),
            "w_gate": _w((lambda *s: torch.randn(*s, dtype=torch.bfloat16) * 0.1)(D, F)),
            "w_up": _w((lambda *s: torch.randn(*s, dtype=torch.bfloat16) * 0.1)(D, F)),
            "w_down": _w((lambda *s: torch.randn(*s, dtype=torch.bfloat16) * 0.1)(F, D)),
        }
    _B_in = _B_setup()

    return {
        "A_x": _A_in["x"],
        "A_c_kv": _A_in["c_kv"],
        "A_w_q": _A_in["w_q"],
        "A_w_uk": _A_in["w_uk"],
        "A_w_uv": _A_in["w_uv"],
        "A_w_o": _A_in["w_o"],
        "B_x": _B_in["x"],
        "B_w_gate": _B_in["w_gate"],
        "B_w_up": _B_in["w_up"],
        "B_w_down": _B_in["w_down"],
    }

def reference(A_x, A_c_kv, A_w_q, A_w_uk, A_w_uv, A_w_o, B_x, B_w_gate, B_w_up, B_w_down):
    def _A_ref(x, c_kv, w_q, w_uk, w_uv, w_o):
        B, Sq, M = x.shape[0], x.shape[1], x.shape[2]
        Sk = c_kv.shape[1]
        H, D = w_q.shape[1], w_q.shape[2]
        q = torch.einsum("bqm,mhd->bhqd", x, w_q)
        k = torch.einsum("bkc,chd->bhkd", c_kv, w_uk)
        v = torch.einsum("bkc,chd->bhkd", c_kv, w_uv)
        scores = torch.einsum("bhqd,bhkd->bhqk", q, k) * (D ** -0.5)
        off = Sk - Sq
        qi = torch.arange(Sq)[:, None]
        kj = torch.arange(Sk)[None, :]
        cmask = kj <= (qi + off)
        scores = torch.where(cmask[None, None], scores, -1e9)
        p = _softmax(scores, axis=-1)
        ctx = torch.einsum("bhqk,bhkd->bhqd", p, v)
        return torch.einsum("bhqd,hdm->bqm", ctx, w_o)

    def _B_ref(x, w_gate, w_up, w_down):
        g = _silu(x @ w_gate)
        u = x @ w_up
        return (g * u) @ w_down

    x = A_x
    xn = _layernorm(x, axis=-1)
    s = _A_ref(xn, A_c_kv, A_w_q, A_w_uk, A_w_uv, A_w_o)
    g = _silu(_B_ref(xn, B_w_gate, B_w_up, B_w_down))
    return x + s * g
```

```cpp
#include <hip/hip_runtime.h>
#include <stdint.h>
extern "C" __global__ __launch_bounds__(256, 1) void mla_mega(const void* x, const void* ckv, const void* wq, const void* wuk, const void* wuv, const void* wo, const void* wg, const void* wu, const void* wd, void* ws, void* out) {
  asm volatile(R"ASM(
MEGA_START_%=:
s_load_dwordx16 s[76:91], %0, 0
s_load_dwordx4 s[92:95], %0, 64
s_load_dwordx2 s[96:97], %0, 80
s_mov_b32 s29, %2
v_and_b32 v1, 63, %1
v_lshrrev_b32 v64, 6, %1
s_getpc_b64 s[4:5]
CPF0_%=:
s_sub_u32 s4, s4, CPF0_%=-MEGA_START_%=
s_subb_u32 s5, s5, 0
v_lshlrev_b32 v65, 7, %1
global_load_dword v61, v65, s[4:5]
v_add_u32 v65, 32768, v65
global_load_dword v62, v65, s[4:5]
s_nop 1
v_readfirstlane_b32 s28, v64
s_waitcnt lgkmcnt(0)
s_and_b32 s35, s29, 7
s_lshr_b32 s36, s29, 3
s_lshr_b32 s37, s36, 2
s_and_b32 s38, s36, 3
s_lshr_b32 s39, s38, 1
s_lshl_b32 s40, s35, 1
s_add_u32 s40, s40, s39
s_and_b32 s41, s38, 1
s_mov_b64 s[16:17], s[78:79]
s_mov_b32 s18, 0x400000
s_mov_b32 s19, 0x20000
s_cmp_eq_u32 s41, 0
s_cselect_b32 s20, s82, s84
s_cselect_b32 s21, s83, s85
s_mov_b32 s22, 0x800000
s_mov_b32 s23, 0x20000
s_mov_b64 s[48:49], s[76:77]
s_mov_b32 s50, 1835008
s_mov_b32 s51, 0x20000
s_mov_b64 s[52:53], s[94:95]
s_mov_b32 s54, 0x10000000
s_mov_b32 s55, 0x20000
v_mov_b32 v78, 0
s_mov_b32 s43, 103153664
buffer_load_dword v13, v78, s[52:55], s43 offen sc1
v_lshrrev_b32 v66, 5, v1
v_and_b32 v67, 31, v1
s_lshl_b32 s43, s29, 3
s_lshl_b32 s44, s28, 1
s_add_u32 s43, s43, s44
v_add_u32 v68, s43, v66
v_mul_u32_u24 v69, 896, v68
v_lshrrev_b32 v70, 2, v67
v_and_b32 v71, 3, v67
v_lshlrev_b32 v72, 6, v70
v_lshl_add_u32 v72, v71, 3, v72
v_add_u32 v73, v69, v72
v_add_u32 v74, 512, v73
v_cmp_gt_u32 vcc, 24, v67
v_mov_b32 v75, 0x7fffffff
s_nop 1
v_cndmask_b32 v74, v75, v74, vcc
buffer_load_dwordx2 v[2:3], v73, s[48:51], 0 offen
buffer_load_dwordx2 v[4:5], v73, s[48:51], 0 offen offset:32
buffer_load_dwordx2 v[6:7], v74, s[48:51], 0 offen
buffer_load_dwordx2 v[8:9], v74, s[48:51], 0 offen offset:32
v_lshrrev_b32 v76, 4, v68
v_mul_u32_u24 v76, 14336, v76
v_lshl_add_u32 v76, v70, 10, v76
v_lshl_add_u32 v76, v71, 8, v76
v_and_b32 v77, 15, v68
v_lshl_add_u32 v10, v77, 4, v76
v_add_u32 v11, 8192, v10
v_cndmask_b32 v11, v75, v11, vcc
v_mov_b32 v12, 1.0
v_cndmask_b32 v12, 0, v12, vcc
v_lshrrev_b32 v66, 3, v1
v_and_b32 v67, 7, v1
v_lshrrev_b32 v68, 1, v66
v_xor_b32 v69, v67, v68
v_xor_b32 v70, 4, v69
v_lshlrev_b32 v71, 11, v66
v_lshl_add_u32 v69, v69, 4, v71
v_lshl_add_u32 v70, v70, 4, v71
v_and_b32 v72, 1, v68
v_lshlrev_b32 v72, 1, v72
v_xor_b32 v73, v67, v72
v_xor_b32 v74, 4, v73
v_lshlrev_b32 v71, 13, v66
v_lshl_add_u32 v73, v73, 4, v71
v_lshl_add_u32 v74, v74, 4, v71
v_add_u32 v192, 0, v69
v_add_u32 v200, 0, v73
v_add_u32 v193, 16384, v70
v_add_u32 v201, 65536, v74
v_add_u32 v194, 32768, v69
v_add_u32 v202, 131072, v73
v_add_u32 v195, 49152, v70
v_add_u32 v203, 196608, v74
v_add_u32 v196, 65536, v69
v_add_u32 v204, 262144, v73
v_add_u32 v197, 81920, v70
v_add_u32 v205, 327680, v74
v_add_u32 v198, 98304, v69
v_add_u32 v206, 393216, v73
v_add_u32 v199, 114688, v70
v_add_u32 v207, 458752, v74
s_lshl_b32 s30, s37, 19
s_lshl_b32 s35, s28, 17
s_add_u32 s30, s30, s35
s_lshl_b32 s31, s40, 9
s_lshl_b32 s35, s28, 7
s_add_u32 s31, s31, s35
s_lshl_b32 s33, s28, 13
s_add_u32 s62, s33, 32768
s_add_u32 m0, s33, 0
s_nop 0
buffer_load_dwordx4 v192, s[16:19], s30 offen lds
s_add_u32 m0, s62, 0
s_nop 0
buffer_load_dwordx4 v200, s[20:23], s31 offen lds
s_add_u32 m0, s33, 1024
s_nop 0
buffer_load_dwordx4 v193, s[16:19], s30 offen lds
s_add_u32 m0, s62, 1024
s_nop 0
buffer_load_dwordx4 v201, s[20:23], s31 offen lds
s_add_u32 m0, s33, 2048
s_nop 0
buffer_load_dwordx4 v194, s[16:19], s30 offen lds
s_add_u32 m0, s62, 2048
s_nop 0
buffer_load_dwordx4 v202, s[20:23], s31 offen lds
s_add_u32 m0, s33, 3072
s_nop 0
buffer_load_dwordx4 v195, s[16:19], s30 offen lds
s_add_u32 m0, s62, 3072
s_nop 0
buffer_load_dwordx4 v203, s[20:23], s31 offen lds
s_add_u32 m0, s33, 4096
s_nop 0
buffer_load_dwordx4 v196, s[16:19], s30 offen lds
s_add_u32 m0, s62, 4096
s_nop 0
buffer_load_dwordx4 v204, s[20:23], s31 offen lds
s_add_u32 m0, s33, 5120
s_nop 0
buffer_load_dwordx4 v197, s[16:19], s30 offen lds
s_add_u32 m0, s62, 5120
s_nop 0
buffer_load_dwordx4 v205, s[20:23], s31 offen lds
s_add_u32 m0, s33, 6144
s_nop 0
buffer_load_dwordx4 v198, s[16:19], s30 offen lds
s_add_u32 m0, s62, 6144
s_nop 0
buffer_load_dwordx4 v206, s[20:23], s31 offen lds
s_add_u32 m0, s33, 7168
s_nop 0
buffer_load_dwordx4 v199, s[16:19], s30 offen lds
s_add_u32 m0, s62, 7168
s_nop 0
buffer_load_dwordx4 v207, s[20:23], s31 offen lds
s_add_u32 s30, s30, 128
s_add_u32 s31, s31, 524288
s_add_u32 s62, s33, 65536
s_add_u32 s63, s33, 98304
s_lshr_b32 s35, s28, 1
s_and_b32 s36, s28, 1
v_and_b32 v66, 15, v1
v_lshrrev_b32 v67, 4, v1
v_lshrrev_b32 v68, 1, v66
v_xor_b32 v69, v67, v68
v_xor_b32 v70, 4, v69
v_lshlrev_b32 v71, 7, v66
s_lshl_b32 s37, s35, 14
v_add_u32 v71, s37, v71
v_lshl_add_u32 v208, v69, 4, v71
s_add_u32 m0, s62, 0
s_nop 0
buffer_load_dwordx4 v192, s[16:19], s30 offen lds
v_lshl_add_u32 v209, v70, 4, v71
v_bfe_u32 v68, v1, 2, 2
v_and_b32 v69, 3, v1
v_lshl_add_u32 v70, v67, 3, v68
v_lshlrev_b32 v70, 7, v70
v_lshl_add_u32 v70, v69, 3, v70
s_lshl_b32 s37, s36, 14
v_add_u32 v70, s37, v70
v_lshrrev_b32 v71, 1, v68
v_and_b32 v72, 1, v67
v_lshl_add_u32 v71, v72, 1, v71
s_add_u32 m0, s63, 0
s_nop 0
buffer_load_dwordx4 v200, s[20:23], s31 offen lds
v_xor_b32 v72, 0, v71
v_lshl_add_u32 v212, v72, 5, v70
v_xor_b32 v72, 1, v71
v_lshl_add_u32 v213, v72, 5, v70
v_xor_b32 v72, 2, v71
v_lshl_add_u32 v214, v72, 5, v70
v_xor_b32 v72, 3, v71
v_lshl_add_u32 v215, v72, 5, v70
v_mov_b32 v210, v208
v_mov_b32 v211, v209
v_add_u32 v216, 32768, v212
s_add_u32 m0, s62, 1024
s_nop 0
buffer_load_dwordx4 v193, s[16:19], s30 offen lds
v_add_u32 v217, 32768, v213
v_add_u32 v218, 32768, v214
v_add_u32 v219, 32768, v215
v_lshrrev_b32 v66, 3, v1
v_bfe_u32 v67, v1, 4, 2
v_lshlrev_b32 v67, 1, v67
v_and_b32 v68, 7, v1
v_xor_b32 v67, v67, v68
v_lshlrev_b32 v67, 4, v67
v_lshl_add_u32 v0, v66, 7, v67
s_lshl_b32 s42, s29, 2
s_add_u32 s42, s42, s28
v_sub_u32 v57, 13, v1
s_add_u32 m0, s63, 1024
s_nop 0
buffer_load_dwordx4 v201, s[20:23], s31 offen lds
v_lshlrev_b32 v57, 10, v57
v_add_u32 v57, s42, v57
v_mov_b32 v58, s76
v_mov_b32 v59, s77
s_getpc_b64 s[48:49]
PFPC_%=:
s_sub_u32 s48, s48, PFPC_%=-MEGA_START_%=
s_subb_u32 s49, s49, 0
s_mov_b32 s43, 0
v_subrev_u32 v56, s43, v57
s_mov_b32 s43, 90
v_cmp_gt_u32 vcc, s43, v56
s_add_u32 m0, s62, 2048
s_nop 0
buffer_load_dwordx4 v194, s[16:19], s30 offen lds
v_lshlrev_b32 v56, 10, v56
s_add_u32 s44, s48, 0
s_addc_u32 s45, s49, 0
v_mov_b32 v55, s45
v_add_co_u32 v56, s[46:47], s44, v56
s_nop 1
v_addc_co_u32 v55, s[46:47], 0, v55, s[46:47]
v_cndmask_b32 v58, v58, v56, vcc
v_cndmask_b32 v59, v59, v55, vcc
s_mov_b32 s43, 90
v_subrev_u32 v56, s43, v57
s_add_u32 m0, s63, 2048
s_nop 0
buffer_load_dwordx4 v202, s[20:23], s31 offen lds
s_mov_b32 s43, 1568
v_cmp_gt_u32 vcc, s43, v56
v_lshlrev_b32 v56, 10, v56
s_add_u32 s44, s88, 0
s_addc_u32 s45, s89, 0
v_mov_b32 v55, s45
v_add_co_u32 v56, s[46:47], s44, v56
s_nop 1
v_addc_co_u32 v55, s[46:47], 0, v55, s[46:47]
v_cndmask_b32 v58, v58, v56, vcc
v_cndmask_b32 v59, v59, v55, vcc
s_add_u32 m0, s62, 3072
s_nop 0
buffer_load_dwordx4 v195, s[16:19], s30 offen lds
s_mov_b32 s43, 1658
v_subrev_u32 v56, s43, v57
s_mov_b32 s43, 1568
v_cmp_gt_u32 vcc, s43, v56
v_lshlrev_b32 v56, 10, v56
s_add_u32 s44, s90, 0
s_addc_u32 s45, s91, 0
v_mov_b32 v55, s45
v_add_co_u32 v56, s[46:47], s44, v56
s_nop 1
v_addc_co_u32 v55, s[46:47], 0, v55, s[46:47]
s_add_u32 m0, s63, 3072
s_nop 0
buffer_load_dwordx4 v203, s[20:23], s31 offen lds
v_cndmask_b32 v58, v58, v56, vcc
v_cndmask_b32 v59, v59, v55, vcc
s_mov_b32 s43, 3226
v_subrev_u32 v56, s43, v57
s_mov_b32 s43, 3584
v_cmp_gt_u32 vcc, s43, v56
v_lshlrev_b32 v56, 10, v56
s_add_u32 s44, s80, 0
s_addc_u32 s45, s81, 0
v_mov_b32 v55, s45
v_add_co_u32 v56, s[46:47], s44, v56
s_add_u32 m0, s62, 4096
s_nop 0
buffer_load_dwordx4 v196, s[16:19], s30 offen lds
s_nop 1
v_addc_co_u32 v55, s[46:47], 0, v55, s[46:47]
v_cndmask_b32 v58, v58, v56, vcc
v_cndmask_b32 v59, v59, v55, vcc
s_mov_b32 s43, 6810
v_subrev_u32 v56, s43, v57
s_mov_b32 s43, 1568
v_cmp_gt_u32 vcc, s43, v56
v_lshlrev_b32 v56, 10, v56
s_add_u32 s44, s92, 0
s_addc_u32 s45, s93, 0
v_mov_b32 v55, s45
s_add_u32 m0, s63, 4096
s_nop 0
buffer_load_dwordx4 v204, s[20:23], s31 offen lds
v_add_co_u32 v56, s[46:47], s44, v56
s_nop 1
v_addc_co_u32 v55, s[46:47], 0, v55, s[46:47]
v_cndmask_b32 v58, v58, v56, vcc
v_cndmask_b32 v59, v59, v55, vcc
s_mov_b32 s43, 8378
v_subrev_u32 v56, s43, v57
s_mov_b32 s43, 3584
v_cmp_gt_u32 vcc, s43, v56
v_lshlrev_b32 v56, 10, v56
s_add_u32 s44, s86, 0
s_addc_u32 s45, s87, 0
v_mov_b32 v55, s45
s_add_u32 m0, s62, 5120
s_nop 0
buffer_load_dwordx4 v197, s[16:19], s30 offen lds
v_add_co_u32 v56, s[46:47], s44, v56
s_nop 1
v_addc_co_u32 v55, s[46:47], 0, v55, s[46:47]
v_cndmask_b32 v58, v58, v56, vcc
v_cndmask_b32 v59, v59, v55, vcc
v_lshlrev_b32 v60, 7, v1
v_mov_b32 v56, 0x7fffffff
v_cmp_gt_u32 vcc, 8, v1
s_nop 1
v_cndmask_b32 v60, v56, v60, vcc
s_mov_b32 s46, 0x7fffffff
s_mov_b32 s47, 0x20000
s_lshl_b32 s42, s29, 2
s_add_u32 s42, s42, s28
s_lshl_b32 s43, s42, 3
v_and_b32 v55, 7, v1
s_add_u32 m0, s63, 5120
s_nop 0
buffer_load_dwordx4 v205, s[20:23], s31 offen lds
v_add_u32 v55, s43, v55
v_lshrrev_b32 v56, 12, v55
v_and_b32 v55, 4095, v55
v_lshlrev_b32 v55, 7, v55
v_add_u32 v55, 2097152, v55
v_cmp_eq_u32 vcc, 0, v56
v_mov_b32 v52, s84
v_mov_b32 v53, s85
v_mov_b32 v56, s82
v_mov_b32 v57, s83
v_cndmask_b32 v52, v52, v56, vcc
s_add_u32 m0, s62, 6144
s_nop 0
buffer_load_dwordx4 v198, s[16:19], s30 offen lds
v_cndmask_b32 v53, v53, v57, vcc
v_mov_b32 v54, 524288
s_lshl_b32 s43, s42, 1
v_and_b32 v56, 1, v1
v_add_u32 v56, s43, v56
v_lshlrev_b32 v56, 11, v56
v_add_u32 v56, 512, v56
v_lshrrev_b32 v57, 1, v1
v_cmp_eq_u32 vcc, 4, v57
v_mov_b32 v57, s78
v_mov_b32 v50, s79
s_add_u32 m0, s63, 6144
s_nop 0
buffer_load_dwordx4 v206, s[20:23], s31 offen lds
v_mov_b32 v51, 128
v_cndmask_b32 v52, v52, v57, vcc
v_cndmask_b32 v53, v53, v50, vcc
v_cndmask_b32 v55, v55, v56, vcc
v_cndmask_b32 v54, v54, v51, vcc
v_add_co_u32 v52, vcc, v52, v55
s_nop 1
v_addc_co_u32 v53, vcc, 0, v53, vcc
s_lshr_b32 s42, s29, 3
s_lshr_b32 s43, s42, 1
s_and_b32 s44, s43, 1
s_lshr_b32 s43, s43, 1
s_sub_u32 s45, 15, s43
s_cmp_eq_u32 s44, 0
s_cselect_b32 s43, s45, s43
s_lshl_b32 s43, s43, 12
s_add_u32 s57, s43, 102825984
v_lshlrev_b32 v250, 6, v1
s_add_u32 m0, s62, 7168
s_nop 0
buffer_load_dwordx4 v199, s[16:19], s30 offen lds
s_lshl_b32 s67, s29, 2
s_add_u32 s67, s67, s28
s_lshl_b32 s67, s67, 6
s_add_u32 s67, s67, 102825984
s_mov_b32 s56, 536870912
s_mov_b32 s65, 33554432
s_add_u32 m0, s63, 7168
s_nop 0
buffer_load_dwordx4 v207, s[20:23], s31 offen lds
s_add_u32 s30, s30, 128
s_add_u32 s31, s31, 524288
s_add_u32 s62, s33, 131072
s_waitcnt vmcnt(16)
s_barrier
ds_read_b128 v[64:67], v210 offset:0
ds_read_b64_tr_b16 v[96:97], v216 offset:0
ds_read_b64_tr_b16 v[98:99], v216 offset:512
ds_read_b128 v[68:71], v210 offset:2048
ds_read_b64_tr_b16 v[100:101], v217 offset:0
ds_read_b64_tr_b16 v[102:103], v217 offset:512
ds_read_b128 v[72:75], v210 offset:4096
ds_read_b64_tr_b16 v[104:105], v218 offset:0
ds_read_b64_tr_b16 v[106:107], v218 offset:512
ds_read_b128 v[76:79], v210 offset:6144
ds_read_b64_tr_b16 v[108:109], v219 offset:0
ds_read_b64_tr_b16 v[110:111], v219 offset:512
ds_read_b128 v[80:83], v210 offset:8192
ds_read_b64_tr_b16 v[112:113], v216 offset:8192
ds_read_b64_tr_b16 v[114:115], v216 offset:8704
ds_read_b128 v[84:87], v210 offset:10240
ds_read_b64_tr_b16 v[116:117], v217 offset:8192
ds_read_b64_tr_b16 v[118:119], v217 offset:8704
ds_read_b128 v[88:91], v210 offset:12288
ds_read_b64_tr_b16 v[120:121], v218 offset:8192
ds_read_b64_tr_b16 v[122:123], v218 offset:8704
ds_read_b128 v[92:95], v210 offset:14336
ds_read_b64_tr_b16 v[124:125], v219 offset:8192
ds_read_b64_tr_b16 v[126:127], v219 offset:8704
s_mov_b32 s60, 0
s_waitcnt lgkmcnt(0)
s_cmp_eq_u32 s41, 0
s_cbranch_scc0 K1V_%=
s_mov_b32 s34, 14
v_mfma_f32_16x16x32_bf16 a[0:3], v[96:99], v[64:67], 0
s_add_u32 m0, s62, 0
ds_read_b128 v[128:131], v211 offset:0
v_readfirstlane_b32 s98, v13
v_mfma_f32_16x16x32_bf16 a[4:7], v[96:99], v[68:71], 0
buffer_load_dwordx4 v192, s[16:19], s30 offen lds
v_mfma_f32_16x16x32_bf16 a[8:11], v[96:99], v[72:75], 0
ds_read_b64_tr_b16 v[160:161], v216 offset:4096
v_mov_b32 v249, 0x7fffffff
v_mfma_f32_16x16x32_bf16 a[12:15], v[96:99], v[76:79], 0
v_cmp_eq_u32 vcc, 0, v1
v_mfma_f32_16x16x32_bf16 a[16:19], v[96:99], v[80:83], 0
s_add_u32 s98, s98, 1
ds_read_b64_tr_b16 v[162:163], v216 offset:4608
v_mfma_f32_16x16x32_bf16 a[20:23], v[96:99], v[84:87], 0
s_nop 0
v_cndmask_b32_e64 v249, v249, 0, vcc
v_mov_b32 v248, s98
v_mfma_f32_16x16x32_bf16 a[24:27], v[96:99], v[88:91], 0
ds_read_b128 v[132:135], v211 offset:2048
v_lshlrev_b32 v14, 16, v2
v_mfma_f32_16x16x32_bf16 a[28:31], v[96:99], v[92:95], 0
v_and_b32 v15, 0xffff0000, v2
v_lshlrev_b32 v16, 16, v3
v_mfma_f32_16x16x32_bf16 a[60:63], v[100:103], v[92:95], 0
s_add_u32 m0, s62, 1024
ds_read_b64_tr_b16 v[164:165], v217 offset:4096
v_and_b32 v17, 0xffff0000, v3
v_mfma_f32_16x16x32_bf16 a[56:59], v[100:103], v[88:91], 0
buffer_load_dwordx4 v193, s[16:19], s30 offen lds
v_mfma_f32_16x16x32_bf16 a[52:55], v[100:103], v[84:87], 0
ds_read_b64_tr_b16 v[166:167], v217 offset:4608
v_lshlrev_b32 v18, 16, v4
v_mfma_f32_16x16x32_bf16 a[48:51], v[100:103], v[80:83], 0
v_and_b32 v19, 0xffff0000, v4
v_lshlrev_b32 v20, 16, v5
v_mfma_f32_16x16x32_bf16 a[44:47], v[100:103], v[76:79], 0
ds_read_b128 v[136:139], v211 offset:4096
v_and_b32 v21, 0xffff0000, v5
v_mfma_f32_16x16x32_bf16 a[40:43], v[100:103], v[72:75], 0
v_lshlrev_b32 v22, 16, v6
v_and_b32 v23, 0xffff0000, v6
v_mfma_f32_16x16x32_bf16 a[36:39], v[100:103], v[68:71], 0
ds_read_b64_tr_b16 v[168:169], v218 offset:4096
v_lshlrev_b32 v24, 16, v7
v_mfma_f32_16x16x32_bf16 a[32:35], v[100:103], v[64:67], 0
v_and_b32 v25, 0xffff0000, v7
v_lshlrev_b32 v26, 16, v8
v_mfma_f32_16x16x32_bf16 a[64:67], v[104:107], v[64:67], 0
s_add_u32 m0, s62, 2048
ds_read_b64_tr_b16 v[170:171], v218 offset:4608
v_and_b32 v27, 0xffff0000, v8
v_mfma_f32_16x16x32_bf16 a[68:71], v[104:107], v[68:71], 0
buffer_load_dwordx4 v194, s[16:19], s30 offen lds
v_mfma_f32_16x16x32_bf16 a[72:75], v[104:107], v[72:75], 0
ds_read_b128 v[140:143], v211 offset:6144
v_lshlrev_b32 v28, 16, v9
v_mfma_f32_16x16x32_bf16 a[76:79], v[104:107], v[76:79], 0
v_and_b32 v29, 0xffff0000, v9
v_add_f32 v30, v14, v15
v_mfma_f32_16x16x32_bf16 a[80:83], v[104:107], v[80:83], 0
ds_read_b64_tr_b16 v[172:173], v219 offset:4096
v_add_f32 v31, v16, v17
v_mfma_f32_16x16x32_bf16 a[84:87], v[104:107], v[84:87], 0
v_add_f32 v32, v18, v19
v_add_f32 v33, v20, v21
v_mfma_f32_16x16x32_bf16 a[88:91], v[104:107], v[88:91], 0
ds_read_b64_tr_b16 v[174:175], v219 offset:4608
v_add_f32 v34, v22, v23
v_mfma_f32_16x16x32_bf16 a[92:95], v[104:107], v[92:95], 0
v_add_f32 v35, v24, v25
v_add_f32 v36, v26, v27
v_mfma_f32_16x16x32_bf16 a[124:127], v[108:111], v[92:95], 0
s_add_u32 m0, s62, 3072
ds_read_b128 v[144:147], v211 offset:8192
v_add_f32 v37, v28, v29
v_mfma_f32_16x16x32_bf16 a[120:123], v[108:111], v[88:91], 0
buffer_load_dwordx4 v195, s[16:19], s30 offen lds
v_mfma_f32_16x16x32_bf16 a[116:119], v[108:111], v[84:87], 0
ds_read_b64_tr_b16 v[176:177], v216 offset:12288
v_add_f32 v30, v30, v31
v_mfma_f32_16x16x32_bf16 a[112:115], v[108:111], v[80:83], 0
v_add_f32 v32, v32, v33
v_add_f32 v34, v34, v35
v_mfma_f32_16x16x32_bf16 a[108:111], v[108:111], v[76:79], 0
ds_read_b64_tr_b16 v[178:179], v216 offset:12800
v_add_f32 v36, v36, v37
v_mfma_f32_16x16x32_bf16 a[104:107], v[108:111], v[72:75], 0
v_add_f32 v30, v30, v32
v_add_f32 v34, v34, v36
v_mfma_f32_16x16x32_bf16 a[100:103], v[108:111], v[68:71], 0
ds_read_b128 v[148:151], v211 offset:10240
v_add_f32 v30, v30, v34
v_mfma_f32_16x16x32_bf16 a[96:99], v[108:111], v[64:67], 0
s_nop 1
v_add_f32_dpp v30, v30, v30 row_ror:8 row_mask:0xf bank_mask:0xf
v_mfma_f32_16x16x32_bf16 a[128:131], v[112:115], v[64:67], 0
s_add_u32 m0, s62, 4096
ds_read_b64_tr_b16 v[180:181], v217 offset:12288
v_mfma_f32_16x16x32_bf16 a[132:135], v[112:115], v[68:71], 0
buffer_load_dwordx4 v196, s[16:19], s30 offen lds
v_mfma_f32_16x16x32_bf16 a[136:139], v[112:115], v[72:75], 0
s_nop 1
ds_read_b64_tr_b16 v[182:183], v217 offset:12800
v_add_f32_dpp v30, v30, v30 row_ror:4 row_mask:0xf bank_mask:0xf
v_mfma_f32_16x16x32_bf16 a[140:143], v[112:115], v[76:79], 0
s_nop 1
v_add_f32_dpp v30, v30, v30 row_ror:2 row_mask:0xf bank_mask:0xf
v_mfma_f32_16x16x32_bf16 a[144:147], v[112:115], v[80:83], 0
s_nop 1
ds_read_b128 v[152:155], v211 offset:12288
v_add_f32_dpp v30, v30, v30 row_ror:1 row_mask:0xf bank_mask:0xf
v_mfma_f32_16x16x32_bf16 a[148:151], v[112:115], v[84:87], 0
v_mov_b32 v31, v30
v_mfma_f32_16x16x32_bf16 a[152:155], v[112:115], v[88:91], 0
s_nop 1
ds_read_b64_tr_b16 v[184:185], v218 offset:12288
v_permlane16_swap_b32 v30, v31
v_mfma_f32_16x16x32_bf16 a[156:159], v[112:115], v[92:95], 0
s_nop 1
v_add_f32 v30, v30, v31
v_mul_f32 v30, 0x3b124925, v30
v_mfma_f32_16x16x32_bf16 a[188:191], v[116:119], v[92:95], 0
s_add_u32 m0, s62, 5120
ds_read_b64_tr_b16 v[186:187], v218 offset:12800
v_sub_f32 v34, v14, v30
v_mfma_f32_16x16x32_bf16 a[184:187], v[116:119], v[88:91], 0
buffer_load_dwordx4 v197, s[16:19], s30 offen lds
v_mfma_f32_16x16x32_bf16 a[180:183], v[116:119], v[84:87], 0
ds_read_b128 v[156:159], v211 offset:14336
v_sub_f32 v35, v15, v30
v_mfma_f32_16x16x32_bf16 a[176:179], v[116:119], v[80:83], 0
v_sub_f32 v36, v16, v30
v_sub_f32 v37, v17, v30
v_mfma_f32_16x16x32_bf16 a[172:175], v[116:119], v[76:79], 0
ds_read_b64_tr_b16 v[188:189], v219 offset:12288
v_sub_f32 v38, v18, v30
v_mfma_f32_16x16x32_bf16 a[168:171], v[116:119], v[72:75], 0
v_sub_f32 v39, v19, v30
v_sub_f32 v40, v20, v30
v_mfma_f32_16x16x32_bf16 a[164:167], v[116:119], v[68:71], 0
ds_read_b64_tr_b16 v[190:191], v219 offset:12800
v_sub_f32 v41, v21, v30
v_mfma_f32_16x16x32_bf16 a[160:163], v[116:119], v[64:67], 0
v_sub_f32 v42, v22, v30
v_sub_f32 v43, v23, v30
v_mfma_f32_16x16x32_bf16 a[192:195], v[120:123], v[64:67], 0
s_add_u32 m0, s62, 6144
v_mfma_f32_16x16x32_bf16 a[196:199], v[120:123], v[68:71], 0
buffer_load_dwordx4 v198, s[16:19], s30 offen lds
v_mfma_f32_16x16x32_bf16 a[200:203], v[120:123], v[72:75], 0
s_add_u32 s61, s60, 65536
v_mfma_f32_16x16x32_bf16 a[204:207], v[120:123], v[76:79], 0
s_sub_u32 s35, s61, 163840
v_mfma_f32_16x16x32_bf16 a[208:211], v[120:123], v[80:83], 0
s_cselect_b32 s61, s61, s35
v_mfma_f32_16x16x32_bf16 a[212:215], v[120:123], v[84:87], 0
s_add_u32 s64, s61, 32768
v_mfma_f32_16x16x32_bf16 a[216:219], v[120:123], v[88:91], 0
s_sub_u32 s35, s64, 163840
v_mfma_f32_16x16x32_bf16 a[220:223], v[120:123], v[92:95], 0
s_cselect_b32 s64, s64, s35
v_mfma_f32_16x16x32_bf16 a[252:255], v[124:127], v[92:95], 0
s_add_u32 m0, s62, 7168
v_mfma_f32_16x16x32_bf16 a[248:251], v[124:127], v[88:91], 0
buffer_load_dwordx4 v199, s[16:19], s30 offen lds
v_mfma_f32_16x16x32_bf16 a[244:247], v[124:127], v[84:87], 0
s_add_u32 s63, s60, s33
v_mfma_f32_16x16x32_bf16 a[240:243], v[124:127], v[80:83], 0
s_add_u32 s30, s30, 128
v_mfma_f32_16x16x32_bf16 a[236:239], v[124:127], v[76:79], 0
v_add_u32 v210, s61, v208
v_mfma_f32_16x16x32_bf16 a[232:235], v[124:127], v[72:75], 0
v_add_u32 v216, s64, v212
v_add_u32 v217, s64, v213
v_mfma_f32_16x16x32_bf16 a[228:231], v[124:127], v[68:71], 0
v_add_u32 v218, s64, v214
v_add_u32 v219, s64, v215
v_mfma_f32_16x16x32_bf16 a[224:227], v[124:127], v[64:67], 0
s_waitcnt lgkmcnt(0) vmcnt(8)
s_barrier
v_mfma_f32_16x16x32_bf16 a[0:3], v[160:163], v[128:131], a[0:3]
s_add_u32 m0, s63, 0
ds_read_b128 v[64:67], v210 offset:0
v_sub_f32 v44, v24, v30
v_mfma_f32_16x16x32_bf16 a[4:7], v[160:163], v[132:135], a[4:7]
buffer_load_dwordx4 v200, s[20:23], s31 offen lds
v_mfma_f32_16x16x32_bf16 a[8:11], v[160:163], v[136:139], a[8:11]
ds_read_b64_tr_b16 v[96:97], v216 offset:0
v_sub_f32 v45, v25, v30
v_mfma_f32_16x16x32_bf16 a[12:15], v[160:163], v[140:143], a[12:15]
v_sub_f32 v46, v26, v30
v_sub_f32 v47, v27, v30
v_mfma_f32_16x16x32_bf16 a[16:19], v[160:163], v[144:147], a[16:19]
ds_read_b64_tr_b16 v[98:99], v216 offset:512
v_sub_f32 v48, v28, v30
v_mfma_f32_16x16x32_bf16 a[20:23], v[160:163], v[148:151], a[20:23]
v_sub_f32 v49, v29, v30
v_mul_f32 v42, v12, v42
v_mfma_f32_16x16x32_bf16 a[24:27], v[160:163], v[152:155], a[24:27]
ds_read_b128 v[68:71], v210 offset:2048
v_mul_f32 v43, v12, v43
v_mfma_f32_16x16x32_bf16 a[28:31], v[160:163], v[156:159], a[28:31]
v_mul_f32 v44, v12, v44
v_mul_f32 v45, v12, v45
v_mfma_f32_16x16x32_bf16 a[60:63], v[164:167], v[156:159], a[60:63]
s_add_u32 m0, s63, 1024
ds_read_b64_tr_b16 v[100:101], v217 offset:0
v_mul_f32 v46, v12, v46
v_mfma_f32_16x16x32_bf16 a[56:59], v[164:167], v[152:155], a[56:59]
buffer_load_dwordx4 v201, s[20:23], s31 offen lds
v_mfma_f32_16x16x32_bf16 a[52:55], v[164:167], v[148:151], a[52:55]
s_cmp_ge_u32 s34, 4
ds_read_b64_tr_b16 v[102:103], v217 offset:512
v_mul_f32 v47, v12, v47
v_mfma_f32_16x16x32_bf16 a[48:51], v[164:167], v[144:147], a[48:51]
s_cselect_b32 s66, -1, 0
v_mul_f32 v48, v12, v48
v_mul_f32 v49, v12, v49
v_mfma_f32_16x16x32_bf16 a[44:47], v[164:167], v[140:143], a[44:47]
ds_read_b128 v[72:75], v210 offset:4096
v_mul_f32 v31, v34, v34
v_mfma_f32_16x16x32_bf16 a[40:43], v[164:167], v[136:139], a[40:43]
v_fmac_f32 v31, v35, v35
v_fmac_f32 v31, v36, v36
v_mfma_f32_16x16x32_bf16 a[36:39], v[164:167], v[132:135], a[36:39]
ds_read_b64_tr_b16 v[104:105], v218 offset:0
v_fmac_f32 v31, v37, v37
v_mfma_f32_16x16x32_bf16 a[32:35], v[164:167], v[128:131], a[32:35]
v_fmac_f32 v31, v38, v38
v_fmac_f32 v31, v39, v39
v_mfma_f32_16x16x32_bf16 a[64:67], v[168:171], v[128:131], a[64:67]
s_add_u32 m0, s63, 2048
ds_read_b64_tr_b16 v[106:107], v218 offset:512
v_fmac_f32 v31, v40, v40
v_mfma_f32_16x16x32_bf16 a[68:71], v[168:171], v[132:135], a[68:71]
buffer_load_dwordx4 v202, s[20:23], s31 offen lds
v_mfma_f32_16x16x32_bf16 a[72:75], v[168:171], v[136:139], a[72:75]
ds_read_b128 v[76:79], v210 offset:6144
v_fmac_f32 v31, v41, v41
v_mfma_f32_16x16x32_bf16 a[76:79], v[168:171], v[140:143], a[76:79]
v_fmac_f32 v31, v42, v42
v_fmac_f32 v31, v43, v43
v_mfma_f32_16x16x32_bf16 a[80:83], v[168:171], v[144:147], a[80:83]
ds_read_b64_tr_b16 v[108:109], v219 offset:0
v_readlane_b32 s44, v58, s34
v_mfma_f32_16x16x32_bf16 a[84:87], v[168:171], v[148:151], a[84:87]
v_readlane_b32 s45, v59, s34
v_fmac_f32 v31, v44, v44
v_mfma_f32_16x16x32_bf16 a[88:91], v[168:171], v[152:155], a[88:91]
ds_read_b64_tr_b16 v[110:111], v219 offset:512
v_fmac_f32 v31, v45, v45
v_mfma_f32_16x16x32_bf16 a[92:95], v[168:171], v[156:159], a[92:95]
v_fmac_f32 v31, v46, v46
v_fmac_f32 v31, v47, v47
v_mfma_f32_16x16x32_bf16 a[124:127], v[172:175], v[156:159], a[124:127]
s_add_u32 m0, s63, 3072
ds_read_b128 v[80:83], v210 offset:8192
v_fmac_f32 v31, v48, v48
v_mfma_f32_16x16x32_bf16 a[120:123], v[172:175], v[152:155], a[120:123]
buffer_load_dwordx4 v203, s[20:23], s31 offen lds
v_mfma_f32_16x16x32_bf16 a[116:119], v[172:175], v[148:151], a[116:119]
ds_read_b64_tr_b16 v[112:113], v216 offset:8192
v_fmac_f32 v31, v49, v49
v_mfma_f32_16x16x32_bf16 a[112:115], v[172:175], v[144:147], a[112:115]
s_nop 1
v_add_f32_dpp v31, v31, v31 row_ror:8 row_mask:0xf bank_mask:0xf
v_mfma_f32_16x16x32_bf16 a[108:111], v[172:175], v[140:143], a[108:111]
s_nop 1
ds_read_b64_tr_b16 v[114:115], v216 offset:8704
v_add_f32_dpp v31, v31, v31 row_ror:4 row_mask:0xf bank_mask:0xf
v_mfma_f32_16x16x32_bf16 a[104:107], v[172:175], v[136:139], a[104:107]
s_nop 1
v_add_f32_dpp v31, v31, v31 row_ror:2 row_mask:0xf bank_mask:0xf
v_mfma_f32_16x16x32_bf16 a[100:103], v[172:175], v[132:135], a[100:103]
s_nop 1
ds_read_b128 v[84:87], v210 offset:10240
v_add_f32_dpp v31, v31, v31 row_ror:1 row_mask:0xf bank_mask:0xf
v_mfma_f32_16x16x32_bf16 a[96:99], v[172:175], v[128:131], a[96:99]
v_mov_b32 v32, v31
v_mfma_f32_16x16x32_bf16 a[128:131], v[176:179], v[128:131], a[128:131]
s_add_u32 m0, s63, 4096
ds_read_b64_tr_b16 v[116:117], v217 offset:8192
v_mfma_f32_16x16x32_bf16 a[132:135], v[176:179], v[132:135], a[132:135]
buffer_load_dwordx4 v204, s[20:23], s31 offen lds
v_mfma_f32_16x16x32_bf16 a[136:139], v[176:179], v[136:139], a[136:139]
s_nop 1
ds_read_b64_tr_b16 v[118:119], v217 offset:8704
v_permlane16_swap_b32 v31, v32
v_mfma_f32_16x16x32_bf16 a[140:143], v[176:179], v[140:143], a[140:143]
s_nop 1
v_add_f32 v31, v31, v32
v_mul_f32 v31, 0x3b124925, v31
v_mfma_f32_16x16x32_bf16 a[144:147], v[176:179], v[144:147], a[144:147]
ds_read_b128 v[88:91], v210 offset:12288
v_cvt_pk_bf16_f32 v32, v30, v31
v_mfma_f32_16x16x32_bf16 a[148:151], v[176:179], v[148:151], a[148:151]
v_and_b32 v31, 0xffff0000, v32
v_lshlrev_b32 v30, 16, v32
v_mfma_f32_16x16x32_bf16 a[152:155], v[176:179], v[152:155], a[152:155]
ds_read_b64_tr_b16 v[120:121], v218 offset:8192
v_add_f32 v31, 0x3727c5ac, v31
v_mfma_f32_16x16x32_bf16 a[156:159], v[176:179], v[156:159], a[156:159]
v_cvt_pk_bf16_f32 v31, v31, v31
v_and_b32 v31, 0xffff0000, v31
v_mfma_f32_16x16x32_bf16 a[188:191], v[180:183], v[156:159], a[188:191]
s_add_u32 m0, s63, 5120
ds_read_b64_tr_b16 v[122:123], v218 offset:8704
v_mfma_f32_16x16x32_bf16 a[184:187], v[180:183], v[152:155], a[184:187]
buffer_load_dwordx4 v205, s[20:23], s31 offen lds
v_mfma_f32_16x16x32_bf16 a[180:183], v[180:183], v[148:151], a[180:183]
ds_read_b128 v[92:95], v210 offset:14336
v_mfma_f32_16x16x32_bf16 a[176:179], v[180:183], v[144:147], a[176:179]
v_sqrt_f32 v31, v31
v_mfma_f32_16x16x32_bf16 a[172:175], v[180:183], v[140:143], a[172:175]
s_nop 0
ds_read_b64_tr_b16 v[124:125], v219 offset:8192
v_cvt_pk_bf16_f32 v31, v31, v31
v_mfma_f32_16x16x32_bf16 a[168:171], v[180:183], v[136:139], a[168:171]
v_and_b32 v31, 0xffff0000, v31
v_mfma_f32_16x16x32_bf16 a[164:167], v[180:183], v[132:135], a[164:167]
ds_read_b64_tr_b16 v[126:127], v219 offset:8704
v_mfma_f32_16x16x32_bf16 a[160:163], v[180:183], v[128:131], a[160:163]
v_rcp_f32 v31, v31
v_mfma_f32_16x16x32_bf16 a[192:195], v[184:187], v[128:131], a[192:195]
s_add_u32 m0, s63, 6144
v_mfma_f32_16x16x32_bf16 a[196:199], v[184:187], v[132:135], a[196:199]
buffer_load_dwordx4 v206, s[20:23], s31 offen lds
v_mfma_f32_16x16x32_bf16 a[200:203], v[184:187], v[136:139], a[200:203]
s_mov_b32 s60, s61
v_mfma_f32_16x16x32_bf16 a[204:207], v[184:187], v[140:143], a[204:207]
s_add_u32 s62, s61, 131072
v_mfma_f32_16x16x32_bf16 a[208:211], v[184:187], v[144:147], a[208:211]
s_sub_u32 s35, s62, 163840
v_mfma_f32_16x16x32_bf16 a[212:215], v[184:187], v[148:151], a[212:215]
s_cselect_b32 s62, s62, s35
v_mfma_f32_16x16x32_bf16 a[216:219], v[184:187], v[152:155], a[216:219]
s_add_u32 s62, s62, s33
v_mfma_f32_16x16x32_bf16 a[220:223], v[184:187], v[156:159], a[220:223]
v_mfma_f32_16x16x32_bf16 a[252:255], v[188:191], v[156:159], a[252:255]
s_add_u32 m0, s63, 7168
v_mfma_f32_16x16x32_bf16 a[248:251], v[188:191], v[152:155], a[248:251]
buffer_load_dwordx4 v207, s[20:23], s31 offen lds
v_mfma_f32_16x16x32_bf16 a[244:247], v[188:191], v[148:151], a[244:247]
v_mfma_f32_16x16x32_bf16 a[240:243], v[188:191], v[144:147], a[240:243]
s_add_u32 s31, s31, 524288
v_mfma_f32_16x16x32_bf16 a[236:239], v[188:191], v[140:143], a[236:239]
v_add_u32 v211, s61, v209
v_mfma_f32_16x16x32_bf16 a[232:235], v[188:191], v[136:139], a[232:235]
buffer_store_dword v248, v249, s[52:55], s56 offen sc1
v_mfma_f32_16x16x32_bf16 a[228:231], v[188:191], v[132:135], a[228:231]
global_load_dword v62, v[52:53], off
v_mfma_f32_16x16x32_bf16 a[224:227], v[188:191], v[128:131], a[224:227]
buffer_load_dword v61, v60, s[44:47], 0 offen
v_sub_f32 v34, v14, v30
v_sub_f32 v35, v15, v30
v_sub_f32 v36, v16, v30
v_sub_f32 v37, v17, v30
v_sub_f32 v38, v18, v30
v_sub_f32 v39, v19, v30
v_sub_f32 v40, v20, v30
v_sub_f32 v41, v21, v30
v_sub_f32 v42, v22, v30
v_sub_f32 v43, v23, v30
v_sub_f32 v44, v24, v30
v_sub_f32 v45, v25, v30
v_sub_f32 v46, v26, v30
v_sub_f32 v47, v27, v30
v_sub_f32 v48, v28, v30
v_sub_f32 v49, v29, v30
v_cvt_pk_bf16_f32 v2, v34, v35
v_cvt_pk_bf16_f32 v3, v36, v37
v_cvt_pk_bf16_f32 v4, v38, v39
v_cvt_pk_bf16_f32 v5, v40, v41
v_cvt_pk_bf16_f32 v6, v42, v43
v_cvt_pk_bf16_f32 v7, v44, v45
v_cvt_pk_bf16_f32 v8, v46, v47
v_cvt_pk_bf16_f32 v9, v48, v49
v_lshlrev_b32 v14, 16, v2
v_and_b32 v15, 0xffff0000, v2
v_lshlrev_b32 v16, 16, v3
v_and_b32 v17, 0xffff0000, v3
v_lshlrev_b32 v18, 16, v4
v_and_b32 v19, 0xffff0000, v4
v_lshlrev_b32 v20, 16, v5
v_and_b32 v21, 0xffff0000, v5
v_lshlrev_b32 v22, 16, v6
v_and_b32 v23, 0xffff0000, v6
v_lshlrev_b32 v24, 16, v7
v_and_b32 v25, 0xffff0000, v7
v_lshlrev_b32 v26, 16, v8
v_and_b32 v27, 0xffff0000, v8
v_lshlrev_b32 v28, 16, v9
v_and_b32 v29, 0xffff0000, v9
v_mul_f32 v14, v31, v14
v_mul_f32 v15, v31, v15
v_mul_f32 v16, v31, v16
v_mul_f32 v17, v31, v17
v_mul_f32 v18, v31, v18
v_mul_f32 v19, v31, v19
v_mul_f32 v20, v31, v20
v_mul_f32 v21, v31, v21
v_mul_f32 v22, v31, v22
v_mul_f32 v23, v31, v23
v_mul_f32 v24, v31, v24
v_mul_f32 v25, v31, v25
v_mul_f32 v26, v31, v26
v_mul_f32 v27, v31, v27
v_mul_f32 v28, v31, v28
v_mul_f32 v29, v31, v29
v_cvt_pk_bf16_f32 v2, v14, v15
v_cvt_pk_bf16_f32 v3, v16, v17
v_cvt_pk_bf16_f32 v4, v18, v19
v_cvt_pk_bf16_f32 v5, v20, v21
v_cvt_pk_bf16_f32 v6, v22, v23
v_cvt_pk_bf16_f32 v7, v24, v25
v_cvt_pk_bf16_f32 v8, v26, v27
v_cvt_pk_bf16_f32 v9, v28, v29
buffer_store_dwordx4 v[2:5], v10, s[52:55], s65 offen sc1
buffer_store_dwordx4 v[6:9], v11, s[52:55], s65 offen sc1
s_waitcnt lgkmcnt(0)
s_mov_b32 s34, 13
K1L0_%=:
v_mfma_f32_16x16x32_bf16 a[0:3], v[96:99], v[64:67], a[0:3]
s_add_u32 m0, s62, 0
ds_read_b128 v[128:131], v211 offset:0
v_mfma_f32_16x16x32_bf16 a[4:7], v[96:99], v[68:71], a[4:7]
buffer_load_dwordx4 v192, s[16:19], s30 offen lds
v_mfma_f32_16x16x32_bf16 a[8:11], v[96:99], v[72:75], a[8:11]
ds_read_b64_tr_b16 v[160:161], v216 offset:4096
v_mfma_f32_16x16x32_bf16 a[12:15], v[96:99], v[76:79], a[12:15]
v_mfma_f32_16x16x32_bf16 a[16:19], v[96:99], v[80:83], a[16:19]
ds_read_b64_tr_b16 v[162:163], v216 offset:4608
v_mfma_f32_16x16x32_bf16 a[20:23], v[96:99], v[84:87], a[20:23]
v_mfma_f32_16x16x32_bf16 a[24:27], v[96:99], v[88:91], a[24:27]
ds_read_b128 v[132:135], v211 offset:2048
v_mfma_f32_16x16x32_bf16 a[28:31], v[96:99], v[92:95], a[28:31]
v_mfma_f32_16x16x32_bf16 a[60:63], v[100:103], v[92:95], a[60:63]
s_add_u32 m0, s62, 1024
ds_read_b64_tr_b16 v[164:165], v217 offset:4096
v_mfma_f32_16x16x32_bf16 a[56:59], v[100:103], v[88:91], a[56:59]
buffer_load_dwordx4 v193, s[16:19], s30 offen lds
v_mfma_f32_16x16x32_bf16 a[52:55], v[100:103], v[84:87], a[52:55]
ds_read_b64_tr_b16 v[166:167], v217 offset:4608
v_mfma_f32_16x16x32_bf16 a[48:51], v[100:103], v[80:83], a[48:51]
v_mfma_f32_16x16x32_bf16 a[44:47], v[100:103], v[76:79], a[44:47]
ds_read_b128 v[136:139], v211 offset:4096
v_mfma_f32_16x16x32_bf16 a[40:43], v[100:103], v[72:75], a[40:43]
v_mfma_f32_16x16x32_bf16 a[36:39], v[100:103], v[68:71], a[36:39]
ds_read_b64_tr_b16 v[168:169], v218 offset:4096
v_mfma_f32_16x16x32_bf16 a[32:35], v[100:103], v[64:67], a[32:35]
v_mfma_f32_16x16x32_bf16 a[64:67], v[104:107], v[64:67], a[64:67]
s_add_u32 m0, s62, 2048
ds_read_b64_tr_b16 v[170:171], v218 offset:4608
v_mfma_f32_16x16x32_bf16 a[68:71], v[104:107], v[68:71], a[68:71]
buffer_load_dwordx4 v194, s[16:19], s30 offen lds
v_mfma_f32_16x16x32_bf16 a[72:75], v[104:107], v[72:75], a[72:75]
ds_read_b128 v[140:143], v211 offset:6144
v_mfma_f32_16x16x32_bf16 a[76:79], v[104:107], v[76:79], a[76:79]
v_mfma_f32_16x16x32_bf16 a[80:83], v[104:107], v[80:83], a[80:83]
ds_read_b64_tr_b16 v[172:173], v219 offset:4096
v_mfma_f32_16x16x32_bf16 a[84:87], v[104:107], v[84:87], a[84:87]
v_mfma_f32_16x16x32_bf16 a[88:91], v[104:107], v[88:91], a[88:91]
ds_read_b64_tr_b16 v[174:175], v219 offset:4608
v_mfma_f32_16x16x32_bf16 a[92:95], v[104:107], v[92:95], a[92:95]
v_mfma_f32_16x16x32_bf16 a[124:127], v[108:111], v[92:95], a[124:127]
s_add_u32 m0, s62, 3072
ds_read_b128 v[144:147], v211 offset:8192
v_mfma_f32_16x16x32_bf16 a[120:123], v[108:111], v[88:91], a[120:123]
buffer_load_dwordx4 v195, s[16:19], s30 offen lds
v_mfma_f32_16x16x32_bf16 a[116:119], v[108:111], v[84:87], a[116:119]
ds_read_b64_tr_b16 v[176:177], v216 offset:12288
v_mfma_f32_16x16x32_bf16 a[112:115], v[108:111], v[80:83], a[112:115]
v_mfma_f32_16x16x32_bf16 a[108:111], v[108:111], v[76:79], a[108:111]
ds_read_b64_tr_b16 v[178:179], v216 offset:12800
v_mfma_f32_16x16x32_bf16 a[104:107], v[108:111], v[72:75], a[104:107]
v_mfma_f32_16x16x32_bf16 a[100:103], v[108:111], v[68:71], a[100:103]
ds_read_b128 v[148:151], v211 offset:10240
v_mfma_f32_16x16x32_bf16 a[96:99], v[108:111], v[64:67], a[96:99]
v_and_b32 v50, s66, v54
v_mfma_f32_16x16x32_bf16 a[128:131], v[112:115], v[64:67], a[128:131]
s_add_u32 m0, s62, 4096
ds_read_b64_tr_b16 v[180:181], v217 offset:12288
v_mfma_f32_16x16x32_bf16 a[132:135], v[112:115], v[68:71], a[132:135]
buffer_load_dwordx4 v196, s[16:19], s30 offen lds
v_add_co_u32 v52, vcc, v52, v50
v_mfma_f32_16x16x32_bf16 a[136:139], v[112:115], v[72:75], a[136:139]
ds_read_b64_tr_b16 v[182:183], v217 offset:12800
v_mfma_f32_16x16x32_bf16 a[140:143], v[112:115], v[76:79], a[140:143]
v_addc_co_u32 v53, vcc, 0, v53, vcc
v_mfma_f32_16x16x32_bf16 a[144:147], v[112:115], v[80:83], a[144:147]
ds_read_b128 v[152:155], v211 offset:12288
v_mfma_f32_16x16x32_bf16 a[148:151], v[112:115], v[84:87], a[148:151]
v_mfma_f32_16x16x32_bf16 a[152:155], v[112:115], v[88:91], a[152:155]
ds_read_b64_tr_b16 v[184:185], v218 offset:12288
v_mfma_f32_16x16x32_bf16 a[156:159], v[112:115], v[92:95], a[156:159]
v_mfma_f32_16x16x32_bf16 a[188:191], v[116:119], v[92:95], a[188:191]
s_add_u32 m0, s62, 5120
ds_read_b64_tr_b16 v[186:187], v218 offset:12800
v_mfma_f32_16x16x32_bf16 a[184:187], v[116:119], v[88:91], a[184:187]
buffer_load_dwordx4 v197, s[16:19], s30 offen lds
v_mfma_f32_16x16x32_bf16 a[180:183], v[116:119], v[84:87], a[180:183]
ds_read_b128 v[156:159], v211 offset:14336
v_mfma_f32_16x16x32_bf16 a[176:179], v[116:119], v[80:83], a[176:179]
v_mfma_f32_16x16x32_bf16 a[172:175], v[116:119], v[76:79], a[172:175]
ds_read_b64_tr_b16 v[188:189], v219 offset:12288
v_mfma_f32_16x16x32_bf16 a[168:171], v[116:119], v[72:75], a[168:171]
v_mfma_f32_16x16x32_bf16 a[164:167], v[116:119], v[68:71], a[164:167]
ds_read_b64_tr_b16 v[190:191], v219 offset:12800
v_mfma_f32_16x16x32_bf16 a[160:163], v[116:119], v[64:67], a[160:163]
v_mfma_f32_16x16x32_bf16 a[192:195], v[120:123], v[64:67], a[192:195]
s_add_u32 m0, s62, 6144
v_mfma_f32_16x16x32_bf16 a[196:199], v[120:123], v[68:71], a[196:199]
buffer_load_dwordx4 v198, s[16:19], s30 offen lds
v_mfma_f32_16x16x32_bf16 a[200:203], v[120:123], v[72:75], a[200:203]
s_add_u32 s61, s60, 65536
v_mfma_f32_16x16x32_bf16 a[204:207], v[120:123], v[76:79], a[204:207]
s_sub_u32 s35, s61, 163840
v_mfma_f32_16x16x32_bf16 a[208:211], v[120:123], v[80:83], a[208:211]
s_cselect_b32 s61, s61, s35
v_mfma_f32_16x16x32_bf16 a[212:215], v[120:123], v[84:87], a[212:215]
s_add_u32 s64, s61, 32768
v_mfma_f32_16x16x32_bf16 a[216:219], v[120:123], v[88:91], a[216:219]
s_sub_u32 s35, s64, 163840
v_mfma_f32_16x16x32_bf16 a[220:223], v[120:123], v[92:95], a[220:223]
s_cselect_b32 s64, s64, s35
v_mfma_f32_16x16x32_bf16 a[252:255], v[124:127], v[92:95], a[252:255]
s_add_u32 m0, s62, 7168
v_mfma_f32_16x16x32_bf16 a[248:251], v[124:127], v[88:91], a[248:251]
buffer_load_dwordx4 v199, s[16:19], s30 offen lds
v_mfma_f32_16x16x32_bf16 a[244:247], v[124:127], v[84:87], a[244:247]
s_add_u32 s63, s60, s33
v_mfma_f32_16x16x32_bf16 a[240:243], v[124:127], v[80:83], a[240:243]
s_add_u32 s30, s30, 128
v_mfma_f32_16x16x32_bf16 a[236:239], v[124:127], v[76:79], a[236:239]
v_add_u32 v210, s61, v208
v_mfma_f32_16x16x32_bf16 a[232:235], v[124:127], v[72:75], a[232:235]
v_add_u32 v216, s64, v212
v_add_u32 v217, s64, v213
v_mfma_f32_16x16x32_bf16 a[228:231], v[124:127], v[68:71], a[228:231]
v_add_u32 v218, s64, v214
v_add_u32 v219, s64, v215
v_mfma_f32_16x16x32_bf16 a[224:227], v[124:127], v[64:67], a[224:227]
s_waitcnt lgkmcnt(0) vmcnt(11)
s_barrier
v_mfma_f32_16x16x32_bf16 a[0:3], v[160:163], v[128:131], a[0:3]
s_add_u32 m0, s63, 0
ds_read_b128 v[64:67], v210 offset:0
v_mfma_f32_16x16x32_bf16 a[4:7], v[160:163], v[132:135], a[4:7]
buffer_load_dwordx4 v200, s[20:23], s31 offen lds
v_mfma_f32_16x16x32_bf16 a[8:11], v[160:163], v[136:139], a[8:11]
ds_read_b64_tr_b16 v[96:97], v216 offset:0
v_mfma_f32_16x16x32_bf16 a[12:15], v[160:163], v[140:143], a[12:15]
v_mfma_f32_16x16x32_bf16 a[16:19], v[160:163], v[144:147], a[16:19]
ds_read_b64_tr_b16 v[98:99], v216 offset:512
v_mfma_f32_16x16x32_bf16 a[20:23], v[160:163], v[148:151], a[20:23]
v_mfma_f32_16x16x32_bf16 a[24:27], v[160:163], v[152:155], a[24:27]
ds_read_b128 v[68:71], v210 offset:2048
v_mfma_f32_16x16x32_bf16 a[28:31], v[160:163], v[156:159], a[28:31]
v_mfma_f32_16x16x32_bf16 a[60:63], v[164:167], v[156:159], a[60:63]
s_add_u32 m0, s63, 1024
ds_read_b64_tr_b16 v[100:101], v217 offset:0
v_mfma_f32_16x16x32_bf16 a[56:59], v[164:167], v[152:155], a[56:59]
buffer_load_dwordx4 v201, s[20:23], s31 offen lds
v_mfma_f32_16x16x32_bf16 a[52:55], v[164:167], v[148:151], a[52:55]
s_cmp_ge_u32 s34, 4
ds_read_b64_tr_b16 v[102:103], v217 offset:512
v_mfma_f32_16x16x32_bf16 a[48:51], v[164:167], v[144:147], a[48:51]
s_cselect_b32 s66, -1, 0
v_mfma_f32_16x16x32_bf16 a[44:47], v[164:167], v[140:143], a[44:47]
ds_read_b128 v[72:75], v210 offset:4096
v_mfma_f32_16x16x32_bf16 a[40:43], v[164:167], v[136:139], a[40:43]
v_mfma_f32_16x16x32_bf16 a[36:39], v[164:167], v[132:135], a[36:39]
ds_read_b64_tr_b16 v[104:105], v218 offset:0
v_mfma_f32_16x16x32_bf16 a[32:35], v[164:167], v[128:131], a[32:35]
v_mfma_f32_16x16x32_bf16 a[64:67], v[168:171], v[128:131], a[64:67]
s_add_u32 m0, s63, 2048
ds_read_b64_tr_b16 v[106:107], v218 offset:512
v_mfma_f32_16x16x32_bf16 a[68:71], v[168:171], v[132:135], a[68:71]
buffer_load_dwordx4 v202, s[20:23], s31 offen lds
v_mfma_f32_16x16x32_bf16 a[72:75], v[168:171], v[136:139], a[72:75]
ds_read_b128 v[76:79], v210 offset:6144
v_mfma_f32_16x16x32_bf16 a[76:79], v[168:171], v[140:143], a[76:79]
v_mfma_f32_16x16x32_bf16 a[80:83], v[168:171], v[144:147], a[80:83]
ds_read_b64_tr_b16 v[108:109], v219 offset:0
v_readlane_b32 s44, v58, s34
v_mfma_f32_16x16x32_bf16 a[84:87], v[168:171], v[148:151], a[84:87]
v_readlane_b32 s45, v59, s34
v_mfma_f32_16x16x32_bf16 a[88:91], v[168:171], v[152:155], a[88:91]
ds_read_b64_tr_b16 v[110:111], v219 offset:512
v_mfma_f32_16x16x32_bf16 a[92:95], v[168:171], v[156:159], a[92:95]
v_mfma_f32_16x16x32_bf16 a[124:127], v[172:175], v[156:159], a[124:127]
s_add_u32 m0, s63, 3072
ds_read_b128 v[80:83], v210 offset:8192
v_mfma_f32_16x16x32_bf16 a[120:123], v[172:175], v[152:155], a[120:123]
buffer_load_dwordx4 v203, s[20:23], s31 offen lds
v_mfma_f32_16x16x32_bf16 a[116:119], v[172:175], v[148:151], a[116:119]
ds_read_b64_tr_b16 v[112:113], v216 offset:8192
v_mfma_f32_16x16x32_bf16 a[112:115], v[172:175], v[144:147], a[112:115]
v_mfma_f32_16x16x32_bf16 a[108:111], v[172:175], v[140:143], a[108:111]
ds_read_b64_tr_b16 v[114:115], v216 offset:8704
v_mfma_f32_16x16x32_bf16 a[104:107], v[172:175], v[136:139], a[104:107]
v_mfma_f32_16x16x32_bf16 a[100:103], v[172:175], v[132:135], a[100:103]
ds_read_b128 v[84:87], v210 offset:10240
v_mfma_f32_16x16x32_bf16 a[96:99], v[172:175], v[128:131], a[96:99]
v_mfma_f32_16x16x32_bf16 a[128:131], v[176:179], v[128:131], a[128:131]
s_add_u32 m0, s63, 4096
ds_read_b64_tr_b16 v[116:117], v217 offset:8192
v_mfma_f32_16x16x32_bf16 a[132:135], v[176:179], v[132:135], a[132:135]
buffer_load_dwordx4 v204, s[20:23], s31 offen lds
v_mfma_f32_16x16x32_bf16 a[136:139], v[176:179], v[136:139], a[136:139]
ds_read_b64_tr_b16 v[118:119], v217 offset:8704
v_mfma_f32_16x16x32_bf16 a[140:143], v[176:179], v[140:143], a[140:143]
v_mfma_f32_16x16x32_bf16 a[144:147], v[176:179], v[144:147], a[144:147]
ds_read_b128 v[88:91], v210 offset:12288
v_mfma_f32_16x16x32_bf16 a[148:151], v[176:179], v[148:151], a[148:151]
v_mfma_f32_16x16x32_bf16 a[152:155], v[176:179], v[152:155], a[152:155]
ds_read_b64_tr_b16 v[120:121], v218 offset:8192
v_mfma_f32_16x16x32_bf16 a[156:159], v[176:179], v[156:159], a[156:159]
v_mfma_f32_16x16x32_bf16 a[188:191], v[180:183], v[156:159], a[188:191]
s_add_u32 m0, s63, 5120
ds_read_b64_tr_b16 v[122:123], v218 offset:8704
v_mfma_f32_16x16x32_bf16 a[184:187], v[180:183], v[152:155], a[184:187]
buffer_load_dwordx4 v205, s[20:23], s31 offen lds
v_mfma_f32_16x16x32_bf16 a[180:183], v[180:183], v[148:151], a[180:183]
ds_read_b128 v[92:95], v210 offset:14336
v_mfma_f32_16x16x32_bf16 a[176:179], v[180:183], v[144:147], a[176:179]
v_mfma_f32_16x16x32_bf16 a[172:175], v[180:183], v[140:143], a[172:175]
ds_read_b64_tr_b16 v[124:125], v219 offset:8192
v_mfma_f32_16x16x32_bf16 a[168:171], v[180:183], v[136:139], a[168:171]
v_mfma_f32_16x16x32_bf16 a[164:167], v[180:183], v[132:135], a[164:167]
ds_read_b64_tr_b16 v[126:127], v219 offset:8704
v_mfma_f32_16x16x32_bf16 a[160:163], v[180:183], v[128:131], a[160:163]
v_mfma_f32_16x16x32_bf16 a[192:195], v[184:187], v[128:131], a[192:195]
s_add_u32 m0, s63, 6144
v_mfma_f32_16x16x32_bf16 a[196:199], v[184:187], v[132:135], a[196:199]
buffer_load_dwordx4 v206, s[20:23], s31 offen lds
v_mfma_f32_16x16x32_bf16 a[200:203], v[184:187], v[136:139], a[200:203]
s_mov_b32 s60, s61
v_mfma_f32_16x16x32_bf16 a[204:207], v[184:187], v[140:143], a[204:207]
s_add_u32 s62, s61, 131072
v_mfma_f32_16x16x32_bf16 a[208:211], v[184:187], v[144:147], a[208:211]
s_sub_u32 s35, s62, 163840
v_mfma_f32_16x16x32_bf16 a[212:215], v[184:187], v[148:151], a[212:215]
s_cselect_b32 s62, s62, s35
v_mfma_f32_16x16x32_bf16 a[216:219], v[184:187], v[152:155], a[216:219]
s_add_u32 s62, s62, s33
v_mfma_f32_16x16x32_bf16 a[220:223], v[184:187], v[156:159], a[220:223]
v_mfma_f32_16x16x32_bf16 a[252:255], v[188:191], v[156:159], a[252:255]
s_add_u32 m0, s63, 7168
v_mfma_f32_16x16x32_bf16 a[248:251], v[188:191], v[152:155], a[248:251]
buffer_load_dwordx4 v207, s[20:23], s31 offen lds
v_mfma_f32_16x16x32_bf16 a[244:247], v[188:191], v[148:151], a[244:247]
v_mfma_f32_16x16x32_bf16 a[240:243], v[188:191], v[144:147], a[240:243]
s_add_u32 s31, s31, 524288
v_mfma_f32_16x16x32_bf16 a[236:239], v[188:191], v[140:143], a[236:239]
v_add_u32 v211, s61, v209
v_mfma_f32_16x16x32_bf16 a[232:235], v[188:191], v[136:139], a[232:235]
buffer_store_dword v248, v249, s[52:55], s56 offen sc1
v_mfma_f32_16x16x32_bf16 a[228:231], v[188:191], v[132:135], a[228:231]
s_mov_b32 s56, s67
global_load_dword v62, v[52:53], off
v_mfma_f32_16x16x32_bf16 a[224:227], v[188:191], v[128:131], a[224:227]
s_mov_b32 s67, 536870912
buffer_load_dword v61, v60, s[44:47], 0 offen
s_waitcnt lgkmcnt(0)
s_sub_u32 s34, s34, 1
s_cmp_lg_u32 s34, 0
s_cbranch_scc1 K1L0_%=
v_mfma_f32_16x16x32_bf16 a[0:3], v[96:99], v[64:67], a[0:3]
s_lshr_b32 s36, s29, 3
ds_read_b128 v[128:131], v211 offset:0
v_lshlrev_b32 v2, 4, v1
v_mfma_f32_16x16x32_bf16 a[4:7], v[96:99], v[68:71], a[4:7]
buffer_load_dword v63, v250, s[52:55], s57 offen sc1
v_mfma_f32_16x16x32_bf16 a[8:11], v[96:99], v[72:75], a[8:11]
s_lshr_b32 s37, s36, 2
ds_read_b64_tr_b16 v[160:161], v216 offset:4096
v_lshrrev_b32 v12, 3, v1
v_mfma_f32_16x16x32_bf16 a[12:15], v[96:99], v[76:79], a[12:15]
s_lshl_b32 s42, s41, 24
v_bfe_u32 v33, v1, 4, 2
v_mfma_f32_16x16x32_bf16 a[16:19], v[96:99], v[80:83], a[16:19]
s_lshl_b32 s43, s40, 20
ds_read_b64_tr_b16 v[162:163], v216 offset:4608
v_lshlrev_b32 v33, 1, v33
v_mfma_f32_16x16x32_bf16 a[20:23], v[96:99], v[84:87], a[20:23]
s_add_u32 s42, s42, s43
v_and_b32 v34, 7, v1
v_mfma_f32_16x16x32_bf16 a[24:27], v[96:99], v[88:91], a[24:27]
s_lshl_b32 s43, s37, 17
ds_read_b128 v[132:135], v211 offset:2048
v_xor_b32 v33, v33, v34
v_mfma_f32_16x16x32_bf16 a[28:31], v[96:99], v[92:95], a[28:31]
s_add_u32 s42, s42, s43
v_lshlrev_b32 v13, 4, v33
v_mfma_f32_16x16x32_bf16 a[60:63], v[100:103], v[92:95], a[60:63]
s_add_u32 s42, s42, 0
ds_read_b64_tr_b16 v[164:165], v217 offset:4096
v_lshrrev_b32 v34, 4, v1
v_mfma_f32_16x16x32_bf16 a[56:59], v[100:103], v[88:91], a[56:59]
s_lshr_b32 s43, s28, 1
v_and_b32 v35, 15, v1
v_mfma_f32_16x16x32_bf16 a[52:55], v[100:103], v[84:87], a[52:55]
s_lshl_b32 s43, s43, 16
ds_read_b64_tr_b16 v[166:167], v217 offset:4608
v_lshlrev_b32 v36, 2, v34
v_mfma_f32_16x16x32_bf16 a[48:51], v[100:103], v[80:83], a[48:51]
s_add_u32 s42, s42, s43
v_sub_u32 v19, v36, v35
v_mfma_f32_16x16x32_bf16 a[44:47], v[100:103], v[76:79], a[44:47]
s_and_b32 s43, s28, 1
ds_read_b128 v[136:139], v211 offset:4096
v_bfe_u32 v35, v1, 2, 2
v_mfma_f32_16x16x32_bf16 a[40:43], v[100:103], v[72:75], a[40:43]
s_cmp_eq_u32 s41, 0
v_and_b32 v37, 3, v1
v_mfma_f32_16x16x32_bf16 a[36:39], v[100:103], v[68:71], a[36:39]
s_cselect_b32 s44, 14, 13
ds_read_b64_tr_b16 v[168:169], v218 offset:4096
v_add_u32 v36, v36, v35
v_mfma_f32_16x16x32_bf16 a[32:35], v[100:103], v[64:67], a[32:35]
s_lshl_b32 s43, s43, s44
v_bfe_u32 v38, v36, 1, 2
v_mfma_f32_16x16x32_bf16 a[64:67], v[104:107], v[64:67], a[64:67]
s_add_u32 s75, s42, s43
ds_read_b64_tr_b16 v[170:171], v218 offset:4608
v_lshlrev_b32 v36, 7, v36
v_mfma_f32_16x16x32_bf16 a[68:71], v[104:107], v[68:71], a[68:71]
s_mov_b32 s99, 16384
v_lshl_add_u32 v36, v37, 3, v36
v_mfma_f32_16x16x32_bf16 a[72:75], v[104:107], v[72:75], a[72:75]
s_cmp_eq_u32 s41, 0
ds_read_b128 v[140:143], v211 offset:6144
v_xor_b32 v39, 0, v38
v_mfma_f32_16x16x32_bf16 a[76:79], v[104:107], v[76:79], a[76:79]
s_cselect_b32 s99, 4096, s99
v_lshl_add_u32 v4, v39, 5, v36
v_mfma_f32_16x16x32_bf16 a[80:83], v[104:107], v[80:83], a[80:83]
s_mov_b32 s69, s41
ds_read_b64_tr_b16 v[172:173], v219 offset:4096
v_xor_b32 v39, 1, v38
v_mfma_f32_16x16x32_bf16 a[84:87], v[104:107], v[84:87], a[84:87]
s_lshr_b32 s43, s36, 1
v_lshl_add_u32 v5, v39, 5, v36
v_mfma_f32_16x16x32_bf16 a[88:91], v[104:107], v[88:91], a[88:91]
s_and_b32 s44, s43, 1
ds_read_b64_tr_b16 v[174:175], v219 offset:4608
v_xor_b32 v39, 2, v38
v_mfma_f32_16x16x32_bf16 a[92:95], v[104:107], v[92:95], a[92:95]
s_lshr_b32 s43, s43, 1
v_lshl_add_u32 v6, v39, 5, v36
v_mfma_f32_16x16x32_bf16 a[124:127], v[108:111], v[92:95], a[124:127]
s_sub_u32 s45, 15, s43
ds_read_b128 v[144:147], v211 offset:8192
v_xor_b32 v39, 3, v38
v_mfma_f32_16x16x32_bf16 a[120:123], v[108:111], v[88:91], a[120:123]
s_cmp_eq_u32 s44, 0
v_lshl_add_u32 v7, v39, 5, v36
v_mfma_f32_16x16x32_bf16 a[116:119], v[108:111], v[84:87], a[116:119]
s_cselect_b32 s43, s45, s43
ds_read_b64_tr_b16 v[176:177], v216 offset:12288
v_mfma_f32_16x16x32_bf16 a[112:115], v[108:111], v[80:83], a[112:115]
s_lshl_b32 s43, s43, 3
s_lshl_b32 s44, s28, 1
s_add_u32 s43, s43, s44
v_mfma_f32_16x16x32_bf16 a[108:111], v[108:111], v[76:79], a[108:111]
s_mul_i32 s45, s43, 14336
ds_read_b64_tr_b16 v[178:179], v216 offset:12800
v_mfma_f32_16x16x32_bf16 a[104:107], v[108:111], v[72:75], a[104:107]
s_add_u32 s45, s45, 33554432
s_add_u32 s46, s45, 14336
v_mfma_f32_16x16x32_bf16 a[100:103], v[108:111], v[68:71], a[100:103]
ds_read_b128 v[148:151], v211 offset:10240
v_mfma_f32_16x16x32_bf16 a[96:99], v[108:111], v[64:67], a[96:99]
v_mfma_f32_16x16x32_bf16 a[128:131], v[112:115], v[64:67], a[128:131]
ds_read_b64_tr_b16 v[180:181], v217 offset:12288
v_mfma_f32_16x16x32_bf16 a[132:135], v[112:115], v[68:71], a[132:135]
v_mfma_f32_16x16x32_bf16 a[136:139], v[112:115], v[72:75], a[136:139]
ds_read_b64_tr_b16 v[182:183], v217 offset:12800
v_mfma_f32_16x16x32_bf16 a[140:143], v[112:115], v[76:79], a[140:143]
v_mfma_f32_16x16x32_bf16 a[144:147], v[112:115], v[80:83], a[144:147]
ds_read_b128 v[152:155], v211 offset:12288
v_mfma_f32_16x16x32_bf16 a[148:151], v[112:115], v[84:87], a[148:151]
v_mfma_f32_16x16x32_bf16 a[152:155], v[112:115], v[88:91], a[152:155]
ds_read_b64_tr_b16 v[184:185], v218 offset:12288
v_mfma_f32_16x16x32_bf16 a[156:159], v[112:115], v[92:95], a[156:159]
v_mfma_f32_16x16x32_bf16 a[188:191], v[116:119], v[92:95], a[188:191]
ds_read_b64_tr_b16 v[186:187], v218 offset:12800
v_mfma_f32_16x16x32_bf16 a[184:187], v[116:119], v[88:91], a[184:187]
v_mfma_f32_16x16x32_bf16 a[180:183], v[116:119], v[84:87], a[180:183]
ds_read_b128 v[156:159], v211 offset:14336
v_mfma_f32_16x16x32_bf16 a[176:179], v[116:119], v[80:83], a[176:179]
v_mfma_f32_16x16x32_bf16 a[172:175], v[116:119], v[76:79], a[172:175]
ds_read_b64_tr_b16 v[188:189], v219 offset:12288
v_mfma_f32_16x16x32_bf16 a[168:171], v[116:119], v[72:75], a[168:171]
v_mfma_f32_16x16x32_bf16 a[164:167], v[116:119], v[68:71], a[164:167]
ds_read_b64_tr_b16 v[190:191], v219 offset:12800
v_mfma_f32_16x16x32_bf16 a[160:163], v[116:119], v[64:67], a[160:163]
v_mfma_f32_16x16x32_bf16 a[192:195], v[120:123], v[64:67], a[192:195]
v_mfma_f32_16x16x32_bf16 a[196:199], v[120:123], v[68:71], a[196:199]
v_mfma_f32_16x16x32_bf16 a[200:203], v[120:123], v[72:75], a[200:203]
s_add_u32 s61, s60, 65536
v_mfma_f32_16x16x32_bf16 a[204:207], v[120:123], v[76:79], a[204:207]
s_sub_u32 s35, s61, 163840
v_mfma_f32_16x16x32_bf16 a[208:211], v[120:123], v[80:83], a[208:211]
s_cselect_b32 s61, s61, s35
v_mfma_f32_16x16x32_bf16 a[212:215], v[120:123], v[84:87], a[212:215]
s_add_u32 s64, s61, 32768
v_mfma_f32_16x16x32_bf16 a[216:219], v[120:123], v[88:91], a[216:219]
s_sub_u32 s35, s64, 163840
v_mfma_f32_16x16x32_bf16 a[220:223], v[120:123], v[92:95], a[220:223]
s_cselect_b32 s64, s64, s35
v_mfma_f32_16x16x32_bf16 a[252:255], v[124:127], v[92:95], a[252:255]
v_mfma_f32_16x16x32_bf16 a[248:251], v[124:127], v[88:91], a[248:251]
v_mfma_f32_16x16x32_bf16 a[244:247], v[124:127], v[84:87], a[244:247]
v_mfma_f32_16x16x32_bf16 a[240:243], v[124:127], v[80:83], a[240:243]
v_mfma_f32_16x16x32_bf16 a[236:239], v[124:127], v[76:79], a[236:239]
v_add_u32 v210, s61, v208
v_mfma_f32_16x16x32_bf16 a[232:235], v[124:127], v[72:75], a[232:235]
v_add_u32 v216, s64, v212
v_add_u32 v217, s64, v213
v_mfma_f32_16x16x32_bf16 a[228:231], v[124:127], v[68:71], a[228:231]
v_add_u32 v218, s64, v214
v_add_u32 v219, s64, v215
v_mfma_f32_16x16x32_bf16 a[224:227], v[124:127], v[64:67], a[224:227]
s_waitcnt lgkmcnt(0) vmcnt(0)
s_barrier
v_cmp_eq_u32 vcc, s98, v63
s_nop 4
s_cmp_eq_u64 vcc, -1
s_cbranch_scc1 ALLOKxn0_%=
s_waitcnt vmcnt(0)
s_mov_b32 s47, 0
ALLPxn0_%=:
buffer_load_dword v63, v250, s[52:55], s57 offen sc1
s_waitcnt vmcnt(0)
v_cmp_eq_u32 vcc, s98, v63
s_add_u32 s47, s47, 1
s_nop 4
s_cmp_eq_u64 vcc, -1
s_cbranch_scc1 ALLOKxn0_%=
s_sleep 1
s_cmp_lt_u32 s47, 0x40000
s_cbranch_scc1 ALLPxn0_%=
ALLOKxn0_%=:
v_mfma_f32_16x16x32_bf16 a[0:3], v[160:163], v[128:131], a[0:3]
s_and_b32 s35, s29, 7
ds_read_b128 v[64:67], v210 offset:0
v_mfma_f32_16x16x32_bf16 a[4:7], v[160:163], v[132:135], a[4:7]
buffer_load_dwordx4 v[20:23], v2, s[52:55], s45 offen offset:0 sc1
v_mfma_f32_16x16x32_bf16 a[8:11], v[160:163], v[136:139], a[8:11]
s_lshr_b32 s47, s29, 3
ds_read_b64_tr_b16 v[96:97], v216 offset:0
v_mfma_f32_16x16x32_bf16 a[12:15], v[160:163], v[140:143], a[12:15]
s_and_b32 s48, s47, 1
s_lshl_b32 s30, s35, 1
s_add_u32 s30, s30, s48
v_mfma_f32_16x16x32_bf16 a[16:19], v[160:163], v[144:147], a[16:19]
s_lshr_b32 s47, s47, 1
ds_read_b64_tr_b16 v[98:99], v216 offset:512
v_mfma_f32_16x16x32_bf16 a[20:23], v[160:163], v[148:151], a[20:23]
s_and_b32 s40, s47, 1
s_lshr_b32 s31, s47, 1
s_lshl_b32 s42, s30, 3
v_mfma_f32_16x16x32_bf16 a[24:27], v[160:163], v[152:155], a[24:27]
s_add_u32 s42, s42, s31
ds_read_b128 v[68:71], v210 offset:2048
v_mfma_f32_16x16x32_bf16 a[28:31], v[160:163], v[156:159], a[28:31]
s_sub_u32 s47, 15, s31
s_cmp_eq_u32 s40, 0
s_cselect_b32 s41, s47, s31
v_mfma_f32_16x16x32_bf16 a[60:63], v[164:167], v[156:159], a[60:63]
s_cmp_lt_u32 s30, 14
ds_read_b64_tr_b16 v[100:101], v217 offset:0
v_mfma_f32_16x16x32_bf16 a[56:59], v[164:167], v[152:155], a[56:59]
buffer_load_dwordx4 v[24:27], v2, s[52:55], s46 offen offset:0 sc1
v_mfma_f32_16x16x32_bf16 a[52:55], v[164:167], v[148:151], a[52:55]
s_cselect_b32 s43, 7, 0
ds_read_b64_tr_b16 v[102:103], v217 offset:512
v_mfma_f32_16x16x32_bf16 a[48:51], v[164:167], v[144:147], a[48:51]
s_lshl_b32 s33, s28, 13
s_mov_b64 s[36:37], s[94:95]
s_mov_b32 s38, 0x10000000
v_mfma_f32_16x16x32_bf16 a[44:47], v[164:167], v[140:143], a[44:47]
s_mov_b32 s39, 0x20000
ds_read_b128 v[72:75], v210 offset:4096
v_mfma_f32_16x16x32_bf16 a[40:43], v[164:167], v[136:139], a[40:43]
s_mov_b32 s18, 0x7fffffff
s_mov_b32 s19, 0x20000
s_cmp_lt_u32 s31, 7
v_mfma_f32_16x16x32_bf16 a[36:39], v[164:167], v[132:135], a[36:39]
s_cselect_b32 s68, 18, 17
ds_read_b64_tr_b16 v[104:105], v218 offset:0
v_mfma_f32_16x16x32_bf16 a[32:35], v[164:167], v[128:131], a[32:35]
s_sub_u32 s47, s29, 224
s_cselect_b32 s47, s29, s47
s_and_b32 s10, s47, 7
v_mfma_f32_16x16x32_bf16 a[64:67], v[168:171], v[128:131], a[64:67]
s_lshr_b32 s11, s47, 3
ds_read_b64_tr_b16 v[106:107], v218 offset:512
v_mfma_f32_16x16x32_bf16 a[68:71], v[168:171], v[132:135], a[68:71]
buffer_load_dwordx4 v[28:31], v2, s[52:55], s45 offen offset:1024 sc1
v_mfma_f32_16x16x32_bf16 a[72:75], v[168:171], v[136:139], a[72:75]
s_mul_i32 s48, s11, 37
ds_read_b128 v[76:79], v210 offset:6144
v_mfma_f32_16x16x32_bf16 a[76:79], v[168:171], v[140:143], a[76:79]
s_lshr_b32 s48, s48, 8
s_lshl_b32 s10, s10, 2
s_add_u32 s10, s10, s48
v_mfma_f32_16x16x32_bf16 a[80:83], v[168:171], v[144:147], a[80:83]
s_mul_i32 s48, s48, 7
ds_read_b64_tr_b16 v[108:109], v219 offset:0
v_mfma_f32_16x16x32_bf16 a[84:87], v[168:171], v[148:151], a[84:87]
s_sub_u32 s11, s11, s48
s_lshl_b32 s47, s30, 9
s_lshl_b32 s48, s28, 7
v_mfma_f32_16x16x32_bf16 a[88:91], v[168:171], v[152:155], a[88:91]
s_add_u32 s47, s47, s48
ds_read_b64_tr_b16 v[110:111], v219 offset:512
v_mfma_f32_16x16x32_bf16 a[92:95], v[168:171], v[156:159], a[92:95]
s_lshl_b32 s49, s30, 8
s_and_b32 s48, s28, 1
s_lshl_b32 s48, s48, 7
v_mfma_f32_16x16x32_bf16 a[124:127], v[172:175], v[156:159], a[124:127]
s_add_u32 s49, s49, s48
ds_read_b128 v[80:83], v210 offset:8192
v_mfma_f32_16x16x32_bf16 a[120:123], v[172:175], v[152:155], a[120:123]
buffer_load_dwordx4 v[32:35], v2, s[52:55], s46 offen offset:1024 sc1
v_mfma_f32_16x16x32_bf16 a[116:119], v[172:175], v[148:151], a[116:119]
s_cmp_lt_u32 s28, 2
ds_read_b64_tr_b16 v[112:113], v216 offset:8192
v_mfma_f32_16x16x32_bf16 a[112:115], v[172:175], v[144:147], a[112:115]
s_cselect_b32 s50, s88, s90
s_cselect_b32 s51, s89, s91
s_cmp_eq_u32 s43, 7
v_mfma_f32_16x16x32_bf16 a[108:111], v[172:175], v[140:143], a[108:111]
s_cselect_b32 s16, s50, s80
ds_read_b64_tr_b16 v[114:115], v216 offset:8704
v_mfma_f32_16x16x32_bf16 a[104:107], v[172:175], v[136:139], a[104:107]
s_cselect_b32 s17, s51, s81
s_cselect_b32 s47, s49, s47
s_mov_b32 s25, 8192
v_mfma_f32_16x16x32_bf16 a[100:103], v[172:175], v[132:135], a[100:103]
s_cselect_b32 s25, 3584, s25
ds_read_b128 v[84:87], v210 offset:10240
v_mfma_f32_16x16x32_bf16 a[96:99], v[172:175], v[128:131], a[96:99]
s_add_u32 s16, s16, s47
s_addc_u32 s17, s17, 0
s_lshl_b32 s21, s25, 3
v_mfma_f32_16x16x32_bf16 a[128:131], v[176:179], v[128:131], a[128:131]
s_lshl_b32 s14, s25, 6
ds_read_b64_tr_b16 v[116:117], v217 offset:8192
v_mfma_f32_16x16x32_bf16 a[132:135], v[176:179], v[132:135], a[132:135]
buffer_load_dwordx4 v[36:39], v2, s[52:55], s45 offen offset:2048 sc1
v_mfma_f32_16x16x32_bf16 a[136:139], v[176:179], v[136:139], a[136:139]
s_nop 0
ds_read_b64_tr_b16 v[118:119], v217 offset:8704
v_mad_u32_u24 v14, v12, s25, v13
v_mfma_f32_16x16x32_bf16 a[140:143], v[176:179], v[140:143], a[140:143]
v_mfma_f32_16x16x32_bf16 a[144:147], v[176:179], v[144:147], a[144:147]
ds_read_b128 v[88:91], v210 offset:12288
v_mfma_f32_16x16x32_bf16 a[148:151], v[176:179], v[148:151], a[148:151]
v_mfma_f32_16x16x32_bf16 a[152:155], v[176:179], v[152:155], a[152:155]
ds_read_b64_tr_b16 v[120:121], v218 offset:8192
v_mfma_f32_16x16x32_bf16 a[156:159], v[176:179], v[156:159], a[156:159]
v_mfma_f32_16x16x32_bf16 a[188:191], v[180:183], v[156:159], a[188:191]
ds_read_b64_tr_b16 v[122:123], v218 offset:8704
v_mfma_f32_16x16x32_bf16 a[184:187], v[180:183], v[152:155], a[184:187]
buffer_load_dwordx4 v[48:51], v2, s[52:55], s46 offen offset:2048 sc1
v_mfma_f32_16x16x32_bf16 a[180:183], v[180:183], v[148:151], a[180:183]
ds_read_b128 v[92:95], v210 offset:14336
v_mfma_f32_16x16x32_bf16 a[176:179], v[180:183], v[144:147], a[176:179]
v_mfma_f32_16x16x32_bf16 a[172:175], v[180:183], v[140:143], a[172:175]
ds_read_b64_tr_b16 v[124:125], v219 offset:8192
v_mfma_f32_16x16x32_bf16 a[168:171], v[180:183], v[136:139], a[168:171]
v_mfma_f32_16x16x32_bf16 a[164:167], v[180:183], v[132:135], a[164:167]
ds_read_b64_tr_b16 v[126:127], v219 offset:8704
v_mfma_f32_16x16x32_bf16 a[160:163], v[180:183], v[128:131], a[160:163]
v_mfma_f32_16x16x32_bf16 a[192:195], v[184:187], v[128:131], a[192:195]
v_mfma_f32_16x16x32_bf16 a[196:199], v[184:187], v[132:135], a[196:199]
buffer_load_dwordx4 v[52:55], v2, s[52:55], s45 offen offset:3072 sc1
v_mfma_f32_16x16x32_bf16 a[200:203], v[184:187], v[136:139], a[200:203]
v_mfma_f32_16x16x32_bf16 a[204:207], v[184:187], v[140:143], a[204:207]
v_mfma_f32_16x16x32_bf16 a[208:211], v[184:187], v[144:147], a[208:211]
v_mfma_f32_16x16x32_bf16 a[212:215], v[184:187], v[148:151], a[212:215]
v_mfma_f32_16x16x32_bf16 a[216:219], v[184:187], v[152:155], a[216:219]
v_mfma_f32_16x16x32_bf16 a[220:223], v[184:187], v[156:159], a[220:223]
v_mfma_f32_16x16x32_bf16 a[252:255], v[188:191], v[156:159], a[252:255]
v_mfma_f32_16x16x32_bf16 a[248:251], v[188:191], v[152:155], a[248:251]
buffer_load_dwordx4 v[56:59], v2, s[52:55], s46 offen offset:3072 sc1
v_mfma_f32_16x16x32_bf16 a[244:247], v[188:191], v[148:151], a[244:247]
v_mfma_f32_16x16x32_bf16 a[240:243], v[188:191], v[144:147], a[240:243]
v_mfma_f32_16x16x32_bf16 a[236:239], v[188:191], v[140:143], a[236:239]
v_add_u32 v211, s61, v209
v_mfma_f32_16x16x32_bf16 a[232:235], v[188:191], v[136:139], a[232:235]
v_mfma_f32_16x16x32_bf16 a[228:231], v[188:191], v[132:135], a[228:231]
v_mfma_f32_16x16x32_bf16 a[224:227], v[188:191], v[128:131], a[224:227]
s_waitcnt lgkmcnt(0)
v_mfma_f32_16x16x32_bf16 a[0:3], v[96:99], v[64:67], a[0:3]
s_mov_b32 s50, s43
ds_read_b128 v[128:131], v211 offset:0
v_mfma_f32_16x16x32_bf16 a[4:7], v[96:99], v[68:71], a[4:7]
s_mov_b32 s51, 0
s_add_u32 s52, s43, 14
s_lshl_b32 s47, s31, 1
v_mfma_f32_16x16x32_bf16 a[8:11], v[96:99], v[72:75], a[8:11]
s_sub_u32 s48, 32, s47
ds_read_b64_tr_b16 v[160:161], v216 offset:4096
v_mfma_f32_16x16x32_bf16 a[12:15], v[96:99], v[76:79], a[12:15]
s_sub_u32 s48, s48, s68
s_cmp_eq_u32 s40, 0
s_cselect_b32 s53, s68, s48
v_mfma_f32_16x16x32_bf16 a[16:19], v[96:99], v[80:83], a[16:19]
s_cselect_b32 s54, 0, s68
ds_read_b64_tr_b16 v[162:163], v216 offset:4608
v_mfma_f32_16x16x32_bf16 a[20:23], v[96:99], v[84:87], a[20:23]
s_lshl_b32 s55, s53, 1
s_add_u32 s55, s55, s52
s_add_u32 s47, s47, 2
v_mfma_f32_16x16x32_bf16 a[24:27], v[96:99], v[88:91], a[24:27]
s_cselect_b32 s56, 0, 0
ds_read_b128 v[132:135], v211 offset:2048
v_mfma_f32_16x16x32_bf16 a[28:31], v[96:99], v[92:95], a[28:31]
s_cmp_eq_u32 s40, 0
s_cselect_b32 s56, 0, s47
s_lshl_b32 s57, s56, 1
v_mfma_f32_16x16x32_bf16 a[60:63], v[100:103], v[92:95], a[60:63]
s_add_u32 s57, s57, s55
ds_read_b64_tr_b16 v[164:165], v217 offset:4096
v_cmp_le_u32 vcc, s55, v1
v_mfma_f32_16x16x32_bf16 a[56:59], v[100:103], v[88:91], a[56:59]
v_mov_b32 v224, s52
v_mov_b32 v225, s55
v_mfma_f32_16x16x32_bf16 a[52:55], v[100:103], v[84:87], a[52:55]
ds_read_b64_tr_b16 v[166:167], v217 offset:4608
v_cndmask_b32 v224, v224, v225, vcc
v_mfma_f32_16x16x32_bf16 a[48:51], v[100:103], v[80:83], a[48:51]
v_mov_b32 v225, s53
v_mov_b32 v226, s56
v_mfma_f32_16x16x32_bf16 a[44:47], v[100:103], v[76:79], a[44:47]
ds_read_b128 v[136:139], v211 offset:4096
v_cndmask_b32 v225, v225, v226, vcc
v_mfma_f32_16x16x32_bf16 a[40:43], v[100:103], v[72:75], a[40:43]
v_mov_b32 v226, s54
v_mov_b32 v227, 0
v_mfma_f32_16x16x32_bf16 a[36:39], v[100:103], v[68:71], a[36:39]
ds_read_b64_tr_b16 v[168:169], v218 offset:4096
v_cndmask_b32 v226, v226, v227, vcc
v_mfma_f32_16x16x32_bf16 a[32:35], v[100:103], v[64:67], a[32:35]
v_sub_u32 v224, v1, v224
v_lshlrev_b32 v225, 1, v225
v_mfma_f32_16x16x32_bf16 a[64:67], v[104:107], v[64:67], a[64:67]
ds_read_b64_tr_b16 v[170:171], v218 offset:4608
v_add_u32 v225, -1, v225
v_mfma_f32_16x16x32_bf16 a[68:71], v[104:107], v[68:71], a[68:71]
v_and_b32 v227, 1, v224
v_cmp_eq_u32 s[48:49], 1, v227
v_mfma_f32_16x16x32_bf16 a[72:75], v[104:107], v[72:75], a[72:75]
ds_read_b128 v[140:143], v211 offset:6144
v_cmp_ne_u32 s[58:59], v224, v225
v_mfma_f32_16x16x32_bf16 a[76:79], v[104:107], v[76:79], a[76:79]
v_cmp_eq_u32 s[60:61], 0, v224
v_mfma_f32_16x16x32_bf16 a[80:83], v[104:107], v[80:83], a[80:83]
s_and_b64 s[48:49], s[48:49], s[58:59]
ds_read_b64_tr_b16 v[172:173], v219 offset:4096
v_mfma_f32_16x16x32_bf16 a[84:87], v[104:107], v[84:87], a[84:87]
s_or_b64 s[48:49], s[48:49], s[60:61]
v_add_u32 v227, 1, v224
v_lshrrev_b32 v227, 1, v227
v_mfma_f32_16x16x32_bf16 a[88:91], v[104:107], v[88:91], a[88:91]
ds_read_b64_tr_b16 v[174:175], v219 offset:4608
v_add_u32 v228, -1, v224
v_mfma_f32_16x16x32_bf16 a[92:95], v[104:107], v[92:95], a[92:95]
v_lshrrev_b32 v228, 1, v228
v_cndmask_b32_e64 v227, v228, v227, s[48:49]
v_mfma_f32_16x16x32_bf16 a[124:127], v[108:111], v[92:95], a[124:127]
ds_read_b128 v[144:147], v211 offset:8192
v_add_u32 v227, v226, v227
v_mfma_f32_16x16x32_bf16 a[120:123], v[108:111], v[88:91], a[120:123]
v_and_b32 v227, 31, v227
v_lshlrev_b32 v231, 15, v227
v_mfma_f32_16x16x32_bf16 a[116:119], v[108:111], v[84:87], a[116:119]
ds_read_b64_tr_b16 v[176:177], v216 offset:12288
v_mov_b32 v228, 16777216
v_mfma_f32_16x16x32_bf16 a[112:115], v[108:111], v[80:83], a[112:115]
v_mov_b32 v229, 0
v_cndmask_b32_e64 v228, v228, v229, s[48:49]
v_mfma_f32_16x16x32_bf16 a[108:111], v[108:111], v[76:79], a[108:111]
ds_read_b64_tr_b16 v[178:179], v216 offset:12800
v_add_u32 v231, v231, v228
v_mfma_f32_16x16x32_bf16 a[104:107], v[108:111], v[72:75], a[104:107]
s_lshl_b32 s47, s30, 20
s_add_u32 s47, s47, s33
v_add_u32 v231, s47, v231
v_mfma_f32_16x16x32_bf16 a[100:103], v[108:111], v[68:71], a[100:103]
ds_read_b128 v[148:151], v211 offset:10240
v_mov_b32 v232, s36
v_mfma_f32_16x16x32_bf16 a[96:99], v[108:111], v[64:67], a[96:99]
v_mov_b32 v233, s37
v_mov_b32 v18, 128
v_mfma_f32_16x16x32_bf16 a[128:131], v[112:115], v[64:67], a[128:131]
s_mov_b64 s[62:63], s[80:81]
ds_read_b64_tr_b16 v[180:181], v217 offset:12288
v_mfma_f32_16x16x32_bf16 a[132:135], v[112:115], v[68:71], a[132:135]
s_add_u32 s58, s50, 7
s_mov_b32 s64, s58
s_lshl_b32 s47, s30, 9
v_mfma_f32_16x16x32_bf16 a[136:139], v[112:115], v[72:75], a[136:139]
s_lshl_b32 s65, s28, 7
ds_read_b64_tr_b16 v[182:183], v217 offset:12800
v_mfma_f32_16x16x32_bf16 a[140:143], v[112:115], v[76:79], a[140:143]
s_add_u32 s65, s65, s47
v_cmp_le_u32 s[48:49], s50, v1
v_cmp_gt_u32 s[58:59], s64, v1
v_mfma_f32_16x16x32_bf16 a[144:147], v[112:115], v[80:83], a[144:147]
s_and_b64 s[48:49], s[48:49], s[58:59]
ds_read_b128 v[152:155], v211 offset:12288
v_subrev_u32 v224, s50, v1
v_mfma_f32_16x16x32_bf16 a[148:151], v[112:115], v[84:87], a[148:151]
s_mov_b32 s60, 524288
v_mul_lo_u32 v224, s60, v224
v_add_u32 v224, s65, v224
v_mfma_f32_16x16x32_bf16 a[152:155], v[112:115], v[88:91], a[152:155]
ds_read_b64_tr_b16 v[184:185], v218 offset:12288
v_cndmask_b32_e64 v231, v231, v224, s[48:49]
v_mfma_f32_16x16x32_bf16 a[156:159], v[112:115], v[92:95], a[156:159]
v_mov_b32 v224, s62
v_mov_b32 v225, s63
v_mfma_f32_16x16x32_bf16 a[188:191], v[116:119], v[92:95], a[188:191]
ds_read_b64_tr_b16 v[186:187], v218 offset:12800
v_mov_b32 v226, 8192
v_mfma_f32_16x16x32_bf16 a[184:187], v[116:119], v[88:91], a[184:187]
v_cndmask_b32_e64 v232, v232, v224, s[48:49]
v_cndmask_b32_e64 v233, v233, v225, s[48:49]
v_mfma_f32_16x16x32_bf16 a[180:183], v[116:119], v[84:87], a[180:183]
ds_read_b128 v[156:159], v211 offset:14336
v_cndmask_b32_e64 v18, v18, v226, s[48:49]
v_mfma_f32_16x16x32_bf16 a[176:179], v[116:119], v[80:83], a[176:179]
s_mov_b64 s[62:63], s[88:89]
s_mov_b64 s[66:67], s[90:91]
s_cmp_lt_u32 s28, 2
v_mfma_f32_16x16x32_bf16 a[172:175], v[116:119], v[76:79], a[172:175]
s_cselect_b32 s62, s62, s66
ds_read_b64_tr_b16 v[188:189], v219 offset:12288
v_mfma_f32_16x16x32_bf16 a[168:171], v[116:119], v[72:75], a[168:171]
s_cselect_b32 s63, s63, s67
s_add_u32 s64, s51, s43
s_lshl_b32 s47, s30, 8
v_mfma_f32_16x16x32_bf16 a[164:167], v[116:119], v[68:71], a[164:167]
s_and_b32 s65, s28, 1
ds_read_b64_tr_b16 v[190:191], v219 offset:12800
v_mfma_f32_16x16x32_bf16 a[160:163], v[116:119], v[64:67], a[160:163]
s_lshl_b32 s65, s65, 7
s_add_u32 s65, s65, s47
v_cmp_le_u32 s[48:49], s51, v1
v_mfma_f32_16x16x32_bf16 a[192:195], v[120:123], v[64:67], a[192:195]
v_cmp_gt_u32 s[58:59], s64, v1
v_mfma_f32_16x16x32_bf16 a[196:199], v[120:123], v[68:71], a[196:199]
s_and_b64 s[48:49], s[48:49], s[58:59]
v_subrev_u32 v224, s51, v1
v_mfma_f32_16x16x32_bf16 a[200:203], v[120:123], v[72:75], a[200:203]
s_mov_b32 s60, 229376
v_mul_lo_u32 v224, s60, v224
v_add_u32 v224, s65, v224
v_mfma_f32_16x16x32_bf16 a[204:207], v[120:123], v[76:79], a[204:207]
v_cndmask_b32_e64 v231, v231, v224, s[48:49]
v_mov_b32 v224, s62
v_mfma_f32_16x16x32_bf16 a[208:211], v[120:123], v[80:83], a[208:211]
v_mov_b32 v225, s63
v_mov_b32 v226, 3584
v_mfma_f32_16x16x32_bf16 a[212:215], v[120:123], v[84:87], a[212:215]
v_cndmask_b32_e64 v232, v232, v224, s[48:49]
v_cndmask_b32_e64 v233, v233, v225, s[48:49]
v_mfma_f32_16x16x32_bf16 a[216:219], v[120:123], v[88:91], a[216:219]
v_cndmask_b32_e64 v18, v18, v226, s[48:49]
v_mfma_f32_16x16x32_bf16 a[220:223], v[120:123], v[92:95], a[220:223]
s_mov_b64 s[62:63], s[92:93]
s_add_u32 s51, s43, 7
s_mov_b32 s64, s52
v_mfma_f32_16x16x32_bf16 a[252:255], v[124:127], v[92:95], a[252:255]
s_mul_i32 s65, s28, 57344
s_lshl_b32 s47, s11, 7
s_add_u32 s65, s65, s47
v_mfma_f32_16x16x32_bf16 a[248:251], v[124:127], v[88:91], a[248:251]
v_cmp_le_u32 s[48:49], s51, v1
v_cmp_gt_u32 s[58:59], s64, v1
v_mfma_f32_16x16x32_bf16 a[244:247], v[124:127], v[84:87], a[244:247]
s_and_b64 s[48:49], s[48:49], s[58:59]
v_subrev_u32 v224, s51, v1
v_mfma_f32_16x16x32_bf16 a[240:243], v[124:127], v[80:83], a[240:243]
s_mov_b32 s60, 229376
v_mul_lo_u32 v224, s60, v224
v_add_u32 v224, s65, v224
v_mfma_f32_16x16x32_bf16 a[236:239], v[124:127], v[76:79], a[236:239]
v_cndmask_b32_e64 v231, v231, v224, s[48:49]
v_mov_b32 v224, s62
v_mfma_f32_16x16x32_bf16 a[232:235], v[124:127], v[72:75], a[232:235]
v_mov_b32 v225, s63
v_mov_b32 v226, 896
v_mfma_f32_16x16x32_bf16 a[228:231], v[124:127], v[68:71], a[228:231]
v_cndmask_b32_e64 v232, v232, v224, s[48:49]
v_cndmask_b32_e64 v233, v233, v225, s[48:49]
v_mfma_f32_16x16x32_bf16 a[224:227], v[124:127], v[64:67], a[224:227]
v_cndmask_b32_e64 v18, v18, v226, s[48:49]
s_waitcnt lgkmcnt(0) vmcnt(0)
s_barrier
v_mfma_f32_16x16x32_bf16 a[0:3], v[160:163], v[128:131], a[0:3]
s_mov_b32 s20, 0
v_mfma_f32_16x16x32_bf16 a[4:7], v[160:163], v[132:135], a[4:7]
s_add_u32 m0, s33, 0
v_mfma_f32_16x16x32_bf16 a[8:11], v[160:163], v[136:139], a[8:11]
buffer_load_dwordx4 v14, s[16:19], s20 offen sc1 lds
v_mfma_f32_16x16x32_bf16 a[12:15], v[160:163], v[140:143], a[12:15]
v_mfma_f32_16x16x32_bf16 a[16:19], v[160:163], v[144:147], a[16:19]
s_add_u32 s20, s20, s21
v_mfma_f32_16x16x32_bf16 a[20:23], v[160:163], v[148:151], a[20:23]
s_add_u32 m0, s33, 1024
v_mfma_f32_16x16x32_bf16 a[24:27], v[160:163], v[152:155], a[24:27]
buffer_load_dwordx4 v14, s[16:19], s20 offen sc1 lds
v_mfma_f32_16x16x32_bf16 a[28:31], v[160:163], v[156:159], a[28:31]
v_mfma_f32_16x16x32_bf16 a[60:63], v[164:167], v[156:159], a[60:63]
s_add_u32 s20, s20, s21
v_mfma_f32_16x16x32_bf16 a[56:59], v[164:167], v[152:155], a[56:59]
s_add_u32 m0, s33, 2048
v_mfma_f32_16x16x32_bf16 a[52:55], v[164:167], v[148:151], a[52:55]
buffer_load_dwordx4 v14, s[16:19], s20 offen sc1 lds
v_mfma_f32_16x16x32_bf16 a[48:51], v[164:167], v[144:147], a[48:51]
v_mfma_f32_16x16x32_bf16 a[44:47], v[164:167], v[140:143], a[44:47]
s_add_u32 s20, s20, s21
v_mfma_f32_16x16x32_bf16 a[40:43], v[164:167], v[136:139], a[40:43]
s_add_u32 m0, s33, 3072
v_mfma_f32_16x16x32_bf16 a[36:39], v[164:167], v[132:135], a[36:39]
buffer_load_dwordx4 v14, s[16:19], s20 offen sc1 lds
v_mfma_f32_16x16x32_bf16 a[32:35], v[164:167], v[128:131], a[32:35]
v_mfma_f32_16x16x32_bf16 a[64:67], v[168:171], v[128:131], a[64:67]
s_add_u32 s20, s20, s21
v_mfma_f32_16x16x32_bf16 a[68:71], v[168:171], v[132:135], a[68:71]
s_add_u32 m0, s33, 4096
v_mfma_f32_16x16x32_bf16 a[72:75], v[168:171], v[136:139], a[72:75]
buffer_load_dwordx4 v14, s[16:19], s20 offen sc1 lds
v_mfma_f32_16x16x32_bf16 a[76:79], v[168:171], v[140:143], a[76:79]
v_mfma_f32_16x16x32_bf16 a[80:83], v[168:171], v[144:147], a[80:83]
s_add_u32 s20, s20, s21
v_mfma_f32_16x16x32_bf16 a[84:87], v[168:171], v[148:151], a[84:87]
s_add_u32 m0, s33, 5120
v_mfma_f32_16x16x32_bf16 a[88:91], v[168:171], v[152:155], a[88:91]
buffer_load_dwordx4 v14, s[16:19], s20 offen sc1 lds
v_mfma_f32_16x16x32_bf16 a[92:95], v[168:171], v[156:159], a[92:95]
v_mfma_f32_16x16x32_bf16 a[124:127], v[172:175], v[156:159], a[124:127]
s_add_u32 s20, s20, s21
v_mfma_f32_16x16x32_bf16 a[120:123], v[172:175], v[152:155], a[120:123]
s_add_u32 m0, s33, 6144
v_mfma_f32_16x16x32_bf16 a[116:119], v[172:175], v[148:151], a[116:119]
buffer_load_dwordx4 v14, s[16:19], s20 offen sc1 lds
v_mfma_f32_16x16x32_bf16 a[112:115], v[172:175], v[144:147], a[112:115]
v_mfma_f32_16x16x32_bf16 a[108:111], v[172:175], v[140:143], a[108:111]
s_add_u32 s20, s20, s21
v_mfma_f32_16x16x32_bf16 a[104:107], v[172:175], v[136:139], a[104:107]
s_add_u32 m0, s33, 7168
v_mfma_f32_16x16x32_bf16 a[100:103], v[172:175], v[132:135], a[100:103]
buffer_load_dwordx4 v14, s[16:19], s20 offen sc1 lds
v_mfma_f32_16x16x32_bf16 a[96:99], v[172:175], v[128:131], a[96:99]
v_mfma_f32_16x16x32_bf16 a[128:131], v[176:179], v[128:131], a[128:131]
s_mov_b32 s20, s14
v_mfma_f32_16x16x32_bf16 a[132:135], v[176:179], v[132:135], a[132:135]
s_add_u32 m0, s33, 32768
v_mfma_f32_16x16x32_bf16 a[136:139], v[176:179], v[136:139], a[136:139]
buffer_load_dwordx4 v14, s[16:19], s20 offen sc1 lds
v_mfma_f32_16x16x32_bf16 a[140:143], v[176:179], v[140:143], a[140:143]
v_mfma_f32_16x16x32_bf16 a[144:147], v[176:179], v[144:147], a[144:147]
s_add_u32 s20, s20, s21
v_mfma_f32_16x16x32_bf16 a[148:151], v[176:179], v[148:151], a[148:151]
s_add_u32 m0, s33, 33792
v_mfma_f32_16x16x32_bf16 a[152:155], v[176:179], v[152:155], a[152:155]
buffer_load_dwordx4 v14, s[16:19], s20 offen sc1 lds
v_mfma_f32_16x16x32_bf16 a[156:159], v[176:179], v[156:159], a[156:159]
v_mfma_f32_16x16x32_bf16 a[188:191], v[180:183], v[156:159], a[188:191]
s_add_u32 s20, s20, s21
v_mfma_f32_16x16x32_bf16 a[184:187], v[180:183], v[152:155], a[184:187]
s_add_u32 m0, s33, 34816
v_mfma_f32_16x16x32_bf16 a[180:183], v[180:183], v[148:151], a[180:183]
buffer_load_dwordx4 v14, s[16:19], s20 offen sc1 lds
v_mfma_f32_16x16x32_bf16 a[176:179], v[180:183], v[144:147], a[176:179]
v_mfma_f32_16x16x32_bf16 a[172:175], v[180:183], v[140:143], a[172:175]
s_add_u32 s20, s20, s21
v_mfma_f32_16x16x32_bf16 a[168:171], v[180:183], v[136:139], a[168:171]
s_add_u32 m0, s33, 35840
v_mfma_f32_16x16x32_bf16 a[164:167], v[180:183], v[132:135], a[164:167]
buffer_load_dwordx4 v14, s[16:19], s20 offen sc1 lds
v_mfma_f32_16x16x32_bf16 a[160:163], v[180:183], v[128:131], a[160:163]
v_mfma_f32_16x16x32_bf16 a[192:195], v[184:187], v[128:131], a[192:195]
s_add_u32 s20, s20, s21
v_mfma_f32_16x16x32_bf16 a[196:199], v[184:187], v[132:135], a[196:199]
s_add_u32 m0, s33, 36864
v_mfma_f32_16x16x32_bf16 a[200:203], v[184:187], v[136:139], a[200:203]
buffer_load_dwordx4 v14, s[16:19], s20 offen sc1 lds
v_mfma_f32_16x16x32_bf16 a[204:207], v[184:187], v[140:143], a[204:207]
v_mfma_f32_16x16x32_bf16 a[208:211], v[184:187], v[144:147], a[208:211]
s_add_u32 s20, s20, s21
v_mfma_f32_16x16x32_bf16 a[212:215], v[184:187], v[148:151], a[212:215]
s_add_u32 m0, s33, 37888
v_mfma_f32_16x16x32_bf16 a[216:219], v[184:187], v[152:155], a[216:219]
buffer_load_dwordx4 v14, s[16:19], s20 offen sc1 lds
v_mfma_f32_16x16x32_bf16 a[220:223], v[184:187], v[156:159], a[220:223]
v_mfma_f32_16x16x32_bf16 a[252:255], v[188:191], v[156:159], a[252:255]
s_add_u32 s20, s20, s21
v_mfma_f32_16x16x32_bf16 a[248:251], v[188:191], v[152:155], a[248:251]
s_add_u32 m0, s33, 38912
v_mfma_f32_16x16x32_bf16 a[244:247], v[188:191], v[148:151], a[244:247]
buffer_load_dwordx4 v14, s[16:19], s20 offen sc1 lds
v_mfma_f32_16x16x32_bf16 a[240:243], v[188:191], v[144:147], a[240:243]
s_add_u32 s20, s20, s21
v_mfma_f32_16x16x32_bf16 a[236:239], v[188:191], v[140:143], a[236:239]
s_add_u32 m0, s33, 39936
v_mfma_f32_16x16x32_bf16 a[232:235], v[188:191], v[136:139], a[232:235]
buffer_load_dwordx4 v14, s[16:19], s20 offen sc1 lds
v_mfma_f32_16x16x32_bf16 a[228:231], v[188:191], v[132:135], a[228:231]
v_mfma_f32_16x16x32_bf16 a[224:227], v[188:191], v[128:131], a[224:227]
s_branch SEAM_%=
K1V_%=:
s_mov_b32 s34, 14
v_mfma_f32_16x16x32_bf16 a[0:3], v[64:67], v[96:99], 0
s_add_u32 m0, s62, 0
ds_read_b128 v[128:131], v211 offset:0
v_readfirstlane_b32 s98, v13
v_mfma_f32_16x16x32_bf16 a[4:7], v[68:71], v[96:99], 0
buffer_load_dwordx4 v192, s[16:19], s30 offen lds
v_mfma_f32_16x16x32_bf16 a[8:11], v[72:75], v[96:99], 0
ds_read_b64_tr_b16 v[160:161], v216 offset:4096
v_mov_b32 v249, 0x7fffffff
v_mfma_f32_16x16x32_bf16 a[12:15], v[76:79], v[96:99], 0
v_cmp_eq_u32 vcc, 0, v1
v_mfma_f32_16x16x32_bf16 a[16:19], v[80:83], v[96:99], 0
s_add_u32 s98, s98, 1
ds_read_b64_tr_b16 v[162:163], v216 offset:4608
v_mfma_f32_16x16x32_bf16 a[20:23], v[84:87], v[96:99], 0
s_nop 0
v_cndmask_b32_e64 v249, v249, 0, vcc
v_mov_b32 v248, s98
v_mfma_f32_16x16x32_bf16 a[24:27], v[88:91], v[96:99], 0
ds_read_b128 v[132:135], v211 offset:2048
v_lshlrev_b32 v14, 16, v2
v_mfma_f32_16x16x32_bf16 a[28:31], v[92:95], v[96:99], 0
v_and_b32 v15, 0xffff0000, v2
v_lshlrev_b32 v16, 16, v3
v_mfma_f32_16x16x32_bf16 a[60:63], v[92:95], v[100:103], 0
s_add_u32 m0, s62, 1024
ds_read_b64_tr_b16 v[164:165], v217 offset:4096
v_and_b32 v17, 0xffff0000, v3
v_mfma_f32_16x16x32_bf16 a[56:59], v[88:91], v[100:103], 0
buffer_load_dwordx4 v193, s[16:19], s30 offen lds
v_mfma_f32_16x16x32_bf16 a[52:55], v[84:87], v[100:103], 0
ds_read_b64_tr_b16 v[166:167], v217 offset:4608
v_lshlrev_b32 v18, 16, v4
v_mfma_f32_16x16x32_bf16 a[48:51], v[80:83], v[100:103], 0
v_and_b32 v19, 0xffff0000, v4
v_lshlrev_b32 v20, 16, v5
v_mfma_f32_16x16x32_bf16 a[44:47], v[76:79], v[100:103], 0
ds_read_b128 v[136:139], v211 offset:4096
v_and_b32 v21, 0xffff0000, v5
v_mfma_f32_16x16x32_bf16 a[40:43], v[72:75], v[100:103], 0
v_lshlrev_b32 v22, 16, v6
v_and_b32 v23, 0xffff0000, v6
v_mfma_f32_16x16x32_bf16 a[36:39], v[68:71], v[100:103], 0
ds_read_b64_tr_b16 v[168:169], v218 offset:4096
v_lshlrev_b32 v24, 16, v7
v_mfma_f32_16x16x32_bf16 a[32:35], v[64:67], v[100:103], 0
v_and_b32 v25, 0xffff0000, v7
v_lshlrev_b32 v26, 16, v8
v_mfma_f32_16x16x32_bf16 a[64:67], v[64:67], v[104:107], 0
s_add_u32 m0, s62, 2048
ds_read_b64_tr_b16 v[170:171], v218 offset:4608
v_and_b32 v27, 0xffff0000, v8
v_mfma_f32_16x16x32_bf16 a[68:71], v[68:71], v[104:107], 0
buffer_load_dwordx4 v194, s[16:19], s30 offen lds
v_mfma_f32_16x16x32_bf16 a[72:75], v[72:75], v[104:107], 0
ds_read_b128 v[140:143], v211 offset:6144
v_lshlrev_b32 v28, 16, v9
v_mfma_f32_16x16x32_bf16 a[76:79], v[76:79], v[104:107], 0
v_and_b32 v29, 0xffff0000, v9
v_add_f32 v30, v14, v15
v_mfma_f32_16x16x32_bf16 a[80:83], v[80:83], v[104:107], 0
ds_read_b64_tr_b16 v[172:173], v219 offset:4096
v_add_f32 v31, v16, v17
v_mfma_f32_16x16x32_bf16 a[84:87], v[84:87], v[104:107], 0
v_add_f32 v32, v18, v19
v_add_f32 v33, v20, v21
v_mfma_f32_16x16x32_bf16 a[88:91], v[88:91], v[104:107], 0
ds_read_b64_tr_b16 v[174:175], v219 offset:4608
v_add_f32 v34, v22, v23
v_mfma_f32_16x16x32_bf16 a[92:95], v[92:95], v[104:107], 0
v_add_f32 v35, v24, v25
v_add_f32 v36, v26, v27
v_mfma_f32_16x16x32_bf16 a[124:127], v[92:95], v[108:111], 0
s_add_u32 m0, s62, 3072
ds_read_b128 v[144:147], v211 offset:8192
v_add_f32 v37, v28, v29
v_mfma_f32_16x16x32_bf16 a[120:123], v[88:91], v[108:111], 0
buffer_load_dwordx4 v195, s[16:19], s30 offen lds
v_mfma_f32_16x16x32_bf16 a[116:119], v[84:87], v[108:111], 0
ds_read_b64_tr_b16 v[176:177], v216 offset:12288
v_add_f32 v30, v30, v31
v_mfma_f32_16x16x32_bf16 a[112:115], v[80:83], v[108:111], 0
v_add_f32 v32, v32, v33
v_add_f32 v34, v34, v35
v_mfma_f32_16x16x32_bf16 a[108:111], v[76:79], v[108:111], 0
ds_read_b64_tr_b16 v[178:179], v216 offset:12800
v_add_f32 v36, v36, v37
v_mfma_f32_16x16x32_bf16 a[104:107], v[72:75], v[108:111], 0
v_add_f32 v30, v30, v32
v_add_f32 v34, v34, v36
v_mfma_f32_16x16x32_bf16 a[100:103], v[68:71], v[108:111], 0
ds_read_b128 v[148:151], v211 offset:10240
v_add_f32 v30, v30, v34
v_mfma_f32_16x16x32_bf16 a[96:99], v[64:67], v[108:111], 0
s_nop 1
v_add_f32_dpp v30, v30, v30 row_ror:8 row_mask:0xf bank_mask:0xf
v_mfma_f32_16x16x32_bf16 a[128:131], v[64:67], v[112:115], 0
s_add_u32 m0, s62, 4096
ds_read_b64_tr_b16 v[180:181], v217 offset:12288
v_mfma_f32_16x16x32_bf16 a[132:135], v[68:71], v[112:115], 0
buffer_load_dwordx4 v196, s[16:19], s30 offen lds
v_mfma_f32_16x16x32_bf16 a[136:139], v[72:75], v[112:115], 0
s_nop 1
ds_read_b64_tr_b16 v[182:183], v217 offset:12800
v_add_f32_dpp v30, v30, v30 row_ror:4 row_mask:0xf bank_mask:0xf
v_mfma_f32_16x16x32_bf16 a[140:143], v[76:79], v[112:115], 0
s_nop 1
v_add_f32_dpp v30, v30, v30 row_ror:2 row_mask:0xf bank_mask:0xf
v_mfma_f32_16x16x32_bf16 a[144:147], v[80:83], v[112:115], 0
s_nop 1
ds_read_b128 v[152:155], v211 offset:12288
v_add_f32_dpp v30, v30, v30 row_ror:1 row_mask:0xf bank_mask:0xf
v_mfma_f32_16x16x32_bf16 a[148:151], v[84:87], v[112:115], 0
v_mov_b32 v31, v30
v_mfma_f32_16x16x32_bf16 a[152:155], v[88:91], v[112:115], 0
s_nop 1
ds_read_b64_tr_b16 v[184:185], v218 offset:12288
v_permlane16_swap_b32 v30, v31
v_mfma_f32_16x16x32_bf16 a[156:159], v[92:95], v[112:115], 0
s_nop 1
v_add_f32 v30, v30, v31
v_mul_f32 v30, 0x3b124925, v30
v_mfma_f32_16x16x32_bf16 a[188:191], v[92:95], v[116:119], 0
s_add_u32 m0, s62, 5120
ds_read_b64_tr_b16 v[186:187], v218 offset:12800
v_sub_f32 v34, v14, v30
v_mfma_f32_16x16x32_bf16 a[184:187], v[88:91], v[116:119], 0
buffer_load_dwordx4 v197, s[16:19], s30 offen lds
v_mfma_f32_16x16x32_bf16 a[180:183], v[84:87], v[116:119], 0
ds_read_b128 v[156:159], v211 offset:14336
v_sub_f32 v35, v15, v30
v_mfma_f32_16x16x32_bf16 a[176:179], v[80:83], v[116:119], 0
v_sub_f32 v36, v16, v30
v_sub_f32 v37, v17, v30
v_mfma_f32_16x16x32_bf16 a[172:175], v[76:79], v[116:119], 0
ds_read_b64_tr_b16 v[188:189], v219 offset:12288
v_sub_f32 v38, v18, v30
v_mfma_f32_16x16x32_bf16 a[168:171], v[72:75], v[116:119], 0
v_sub_f32 v39, v19, v30
v_sub_f32 v40, v20, v30
v_mfma_f32_16x16x32_bf16 a[164:167], v[68:71], v[116:119], 0
ds_read_b64_tr_b16 v[190:191], v219 offset:12800
v_sub_f32 v41, v21, v30
v_mfma_f32_16x16x32_bf16 a[160:163], v[64:67], v[116:119], 0
v_sub_f32 v42, v22, v30
v_sub_f32 v43, v23, v30
v_mfma_f32_16x16x32_bf16 a[192:195], v[64:67], v[120:123], 0
s_add_u32 m0, s62, 6144
v_mfma_f32_16x16x32_bf16 a[196:199], v[68:71], v[120:123], 0
buffer_load_dwordx4 v198, s[16:19], s30 offen lds
v_mfma_f32_16x16x32_bf16 a[200:203], v[72:75], v[120:123], 0
s_add_u32 s61, s60, 65536
v_mfma_f32_16x16x32_bf16 a[204:207], v[76:79], v[120:123], 0
s_sub_u32 s35, s61, 163840
v_mfma_f32_16x16x32_bf16 a[208:211], v[80:83], v[120:123], 0
s_cselect_b32 s61, s61, s35
v_mfma_f32_16x16x32_bf16 a[212:215], v[84:87], v[120:123], 0
s_add_u32 s64, s61, 32768
v_mfma_f32_16x16x32_bf16 a[216:219], v[88:91], v[120:123], 0
s_sub_u32 s35, s64, 163840
v_mfma_f32_16x16x32_bf16 a[220:223], v[92:95], v[120:123], 0
s_cselect_b32 s64, s64, s35
v_mfma_f32_16x16x32_bf16 a[252:255], v[92:95], v[124:127], 0
s_add_u32 m0, s62, 7168
v_mfma_f32_16x16x32_bf16 a[248:251], v[88:91], v[124:127], 0
buffer_load_dwordx4 v199, s[16:19], s30 offen lds
v_mfma_f32_16x16x32_bf16 a[244:247], v[84:87], v[124:127], 0
s_add_u32 s63, s60, s33
v_mfma_f32_16x16x32_bf16 a[240:243], v[80:83], v[124:127], 0
s_add_u32 s30, s30, 128
v_mfma_f32_16x16x32_bf16 a[236:239], v[76:79], v[124:127], 0
v_add_u32 v210, s61, v208
v_mfma_f32_16x16x32_bf16 a[232:235], v[72:75], v[124:127], 0
v_add_u32 v216, s64, v212
v_add_u32 v217, s64, v213
v_mfma_f32_16x16x32_bf16 a[228:231], v[68:71], v[124:127], 0
v_add_u32 v218, s64, v214
v_add_u32 v219, s64, v215
v_mfma_f32_16x16x32_bf16 a[224:227], v[64:67], v[124:127], 0
s_waitcnt lgkmcnt(0) vmcnt(8)
s_barrier
v_mfma_f32_16x16x32_bf16 a[0:3], v[128:131], v[160:163], a[0:3]
s_add_u32 m0, s63, 0
ds_read_b128 v[64:67], v210 offset:0
v_sub_f32 v44, v24, v30
v_mfma_f32_16x16x32_bf16 a[4:7], v[132:135], v[160:163], a[4:7]
buffer_load_dwordx4 v200, s[20:23], s31 offen lds
v_mfma_f32_16x16x32_bf16 a[8:11], v[136:139], v[160:163], a[8:11]
ds_read_b64_tr_b16 v[96:97], v216 offset:0
v_sub_f32 v45, v25, v30
v_mfma_f32_16x16x32_bf16 a[12:15], v[140:143], v[160:163], a[12:15]
v_sub_f32 v46, v26, v30
v_sub_f32 v47, v27, v30
v_mfma_f32_16x16x32_bf16 a[16:19], v[144:147], v[160:163], a[16:19]
ds_read_b64_tr_b16 v[98:99], v216 offset:512
v_sub_f32 v48, v28, v30
v_mfma_f32_16x16x32_bf16 a[20:23], v[148:151], v[160:163], a[20:23]
v_sub_f32 v49, v29, v30
v_mul_f32 v42, v12, v42
v_mfma_f32_16x16x32_bf16 a[24:27], v[152:155], v[160:163], a[24:27]
ds_read_b128 v[68:71], v210 offset:2048
v_mul_f32 v43, v12, v43
v_mfma_f32_16x16x32_bf16 a[28:31], v[156:159], v[160:163], a[28:31]
v_mul_f32 v44, v12, v44
v_mul_f32 v45, v12, v45
v_mfma_f32_16x16x32_bf16 a[60:63], v[156:159], v[164:167], a[60:63]
s_add_u32 m0, s63, 1024
ds_read_b64_tr_b16 v[100:101], v217 offset:0
v_mul_f32 v46, v12, v46
v_mfma_f32_16x16x32_bf16 a[56:59], v[152:155], v[164:167], a[56:59]
buffer_load_dwordx4 v201, s[20:23], s31 offen lds
v_mfma_f32_16x16x32_bf16 a[52:55], v[148:151], v[164:167], a[52:55]
s_cmp_ge_u32 s34, 4
ds_read_b64_tr_b16 v[102:103], v217 offset:512
v_mul_f32 v47, v12, v47
v_mfma_f32_16x16x32_bf16 a[48:51], v[144:147], v[164:167], a[48:51]
s_cselect_b32 s66, -1, 0
v_mul_f32 v48, v12, v48
v_mul_f32 v49, v12, v49
v_mfma_f32_16x16x32_bf16 a[44:47], v[140:143], v[164:167], a[44:47]
ds_read_b128 v[72:75], v210 offset:4096
v_mul_f32 v31, v34, v34
v_mfma_f32_16x16x32_bf16 a[40:43], v[136:139], v[164:167], a[40:43]
v_fmac_f32 v31, v35, v35
v_fmac_f32 v31, v36, v36
v_mfma_f32_16x16x32_bf16 a[36:39], v[132:135], v[164:167], a[36:39]
ds_read_b64_tr_b16 v[104:105], v218 offset:0
v_fmac_f32 v31, v37, v37
v_mfma_f32_16x16x32_bf16 a[32:35], v[128:131], v[164:167], a[32:35]
v_fmac_f32 v31, v38, v38
v_fmac_f32 v31, v39, v39
v_mfma_f32_16x16x32_bf16 a[64:67], v[128:131], v[168:171], a[64:67]
s_add_u32 m0, s63, 2048
ds_read_b64_tr_b16 v[106:107], v218 offset:512
v_fmac_f32 v31, v40, v40
v_mfma_f32_16x16x32_bf16 a[68:71], v[132:135], v[168:171], a[68:71]
buffer_load_dwordx4 v202, s[20:23], s31 offen lds
v_mfma_f32_16x16x32_bf16 a[72:75], v[136:139], v[168:171], a[72:75]
ds_read_b128 v[76:79], v210 offset:6144
v_fmac_f32 v31, v41, v41
v_mfma_f32_16x16x32_bf16 a[76:79], v[140:143], v[168:171], a[76:79]
v_fmac_f32 v31, v42, v42
v_fmac_f32 v31, v43, v43
v_mfma_f32_16x16x32_bf16 a[80:83], v[144:147], v[168:171], a[80:83]
ds_read_b64_tr_b16 v[108:109], v219 offset:0
v_readlane_b32 s44, v58, s34
v_mfma_f32_16x16x32_bf16 a[84:87], v[148:151], v[168:171], a[84:87]
v_readlane_b32 s45, v59, s34
v_fmac_f32 v31, v44, v44
v_mfma_f32_16x16x32_bf16 a[88:91], v[152:155], v[168:171], a[88:91]
ds_read_b64_tr_b16 v[110:111], v219 offset:512
v_fmac_f32 v31, v45, v45
v_mfma_f32_16x16x32_bf16 a[92:95], v[156:159], v[168:171], a[92:95]
v_fmac_f32 v31, v46, v46
v_fmac_f32 v31, v47, v47
v_mfma_f32_16x16x32_bf16 a[124:127], v[156:159], v[172:175], a[124:127]
s_add_u32 m0, s63, 3072
ds_read_b128 v[80:83], v210 offset:8192
v_fmac_f32 v31, v48, v48
v_mfma_f32_16x16x32_bf16 a[120:123], v[152:155], v[172:175], a[120:123]
buffer_load_dwordx4 v203, s[20:23], s31 offen lds
v_mfma_f32_16x16x32_bf16 a[116:119], v[148:151], v[172:175], a[116:119]
ds_read_b64_tr_b16 v[112:113], v216 offset:8192
v_fmac_f32 v31, v49, v49
v_mfma_f32_16x16x32_bf16 a[112:115], v[144:147], v[172:175], a[112:115]
s_nop 1
v_add_f32_dpp v31, v31, v31 row_ror:8 row_mask:0xf bank_mask:0xf
v_mfma_f32_16x16x32_bf16 a[108:111], v[140:143], v[172:175], a[108:111]
s_nop 1
ds_read_b64_tr_b16 v[114:115], v216 offset:8704
v_add_f32_dpp v31, v31, v31 row_ror:4 row_mask:0xf bank_mask:0xf
v_mfma_f32_16x16x32_bf16 a[104:107], v[136:139], v[172:175], a[104:107]
s_nop 1
v_add_f32_dpp v31, v31, v31 row_ror:2 row_mask:0xf bank_mask:0xf
v_mfma_f32_16x16x32_bf16 a[100:103], v[132:135], v[172:175], a[100:103]
s_nop 1
ds_read_b128 v[84:87], v210 offset:10240
v_add_f32_dpp v31, v31, v31 row_ror:1 row_mask:0xf bank_mask:0xf
v_mfma_f32_16x16x32_bf16 a[96:99], v[128:131], v[172:175], a[96:99]
v_mov_b32 v32, v31
v_mfma_f32_16x16x32_bf16 a[128:131], v[128:131], v[176:179], a[128:131]
s_add_u32 m0, s63, 4096
ds_read_b64_tr_b16 v[116:117], v217 offset:8192
v_mfma_f32_16x16x32_bf16 a[132:135], v[132:135], v[176:179], a[132:135]
buffer_load_dwordx4 v204, s[20:23], s31 offen lds
v_mfma_f32_16x16x32_bf16 a[136:139], v[136:139], v[176:179], a[136:139]
s_nop 1
ds_read_b64_tr_b16 v[118:119], v217 offset:8704
v_permlane16_swap_b32 v31, v32
v_mfma_f32_16x16x32_bf16 a[140:143], v[140:143], v[176:179], a[140:143]
s_nop 1
v_add_f32 v31, v31, v32
v_mul_f32 v31, 0x3b124925, v31
v_mfma_f32_16x16x32_bf16 a[144:147], v[144:147], v[176:179], a[144:147]
ds_read_b128 v[88:91], v210 offset:12288
v_cvt_pk_bf16_f32 v32, v30, v31
v_mfma_f32_16x16x32_bf16 a[148:151], v[148:151], v[176:179], a[148:151]
v_and_b32 v31, 0xffff0000, v32
v_lshlrev_b32 v30, 16, v32
v_mfma_f32_16x16x32_bf16 a[152:155], v[152:155], v[176:179], a[152:155]
ds_read_b64_tr_b16 v[120:121], v218 offset:8192
v_add_f32 v31, 0x3727c5ac, v31
v_mfma_f32_16x16x32_bf16 a[156:159], v[156:159], v[176:179], a[156:159]
v_cvt_pk_bf16_f32 v31, v31, v31
v_and_b32 v31, 0xffff0000, v31
v_mfma_f32_16x16x32_bf16 a[188:191], v[156:159], v[180:183], a[188:191]
s_add_u32 m0, s63, 5120
ds_read_b64_tr_b16 v[122:123], v218 offset:8704
v_mfma_f32_16x16x32_bf16 a[184:187], v[152:155], v[180:183], a[184:187]
buffer_load_dwordx4 v205, s[20:23], s31 offen lds
v_mfma_f32_16x16x32_bf16 a[180:183], v[148:151], v[180:183], a[180:183]
ds_read_b128 v[92:95], v210 offset:14336
v_mfma_f32_16x16x32_bf16 a[176:179], v[144:147], v[180:183], a[176:179]
v_sqrt_f32 v31, v31
v_mfma_f32_16x16x32_bf16 a[172:175], v[140:143], v[180:183], a[172:175]
s_nop 0
ds_read_b64_tr_b16 v[124:125], v219 offset:8192
v_cvt_pk_bf16_f32 v31, v31, v31
v_mfma_f32_16x16x32_bf16 a[168:171], v[136:139], v[180:183], a[168:171]
v_and_b32 v31, 0xffff0000, v31
v_mfma_f32_16x16x32_bf16 a[164:167], v[132:135], v[180:183], a[164:167]
ds_read_b64_tr_b16 v[126:127], v219 offset:8704
v_mfma_f32_16x16x32_bf16 a[160:163], v[128:131], v[180:183], a[160:163]
v_rcp_f32 v31, v31
v_mfma_f32_16x16x32_bf16 a[192:195], v[128:131], v[184:187], a[192:195]
s_add_u32 m0, s63, 6144
v_mfma_f32_16x16x32_bf16 a[196:199], v[132:135], v[184:187], a[196:199]
buffer_load_dwordx4 v206, s[20:23], s31 offen lds
v_mfma_f32_16x16x32_bf16 a[200:203], v[136:139], v[184:187], a[200:203]
s_mov_b32 s60, s61
v_mfma_f32_16x16x32_bf16 a[204:207], v[140:143], v[184:187], a[204:207]
s_add_u32 s62, s61, 131072
v_mfma_f32_16x16x32_bf16 a[208:211], v[144:147], v[184:187], a[208:211]
s_sub_u32 s35, s62, 163840
v_mfma_f32_16x16x32_bf16 a[212:215], v[148:151], v[184:187], a[212:215]
s_cselect_b32 s62, s62, s35
v_mfma_f32_16x16x32_bf16 a[216:219], v[152:155], v[184:187], a[216:219]
s_add_u32 s62, s62, s33
v_mfma_f32_16x16x32_bf16 a[220:223], v[156:159], v[184:187], a[220:223]
v_mfma_f32_16x16x32_bf16 a[252:255], v[156:159], v[188:191], a[252:255]
s_add_u32 m0, s63, 7168
v_mfma_f32_16x16x32_bf16 a[248:251], v[152:155], v[188:191], a[248:251]
buffer_load_dwordx4 v207, s[20:23], s31 offen lds
v_mfma_f32_16x16x32_bf16 a[244:247], v[148:151], v[188:191], a[244:247]
v_mfma_f32_16x16x32_bf16 a[240:243], v[144:147], v[188:191], a[240:243]
s_add_u32 s31, s31, 524288
v_mfma_f32_16x16x32_bf16 a[236:239], v[140:143], v[188:191], a[236:239]
v_add_u32 v211, s61, v209
v_mfma_f32_16x16x32_bf16 a[232:235], v[136:139], v[188:191], a[232:235]
buffer_store_dword v248, v249, s[52:55], s56 offen sc1
v_mfma_f32_16x16x32_bf16 a[228:231], v[132:135], v[188:191], a[228:231]
global_load_dword v62, v[52:53], off
v_mfma_f32_16x16x32_bf16 a[224:227], v[128:131], v[188:191], a[224:227]
buffer_load_dword v61, v60, s[44:47], 0 offen
v_sub_f32 v34, v14, v30
v_sub_f32 v35, v15, v30
v_sub_f32 v36, v16, v30
v_sub_f32 v37, v17, v30
v_sub_f32 v38, v18, v30
v_sub_f32 v39, v19, v30
v_sub_f32 v40, v20, v30
v_sub_f32 v41, v21, v30
v_sub_f32 v42, v22, v30
v_sub_f32 v43, v23, v30
v_sub_f32 v44, v24, v30
v_sub_f32 v45, v25, v30
v_sub_f32 v46, v26, v30
v_sub_f32 v47, v27, v30
v_sub_f32 v48, v28, v30
v_sub_f32 v49, v29, v30
v_cvt_pk_bf16_f32 v2, v34, v35
v_cvt_pk_bf16_f32 v3, v36, v37
v_cvt_pk_bf16_f32 v4, v38, v39
v_cvt_pk_bf16_f32 v5, v40, v41
v_cvt_pk_bf16_f32 v6, v42, v43
v_cvt_pk_bf16_f32 v7, v44, v45
v_cvt_pk_bf16_f32 v8, v46, v47
v_cvt_pk_bf16_f32 v9, v48, v49
v_lshlrev_b32 v14, 16, v2
v_and_b32 v15, 0xffff0000, v2
v_lshlrev_b32 v16, 16, v3
v_and_b32 v17, 0xffff0000, v3
v_lshlrev_b32 v18, 16, v4
v_and_b32 v19, 0xffff0000, v4
v_lshlrev_b32 v20, 16, v5
v_and_b32 v21, 0xffff0000, v5
v_lshlrev_b32 v22, 16, v6
v_and_b32 v23, 0xffff0000, v6
v_lshlrev_b32 v24, 16, v7
v_and_b32 v25, 0xffff0000, v7
v_lshlrev_b32 v26, 16, v8
v_and_b32 v27, 0xffff0000, v8
v_lshlrev_b32 v28, 16, v9
v_and_b32 v29, 0xffff0000, v9
v_mul_f32 v14, v31, v14
v_mul_f32 v15, v31, v15
v_mul_f32 v16, v31, v16
v_mul_f32 v17, v31, v17
v_mul_f32 v18, v31, v18
v_mul_f32 v19, v31, v19
v_mul_f32 v20, v31, v20
v_mul_f32 v21, v31, v21
v_mul_f32 v22, v31, v22
v_mul_f32 v23, v31, v23
v_mul_f32 v24, v31, v24
v_mul_f32 v25, v31, v25
v_mul_f32 v26, v31, v26
v_mul_f32 v27, v31, v27
v_mul_f32 v28, v31, v28
v_mul_f32 v29, v31, v29
v_cvt_pk_bf16_f32 v2, v14, v15
v_cvt_pk_bf16_f32 v3, v16, v17
v_cvt_pk_bf16_f32 v4, v18, v19
v_cvt_pk_bf16_f32 v5, v20, v21
v_cvt_pk_bf16_f32 v6, v22, v23
v_cvt_pk_bf16_f32 v7, v24, v25
v_cvt_pk_bf16_f32 v8, v26, v27
v_cvt_pk_bf16_f32 v9, v28, v29
buffer_store_dwordx4 v[2:5], v10, s[52:55], s65 offen sc1
buffer_store_dwordx4 v[6:9], v11, s[52:55], s65 offen sc1
s_waitcnt lgkmcnt(0)
s_mov_b32 s34, 13
K1L1_%=:
v_mfma_f32_16x16x32_bf16 a[0:3], v[64:67], v[96:99], a[0:3]
s_add_u32 m0, s62, 0
ds_read_b128 v[128:131], v211 offset:0
v_mfma_f32_16x16x32_bf16 a[4:7], v[68:71], v[96:99], a[4:7]
buffer_load_dwordx4 v192, s[16:19], s30 offen lds
v_mfma_f32_16x16x32_bf16 a[8:11], v[72:75], v[96:99], a[8:11]
ds_read_b64_tr_b16 v[160:161], v216 offset:4096
v_mfma_f32_16x16x32_bf16 a[12:15], v[76:79], v[96:99], a[12:15]
v_mfma_f32_16x16x32_bf16 a[16:19], v[80:83], v[96:99], a[16:19]
ds_read_b64_tr_b16 v[162:163], v216 offset:4608
v_mfma_f32_16x16x32_bf16 a[20:23], v[84:87], v[96:99], a[20:23]
v_mfma_f32_16x16x32_bf16 a[24:27], v[88:91], v[96:99], a[24:27]
ds_read_b128 v[132:135], v211 offset:2048
v_mfma_f32_16x16x32_bf16 a[28:31], v[92:95], v[96:99], a[28:31]
v_mfma_f32_16x16x32_bf16 a[60:63], v[92:95], v[100:103], a[60:63]
s_add_u32 m0, s62, 1024
ds_read_b64_tr_b16 v[164:165], v217 offset:4096
v_mfma_f32_16x16x32_bf16 a[56:59], v[88:91], v[100:103], a[56:59]
buffer_load_dwordx4 v193, s[16:19], s30 offen lds
v_mfma_f32_16x16x32_bf16 a[52:55], v[84:87], v[100:103], a[52:55]
ds_read_b64_tr_b16 v[166:167], v217 offset:4608
v_mfma_f32_16x16x32_bf16 a[48:51], v[80:83], v[100:103], a[48:51]
v_mfma_f32_16x16x32_bf16 a[44:47], v[76:79], v[100:103], a[44:47]
ds_read_b128 v[136:139], v211 offset:4096
v_mfma_f32_16x16x32_bf16 a[40:43], v[72:75], v[100:103], a[40:43]
v_mfma_f32_16x16x32_bf16 a[36:39], v[68:71], v[100:103], a[36:39]
ds_read_b64_tr_b16 v[168:169], v218 offset:4096
v_mfma_f32_16x16x32_bf16 a[32:35], v[64:67], v[100:103], a[32:35]
v_mfma_f32_16x16x32_bf16 a[64:67], v[64:67], v[104:107], a[64:67]
s_add_u32 m0, s62, 2048
ds_read_b64_tr_b16 v[170:171], v218 offset:4608
v_mfma_f32_16x16x32_bf16 a[68:71], v[68:71], v[104:107], a[68:71]
buffer_load_dwordx4 v194, s[16:19], s30 offen lds
v_mfma_f32_16x16x32_bf16 a[72:75], v[72:75], v[104:107], a[72:75]
ds_read_b128 v[140:143], v211 offset:6144
v_mfma_f32_16x16x32_bf16 a[76:79], v[76:79], v[104:107], a[76:79]
v_mfma_f32_16x16x32_bf16 a[80:83], v[80:83], v[104:107], a[80:83]
ds_read_b64_tr_b16 v[172:173], v219 offset:4096
v_mfma_f32_16x16x32_bf16 a[84:87], v[84:87], v[104:107], a[84:87]
v_mfma_f32_16x16x32_bf16 a[88:91], v[88:91], v[104:107], a[88:91]
ds_read_b64_tr_b16 v[174:175], v219 offset:4608
v_mfma_f32_16x16x32_bf16 a[92:95], v[92:95], v[104:107], a[92:95]
v_mfma_f32_16x16x32_bf16 a[124:127], v[92:95], v[108:111], a[124:127]
s_add_u32 m0, s62, 3072
ds_read_b128 v[144:147], v211 offset:8192
v_mfma_f32_16x16x32_bf16 a[120:123], v[88:91], v[108:111], a[120:123]
buffer_load_dwordx4 v195, s[16:19], s30 offen lds
v_mfma_f32_16x16x32_bf16 a[116:119], v[84:87], v[108:111], a[116:119]
ds_read_b64_tr_b16 v[176:177], v216 offset:12288
v_mfma_f32_16x16x32_bf16 a[112:115], v[80:83], v[108:111], a[112:115]
v_mfma_f32_16x16x32_bf16 a[108:111], v[76:79], v[108:111], a[108:111]
ds_read_b64_tr_b16 v[178:179], v216 offset:12800
v_mfma_f32_16x16x32_bf16 a[104:107], v[72:75], v[108:111], a[104:107]
v_mfma_f32_16x16x32_bf16 a[100:103], v[68:71], v[108:111], a[100:103]
ds_read_b128 v[148:151], v211 offset:10240
v_mfma_f32_16x16x32_bf16 a[96:99], v[64:67], v[108:111], a[96:99]
v_and_b32 v50, s66, v54
v_mfma_f32_16x16x32_bf16 a[128:131], v[64:67], v[112:115], a[128:131]
s_add_u32 m0, s62, 4096
ds_read_b64_tr_b16 v[180:181], v217 offset:12288
v_mfma_f32_16x16x32_bf16 a[132:135], v[68:71], v[112:115], a[132:135]
buffer_load_dwordx4 v196, s[16:19], s30 offen lds
v_add_co_u32 v52, vcc, v52, v50
v_mfma_f32_16x16x32_bf16 a[136:139], v[72:75], v[112:115], a[136:139]
ds_read_b64_tr_b16 v[182:183], v217 offset:12800
v_mfma_f32_16x16x32_bf16 a[140:143], v[76:79], v[112:115], a[140:143]
v_addc_co_u32 v53, vcc, 0, v53, vcc
v_mfma_f32_16x16x32_bf16 a[144:147], v[80:83], v[112:115], a[144:147]
ds_read_b128 v[152:155], v211 offset:12288
v_mfma_f32_16x16x32_bf16 a[148:151], v[84:87], v[112:115], a[148:151]
v_mfma_f32_16x16x32_bf16 a[152:155], v[88:91], v[112:115], a[152:155]
ds_read_b64_tr_b16 v[184:185], v218 offset:12288
v_mfma_f32_16x16x32_bf16 a[156:159], v[92:95], v[112:115], a[156:159]
v_mfma_f32_16x16x32_bf16 a[188:191], v[92:95], v[116:119], a[188:191]
s_add_u32 m0, s62, 5120
ds_read_b64_tr_b16 v[186:187], v218 offset:12800
v_mfma_f32_16x16x32_bf16 a[184:187], v[88:91], v[116:119], a[184:187]
buffer_load_dwordx4 v197, s[16:19], s30 offen lds
v_mfma_f32_16x16x32_bf16 a[180:183], v[84:87], v[116:119], a[180:183]
ds_read_b128 v[156:159], v211 offset:14336
v_mfma_f32_16x16x32_bf16 a[176:179], v[80:83], v[116:119], a[176:179]
v_mfma_f32_16x16x32_bf16 a[172:175], v[76:79], v[116:119], a[172:175]
ds_read_b64_tr_b16 v[188:189], v219 offset:12288
v_mfma_f32_16x16x32_bf16 a[168:171], v[72:75], v[116:119], a[168:171]
v_mfma_f32_16x16x32_bf16 a[164:167], v[68:71], v[116:119], a[164:167]
ds_read_b64_tr_b16 v[190:191], v219 offset:12800
v_mfma_f32_16x16x32_bf16 a[160:163], v[64:67], v[116:119], a[160:163]
v_mfma_f32_16x16x32_bf16 a[192:195], v[64:67], v[120:123], a[192:195]
s_add_u32 m0, s62, 6144
v_mfma_f32_16x16x32_bf16 a[196:199], v[68:71], v[120:123], a[196:199]
buffer_load_dwordx4 v198, s[16:19], s30 offen lds
v_mfma_f32_16x16x32_bf16 a[200:203], v[72:75], v[120:123], a[200:203]
s_add_u32 s61, s60, 65536
v_mfma_f32_16x16x32_bf16 a[204:207], v[76:79], v[120:123], a[204:207]
s_sub_u32 s35, s61, 163840
v_mfma_f32_16x16x32_bf16 a[208:211], v[80:83], v[120:123], a[208:211]
s_cselect_b32 s61, s61, s35
v_mfma_f32_16x16x32_bf16 a[212:215], v[84:87], v[120:123], a[212:215]
s_add_u32 s64, s61, 32768
v_mfma_f32_16x16x32_bf16 a[216:219], v[88:91], v[120:123], a[216:219]
s_sub_u32 s35, s64, 163840
v_mfma_f32_16x16x32_bf16 a[220:223], v[92:95], v[120:123], a[220:223]
s_cselect_b32 s64, s64, s35
v_mfma_f32_16x16x32_bf16 a[252:255], v[92:95], v[124:127], a[252:255]
s_add_u32 m0, s62, 7168
v_mfma_f32_16x16x32_bf16 a[248:251], v[88:91], v[124:127], a[248:251]
buffer_load_dwordx4 v199, s[16:19], s30 offen lds
v_mfma_f32_16x16x32_bf16 a[244:247], v[84:87], v[124:127], a[244:247]
s_add_u32 s63, s60, s33
v_mfma_f32_16x16x32_bf16 a[240:243], v[80:83], v[124:127], a[240:243]
s_add_u32 s30, s30, 128
v_mfma_f32_16x16x32_bf16 a[236:239], v[76:79], v[124:127], a[236:239]
v_add_u32 v210, s61, v208
v_mfma_f32_16x16x32_bf16 a[232:235], v[72:75], v[124:127], a[232:235]
v_add_u32 v216, s64, v212
v_add_u32 v217, s64, v213
v_mfma_f32_16x16x32_bf16 a[228:231], v[68:71], v[124:127], a[228:231]
v_add_u32 v218, s64, v214
v_add_u32 v219, s64, v215
v_mfma_f32_16x16x32_bf16 a[224:227], v[64:67], v[124:127], a[224:227]
s_waitcnt lgkmcnt(0) vmcnt(11)
s_barrier
v_mfma_f32_16x16x32_bf16 a[0:3], v[128:131], v[160:163], a[0:3]
s_add_u32 m0, s63, 0
ds_read_b128 v[64:67], v210 offset:0
v_mfma_f32_16x16x32_bf16 a[4:7], v[132:135], v[160:163], a[4:7]
buffer_load_dwordx4 v200, s[20:23], s31 offen lds
v_mfma_f32_16x16x32_bf16 a[8:11], v[136:139], v[160:163], a[8:11]
ds_read_b64_tr_b16 v[96:97], v216 offset:0
v_mfma_f32_16x16x32_bf16 a[12:15], v[140:143], v[160:163], a[12:15]
v_mfma_f32_16x16x32_bf16 a[16:19], v[144:147], v[160:163], a[16:19]
ds_read_b64_tr_b16 v[98:99], v216 offset:512
v_mfma_f32_16x16x32_bf16 a[20:23], v[148:151], v[160:163], a[20:23]
v_mfma_f32_16x16x32_bf16 a[24:27], v[152:155], v[160:163], a[24:27]
ds_read_b128 v[68:71], v210 offset:2048
v_mfma_f32_16x16x32_bf16 a[28:31], v[156:159], v[160:163], a[28:31]
v_mfma_f32_16x16x32_bf16 a[60:63], v[156:159], v[164:167], a[60:63]
s_add_u32 m0, s63, 1024
ds_read_b64_tr_b16 v[100:101], v217 offset:0
v_mfma_f32_16x16x32_bf16 a[56:59], v[152:155], v[164:167], a[56:59]
buffer_load_dwordx4 v201, s[20:23], s31 offen lds
v_mfma_f32_16x16x32_bf16 a[52:55], v[148:151], v[164:167], a[52:55]
s_cmp_ge_u32 s34, 4
ds_read_b64_tr_b16 v[102:103], v217 offset:512
v_mfma_f32_16x16x32_bf16 a[48:51], v[144:147], v[164:167], a[48:51]
s_cselect_b32 s66, -1, 0
v_mfma_f32_16x16x32_bf16 a[44:47], v[140:143], v[164:167], a[44:47]
ds_read_b128 v[72:75], v210 offset:4096
v_mfma_f32_16x16x32_bf16 a[40:43], v[136:139], v[164:167], a[40:43]
v_mfma_f32_16x16x32_bf16 a[36:39], v[132:135], v[164:167], a[36:39]
ds_read_b64_tr_b16 v[104:105], v218 offset:0
v_mfma_f32_16x16x32_bf16 a[32:35], v[128:131], v[164:167], a[32:35]
v_mfma_f32_16x16x32_bf16 a[64:67], v[128:131], v[168:171], a[64:67]
s_add_u32 m0, s63, 2048
ds_read_b64_tr_b16 v[106:107], v218 offset:512
v_mfma_f32_16x16x32_bf16 a[68:71], v[132:135], v[168:171], a[68:71]
buffer_load_dwordx4 v202, s[20:23], s31 offen lds
v_mfma_f32_16x16x32_bf16 a[72:75], v[136:139], v[168:171], a[72:75]
ds_read_b128 v[76:79], v210 offset:6144
v_mfma_f32_16x16x32_bf16 a[76:79], v[140:143], v[168:171], a[76:79]
v_mfma_f32_16x16x32_bf16 a[80:83], v[144:147], v[168:171], a[80:83]
ds_read_b64_tr_b16 v[108:109], v219 offset:0
v_readlane_b32 s44, v58, s34
v_mfma_f32_16x16x32_bf16 a[84:87], v[148:151], v[168:171], a[84:87]
v_readlane_b32 s45, v59, s34
v_mfma_f32_16x16x32_bf16 a[88:91], v[152:155], v[168:171], a[88:91]
ds_read_b64_tr_b16 v[110:111], v219 offset:512
v_mfma_f32_16x16x32_bf16 a[92:95], v[156:159], v[168:171], a[92:95]
v_mfma_f32_16x16x32_bf16 a[124:127], v[156:159], v[172:175], a[124:127]
s_add_u32 m0, s63, 3072
ds_read_b128 v[80:83], v210 offset:8192
v_mfma_f32_16x16x32_bf16 a[120:123], v[152:155], v[172:175], a[120:123]
buffer_load_dwordx4 v203, s[20:23], s31 offen lds
v_mfma_f32_16x16x32_bf16 a[116:119], v[148:151], v[172:175], a[116:119]
ds_read_b64_tr_b16 v[112:113], v216 offset:8192
v_mfma_f32_16x16x32_bf16 a[112:115], v[144:147], v[172:175], a[112:115]
v_mfma_f32_16x16x32_bf16 a[108:111], v[140:143], v[172:175], a[108:111]
ds_read_b64_tr_b16 v[114:115], v216 offset:8704
v_mfma_f32_16x16x32_bf16 a[104:107], v[136:139], v[172:175], a[104:107]
v_mfma_f32_16x16x32_bf16 a[100:103], v[132:135], v[172:175], a[100:103]
ds_read_b128 v[84:87], v210 offset:10240
v_mfma_f32_16x16x32_bf16 a[96:99], v[128:131], v[172:175], a[96:99]
v_mfma_f32_16x16x32_bf16 a[128:131], v[128:131], v[176:179], a[128:131]
s_add_u32 m0, s63, 4096
ds_read_b64_tr_b16 v[116:117], v217 offset:8192
v_mfma_f32_16x16x32_bf16 a[132:135], v[132:135], v[176:179], a[132:135]
buffer_load_dwordx4 v204, s[20:23], s31 offen lds
v_mfma_f32_16x16x32_bf16 a[136:139], v[136:139], v[176:179], a[136:139]
ds_read_b64_tr_b16 v[118:119], v217 offset:8704
v_mfma_f32_16x16x32_bf16 a[140:143], v[140:143], v[176:179], a[140:143]
v_mfma_f32_16x16x32_bf16 a[144:147], v[144:147], v[176:179], a[144:147]
ds_read_b128 v[88:91], v210 offset:12288
v_mfma_f32_16x16x32_bf16 a[148:151], v[148:151], v[176:179], a[148:151]
v_mfma_f32_16x16x32_bf16 a[152:155], v[152:155], v[176:179], a[152:155]
ds_read_b64_tr_b16 v[120:121], v218 offset:8192
v_mfma_f32_16x16x32_bf16 a[156:159], v[156:159], v[176:179], a[156:159]
v_mfma_f32_16x16x32_bf16 a[188:191], v[156:159], v[180:183], a[188:191]
s_add_u32 m0, s63, 5120
ds_read_b64_tr_b16 v[122:123], v218 offset:8704
v_mfma_f32_16x16x32_bf16 a[184:187], v[152:155], v[180:183], a[184:187]
buffer_load_dwordx4 v205, s[20:23], s31 offen lds
v_mfma_f32_16x16x32_bf16 a[180:183], v[148:151], v[180:183], a[180:183]
ds_read_b128 v[92:95], v210 offset:14336
v_mfma_f32_16x16x32_bf16 a[176:179], v[144:147], v[180:183], a[176:179]
v_mfma_f32_16x16x32_bf16 a[172:175], v[140:143], v[180:183], a[172:175]
ds_read_b64_tr_b16 v[124:125], v219 offset:8192
v_mfma_f32_16x16x32_bf16 a[168:171], v[136:139], v[180:183], a[168:171]
v_mfma_f32_16x16x32_bf16 a[164:167], v[132:135], v[180:183], a[164:167]
ds_read_b64_tr_b16 v[126:127], v219 offset:8704
v_mfma_f32_16x16x32_bf16 a[160:163], v[128:131], v[180:183], a[160:163]
v_mfma_f32_16x16x32_bf16 a[192:195], v[128:131], v[184:187], a[192:195]
s_add_u32 m0, s63, 6144
v_mfma_f32_16x16x32_bf16 a[196:199], v[132:135], v[184:187], a[196:199]
buffer_load_dwordx4 v206, s[20:23], s31 offen lds
v_mfma_f32_16x16x32_bf16 a[200:203], v[136:139], v[184:187], a[200:203]
s_mov_b32 s60, s61
v_mfma_f32_16x16x32_bf16 a[204:207], v[140:143], v[184:187], a[204:207]
s_add_u32 s62, s61, 131072
v_mfma_f32_16x16x32_bf16 a[208:211], v[144:147], v[184:187], a[208:211]
s_sub_u32 s35, s62, 163840
v_mfma_f32_16x16x32_bf16 a[212:215], v[148:151], v[184:187], a[212:215]
s_cselect_b32 s62, s62, s35
v_mfma_f32_16x16x32_bf16 a[216:219], v[152:155], v[184:187], a[216:219]
s_add_u32 s62, s62, s33
v_mfma_f32_16x16x32_bf16 a[220:223], v[156:159], v[184:187], a[220:223]
v_mfma_f32_16x16x32_bf16 a[252:255], v[156:159], v[188:191], a[252:255]
s_add_u32 m0, s63, 7168
v_mfma_f32_16x16x32_bf16 a[248:251], v[152:155], v[188:191], a[248:251]
buffer_load_dwordx4 v207, s[20:23], s31 offen lds
v_mfma_f32_16x16x32_bf16 a[244:247], v[148:151], v[188:191], a[244:247]
v_mfma_f32_16x16x32_bf16 a[240:243], v[144:147], v[188:191], a[240:243]
s_add_u32 s31, s31, 524288
v_mfma_f32_16x16x32_bf16 a[236:239], v[140:143], v[188:191], a[236:239]
v_add_u32 v211, s61, v209
v_mfma_f32_16x16x32_bf16 a[232:235], v[136:139], v[188:191], a[232:235]
buffer_store_dword v248, v249, s[52:55], s56 offen sc1
v_mfma_f32_16x16x32_bf16 a[228:231], v[132:135], v[188:191], a[228:231]
s_mov_b32 s56, s67
global_load_dword v62, v[52:53], off
v_mfma_f32_16x16x32_bf16 a[224:227], v[128:131], v[188:191], a[224:227]
s_mov_b32 s67, 536870912
buffer_load_dword v61, v60, s[44:47], 0 offen
s_waitcnt lgkmcnt(0)
s_sub_u32 s34, s34, 1
s_cmp_lg_u32 s34, 0
s_cbranch_scc1 K1L1_%=
v_mfma_f32_16x16x32_bf16 a[0:3], v[64:67], v[96:99], a[0:3]
s_lshr_b32 s36, s29, 3
ds_read_b128 v[128:131], v211 offset:0
v_lshlrev_b32 v2, 4, v1
v_mfma_f32_16x16x32_bf16 a[4:7], v[68:71], v[96:99], a[4:7]
buffer_load_dword v63, v250, s[52:55], s57 offen sc1
v_mfma_f32_16x16x32_bf16 a[8:11], v[72:75], v[96:99], a[8:11]
s_lshr_b32 s37, s36, 2
ds_read_b64_tr_b16 v[160:161], v216 offset:4096
v_lshrrev_b32 v12, 3, v1
v_mfma_f32_16x16x32_bf16 a[12:15], v[76:79], v[96:99], a[12:15]
s_lshl_b32 s42, s41, 24
v_bfe_u32 v33, v1, 4, 2
v_mfma_f32_16x16x32_bf16 a[16:19], v[80:83], v[96:99], a[16:19]
s_lshl_b32 s43, s40, 20
ds_read_b64_tr_b16 v[162:163], v216 offset:4608
v_lshlrev_b32 v33, 1, v33
v_mfma_f32_16x16x32_bf16 a[20:23], v[84:87], v[96:99], a[20:23]
s_add_u32 s42, s42, s43
v_and_b32 v34, 7, v1
v_mfma_f32_16x16x32_bf16 a[24:27], v[88:91], v[96:99], a[24:27]
s_lshl_b32 s43, s37, 17
ds_read_b128 v[132:135], v211 offset:2048
v_xor_b32 v33, v33, v34
v_mfma_f32_16x16x32_bf16 a[28:31], v[92:95], v[96:99], a[28:31]
s_add_u32 s42, s42, s43
v_lshlrev_b32 v13, 4, v33
v_mfma_f32_16x16x32_bf16 a[60:63], v[92:95], v[100:103], a[60:63]
s_add_u32 s42, s42, 0
ds_read_b64_tr_b16 v[164:165], v217 offset:4096
v_lshrrev_b32 v34, 4, v1
v_mfma_f32_16x16x32_bf16 a[56:59], v[88:91], v[100:103], a[56:59]
s_lshr_b32 s43, s28, 1
v_and_b32 v35, 15, v1
v_mfma_f32_16x16x32_bf16 a[52:55], v[84:87], v[100:103], a[52:55]
s_lshl_b32 s43, s43, 16
ds_read_b64_tr_b16 v[166:167], v217 offset:4608
v_lshlrev_b32 v36, 2, v34
v_mfma_f32_16x16x32_bf16 a[48:51], v[80:83], v[100:103], a[48:51]
s_add_u32 s42, s42, s43
v_sub_u32 v19, v36, v35
v_mfma_f32_16x16x32_bf16 a[44:47], v[76:79], v[100:103], a[44:47]
s_and_b32 s43, s28, 1
ds_read_b128 v[136:139], v211 offset:4096
v_bfe_u32 v35, v1, 2, 2
v_mfma_f32_16x16x32_bf16 a[40:43], v[72:75], v[100:103], a[40:43]
s_cmp_eq_u32 s41, 0
v_and_b32 v37, 3, v1
v_mfma_f32_16x16x32_bf16 a[36:39], v[68:71], v[100:103], a[36:39]
s_cselect_b32 s44, 14, 13
ds_read_b64_tr_b16 v[168:169], v218 offset:4096
v_add_u32 v36, v36, v35
v_mfma_f32_16x16x32_bf16 a[32:35], v[64:67], v[100:103], a[32:35]
s_lshl_b32 s43, s43, s44
v_bfe_u32 v38, v36, 1, 2
v_mfma_f32_16x16x32_bf16 a[64:67], v[64:67], v[104:107], a[64:67]
s_add_u32 s75, s42, s43
ds_read_b64_tr_b16 v[170:171], v218 offset:4608
v_lshlrev_b32 v36, 7, v36
v_mfma_f32_16x16x32_bf16 a[68:71], v[68:71], v[104:107], a[68:71]
s_mov_b32 s99, 16384
v_lshl_add_u32 v36, v37, 3, v36
v_mfma_f32_16x16x32_bf16 a[72:75], v[72:75], v[104:107], a[72:75]
s_cmp_eq_u32 s41, 0
ds_read_b128 v[140:143], v211 offset:6144
v_xor_b32 v39, 0, v38
v_mfma_f32_16x16x32_bf16 a[76:79], v[76:79], v[104:107], a[76:79]
s_cselect_b32 s99, 4096, s99
v_lshl_add_u32 v4, v39, 5, v36
v_mfma_f32_16x16x32_bf16 a[80:83], v[80:83], v[104:107], a[80:83]
s_mov_b32 s69, s41
ds_read_b64_tr_b16 v[172:173], v219 offset:4096
v_xor_b32 v39, 1, v38
v_mfma_f32_16x16x32_bf16 a[84:87], v[84:87], v[104:107], a[84:87]
s_lshr_b32 s43, s36, 1
v_lshl_add_u32 v5, v39, 5, v36
v_mfma_f32_16x16x32_bf16 a[88:91], v[88:91], v[104:107], a[88:91]
s_and_b32 s44, s43, 1
ds_read_b64_tr_b16 v[174:175], v219 offset:4608
v_xor_b32 v39, 2, v38
v_mfma_f32_16x16x32_bf16 a[92:95], v[92:95], v[104:107], a[92:95]
s_lshr_b32 s43, s43, 1
v_lshl_add_u32 v6, v39, 5, v36
v_mfma_f32_16x16x32_bf16 a[124:127], v[92:95], v[108:111], a[124:127]
s_sub_u32 s45, 15, s43
ds_read_b128 v[144:147], v211 offset:8192
v_xor_b32 v39, 3, v38
v_mfma_f32_16x16x32_bf16 a[120:123], v[88:91], v[108:111], a[120:123]
s_cmp_eq_u32 s44, 0
v_lshl_add_u32 v7, v39, 5, v36
v_mfma_f32_16x16x32_bf16 a[116:119], v[84:87], v[108:111], a[116:119]
s_cselect_b32 s43, s45, s43
ds_read_b64_tr_b16 v[176:177], v216 offset:12288
v_mfma_f32_16x16x32_bf16 a[112:115], v[80:83], v[108:111], a[112:115]
s_lshl_b32 s43, s43, 3
s_lshl_b32 s44, s28, 1
s_add_u32 s43, s43, s44
v_mfma_f32_16x16x32_bf16 a[108:111], v[76:79], v[108:111], a[108:111]
s_mul_i32 s45, s43, 14336
ds_read_b64_tr_b16 v[178:179], v216 offset:12800
v_mfma_f32_16x16x32_bf16 a[104:107], v[72:75], v[108:111], a[104:107]
s_add_u32 s45, s45, 33554432
s_add_u32 s46, s45, 14336
v_mfma_f32_16x16x32_bf16 a[100:103], v[68:71], v[108:111], a[100:103]
ds_read_b128 v[148:151], v211 offset:10240
v_mfma_f32_16x16x32_bf16 a[96:99], v[64:67], v[108:111], a[96:99]
v_mfma_f32_16x16x32_bf16 a[128:131], v[64:67], v[112:115], a[128:131]
ds_read_b64_tr_b16 v[180:181], v217 offset:12288
v_mfma_f32_16x16x32_bf16 a[132:135], v[68:71], v[112:115], a[132:135]
v_mfma_f32_16x16x32_bf16 a[136:139], v[72:75], v[112:115], a[136:139]
ds_read_b64_tr_b16 v[182:183], v217 offset:12800
v_mfma_f32_16x16x32_bf16 a[140:143], v[76:79], v[112:115], a[140:143]
v_mfma_f32_16x16x32_bf16 a[144:147], v[80:83], v[112:115], a[144:147]
ds_read_b128 v[152:155], v211 offset:12288
v_mfma_f32_16x16x32_bf16 a[148:151], v[84:87], v[112:115], a[148:151]
v_mfma_f32_16x16x32_bf16 a[152:155], v[88:91], v[112:115], a[152:155]
ds_read_b64_tr_b16 v[184:185], v218 offset:12288
v_mfma_f32_16x16x32_bf16 a[156:159], v[92:95], v[112:115], a[156:159]
v_mfma_f32_16x16x32_bf16 a[188:191], v[92:95], v[116:119], a[188:191]
ds_read_b64_tr_b16 v[186:187], v218 offset:12800
v_mfma_f32_16x16x32_bf16 a[184:187], v[88:91], v[116:119], a[184:187]
v_mfma_f32_16x16x32_bf16 a[180:183], v[84:87], v[116:119], a[180:183]
ds_read_b128 v[156:159], v211 offset:14336
v_mfma_f32_16x16x32_bf16 a[176:179], v[80:83], v[116:119], a[176:179]
v_mfma_f32_16x16x32_bf16 a[172:175], v[76:79], v[116:119], a[172:175]
ds_read_b64_tr_b16 v[188:189], v219 offset:12288
v_mfma_f32_16x16x32_bf16 a[168:171], v[72:75], v[116:119], a[168:171]
v_mfma_f32_16x16x32_bf16 a[164:167], v[68:71], v[116:119], a[164:167]
ds_read_b64_tr_b16 v[190:191], v219 offset:12800
v_mfma_f32_16x16x32_bf16 a[160:163], v[64:67], v[116:119], a[160:163]
v_mfma_f32_16x16x32_bf16 a[192:195], v[64:67], v[120:123], a[192:195]
v_mfma_f32_16x16x32_bf16 a[196:199], v[68:71], v[120:123], a[196:199]
v_mfma_f32_16x16x32_bf16 a[200:203], v[72:75], v[120:123], a[200:203]
s_add_u32 s61, s60, 65536
v_mfma_f32_16x16x32_bf16 a[204:207], v[76:79], v[120:123], a[204:207]
s_sub_u32 s35, s61, 163840
v_mfma_f32_16x16x32_bf16 a[208:211], v[80:83], v[120:123], a[208:211]
s_cselect_b32 s61, s61, s35
v_mfma_f32_16x16x32_bf16 a[212:215], v[84:87], v[120:123], a[212:215]
s_add_u32 s64, s61, 32768
v_mfma_f32_16x16x32_bf16 a[216:219], v[88:91], v[120:123], a[216:219]
s_sub_u32 s35, s64, 163840
v_mfma_f32_16x16x32_bf16 a[220:223], v[92:95], v[120:123], a[220:223]
s_cselect_b32 s64, s64, s35
v_mfma_f32_16x16x32_bf16 a[252:255], v[92:95], v[124:127], a[252:255]
v_mfma_f32_16x16x32_bf16 a[248:251], v[88:91], v[124:127], a[248:251]
v_mfma_f32_16x16x32_bf16 a[244:247], v[84:87], v[124:127], a[244:247]
v_mfma_f32_16x16x32_bf16 a[240:243], v[80:83], v[124:127], a[240:243]
v_mfma_f32_16x16x32_bf16 a[236:239], v[76:79], v[124:127], a[236:239]
v_add_u32 v210, s61, v208
v_mfma_f32_16x16x32_bf16 a[232:235], v[72:75], v[124:127], a[232:235]
v_add_u32 v216, s64, v212
v_add_u32 v217, s64, v213
v_mfma_f32_16x16x32_bf16 a[228:231], v[68:71], v[124:127], a[228:231]
v_add_u32 v218, s64, v214
v_add_u32 v219, s64, v215
v_mfma_f32_16x16x32_bf16 a[224:227], v[64:67], v[124:127], a[224:227]
s_waitcnt lgkmcnt(0) vmcnt(0)
s_barrier
v_cmp_eq_u32 vcc, s98, v63
s_nop 4
s_cmp_eq_u64 vcc, -1
s_cbranch_scc1 ALLOKxn1_%=
s_waitcnt vmcnt(0)
s_mov_b32 s47, 0
ALLPxn1_%=:
buffer_load_dword v63, v250, s[52:55], s57 offen sc1
s_waitcnt vmcnt(0)
v_cmp_eq_u32 vcc, s98, v63
s_add_u32 s47, s47, 1
s_nop 4
s_cmp_eq_u64 vcc, -1
s_cbranch_scc1 ALLOKxn1_%=
s_sleep 1
s_cmp_lt_u32 s47, 0x40000
s_cbranch_scc1 ALLPxn1_%=
ALLOKxn1_%=:
v_mfma_f32_16x16x32_bf16 a[0:3], v[128:131], v[160:163], a[0:3]
s_and_b32 s35, s29, 7
ds_read_b128 v[64:67], v210 offset:0
v_mfma_f32_16x16x32_bf16 a[4:7], v[132:135], v[160:163], a[4:7]
buffer_load_dwordx4 v[20:23], v2, s[52:55], s45 offen offset:0 sc1
v_mfma_f32_16x16x32_bf16 a[8:11], v[136:139], v[160:163], a[8:11]
s_lshr_b32 s47, s29, 3
ds_read_b64_tr_b16 v[96:97], v216 offset:0
v_mfma_f32_16x16x32_bf16 a[12:15], v[140:143], v[160:163], a[12:15]
s_and_b32 s48, s47, 1
s_lshl_b32 s30, s35, 1
s_add_u32 s30, s30, s48
v_mfma_f32_16x16x32_bf16 a[16:19], v[144:147], v[160:163], a[16:19]
s_lshr_b32 s47, s47, 1
ds_read_b64_tr_b16 v[98:99], v216 offset:512
v_mfma_f32_16x16x32_bf16 a[20:23], v[148:151], v[160:163], a[20:23]
s_and_b32 s40, s47, 1
s_lshr_b32 s31, s47, 1
s_lshl_b32 s42, s30, 3
v_mfma_f32_16x16x32_bf16 a[24:27], v[152:155], v[160:163], a[24:27]
s_add_u32 s42, s42, s31
ds_read_b128 v[68:71], v210 offset:2048
v_mfma_f32_16x16x32_bf16 a[28:31], v[156:159], v[160:163], a[28:31]
s_sub_u32 s47, 15, s31
s_cmp_eq_u32 s40, 0
s_cselect_b32 s41, s47, s31
v_mfma_f32_16x16x32_bf16 a[60:63], v[156:159], v[164:167], a[60:63]
s_cmp_lt_u32 s30, 14
ds_read_b64_tr_b16 v[100:101], v217 offset:0
v_mfma_f32_16x16x32_bf16 a[56:59], v[152:155], v[164:167], a[56:59]
buffer_load_dwordx4 v[24:27], v2, s[52:55], s46 offen offset:0 sc1
v_mfma_f32_16x16x32_bf16 a[52:55], v[148:151], v[164:167], a[52:55]
s_cselect_b32 s43, 7, 0
ds_read_b64_tr_b16 v[102:103], v217 offset:512
v_mfma_f32_16x16x32_bf16 a[48:51], v[144:147], v[164:167], a[48:51]
s_lshl_b32 s33, s28, 13
s_mov_b64 s[36:37], s[94:95]
s_mov_b32 s38, 0x10000000
v_mfma_f32_16x16x32_bf16 a[44:47], v[140:143], v[164:167], a[44:47]
s_mov_b32 s39, 0x20000
ds_read_b128 v[72:75], v210 offset:4096
v_mfma_f32_16x16x32_bf16 a[40:43], v[136:139], v[164:167], a[40:43]
s_mov_b32 s18, 0x7fffffff
s_mov_b32 s19, 0x20000
s_cmp_lt_u32 s31, 7
v_mfma_f32_16x16x32_bf16 a[36:39], v[132:135], v[164:167], a[36:39]
s_cselect_b32 s68, 18, 17
ds_read_b64_tr_b16 v[104:105], v218 offset:0
v_mfma_f32_16x16x32_bf16 a[32:35], v[128:131], v[164:167], a[32:35]
s_sub_u32 s47, s29, 224
s_cselect_b32 s47, s29, s47
s_and_b32 s10, s47, 7
v_mfma_f32_16x16x32_bf16 a[64:67], v[128:131], v[168:171], a[64:67]
s_lshr_b32 s11, s47, 3
ds_read_b64_tr_b16 v[106:107], v218 offset:512
v_mfma_f32_16x16x32_bf16 a[68:71], v[132:135], v[168:171], a[68:71]
buffer_load_dwordx4 v[28:31], v2, s[52:55], s45 offen offset:1024 sc1
v_mfma_f32_16x16x32_bf16 a[72:75], v[136:139], v[168:171], a[72:75]
s_mul_i32 s48, s11, 37
ds_read_b128 v[76:79], v210 offset:6144
v_mfma_f32_16x16x32_bf16 a[76:79], v[140:143], v[168:171], a[76:79]
s_lshr_b32 s48, s48, 8
s_lshl_b32 s10, s10, 2
s_add_u32 s10, s10, s48
v_mfma_f32_16x16x32_bf16 a[80:83], v[144:147], v[168:171], a[80:83]
s_mul_i32 s48, s48, 7
ds_read_b64_tr_b16 v[108:109], v219 offset:0
v_mfma_f32_16x16x32_bf16 a[84:87], v[148:151], v[168:171], a[84:87]
s_sub_u32 s11, s11, s48
s_lshl_b32 s47, s30, 9
s_lshl_b32 s48, s28, 7
v_mfma_f32_16x16x32_bf16 a[88:91], v[152:155], v[168:171], a[88:91]
s_add_u32 s47, s47, s48
ds_read_b64_tr_b16 v[110:111], v219 offset:512
v_mfma_f32_16x16x32_bf16 a[92:95], v[156:159], v[168:171], a[92:95]
s_lshl_b32 s49, s30, 8
s_and_b32 s48, s28, 1
s_lshl_b32 s48, s48, 7
v_mfma_f32_16x16x32_bf16 a[124:127], v[156:159], v[172:175], a[124:127]
s_add_u32 s49, s49, s48
ds_read_b128 v[80:83], v210 offset:8192
v_mfma_f32_16x16x32_bf16 a[120:123], v[152:155], v[172:175], a[120:123]
buffer_load_dwordx4 v[32:35], v2, s[52:55], s46 offen offset:1024 sc1
v_mfma_f32_16x16x32_bf16 a[116:119], v[148:151], v[172:175], a[116:119]
s_cmp_lt_u32 s28, 2
ds_read_b64_tr_b16 v[112:113], v216 offset:8192
v_mfma_f32_16x16x32_bf16 a[112:115], v[144:147], v[172:175], a[112:115]
s_cselect_b32 s50, s88, s90
s_cselect_b32 s51, s89, s91
s_cmp_eq_u32 s43, 7
v_mfma_f32_16x16x32_bf16 a[108:111], v[140:143], v[172:175], a[108:111]
s_cselect_b32 s16, s50, s80
ds_read_b64_tr_b16 v[114:115], v216 offset:8704
v_mfma_f32_16x16x32_bf16 a[104:107], v[136:139], v[172:175], a[104:107]
s_cselect_b32 s17, s51, s81
s_cselect_b32 s47, s49, s47
s_mov_b32 s25, 8192
v_mfma_f32_16x16x32_bf16 a[100:103], v[132:135], v[172:175], a[100:103]
s_cselect_b32 s25, 3584, s25
ds_read_b128 v[84:87], v210 offset:10240
v_mfma_f32_16x16x32_bf16 a[96:99], v[128:131], v[172:175], a[96:99]
s_add_u32 s16, s16, s47
s_addc_u32 s17, s17, 0
s_lshl_b32 s21, s25, 3
v_mfma_f32_16x16x32_bf16 a[128:131], v[128:131], v[176:179], a[128:131]
s_lshl_b32 s14, s25, 6
ds_read_b64_tr_b16 v[116:117], v217 offset:8192
v_mfma_f32_16x16x32_bf16 a[132:135], v[132:135], v[176:179], a[132:135]
buffer_load_dwordx4 v[36:39], v2, s[52:55], s45 offen offset:2048 sc1
v_mfma_f32_16x16x32_bf16 a[136:139], v[136:139], v[176:179], a[136:139]
s_nop 0
ds_read_b64_tr_b16 v[118:119], v217 offset:8704
v_mad_u32_u24 v14, v12, s25, v13
v_mfma_f32_16x16x32_bf16 a[140:143], v[140:143], v[176:179], a[140:143]
v_mfma_f32_16x16x32_bf16 a[144:147], v[144:147], v[176:179], a[144:147]
ds_read_b128 v[88:91], v210 offset:12288
v_mfma_f32_16x16x32_bf16 a[148:151], v[148:151], v[176:179], a[148:151]
v_mfma_f32_16x16x32_bf16 a[152:155], v[152:155], v[176:179], a[152:155]
ds_read_b64_tr_b16 v[120:121], v218 offset:8192
v_mfma_f32_16x16x32_bf16 a[156:159], v[156:159], v[176:179], a[156:159]
v_mfma_f32_16x16x32_bf16 a[188:191], v[156:159], v[180:183], a[188:191]
ds_read_b64_tr_b16 v[122:123], v218 offset:8704
v_mfma_f32_16x16x32_bf16 a[184:187], v[152:155], v[180:183], a[184:187]
buffer_load_dwordx4 v[48:51], v2, s[52:55], s46 offen offset:2048 sc1
v_mfma_f32_16x16x32_bf16 a[180:183], v[148:151], v[180:183], a[180:183]
ds_read_b128 v[92:95], v210 offset:14336
v_mfma_f32_16x16x32_bf16 a[176:179], v[144:147], v[180:183], a[176:179]
v_mfma_f32_16x16x32_bf16 a[172:175], v[140:143], v[180:183], a[172:175]
ds_read_b64_tr_b16 v[124:125], v219 offset:8192
v_mfma_f32_16x16x32_bf16 a[168:171], v[136:139], v[180:183], a[168:171]
v_mfma_f32_16x16x32_bf16 a[164:167], v[132:135], v[180:183], a[164:167]
ds_read_b64_tr_b16 v[126:127], v219 offset:8704
v_mfma_f32_16x16x32_bf16 a[160:163], v[128:131], v[180:183], a[160:163]
v_mfma_f32_16x16x32_bf16 a[192:195], v[128:131], v[184:187], a[192:195]
v_mfma_f32_16x16x32_bf16 a[196:199], v[132:135], v[184:187], a[196:199]
buffer_load_dwordx4 v[52:55], v2, s[52:55], s45 offen offset:3072 sc1
v_mfma_f32_16x16x32_bf16 a[200:203], v[136:139], v[184:187], a[200:203]
v_mfma_f32_16x16x32_bf16 a[204:207], v[140:143], v[184:187], a[204:207]
v_mfma_f32_16x16x32_bf16 a[208:211], v[144:147], v[184:187], a[208:211]
v_mfma_f32_16x16x32_bf16 a[212:215], v[148:151], v[184:187], a[212:215]
v_mfma_f32_16x16x32_bf16 a[216:219], v[152:155], v[184:187], a[216:219]
v_mfma_f32_16x16x32_bf16 a[220:223], v[156:159], v[184:187], a[220:223]
v_mfma_f32_16x16x32_bf16 a[252:255], v[156:159], v[188:191], a[252:255]
v_mfma_f32_16x16x32_bf16 a[248:251], v[152:155], v[188:191], a[248:251]
buffer_load_dwordx4 v[56:59], v2, s[52:55], s46 offen offset:3072 sc1
v_mfma_f32_16x16x32_bf16 a[244:247], v[148:151], v[188:191], a[244:247]
v_mfma_f32_16x16x32_bf16 a[240:243], v[144:147], v[188:191], a[240:243]
v_mfma_f32_16x16x32_bf16 a[236:239], v[140:143], v[188:191], a[236:239]
v_add_u32 v211, s61, v209
v_mfma_f32_16x16x32_bf16 a[232:235], v[136:139], v[188:191], a[232:235]
v_mfma_f32_16x16x32_bf16 a[228:231], v[132:135], v[188:191], a[228:231]
v_mfma_f32_16x16x32_bf16 a[224:227], v[128:131], v[188:191], a[224:227]
s_waitcnt lgkmcnt(0)
v_mfma_f32_16x16x32_bf16 a[0:3], v[64:67], v[96:99], a[0:3]
s_mov_b32 s50, s43
ds_read_b128 v[128:131], v211 offset:0
v_mfma_f32_16x16x32_bf16 a[4:7], v[68:71], v[96:99], a[4:7]
s_mov_b32 s51, 0
s_add_u32 s52, s43, 14
s_lshl_b32 s47, s31, 1
v_mfma_f32_16x16x32_bf16 a[8:11], v[72:75], v[96:99], a[8:11]
s_sub_u32 s48, 32, s47
ds_read_b64_tr_b16 v[160:161], v216 offset:4096
v_mfma_f32_16x16x32_bf16 a[12:15], v[76:79], v[96:99], a[12:15]
s_sub_u32 s48, s48, s68
s_cmp_eq_u32 s40, 0
s_cselect_b32 s53, s68, s48
v_mfma_f32_16x16x32_bf16 a[16:19], v[80:83], v[96:99], a[16:19]
s_cselect_b32 s54, 0, s68
ds_read_b64_tr_b16 v[162:163], v216 offset:4608
v_mfma_f32_16x16x32_bf16 a[20:23], v[84:87], v[96:99], a[20:23]
s_lshl_b32 s55, s53, 1
s_add_u32 s55, s55, s52
s_add_u32 s47, s47, 2
v_mfma_f32_16x16x32_bf16 a[24:27], v[88:91], v[96:99], a[24:27]
s_cselect_b32 s56, 0, 0
ds_read_b128 v[132:135], v211 offset:2048
v_mfma_f32_16x16x32_bf16 a[28:31], v[92:95], v[96:99], a[28:31]
s_cmp_eq_u32 s40, 0
s_cselect_b32 s56, 0, s47
s_lshl_b32 s57, s56, 1
v_mfma_f32_16x16x32_bf16 a[60:63], v[92:95], v[100:103], a[60:63]
s_add_u32 s57, s57, s55
ds_read_b64_tr_b16 v[164:165], v217 offset:4096
v_cmp_le_u32 vcc, s55, v1
v_mfma_f32_16x16x32_bf16 a[56:59], v[88:91], v[100:103], a[56:59]
v_mov_b32 v224, s52
v_mov_b32 v225, s55
v_mfma_f32_16x16x32_bf16 a[52:55], v[84:87], v[100:103], a[52:55]
ds_read_b64_tr_b16 v[166:167], v217 offset:4608
v_cndmask_b32 v224, v224, v225, vcc
v_mfma_f32_16x16x32_bf16 a[48:51], v[80:83], v[100:103], a[48:51]
v_mov_b32 v225, s53
v_mov_b32 v226, s56
v_mfma_f32_16x16x32_bf16 a[44:47], v[76:79], v[100:103], a[44:47]
ds_read_b128 v[136:139], v211 offset:4096
v_cndmask_b32 v225, v225, v226, vcc
v_mfma_f32_16x16x32_bf16 a[40:43], v[72:75], v[100:103], a[40:43]
v_mov_b32 v226, s54
v_mov_b32 v227, 0
v_mfma_f32_16x16x32_bf16 a[36:39], v[68:71], v[100:103], a[36:39]
ds_read_b64_tr_b16 v[168:169], v218 offset:4096
v_cndmask_b32 v226, v226, v227, vcc
v_mfma_f32_16x16x32_bf16 a[32:35], v[64:67], v[100:103], a[32:35]
v_sub_u32 v224, v1, v224
v_lshlrev_b32 v225, 1, v225
v_mfma_f32_16x16x32_bf16 a[64:67], v[64:67], v[104:107], a[64:67]
ds_read_b64_tr_b16 v[170:171], v218 offset:4608
v_add_u32 v225, -1, v225
v_mfma_f32_16x16x32_bf16 a[68:71], v[68:71], v[104:107], a[68:71]
v_and_b32 v227, 1, v224
v_cmp_eq_u32 s[48:49], 1, v227
v_mfma_f32_16x16x32_bf16 a[72:75], v[72:75], v[104:107], a[72:75]
ds_read_b128 v[140:143], v211 offset:6144
v_cmp_ne_u32 s[58:59], v224, v225
v_mfma_f32_16x16x32_bf16 a[76:79], v[76:79], v[104:107], a[76:79]
v_cmp_eq_u32 s[60:61], 0, v224
v_mfma_f32_16x16x32_bf16 a[80:83], v[80:83], v[104:107], a[80:83]
s_and_b64 s[48:49], s[48:49], s[58:59]
ds_read_b64_tr_b16 v[172:173], v219 offset:4096
v_mfma_f32_16x16x32_bf16 a[84:87], v[84:87], v[104:107], a[84:87]
s_or_b64 s[48:49], s[48:49], s[60:61]
v_add_u32 v227, 1, v224
v_lshrrev_b32 v227, 1, v227
v_mfma_f32_16x16x32_bf16 a[88:91], v[88:91], v[104:107], a[88:91]
ds_read_b64_tr_b16 v[174:175], v219 offset:4608
v_add_u32 v228, -1, v224
v_mfma_f32_16x16x32_bf16 a[92:95], v[92:95], v[104:107], a[92:95]
v_lshrrev_b32 v228, 1, v228
v_cndmask_b32_e64 v227, v228, v227, s[48:49]
v_mfma_f32_16x16x32_bf16 a[124:127], v[92:95], v[108:111], a[124:127]
ds_read_b128 v[144:147], v211 offset:8192
v_add_u32 v227, v226, v227
v_mfma_f32_16x16x32_bf16 a[120:123], v[88:91], v[108:111], a[120:123]
v_and_b32 v227, 31, v227
v_lshlrev_b32 v231, 15, v227
v_mfma_f32_16x16x32_bf16 a[116:119], v[84:87], v[108:111], a[116:119]
ds_read_b64_tr_b16 v[176:177], v216 offset:12288
v_mov_b32 v228, 16777216
v_mfma_f32_16x16x32_bf16 a[112:115], v[80:83], v[108:111], a[112:115]
v_mov_b32 v229, 0
v_cndmask_b32_e64 v228, v228, v229, s[48:49]
v_mfma_f32_16x16x32_bf16 a[108:111], v[76:79], v[108:111], a[108:111]
ds_read_b64_tr_b16 v[178:179], v216 offset:12800
v_add_u32 v231, v231, v228
v_mfma_f32_16x16x32_bf16 a[104:107], v[72:75], v[108:111], a[104:107]
s_lshl_b32 s47, s30, 20
s_add_u32 s47, s47, s33
v_add_u32 v231, s47, v231
v_mfma_f32_16x16x32_bf16 a[100:103], v[68:71], v[108:111], a[100:103]
ds_read_b128 v[148:151], v211 offset:10240
v_mov_b32 v232, s36
v_mfma_f32_16x16x32_bf16 a[96:99], v[64:67], v[108:111], a[96:99]
v_mov_b32 v233, s37
v_mov_b32 v18, 128
v_mfma_f32_16x16x32_bf16 a[128:131], v[64:67], v[112:115], a[128:131]
s_mov_b64 s[62:63], s[80:81]
ds_read_b64_tr_b16 v[180:181], v217 offset:12288
v_mfma_f32_16x16x32_bf16 a[132:135], v[68:71], v[112:115], a[132:135]
s_add_u32 s58, s50, 7
s_mov_b32 s64, s58
s_lshl_b32 s47, s30, 9
v_mfma_f32_16x16x32_bf16 a[136:139], v[72:75], v[112:115], a[136:139]
s_lshl_b32 s65, s28, 7
ds_read_b64_tr_b16 v[182:183], v217 offset:12800
v_mfma_f32_16x16x32_bf16 a[140:143], v[76:79], v[112:115], a[140:143]
s_add_u32 s65, s65, s47
v_cmp_le_u32 s[48:49], s50, v1
v_cmp_gt_u32 s[58:59], s64, v1
v_mfma_f32_16x16x32_bf16 a[144:147], v[80:83], v[112:115], a[144:147]
s_and_b64 s[48:49], s[48:49], s[58:59]
ds_read_b128 v[152:155], v211 offset:12288
v_subrev_u32 v224, s50, v1
v_mfma_f32_16x16x32_bf16 a[148:151], v[84:87], v[112:115], a[148:151]
s_mov_b32 s60, 524288
v_mul_lo_u32 v224, s60, v224
v_add_u32 v224, s65, v224
v_mfma_f32_16x16x32_bf16 a[152:155], v[88:91], v[112:115], a[152:155]
ds_read_b64_tr_b16 v[184:185], v218 offset:12288
v_cndmask_b32_e64 v231, v231, v224, s[48:49]
v_mfma_f32_16x16x32_bf16 a[156:159], v[92:95], v[112:115], a[156:159]
v_mov_b32 v224, s62
v_mov_b32 v225, s63
v_mfma_f32_16x16x32_bf16 a[188:191], v[92:95], v[116:119], a[188:191]
ds_read_b64_tr_b16 v[186:187], v218 offset:12800
v_mov_b32 v226, 8192
v_mfma_f32_16x16x32_bf16 a[184:187], v[88:91], v[116:119], a[184:187]
v_cndmask_b32_e64 v232, v232, v224, s[48:49]
v_cndmask_b32_e64 v233, v233, v225, s[48:49]
v_mfma_f32_16x16x32_bf16 a[180:183], v[84:87], v[116:119], a[180:183]
ds_read_b128 v[156:159], v211 offset:14336
v_cndmask_b32_e64 v18, v18, v226, s[48:49]
v_mfma_f32_16x16x32_bf16 a[176:179], v[80:83], v[116:119], a[176:179]
s_mov_b64 s[62:63], s[88:89]
s_mov_b64 s[66:67], s[90:91]
s_cmp_lt_u32 s28, 2
v_mfma_f32_16x16x32_bf16 a[172:175], v[76:79], v[116:119], a[172:175]
s_cselect_b32 s62, s62, s66
ds_read_b64_tr_b16 v[188:189], v219 offset:12288
v_mfma_f32_16x16x32_bf16 a[168:171], v[72:75], v[116:119], a[168:171]
s_cselect_b32 s63, s63, s67
s_add_u32 s64, s51, s43
s_lshl_b32 s47, s30, 8
v_mfma_f32_16x16x32_bf16 a[164:167], v[68:71], v[116:119], a[164:167]
s_and_b32 s65, s28, 1
ds_read_b64_tr_b16 v[190:191], v219 offset:12800
v_mfma_f32_16x16x32_bf16 a[160:163], v[64:67], v[116:119], a[160:163]
s_lshl_b32 s65, s65, 7
s_add_u32 s65, s65, s47
v_cmp_le_u32 s[48:49], s51, v1
v_mfma_f32_16x16x32_bf16 a[192:195], v[64:67], v[120:123], a[192:195]
v_cmp_gt_u32 s[58:59], s64, v1
v_mfma_f32_16x16x32_bf16 a[196:199], v[68:71], v[120:123], a[196:199]
s_and_b64 s[48:49], s[48:49], s[58:59]
v_subrev_u32 v224, s51, v1
v_mfma_f32_16x16x32_bf16 a[200:203], v[72:75], v[120:123], a[200:203]
s_mov_b32 s60, 229376
v_mul_lo_u32 v224, s60, v224
v_add_u32 v224, s65, v224
v_mfma_f32_16x16x32_bf16 a[204:207], v[76:79], v[120:123], a[204:207]
v_cndmask_b32_e64 v231, v231, v224, s[48:49]
v_mov_b32 v224, s62
v_mfma_f32_16x16x32_bf16 a[208:211], v[80:83], v[120:123], a[208:211]
v_mov_b32 v225, s63
v_mov_b32 v226, 3584
v_mfma_f32_16x16x32_bf16 a[212:215], v[84:87], v[120:123], a[212:215]
v_cndmask_b32_e64 v232, v232, v224, s[48:49]
v_cndmask_b32_e64 v233, v233, v225, s[48:49]
v_mfma_f32_16x16x32_bf16 a[216:219], v[88:91], v[120:123], a[216:219]
v_cndmask_b32_e64 v18, v18, v226, s[48:49]
v_mfma_f32_16x16x32_bf16 a[220:223], v[92:95], v[120:123], a[220:223]
s_mov_b64 s[62:63], s[92:93]
s_add_u32 s51, s43, 7
s_mov_b32 s64, s52
v_mfma_f32_16x16x32_bf16 a[252:255], v[92:95], v[124:127], a[252:255]
s_mul_i32 s65, s28, 57344
s_lshl_b32 s47, s11, 7
s_add_u32 s65, s65, s47
v_mfma_f32_16x16x32_bf16 a[248:251], v[88:91], v[124:127], a[248:251]
v_cmp_le_u32 s[48:49], s51, v1
v_cmp_gt_u32 s[58:59], s64, v1
v_mfma_f32_16x16x32_bf16 a[244:247], v[84:87], v[124:127], a[244:247]
s_and_b64 s[48:49], s[48:49], s[58:59]
v_subrev_u32 v224, s51, v1
v_mfma_f32_16x16x32_bf16 a[240:243], v[80:83], v[124:127], a[240:243]
s_mov_b32 s60, 229376
v_mul_lo_u32 v224, s60, v224
v_add_u32 v224, s65, v224
v_mfma_f32_16x16x32_bf16 a[236:239], v[76:79], v[124:127], a[236:239]
v_cndmask_b32_e64 v231, v231, v224, s[48:49]
v_mov_b32 v224, s62
v_mfma_f32_16x16x32_bf16 a[232:235], v[72:75], v[124:127], a[232:235]
v_mov_b32 v225, s63
v_mov_b32 v226, 896
v_mfma_f32_16x16x32_bf16 a[228:231], v[68:71], v[124:127], a[228:231]
v_cndmask_b32_e64 v232, v232, v224, s[48:49]
v_cndmask_b32_e64 v233, v233, v225, s[48:49]
v_mfma_f32_16x16x32_bf16 a[224:227], v[64:67], v[124:127], a[224:227]
v_cndmask_b32_e64 v18, v18, v226, s[48:49]
s_waitcnt lgkmcnt(0) vmcnt(0)
s_barrier
v_mfma_f32_16x16x32_bf16 a[0:3], v[128:131], v[160:163], a[0:3]
s_mov_b32 s20, 0
v_mfma_f32_16x16x32_bf16 a[4:7], v[132:135], v[160:163], a[4:7]
s_add_u32 m0, s33, 0
v_mfma_f32_16x16x32_bf16 a[8:11], v[136:139], v[160:163], a[8:11]
buffer_load_dwordx4 v14, s[16:19], s20 offen sc1 lds
v_mfma_f32_16x16x32_bf16 a[12:15], v[140:143], v[160:163], a[12:15]
v_mfma_f32_16x16x32_bf16 a[16:19], v[144:147], v[160:163], a[16:19]
s_add_u32 s20, s20, s21
v_mfma_f32_16x16x32_bf16 a[20:23], v[148:151], v[160:163], a[20:23]
s_add_u32 m0, s33, 1024
v_mfma_f32_16x16x32_bf16 a[24:27], v[152:155], v[160:163], a[24:27]
buffer_load_dwordx4 v14, s[16:19], s20 offen sc1 lds
v_mfma_f32_16x16x32_bf16 a[28:31], v[156:159], v[160:163], a[28:31]
v_mfma_f32_16x16x32_bf16 a[60:63], v[156:159], v[164:167], a[60:63]
s_add_u32 s20, s20, s21
v_mfma_f32_16x16x32_bf16 a[56:59], v[152:155], v[164:167], a[56:59]
s_add_u32 m0, s33, 2048
v_mfma_f32_16x16x32_bf16 a[52:55], v[148:151], v[164:167], a[52:55]
buffer_load_dwordx4 v14, s[16:19], s20 offen sc1 lds
v_mfma_f32_16x16x32_bf16 a[48:51], v[144:147], v[164:167], a[48:51]
v_mfma_f32_16x16x32_bf16 a[44:47], v[140:143], v[164:167], a[44:47]
s_add_u32 s20, s20, s21
v_mfma_f32_16x16x32_bf16 a[40:43], v[136:139], v[164:167], a[40:43]
s_add_u32 m0, s33, 3072
v_mfma_f32_16x16x32_bf16 a[36:39], v[132:135], v[164:167], a[36:39]
buffer_load_dwordx4 v14, s[16:19], s20 offen sc1 lds
v_mfma_f32_16x16x32_bf16 a[32:35], v[128:131], v[164:167], a[32:35]
v_mfma_f32_16x16x32_bf16 a[64:67], v[128:131], v[168:171], a[64:67]
s_add_u32 s20, s20, s21
v_mfma_f32_16x16x32_bf16 a[68:71], v[132:135], v[168:171], a[68:71]
s_add_u32 m0, s33, 4096
v_mfma_f32_16x16x32_bf16 a[72:75], v[136:139], v[168:171], a[72:75]
buffer_load_dwordx4 v14, s[16:19], s20 offen sc1 lds
v_mfma_f32_16x16x32_bf16 a[76:79], v[140:143], v[168:171], a[76:79]
v_mfma_f32_16x16x32_bf16 a[80:83], v[144:147], v[168:171], a[80:83]
s_add_u32 s20, s20, s21
v_mfma_f32_16x16x32_bf16 a[84:87], v[148:151], v[168:171], a[84:87]
s_add_u32 m0, s33, 5120
v_mfma_f32_16x16x32_bf16 a[88:91], v[152:155], v[168:171], a[88:91]
buffer_load_dwordx4 v14, s[16:19], s20 offen sc1 lds
v_mfma_f32_16x16x32_bf16 a[92:95], v[156:159], v[168:171], a[92:95]
v_mfma_f32_16x16x32_bf16 a[124:127], v[156:159], v[172:175], a[124:127]
s_add_u32 s20, s20, s21
v_mfma_f32_16x16x32_bf16 a[120:123], v[152:155], v[172:175], a[120:123]
s_add_u32 m0, s33, 6144
v_mfma_f32_16x16x32_bf16 a[116:119], v[148:151], v[172:175], a[116:119]
buffer_load_dwordx4 v14, s[16:19], s20 offen sc1 lds
v_mfma_f32_16x16x32_bf16 a[112:115], v[144:147], v[172:175], a[112:115]
v_mfma_f32_16x16x32_bf16 a[108:111], v[140:143], v[172:175], a[108:111]
s_add_u32 s20, s20, s21
v_mfma_f32_16x16x32_bf16 a[104:107], v[136:139], v[172:175], a[104:107]
s_add_u32 m0, s33, 7168
v_mfma_f32_16x16x32_bf16 a[100:103], v[132:135], v[172:175], a[100:103]
buffer_load_dwordx4 v14, s[16:19], s20 offen sc1 lds
v_mfma_f32_16x16x32_bf16 a[96:99], v[128:131], v[172:175], a[96:99]
v_mfma_f32_16x16x32_bf16 a[128:131], v[128:131], v[176:179], a[128:131]
s_mov_b32 s20, s14
v_mfma_f32_16x16x32_bf16 a[132:135], v[132:135], v[176:179], a[132:135]
s_add_u32 m0, s33, 32768
v_mfma_f32_16x16x32_bf16 a[136:139], v[136:139], v[176:179], a[136:139]
buffer_load_dwordx4 v14, s[16:19], s20 offen sc1 lds
v_mfma_f32_16x16x32_bf16 a[140:143], v[140:143], v[176:179], a[140:143]
v_mfma_f32_16x16x32_bf16 a[144:147], v[144:147], v[176:179], a[144:147]
s_add_u32 s20, s20, s21
v_mfma_f32_16x16x32_bf16 a[148:151], v[148:151], v[176:179], a[148:151]
s_add_u32 m0, s33, 33792
v_mfma_f32_16x16x32_bf16 a[152:155], v[152:155], v[176:179], a[152:155]
buffer_load_dwordx4 v14, s[16:19], s20 offen sc1 lds
v_mfma_f32_16x16x32_bf16 a[156:159], v[156:159], v[176:179], a[156:159]
v_mfma_f32_16x16x32_bf16 a[188:191], v[156:159], v[180:183], a[188:191]
s_add_u32 s20, s20, s21
v_mfma_f32_16x16x32_bf16 a[184:187], v[152:155], v[180:183], a[184:187]
s_add_u32 m0, s33, 34816
v_mfma_f32_16x16x32_bf16 a[180:183], v[148:151], v[180:183], a[180:183]
buffer_load_dwordx4 v14, s[16:19], s20 offen sc1 lds
v_mfma_f32_16x16x32_bf16 a[176:179], v[144:147], v[180:183], a[176:179]
v_mfma_f32_16x16x32_bf16 a[172:175], v[140:143], v[180:183], a[172:175]
s_add_u32 s20, s20, s21
v_mfma_f32_16x16x32_bf16 a[168:171], v[136:139], v[180:183], a[168:171]
s_add_u32 m0, s33, 35840
v_mfma_f32_16x16x32_bf16 a[164:167], v[132:135], v[180:183], a[164:167]
buffer_load_dwordx4 v14, s[16:19], s20 offen sc1 lds
v_mfma_f32_16x16x32_bf16 a[160:163], v[128:131], v[180:183], a[160:163]
v_mfma_f32_16x16x32_bf16 a[192:195], v[128:131], v[184:187], a[192:195]
s_add_u32 s20, s20, s21
v_mfma_f32_16x16x32_bf16 a[196:199], v[132:135], v[184:187], a[196:199]
s_add_u32 m0, s33, 36864
v_mfma_f32_16x16x32_bf16 a[200:203], v[136:139], v[184:187], a[200:203]
buffer_load_dwordx4 v14, s[16:19], s20 offen sc1 lds
v_mfma_f32_16x16x32_bf16 a[204:207], v[140:143], v[184:187], a[204:207]
v_mfma_f32_16x16x32_bf16 a[208:211], v[144:147], v[184:187], a[208:211]
s_add_u32 s20, s20, s21
v_mfma_f32_16x16x32_bf16 a[212:215], v[148:151], v[184:187], a[212:215]
s_add_u32 m0, s33, 37888
v_mfma_f32_16x16x32_bf16 a[216:219], v[152:155], v[184:187], a[216:219]
buffer_load_dwordx4 v14, s[16:19], s20 offen sc1 lds
v_mfma_f32_16x16x32_bf16 a[220:223], v[156:159], v[184:187], a[220:223]
v_mfma_f32_16x16x32_bf16 a[252:255], v[156:159], v[188:191], a[252:255]
s_add_u32 s20, s20, s21
v_mfma_f32_16x16x32_bf16 a[248:251], v[152:155], v[188:191], a[248:251]
s_add_u32 m0, s33, 38912
v_mfma_f32_16x16x32_bf16 a[244:247], v[148:151], v[188:191], a[244:247]
buffer_load_dwordx4 v14, s[16:19], s20 offen sc1 lds
v_mfma_f32_16x16x32_bf16 a[240:243], v[144:147], v[188:191], a[240:243]
s_add_u32 s20, s20, s21
v_mfma_f32_16x16x32_bf16 a[236:239], v[140:143], v[188:191], a[236:239]
s_add_u32 m0, s33, 39936
v_mfma_f32_16x16x32_bf16 a[232:235], v[136:139], v[188:191], a[232:235]
buffer_load_dwordx4 v14, s[16:19], s20 offen sc1 lds
v_mfma_f32_16x16x32_bf16 a[228:231], v[132:135], v[188:191], a[228:231]
v_mfma_f32_16x16x32_bf16 a[224:227], v[128:131], v[188:191], a[224:227]
s_branch SEAM_%=
SEAM_%=:
s_nop 7
s_cmp_eq_u32 s69, 0
s_cbranch_scc0 SEAMVU_%=
v_accvgpr_read_b32 v192, a128
v_accvgpr_read_b32 v193, a129
v_accvgpr_read_b32 v194, a130
v_accvgpr_read_b32 v195, a131
v_accvgpr_read_b32 v196, a160
v_accvgpr_read_b32 v197, a161
v_accvgpr_read_b32 v198, a162
v_accvgpr_read_b32 v199, a163
v_cvt_pk_bf16_f32 v128, v192, v193
v_cvt_pk_bf16_f32 v129, v194, v195
v_cvt_pk_bf16_f32 v130, v196, v197
v_cvt_pk_bf16_f32 v131, v198, v199
v_accvgpr_read_b32 v200, a132
v_accvgpr_read_b32 v201, a133
v_accvgpr_read_b32 v202, a134
v_accvgpr_read_b32 v203, a135
v_accvgpr_read_b32 v204, a164
v_accvgpr_read_b32 v205, a165
s_mul_i32 s20, s14, 2
s_add_u32 m0, s33, 65536
s_nop 0
buffer_load_dwordx4 v14, s[16:19], s20 offen sc1 lds
v_accvgpr_read_b32 v206, a166
v_accvgpr_read_b32 v207, a167
v_cvt_pk_bf16_f32 v132, v200, v201
v_cvt_pk_bf16_f32 v133, v202, v203
v_cvt_pk_bf16_f32 v134, v204, v205
v_cvt_pk_bf16_f32 v135, v206, v207
v_accvgpr_read_b32 v208, a136
v_accvgpr_read_b32 v209, a137
v_accvgpr_read_b32 v210, a138
v_accvgpr_read_b32 v211, a139
v_accvgpr_read_b32 v212, a168
v_accvgpr_read_b32 v213, a169
v_accvgpr_read_b32 v214, a170
v_accvgpr_read_b32 v215, a171
v_cvt_pk_bf16_f32 v136, v208, v209
v_cvt_pk_bf16_f32 v137, v210, v211
v_cvt_pk_bf16_f32 v138, v212, v213
v_cvt_pk_bf16_f32 v139, v214, v215
s_add_u32 s20, s20, s21
s_add_u32 m0, s33, 66560
s_nop 0
buffer_load_dwordx4 v14, s[16:19], s20 offen sc1 lds
v_accvgpr_read_b32 v192, a140
v_accvgpr_read_b32 v193, a141
v_accvgpr_read_b32 v194, a142
v_accvgpr_read_b32 v195, a143
v_accvgpr_read_b32 v196, a172
v_accvgpr_read_b32 v197, a173
v_accvgpr_read_b32 v198, a174
v_accvgpr_read_b32 v199, a175
v_cvt_pk_bf16_f32 v140, v192, v193
v_cvt_pk_bf16_f32 v141, v194, v195
v_cvt_pk_bf16_f32 v142, v196, v197
v_cvt_pk_bf16_f32 v143, v198, v199
v_accvgpr_read_b32 v200, a192
v_accvgpr_read_b32 v201, a193
v_accvgpr_read_b32 v202, a194
v_accvgpr_read_b32 v203, a195
v_accvgpr_read_b32 v204, a224
v_accvgpr_read_b32 v205, a225
s_add_u32 s20, s20, s21
s_add_u32 m0, s33, 67584
s_nop 0
buffer_load_dwordx4 v14, s[16:19], s20 offen sc1 lds
v_accvgpr_read_b32 v206, a226
v_accvgpr_read_b32 v207, a227
v_cvt_pk_bf16_f32 v144, v200, v201
v_cvt_pk_bf16_f32 v145, v202, v203
v_cvt_pk_bf16_f32 v146, v204, v205
v_cvt_pk_bf16_f32 v147, v206, v207
v_accvgpr_read_b32 v208, a196
v_accvgpr_read_b32 v209, a197
v_accvgpr_read_b32 v210, a198
v_accvgpr_read_b32 v211, a199
v_accvgpr_read_b32 v212, a228
v_accvgpr_read_b32 v213, a229
v_accvgpr_read_b32 v214, a230
v_accvgpr_read_b32 v215, a231
v_cvt_pk_bf16_f32 v148, v208, v209
v_cvt_pk_bf16_f32 v149, v210, v211
v_cvt_pk_bf16_f32 v150, v212, v213
v_cvt_pk_bf16_f32 v151, v214, v215
s_add_u32 s20, s20, s21
s_add_u32 m0, s33, 68608
s_nop 0
buffer_load_dwordx4 v14, s[16:19], s20 offen sc1 lds
v_accvgpr_read_b32 v192, a200
v_accvgpr_read_b32 v193, a201
v_accvgpr_read_b32 v194, a202
v_accvgpr_read_b32 v195, a203
v_accvgpr_read_b32 v196, a232
v_accvgpr_read_b32 v197, a233
v_accvgpr_read_b32 v198, a234
v_accvgpr_read_b32 v199, a235
v_cvt_pk_bf16_f32 v152, v192, v193
v_cvt_pk_bf16_f32 v153, v194, v195
v_cvt_pk_bf16_f32 v154, v196, v197
v_cvt_pk_bf16_f32 v155, v198, v199
v_accvgpr_read_b32 v200, a204
v_accvgpr_read_b32 v201, a205
v_accvgpr_read_b32 v202, a206
v_accvgpr_read_b32 v203, a207
v_accvgpr_read_b32 v204, a236
v_accvgpr_read_b32 v205, a237
s_add_u32 s20, s20, s21
s_add_u32 m0, s33, 69632
s_nop 0
buffer_load_dwordx4 v14, s[16:19], s20 offen sc1 lds
v_accvgpr_read_b32 v206, a238
v_accvgpr_read_b32 v207, a239
v_cvt_pk_bf16_f32 v156, v200, v201
v_cvt_pk_bf16_f32 v157, v202, v203
v_cvt_pk_bf16_f32 v158, v204, v205
v_cvt_pk_bf16_f32 v159, v206, v207
v_accvgpr_read_b32 v208, a144
v_accvgpr_read_b32 v209, a145
v_accvgpr_read_b32 v210, a146
v_accvgpr_read_b32 v211, a147
v_accvgpr_read_b32 v212, a176
v_accvgpr_read_b32 v213, a177
v_accvgpr_read_b32 v214, a178
v_accvgpr_read_b32 v215, a179
v_cvt_pk_bf16_f32 v160, v208, v209
v_cvt_pk_bf16_f32 v161, v210, v211
v_cvt_pk_bf16_f32 v162, v212, v213
v_cvt_pk_bf16_f32 v163, v214, v215
s_add_u32 s20, s20, s21
s_add_u32 m0, s33, 70656
s_nop 0
buffer_load_dwordx4 v14, s[16:19], s20 offen sc1 lds
v_accvgpr_read_b32 v192, a148
v_accvgpr_read_b32 v193, a149
v_accvgpr_read_b32 v194, a150
v_accvgpr_read_b32 v195, a151
v_accvgpr_read_b32 v196, a180
v_accvgpr_read_b32 v197, a181
v_accvgpr_read_b32 v198, a182
v_accvgpr_read_b32 v199, a183
v_cvt_pk_bf16_f32 v164, v192, v193
v_cvt_pk_bf16_f32 v165, v194, v195
v_cvt_pk_bf16_f32 v166, v196, v197
v_cvt_pk_bf16_f32 v167, v198, v199
v_accvgpr_read_b32 v200, a152
v_accvgpr_read_b32 v201, a153
v_accvgpr_read_b32 v202, a154
v_accvgpr_read_b32 v203, a155
v_accvgpr_read_b32 v204, a184
v_accvgpr_read_b32 v205, a185
s_add_u32 s20, s20, s21
s_add_u32 m0, s33, 71680
s_nop 0
buffer_load_dwordx4 v14, s[16:19], s20 offen sc1 lds
v_accvgpr_read_b32 v206, a186
v_accvgpr_read_b32 v207, a187
v_cvt_pk_bf16_f32 v168, v200, v201
v_cvt_pk_bf16_f32 v169, v202, v203
v_cvt_pk_bf16_f32 v170, v204, v205
v_cvt_pk_bf16_f32 v171, v206, v207
v_accvgpr_read_b32 v208, a156
v_accvgpr_read_b32 v209, a157
v_accvgpr_read_b32 v210, a158
v_accvgpr_read_b32 v211, a159
v_accvgpr_read_b32 v212, a188
v_accvgpr_read_b32 v213, a189
v_accvgpr_read_b32 v214, a190
v_accvgpr_read_b32 v215, a191
v_cvt_pk_bf16_f32 v172, v208, v209
v_cvt_pk_bf16_f32 v173, v210, v211
v_cvt_pk_bf16_f32 v174, v212, v213
v_cvt_pk_bf16_f32 v175, v214, v215
s_add_u32 s20, s20, s21
s_add_u32 m0, s33, 72704
s_nop 0
buffer_load_dwordx4 v14, s[16:19], s20 offen sc1 lds
v_accvgpr_read_b32 v192, a208
v_accvgpr_read_b32 v193, a209
v_accvgpr_read_b32 v194, a210
v_accvgpr_read_b32 v195, a211
v_accvgpr_read_b32 v196, a240
v_accvgpr_read_b32 v197, a241
v_accvgpr_read_b32 v198, a242
v_accvgpr_read_b32 v199, a243
v_cvt_pk_bf16_f32 v176, v192, v193
v_cvt_pk_bf16_f32 v177, v194, v195
v_cvt_pk_bf16_f32 v178, v196, v197
v_cvt_pk_bf16_f32 v179, v198, v199
v_accvgpr_read_b32 v200, a212
v_accvgpr_read_b32 v201, a213
v_accvgpr_read_b32 v202, a214
v_accvgpr_read_b32 v203, a215
v_accvgpr_read_b32 v204, a244
v_accvgpr_read_b32 v205, a245
s_mul_i32 s20, s14, 3
s_add_u32 m0, s33, 98304
s_nop 0
buffer_load_dwordx4 v14, s[16:19], s20 offen sc1 lds
v_accvgpr_read_b32 v206, a246
v_accvgpr_read_b32 v207, a247
v_cvt_pk_bf16_f32 v180, v200, v201
v_cvt_pk_bf16_f32 v181, v202, v203
v_cvt_pk_bf16_f32 v182, v204, v205
v_cvt_pk_bf16_f32 v183, v206, v207
v_accvgpr_read_b32 v208, a216
v_accvgpr_read_b32 v209, a217
v_accvgpr_read_b32 v210, a218
v_accvgpr_read_b32 v211, a219
v_accvgpr_read_b32 v212, a248
v_accvgpr_read_b32 v213, a249
v_accvgpr_read_b32 v214, a250
v_accvgpr_read_b32 v215, a251
v_cvt_pk_bf16_f32 v184, v208, v209
v_cvt_pk_bf16_f32 v185, v210, v211
v_cvt_pk_bf16_f32 v186, v212, v213
v_cvt_pk_bf16_f32 v187, v214, v215
s_add_u32 s20, s20, s21
s_add_u32 m0, s33, 99328
s_nop 0
buffer_load_dwordx4 v14, s[16:19], s20 offen sc1 lds
v_accvgpr_read_b32 v192, a220
v_accvgpr_read_b32 v193, a221
v_accvgpr_read_b32 v194, a222
v_accvgpr_read_b32 v195, a223
v_accvgpr_read_b32 v196, a252
v_accvgpr_read_b32 v197, a253
v_accvgpr_read_b32 v198, a254
v_accvgpr_read_b32 v199, a255
v_cvt_pk_bf16_f32 v188, v192, v193
v_cvt_pk_bf16_f32 v189, v194, v195
v_cvt_pk_bf16_f32 v190, v196, v197
v_cvt_pk_bf16_f32 v191, v198, v199
s_branch SEAMXU_%=
SEAMVU_%=:
v_accvgpr_read_b32 v192, a128
v_accvgpr_read_b32 v193, a129
v_accvgpr_read_b32 v194, a130
v_accvgpr_read_b32 v195, a131
v_accvgpr_read_b32 v196, a132
v_accvgpr_read_b32 v197, a133
v_accvgpr_read_b32 v198, a134
v_accvgpr_read_b32 v199, a135
v_cvt_pk_bf16_f32 v128, v192, v193
v_cvt_pk_bf16_f32 v129, v194, v195
v_cvt_pk_bf16_f32 v130, v196, v197
v_cvt_pk_bf16_f32 v131, v198, v199
v_accvgpr_read_b32 v200, a160
v_accvgpr_read_b32 v201, a161
v_accvgpr_read_b32 v202, a162
v_accvgpr_read_b32 v203, a163
v_accvgpr_read_b32 v204, a164
v_accvgpr_read_b32 v205, a165
s_mul_i32 s20, s14, 2
s_add_u32 m0, s33, 65536
s_nop 0
buffer_load_dwordx4 v14, s[16:19], s20 offen sc1 lds
v_accvgpr_read_b32 v206, a166
v_accvgpr_read_b32 v207, a167
v_cvt_pk_bf16_f32 v132, v200, v201
v_cvt_pk_bf16_f32 v133, v202, v203
v_cvt_pk_bf16_f32 v134, v204, v205
v_cvt_pk_bf16_f32 v135, v206, v207
v_accvgpr_read_b32 v208, a192
v_accvgpr_read_b32 v209, a193
v_accvgpr_read_b32 v210, a194
v_accvgpr_read_b32 v211, a195
v_accvgpr_read_b32 v212, a196
v_accvgpr_read_b32 v213, a197
v_accvgpr_read_b32 v214, a198
v_accvgpr_read_b32 v215, a199
v_cvt_pk_bf16_f32 v136, v208, v209
v_cvt_pk_bf16_f32 v137, v210, v211
v_cvt_pk_bf16_f32 v138, v212, v213
v_cvt_pk_bf16_f32 v139, v214, v215
s_add_u32 s20, s20, s21
s_add_u32 m0, s33, 66560
s_nop 0
buffer_load_dwordx4 v14, s[16:19], s20 offen sc1 lds
v_accvgpr_read_b32 v192, a224
v_accvgpr_read_b32 v193, a225
v_accvgpr_read_b32 v194, a226
v_accvgpr_read_b32 v195, a227
v_accvgpr_read_b32 v196, a228
v_accvgpr_read_b32 v197, a229
v_accvgpr_read_b32 v198, a230
v_accvgpr_read_b32 v199, a231
v_cvt_pk_bf16_f32 v140, v192, v193
v_cvt_pk_bf16_f32 v141, v194, v195
v_cvt_pk_bf16_f32 v142, v196, v197
v_cvt_pk_bf16_f32 v143, v198, v199
v_accvgpr_read_b32 v200, a136
v_accvgpr_read_b32 v201, a137
v_accvgpr_read_b32 v202, a138
v_accvgpr_read_b32 v203, a139
v_accvgpr_read_b32 v204, a140
v_accvgpr_read_b32 v205, a141
s_add_u32 s20, s20, s21
s_add_u32 m0, s33, 67584
s_nop 0
buffer_load_dwordx4 v14, s[16:19], s20 offen sc1 lds
v_accvgpr_read_b32 v206, a142
v_accvgpr_read_b32 v207, a143
v_cvt_pk_bf16_f32 v144, v200, v201
v_cvt_pk_bf16_f32 v145, v202, v203
v_cvt_pk_bf16_f32 v146, v204, v205
v_cvt_pk_bf16_f32 v147, v206, v207
v_accvgpr_read_b32 v208, a168
v_accvgpr_read_b32 v209, a169
v_accvgpr_read_b32 v210, a170
v_accvgpr_read_b32 v211, a171
v_accvgpr_read_b32 v212, a172
v_accvgpr_read_b32 v213, a173
v_accvgpr_read_b32 v214, a174
v_accvgpr_read_b32 v215, a175
v_cvt_pk_bf16_f32 v148, v208, v209
v_cvt_pk_bf16_f32 v149, v210, v211
v_cvt_pk_bf16_f32 v150, v212, v213
v_cvt_pk_bf16_f32 v151, v214, v215
s_add_u32 s20, s20, s21
s_add_u32 m0, s33, 68608
s_nop 0
buffer_load_dwordx4 v14, s[16:19], s20 offen sc1 lds
v_accvgpr_read_b32 v192, a200
v_accvgpr_read_b32 v193, a201
v_accvgpr_read_b32 v194, a202
v_accvgpr_read_b32 v195, a203
v_accvgpr_read_b32 v196, a204
v_accvgpr_read_b32 v197, a205
v_accvgpr_read_b32 v198, a206
v_accvgpr_read_b32 v199, a207
v_cvt_pk_bf16_f32 v152, v192, v193
v_cvt_pk_bf16_f32 v153, v194, v195
v_cvt_pk_bf16_f32 v154, v196, v197
v_cvt_pk_bf16_f32 v155, v198, v199
v_accvgpr_read_b32 v200, a232
v_accvgpr_read_b32 v201, a233
v_accvgpr_read_b32 v202, a234
v_accvgpr_read_b32 v203, a235
v_accvgpr_read_b32 v204, a236
v_accvgpr_read_b32 v205, a237
s_add_u32 s20, s20, s21
s_add_u32 m0, s33, 69632
s_nop 0
buffer_load_dwordx4 v14, s[16:19], s20 offen sc1 lds
v_accvgpr_read_b32 v206, a238
v_accvgpr_read_b32 v207, a239
v_cvt_pk_bf16_f32 v156, v200, v201
v_cvt_pk_bf16_f32 v157, v202, v203
v_cvt_pk_bf16_f32 v158, v204, v205
v_cvt_pk_bf16_f32 v159, v206, v207
v_accvgpr_read_b32 v208, a144
v_accvgpr_read_b32 v209, a145
v_accvgpr_read_b32 v210, a146
v_accvgpr_read_b32 v211, a147
v_accvgpr_read_b32 v212, a148
v_accvgpr_read_b32 v213, a149
v_accvgpr_read_b32 v214, a150
v_accvgpr_read_b32 v215, a151
v_cvt_pk_bf16_f32 v160, v208, v209
v_cvt_pk_bf16_f32 v161, v210, v211
v_cvt_pk_bf16_f32 v162, v212, v213
v_cvt_pk_bf16_f32 v163, v214, v215
s_add_u32 s20, s20, s21
s_add_u32 m0, s33, 70656
s_nop 0
buffer_load_dwordx4 v14, s[16:19], s20 offen sc1 lds
v_accvgpr_read_b32 v192, a176
v_accvgpr_read_b32 v193, a177
v_accvgpr_read_b32 v194, a178
v_accvgpr_read_b32 v195, a179
v_accvgpr_read_b32 v196, a180
v_accvgpr_read_b32 v197, a181
v_accvgpr_read_b32 v198, a182
v_accvgpr_read_b32 v199, a183
v_cvt_pk_bf16_f32 v164, v192, v193
v_cvt_pk_bf16_f32 v165, v194, v195
v_cvt_pk_bf16_f32 v166, v196, v197
v_cvt_pk_bf16_f32 v167, v198, v199
v_accvgpr_read_b32 v200, a208
v_accvgpr_read_b32 v201, a209
v_accvgpr_read_b32 v202, a210
v_accvgpr_read_b32 v203, a211
v_accvgpr_read_b32 v204, a212
v_accvgpr_read_b32 v205, a213
s_add_u32 s20, s20, s21
s_add_u32 m0, s33, 71680
s_nop 0
buffer_load_dwordx4 v14, s[16:19], s20 offen sc1 lds
v_accvgpr_read_b32 v206, a214
v_accvgpr_read_b32 v207, a215
v_cvt_pk_bf16_f32 v168, v200, v201
v_cvt_pk_bf16_f32 v169, v202, v203
v_cvt_pk_bf16_f32 v170, v204, v205
v_cvt_pk_bf16_f32 v171, v206, v207
v_accvgpr_read_b32 v208, a240
v_accvgpr_read_b32 v209, a241
v_accvgpr_read_b32 v210, a242
v_accvgpr_read_b32 v211, a243
v_accvgpr_read_b32 v212, a244
v_accvgpr_read_b32 v213, a245
v_accvgpr_read_b32 v214, a246
v_accvgpr_read_b32 v215, a247
v_cvt_pk_bf16_f32 v172, v208, v209
v_cvt_pk_bf16_f32 v173, v210, v211
v_cvt_pk_bf16_f32 v174, v212, v213
v_cvt_pk_bf16_f32 v175, v214, v215
s_add_u32 s20, s20, s21
s_add_u32 m0, s33, 72704
s_nop 0
buffer_load_dwordx4 v14, s[16:19], s20 offen sc1 lds
v_accvgpr_read_b32 v192, a152
v_accvgpr_read_b32 v193, a153
v_accvgpr_read_b32 v194, a154
v_accvgpr_read_b32 v195, a155
v_accvgpr_read_b32 v196, a156
v_accvgpr_read_b32 v197, a157
v_accvgpr_read_b32 v198, a158
v_accvgpr_read_b32 v199, a159
v_cvt_pk_bf16_f32 v176, v192, v193
v_cvt_pk_bf16_f32 v177, v194, v195
v_cvt_pk_bf16_f32 v178, v196, v197
v_cvt_pk_bf16_f32 v179, v198, v199
v_accvgpr_read_b32 v200, a184
v_accvgpr_read_b32 v201, a185
v_accvgpr_read_b32 v202, a186
v_accvgpr_read_b32 v203, a187
v_accvgpr_read_b32 v204, a188
v_accvgpr_read_b32 v205, a189
s_mul_i32 s20, s14, 3
s_add_u32 m0, s33, 98304
s_nop 0
buffer_load_dwordx4 v14, s[16:19], s20 offen sc1 lds
v_accvgpr_read_b32 v206, a190
v_accvgpr_read_b32 v207, a191
v_cvt_pk_bf16_f32 v180, v200, v201
v_cvt_pk_bf16_f32 v181, v202, v203
v_cvt_pk_bf16_f32 v182, v204, v205
v_cvt_pk_bf16_f32 v183, v206, v207
v_accvgpr_read_b32 v208, a216
v_accvgpr_read_b32 v209, a217
v_accvgpr_read_b32 v210, a218
v_accvgpr_read_b32 v211, a219
v_accvgpr_read_b32 v212, a220
v_accvgpr_read_b32 v213, a221
v_accvgpr_read_b32 v214, a222
v_accvgpr_read_b32 v215, a223
v_cvt_pk_bf16_f32 v184, v208, v209
v_cvt_pk_bf16_f32 v185, v210, v211
v_cvt_pk_bf16_f32 v186, v212, v213
v_cvt_pk_bf16_f32 v187, v214, v215
s_add_u32 s20, s20, s21
s_add_u32 m0, s33, 99328
s_nop 0
buffer_load_dwordx4 v14, s[16:19], s20 offen sc1 lds
v_accvgpr_read_b32 v192, a248
v_accvgpr_read_b32 v193, a249
v_accvgpr_read_b32 v194, a250
v_accvgpr_read_b32 v195, a251
v_accvgpr_read_b32 v196, a252
v_accvgpr_read_b32 v197, a253
v_accvgpr_read_b32 v198, a254
v_accvgpr_read_b32 v199, a255
v_cvt_pk_bf16_f32 v188, v192, v193
v_cvt_pk_bf16_f32 v189, v194, v195
v_cvt_pk_bf16_f32 v190, v196, v197
v_cvt_pk_bf16_f32 v191, v198, v199
SEAMXU_%=:
s_cmp_eq_u32 s69, 0
s_cbranch_scc0 SEAMVL_%=
v_accvgpr_read_b32 v192, a0
v_accvgpr_read_b32 v193, a1
v_accvgpr_read_b32 v194, a2
v_accvgpr_read_b32 v195, a3
v_accvgpr_read_b32 v196, a32
v_accvgpr_read_b32 v197, a33
v_accvgpr_read_b32 v198, a34
v_accvgpr_read_b32 v199, a35
v_cvt_pk_bf16_f32 v64, v192, v193
v_cvt_pk_bf16_f32 v65, v194, v195
v_cvt_pk_bf16_f32 v66, v196, v197
v_cvt_pk_bf16_f32 v67, v198, v199
v_accvgpr_read_b32 v200, a4
v_accvgpr_read_b32 v201, a5
v_accvgpr_read_b32 v202, a6
s_add_u32 s48, s45, 4096
buffer_load_dwordx4 a[160:163], v2, s[36:39], s48 offen offset:0 sc1
v_accvgpr_read_b32 v203, a7
v_accvgpr_read_b32 v204, a36
v_accvgpr_read_b32 v205, a37
v_accvgpr_read_b32 v206, a38
v_accvgpr_read_b32 v207, a39
v_cvt_pk_bf16_f32 v68, v200, v201
v_cvt_pk_bf16_f32 v69, v202, v203
v_cvt_pk_bf16_f32 v70, v204, v205
v_cvt_pk_bf16_f32 v71, v206, v207
v_accvgpr_read_b32 v208, a8
v_accvgpr_read_b32 v209, a9
v_accvgpr_read_b32 v210, a10
v_accvgpr_read_b32 v211, a11
v_accvgpr_read_b32 v212, a40
v_accvgpr_read_b32 v213, a41
s_add_u32 s48, s46, 4096
buffer_load_dwordx4 a[164:167], v2, s[36:39], s48 offen offset:0 sc1
v_accvgpr_read_b32 v214, a42
v_accvgpr_read_b32 v215, a43
v_cvt_pk_bf16_f32 v72, v208, v209
v_cvt_pk_bf16_f32 v73, v210, v211
v_cvt_pk_bf16_f32 v74, v212, v213
v_cvt_pk_bf16_f32 v75, v214, v215
v_accvgpr_read_b32 v192, a12
v_accvgpr_read_b32 v193, a13
v_accvgpr_read_b32 v194, a14
v_accvgpr_read_b32 v195, a15
v_accvgpr_read_b32 v196, a44
v_accvgpr_read_b32 v197, a45
v_accvgpr_read_b32 v198, a46
v_accvgpr_read_b32 v199, a47
v_cvt_pk_bf16_f32 v76, v192, v193
s_add_u32 s48, s45, 4096
buffer_load_dwordx4 a[168:171], v2, s[36:39], s48 offen offset:1024 sc1
v_cvt_pk_bf16_f32 v77, v194, v195
v_cvt_pk_bf16_f32 v78, v196, v197
v_cvt_pk_bf16_f32 v79, v198, v199
v_accvgpr_read_b32 v200, a64
v_accvgpr_read_b32 v201, a65
v_accvgpr_read_b32 v202, a66
v_accvgpr_read_b32 v203, a67
v_accvgpr_read_b32 v204, a96
v_accvgpr_read_b32 v205, a97
v_accvgpr_read_b32 v206, a98
v_accvgpr_read_b32 v207, a99
v_cvt_pk_bf16_f32 v80, v200, v201
v_cvt_pk_bf16_f32 v81, v202, v203
v_cvt_pk_bf16_f32 v82, v204, v205
v_cvt_pk_bf16_f32 v83, v206, v207
s_add_u32 s48, s46, 4096
buffer_load_dwordx4 a[172:175], v2, s[36:39], s48 offen offset:1024 sc1
v_accvgpr_read_b32 v208, a68
v_accvgpr_read_b32 v209, a69
v_accvgpr_read_b32 v210, a70
v_accvgpr_read_b32 v211, a71
v_accvgpr_read_b32 v212, a100
v_accvgpr_read_b32 v213, a101
v_accvgpr_read_b32 v214, a102
v_accvgpr_read_b32 v215, a103
v_cvt_pk_bf16_f32 v84, v208, v209
v_cvt_pk_bf16_f32 v85, v210, v211
v_cvt_pk_bf16_f32 v86, v212, v213
v_cvt_pk_bf16_f32 v87, v214, v215
v_accvgpr_read_b32 v192, a72
v_accvgpr_read_b32 v193, a73
v_accvgpr_read_b32 v194, a74
s_add_u32 s48, s45, 4096
buffer_load_dwordx4 a[176:179], v2, s[36:39], s48 offen offset:2048 sc1
v_accvgpr_read_b32 v195, a75
v_accvgpr_read_b32 v196, a104
v_accvgpr_read_b32 v197, a105
v_accvgpr_read_b32 v198, a106
v_accvgpr_read_b32 v199, a107
v_cvt_pk_bf16_f32 v88, v192, v193
v_cvt_pk_bf16_f32 v89, v194, v195
v_cvt_pk_bf16_f32 v90, v196, v197
v_cvt_pk_bf16_f32 v91, v198, v199
v_accvgpr_read_b32 v200, a76
v_accvgpr_read_b32 v201, a77
v_accvgpr_read_b32 v202, a78
v_accvgpr_read_b32 v203, a79
v_accvgpr_read_b32 v204, a108
v_accvgpr_read_b32 v205, a109
s_add_u32 s48, s46, 4096
buffer_load_dwordx4 a[180:183], v2, s[36:39], s48 offen offset:2048 sc1
v_accvgpr_read_b32 v206, a110
v_accvgpr_read_b32 v207, a111
v_cvt_pk_bf16_f32 v92, v200, v201
v_cvt_pk_bf16_f32 v93, v202, v203
v_cvt_pk_bf16_f32 v94, v204, v205
v_cvt_pk_bf16_f32 v95, v206, v207
v_accvgpr_read_b32 v208, a16
v_accvgpr_read_b32 v209, a17
v_accvgpr_read_b32 v210, a18
v_accvgpr_read_b32 v211, a19
v_accvgpr_read_b32 v212, a48
v_accvgpr_read_b32 v213, a49
v_accvgpr_read_b32 v214, a50
v_accvgpr_read_b32 v215, a51
v_cvt_pk_bf16_f32 v96, v208, v209
s_add_u32 s48, s45, 4096
buffer_load_dwordx4 a[184:187], v2, s[36:39], s48 offen offset:3072 sc1
v_cvt_pk_bf16_f32 v97, v210, v211
v_cvt_pk_bf16_f32 v98, v212, v213
v_cvt_pk_bf16_f32 v99, v214, v215
v_accvgpr_read_b32 v192, a20
v_accvgpr_read_b32 v193, a21
v_accvgpr_read_b32 v194, a22
v_accvgpr_read_b32 v195, a23
v_accvgpr_read_b32 v196, a52
v_accvgpr_read_b32 v197, a53
v_accvgpr_read_b32 v198, a54
v_accvgpr_read_b32 v199, a55
v_cvt_pk_bf16_f32 v100, v192, v193
v_cvt_pk_bf16_f32 v101, v194, v195
v_cvt_pk_bf16_f32 v102, v196, v197
v_cvt_pk_bf16_f32 v103, v198, v199
s_add_u32 s48, s46, 4096
buffer_load_dwordx4 a[188:191], v2, s[36:39], s48 offen offset:3072 sc1
v_accvgpr_read_b32 v200, a24
v_accvgpr_read_b32 v201, a25
v_accvgpr_read_b32 v202, a26
v_accvgpr_read_b32 v203, a27
v_accvgpr_read_b32 v204, a56
v_accvgpr_read_b32 v205, a57
v_accvgpr_read_b32 v206, a58
v_accvgpr_read_b32 v207, a59
v_cvt_pk_bf16_f32 v104, v200, v201
v_cvt_pk_bf16_f32 v105, v202, v203
v_cvt_pk_bf16_f32 v106, v204, v205
v_cvt_pk_bf16_f32 v107, v206, v207
v_accvgpr_read_b32 v208, a28
v_accvgpr_read_b32 v209, a29
v_accvgpr_read_b32 v210, a30
s_add_u32 s48, s45, 8192
buffer_load_dwordx4 a[192:195], v2, s[36:39], s48 offen offset:0 sc1
v_accvgpr_read_b32 v211, a31
v_accvgpr_read_b32 v212, a60
v_accvgpr_read_b32 v213, a61
v_accvgpr_read_b32 v214, a62
v_accvgpr_read_b32 v215, a63
v_cvt_pk_bf16_f32 v108, v208, v209
v_cvt_pk_bf16_f32 v109, v210, v211
v_cvt_pk_bf16_f32 v110, v212, v213
v_cvt_pk_bf16_f32 v111, v214, v215
v_accvgpr_read_b32 v192, a80
v_accvgpr_read_b32 v193, a81
v_accvgpr_read_b32 v194, a82
v_accvgpr_read_b32 v195, a83
v_accvgpr_read_b32 v196, a112
v_accvgpr_read_b32 v197, a113
s_add_u32 s48, s46, 8192
buffer_load_dwordx4 a[196:199], v2, s[36:39], s48 offen offset:0 sc1
v_accvgpr_read_b32 v198, a114
v_accvgpr_read_b32 v199, a115
v_cvt_pk_bf16_f32 v112, v192, v193
v_cvt_pk_bf16_f32 v113, v194, v195
v_cvt_pk_bf16_f32 v114, v196, v197
v_cvt_pk_bf16_f32 v115, v198, v199
v_accvgpr_read_b32 v200, a84
v_accvgpr_read_b32 v201, a85
v_accvgpr_read_b32 v202, a86
v_accvgpr_read_b32 v203, a87
v_accvgpr_read_b32 v204, a116
v_accvgpr_read_b32 v205, a117
v_accvgpr_read_b32 v206, a118
v_accvgpr_read_b32 v207, a119
v_cvt_pk_bf16_f32 v116, v200, v201
s_add_u32 s48, s45, 8192
buffer_load_dwordx4 a[200:203], v2, s[36:39], s48 offen offset:1024 sc1
v_cvt_pk_bf16_f32 v117, v202, v203
v_cvt_pk_bf16_f32 v118, v204, v205
v_cvt_pk_bf16_f32 v119, v206, v207
v_accvgpr_read_b32 v208, a88
v_accvgpr_read_b32 v209, a89
v_accvgpr_read_b32 v210, a90
v_accvgpr_read_b32 v211, a91
v_accvgpr_read_b32 v212, a120
v_accvgpr_read_b32 v213, a121
v_accvgpr_read_b32 v214, a122
v_accvgpr_read_b32 v215, a123
v_cvt_pk_bf16_f32 v120, v208, v209
v_cvt_pk_bf16_f32 v121, v210, v211
v_cvt_pk_bf16_f32 v122, v212, v213
v_cvt_pk_bf16_f32 v123, v214, v215
s_add_u32 s48, s46, 8192
buffer_load_dwordx4 a[204:207], v2, s[36:39], s48 offen offset:1024 sc1
v_accvgpr_read_b32 v192, a92
v_accvgpr_read_b32 v193, a93
v_accvgpr_read_b32 v194, a94
v_accvgpr_read_b32 v195, a95
v_accvgpr_read_b32 v196, a124
v_accvgpr_read_b32 v197, a125
v_accvgpr_read_b32 v198, a126
v_accvgpr_read_b32 v199, a127
v_cvt_pk_bf16_f32 v124, v192, v193
v_cvt_pk_bf16_f32 v125, v194, v195
v_cvt_pk_bf16_f32 v126, v196, v197
v_cvt_pk_bf16_f32 v127, v198, v199
s_branch SEAMXL_%=
SEAMVL_%=:
v_accvgpr_read_b32 v192, a0
v_accvgpr_read_b32 v193, a1
v_accvgpr_read_b32 v194, a2
v_accvgpr_read_b32 v195, a3
v_accvgpr_read_b32 v196, a4
v_accvgpr_read_b32 v197, a5
v_accvgpr_read_b32 v198, a6
v_accvgpr_read_b32 v199, a7
v_cvt_pk_bf16_f32 v64, v192, v193
v_cvt_pk_bf16_f32 v65, v194, v195
v_cvt_pk_bf16_f32 v66, v196, v197
v_cvt_pk_bf16_f32 v67, v198, v199
v_accvgpr_read_b32 v200, a32
v_accvgpr_read_b32 v201, a33
v_accvgpr_read_b32 v202, a34
s_add_u32 s48, s45, 4096
buffer_load_dwordx4 a[160:163], v2, s[36:39], s48 offen offset:0 sc1
v_accvgpr_read_b32 v203, a35
v_accvgpr_read_b32 v204, a36
v_accvgpr_read_b32 v205, a37
v_accvgpr_read_b32 v206, a38
v_accvgpr_read_b32 v207, a39
v_cvt_pk_bf16_f32 v68, v200, v201
v_cvt_pk_bf16_f32 v69, v202, v203
v_cvt_pk_bf16_f32 v70, v204, v205
v_cvt_pk_bf16_f32 v71, v206, v207
v_accvgpr_read_b32 v208, a64
v_accvgpr_read_b32 v209, a65
v_accvgpr_read_b32 v210, a66
v_accvgpr_read_b32 v211, a67
v_accvgpr_read_b32 v212, a68
v_accvgpr_read_b32 v213, a69
s_add_u32 s48, s46, 4096
buffer_load_dwordx4 a[164:167], v2, s[36:39], s48 offen offset:0 sc1
v_accvgpr_read_b32 v214, a70
v_accvgpr_read_b32 v215, a71
v_cvt_pk_bf16_f32 v72, v208, v209
v_cvt_pk_bf16_f32 v73, v210, v211
v_cvt_pk_bf16_f32 v74, v212, v213
v_cvt_pk_bf16_f32 v75, v214, v215
v_accvgpr_read_b32 v192, a96
v_accvgpr_read_b32 v193, a97
v_accvgpr_read_b32 v194, a98
v_accvgpr_read_b32 v195, a99
v_accvgpr_read_b32 v196, a100
v_accvgpr_read_b32 v197, a101
v_accvgpr_read_b32 v198, a102
v_accvgpr_read_b32 v199, a103
v_cvt_pk_bf16_f32 v76, v192, v193
s_add_u32 s48, s45, 4096
buffer_load_dwordx4 a[168:171], v2, s[36:39], s48 offen offset:1024 sc1
v_cvt_pk_bf16_f32 v77, v194, v195
v_cvt_pk_bf16_f32 v78, v196, v197
v_cvt_pk_bf16_f32 v79, v198, v199
v_accvgpr_read_b32 v200, a8
v_accvgpr_read_b32 v201, a9
v_accvgpr_read_b32 v202, a10
v_accvgpr_read_b32 v203, a11
v_accvgpr_read_b32 v204, a12
v_accvgpr_read_b32 v205, a13
v_accvgpr_read_b32 v206, a14
v_accvgpr_read_b32 v207, a15
v_cvt_pk_bf16_f32 v80, v200, v201
v_cvt_pk_bf16_f32 v81, v202, v203
v_cvt_pk_bf16_f32 v82, v204, v205
v_cvt_pk_bf16_f32 v83, v206, v207
s_add_u32 s48, s46, 4096
buffer_load_dwordx4 a[172:175], v2, s[36:39], s48 offen offset:1024 sc1
v_accvgpr_read_b32 v208, a40
v_accvgpr_read_b32 v209, a41
v_accvgpr_read_b32 v210, a42
v_accvgpr_read_b32 v211, a43
v_accvgpr_read_b32 v212, a44
v_accvgpr_read_b32 v213, a45
v_accvgpr_read_b32 v214, a46
v_accvgpr_read_b32 v215, a47
v_cvt_pk_bf16_f32 v84, v208, v209
v_cvt_pk_bf16_f32 v85, v210, v211
v_cvt_pk_bf16_f32 v86, v212, v213
v_cvt_pk_bf16_f32 v87, v214, v215
v_accvgpr_read_b32 v192, a72
v_accvgpr_read_b32 v193, a73
v_accvgpr_read_b32 v194, a74
s_add_u32 s48, s45, 4096
buffer_load_dwordx4 a[176:179], v2, s[36:39], s48 offen offset:2048 sc1
v_accvgpr_read_b32 v195, a75
v_accvgpr_read_b32 v196, a76
v_accvgpr_read_b32 v197, a77
v_accvgpr_read_b32 v198, a78
v_accvgpr_read_b32 v199, a79
v_cvt_pk_bf16_f32 v88, v192, v193
v_cvt_pk_bf16_f32 v89, v194, v195
v_cvt_pk_bf16_f32 v90, v196, v197
v_cvt_pk_bf16_f32 v91, v198, v199
v_accvgpr_read_b32 v200, a104
v_accvgpr_read_b32 v201, a105
v_accvgpr_read_b32 v202, a106
v_accvgpr_read_b32 v203, a107
v_accvgpr_read_b32 v204, a108
v_accvgpr_read_b32 v205, a109
s_add_u32 s48, s46, 4096
buffer_load_dwordx4 a[180:183], v2, s[36:39], s48 offen offset:2048 sc1
v_accvgpr_read_b32 v206, a110
v_accvgpr_read_b32 v207, a111
v_cvt_pk_bf16_f32 v92, v200, v201
v_cvt_pk_bf16_f32 v93, v202, v203
v_cvt_pk_bf16_f32 v94, v204, v205
v_cvt_pk_bf16_f32 v95, v206, v207
v_accvgpr_read_b32 v208, a16
v_accvgpr_read_b32 v209, a17
v_accvgpr_read_b32 v210, a18
v_accvgpr_read_b32 v211, a19
v_accvgpr_read_b32 v212, a20
v_accvgpr_read_b32 v213, a21
v_accvgpr_read_b32 v214, a22
v_accvgpr_read_b32 v215, a23
v_cvt_pk_bf16_f32 v96, v208, v209
s_add_u32 s48, s45, 4096
buffer_load_dwordx4 a[184:187], v2, s[36:39], s48 offen offset:3072 sc1
v_cvt_pk_bf16_f32 v97, v210, v211
v_cvt_pk_bf16_f32 v98, v212, v213
v_cvt_pk_bf16_f32 v99, v214, v215
v_accvgpr_read_b32 v192, a48
v_accvgpr_read_b32 v193, a49
v_accvgpr_read_b32 v194, a50
v_accvgpr_read_b32 v195, a51
v_accvgpr_read_b32 v196, a52
v_accvgpr_read_b32 v197, a53
v_accvgpr_read_b32 v198, a54
v_accvgpr_read_b32 v199, a55
v_cvt_pk_bf16_f32 v100, v192, v193
v_cvt_pk_bf16_f32 v101, v194, v195
v_cvt_pk_bf16_f32 v102, v196, v197
v_cvt_pk_bf16_f32 v103, v198, v199
s_add_u32 s48, s46, 4096
buffer_load_dwordx4 a[188:191], v2, s[36:39], s48 offen offset:3072 sc1
v_accvgpr_read_b32 v200, a80
v_accvgpr_read_b32 v201, a81
v_accvgpr_read_b32 v202, a82
v_accvgpr_read_b32 v203, a83
v_accvgpr_read_b32 v204, a84
v_accvgpr_read_b32 v205, a85
v_accvgpr_read_b32 v206, a86
v_accvgpr_read_b32 v207, a87
v_cvt_pk_bf16_f32 v104, v200, v201
v_cvt_pk_bf16_f32 v105, v202, v203
v_cvt_pk_bf16_f32 v106, v204, v205
v_cvt_pk_bf16_f32 v107, v206, v207
v_accvgpr_read_b32 v208, a112
v_accvgpr_read_b32 v209, a113
v_accvgpr_read_b32 v210, a114
s_add_u32 s48, s45, 8192
buffer_load_dwordx4 a[192:195], v2, s[36:39], s48 offen offset:0 sc1
v_accvgpr_read_b32 v211, a115
v_accvgpr_read_b32 v212, a116
v_accvgpr_read_b32 v213, a117
v_accvgpr_read_b32 v214, a118
v_accvgpr_read_b32 v215, a119
v_cvt_pk_bf16_f32 v108, v208, v209
v_cvt_pk_bf16_f32 v109, v210, v211
v_cvt_pk_bf16_f32 v110, v212, v213
v_cvt_pk_bf16_f32 v111, v214, v215
v_accvgpr_read_b32 v192, a24
v_accvgpr_read_b32 v193, a25
v_accvgpr_read_b32 v194, a26
v_accvgpr_read_b32 v195, a27
v_accvgpr_read_b32 v196, a28
v_accvgpr_read_b32 v197, a29
s_add_u32 s48, s46, 8192
buffer_load_dwordx4 a[196:199], v2, s[36:39], s48 offen offset:0 sc1
v_accvgpr_read_b32 v198, a30
v_accvgpr_read_b32 v199, a31
v_cvt_pk_bf16_f32 v112, v192, v193
v_cvt_pk_bf16_f32 v113, v194, v195
v_cvt_pk_bf16_f32 v114, v196, v197
v_cvt_pk_bf16_f32 v115, v198, v199
v_accvgpr_read_b32 v200, a56
v_accvgpr_read_b32 v201, a57
v_accvgpr_read_b32 v202, a58
v_accvgpr_read_b32 v203, a59
v_accvgpr_read_b32 v204, a60
v_accvgpr_read_b32 v205, a61
v_accvgpr_read_b32 v206, a62
v_accvgpr_read_b32 v207, a63
v_cvt_pk_bf16_f32 v116, v200, v201
s_add_u32 s48, s45, 8192
buffer_load_dwordx4 a[200:203], v2, s[36:39], s48 offen offset:1024 sc1
v_cvt_pk_bf16_f32 v117, v202, v203
v_cvt_pk_bf16_f32 v118, v204, v205
v_cvt_pk_bf16_f32 v119, v206, v207
v_accvgpr_read_b32 v208, a88
v_accvgpr_read_b32 v209, a89
v_accvgpr_read_b32 v210, a90
v_accvgpr_read_b32 v211, a91
v_accvgpr_read_b32 v212, a92
v_accvgpr_read_b32 v213, a93
v_accvgpr_read_b32 v214, a94
v_accvgpr_read_b32 v215, a95
v_cvt_pk_bf16_f32 v120, v208, v209
v_cvt_pk_bf16_f32 v121, v210, v211
v_cvt_pk_bf16_f32 v122, v212, v213
v_cvt_pk_bf16_f32 v123, v214, v215
s_add_u32 s48, s46, 8192
buffer_load_dwordx4 a[204:207], v2, s[36:39], s48 offen offset:1024 sc1
v_accvgpr_read_b32 v192, a120
v_accvgpr_read_b32 v193, a121
v_accvgpr_read_b32 v194, a122
v_accvgpr_read_b32 v195, a123
v_accvgpr_read_b32 v196, a124
v_accvgpr_read_b32 v197, a125
v_accvgpr_read_b32 v198, a126
v_accvgpr_read_b32 v199, a127
v_cvt_pk_bf16_f32 v124, v192, v193
v_cvt_pk_bf16_f32 v125, v194, v195
v_cvt_pk_bf16_f32 v126, v196, v197
v_cvt_pk_bf16_f32 v127, v198, v199
SEAMXL_%=:
s_mov_b64 s[62:63], s[86:87]
s_lshl_b32 s47, s30, 8
s_lshl_b32 s65, s28, 6
s_add_u32 s65, s65, s47
s_mul_i32 s65, s65, 896
v_cmp_le_u32 s[48:49], s57, v1
v_subrev_u32 v224, s57, v1
v_min_u32 v224, 6, v224
s_mov_b32 s60, 128
v_mul_lo_u32 v224, s60, v224
v_add_u32 v224, s65, v224
v_cndmask_b32_e64 v231, v231, v224, s[48:49]
v_mov_b32 v224, s62
v_mov_b32 v225, s63
v_mov_b32 v226, 896
v_cndmask_b32_e64 v232, v232, v224, s[48:49]
v_cndmask_b32_e64 v233, v233, v225, s[48:49]
v_cndmask_b32_e64 v18, v18, v226, s[48:49]
v_add_co_u32 v16, vcc, v232, v231
v_addc_co_u32 v17, vcc, 0, v233, vcc
v_mov_b32 v42, s98
v_mov_b32 v43, v249
s_lshl_b32 s12, s29, 2
s_add_u32 s12, s12, s28
s_lshl_b32 s12, s12, 6
s_add_u32 s12, s12, 102891520
s_mov_b32 s56, 536870912
s_mov_b32 s58, 102760448
s_lshl_b32 s47, s10, 2
s_add_u32 s47, s47, s28
s_mul_i32 s47, s47, 57344
s_add_u32 s13, s47, 62914560
v_lshlrev_b32 v47, 4, v1
s_lshr_b32 s50, s10, 1
s_sub_u32 s51, 15, s50
s_cmp_ge_u32 s50, 8
s_cselect_b32 s51, s51, s50
s_cselect_b32 s52, 0, 1
s_lshl_b32 s51, s51, 1
s_add_u32 s51, s51, s52
s_lshl_b32 s51, s51, 4
s_and_b32 s52, s10, 1
s_lshl_b32 s52, s52, 1
s_lshr_b32 s53, s28, 1
s_add_u32 s52, s52, s53
v_and_b32 v46, 15, v1
v_min_u32 v46, 13, v46
v_lshrrev_b32 v229, 1, v46
v_and_b32 v230, 1, v46
v_lshl_add_u32 v229, v230, 3, v229
v_add_u32 v229, s51, v229
v_lshlrev_b32 v229, 2, v229
v_add_u32 v229, s52, v229
v_lshlrev_b32 v229, 6, v229
v_add_u32 v46, 102957056, v229
s_mov_b32 s47, 4096
s_cmp_eq_u32 s69, 0
s_cselect_b32 s47, 8192, s47
s_add_u32 s53, s75, s47
s_mov_b32 s61, s53
s_add_u32 s62, s53, s99
s_add_u32 s63, s53, 32768
s_add_u32 s64, s63, s99
s_mov_b32 s66, s75
s_add_u32 s67, s75, s99
s_add_u32 s50, s75, 32768
s_add_u32 s51, s50, s99
s_and_b32 s47, s29, 7
s_lshr_b32 s48, s29, 3
s_and_b32 s48, s48, 1
s_lshl_b32 s48, s48, 4
s_add_u32 s47, s47, s48
v_lshrrev_b32 v224, 3, v1
v_lshlrev_b32 v224, 5, v224
v_bfe_u32 v225, v1, 2, 1
v_lshl_add_u32 v224, v225, 3, v224
v_add_u32 v224, s47, v224
v_and_b32 v225, 3, v1
v_lshl_add_u32 v224, v224, 2, v225
v_lshlrev_b32 v224, 6, v224
v_add_u32 v44, 102891520, v224
s_mov_b32 s22, 4
s_mov_b32 s24, 0
s_mov_b32 s27, 131072
s_add_u32 s23, s33, 98304
s_cmp_eq_u32 s43, 0
s_cbranch_scc0 SEAMZ_%=
buffer_store_dwordx4 v[128:131], v0, s[36:39], s61 offen offset:0 sc1
buffer_store_dwordx4 v[132:135], v0, s[36:39], s61 offen offset:1024 sc1
buffer_store_dwordx4 v[136:139], v0, s[36:39], s61 offen offset:2048 sc1
buffer_store_dwordx4 v[140:143], v0, s[36:39], s61 offen offset:3072 sc1
buffer_store_dwordx4 v[144:147], v0, s[36:39], s62 offen offset:0 sc1
buffer_store_dwordx4 v[148:151], v0, s[36:39], s62 offen offset:1024 sc1
buffer_store_dwordx4 v[152:155], v0, s[36:39], s62 offen offset:2048 sc1
buffer_store_dwordx4 v[156:159], v0, s[36:39], s62 offen offset:3072 sc1
buffer_store_dwordx4 v[160:163], v0, s[36:39], s63 offen offset:0 sc1
buffer_store_dwordx4 v[164:167], v0, s[36:39], s63 offen offset:1024 sc1
buffer_store_dwordx4 v[168:171], v0, s[36:39], s63 offen offset:2048 sc1
buffer_store_dwordx4 v[172:175], v0, s[36:39], s63 offen offset:3072 sc1
buffer_store_dwordx4 v[176:179], v0, s[36:39], s64 offen offset:0 sc1
buffer_store_dwordx4 v[180:183], v0, s[36:39], s64 offen offset:1024 sc1
buffer_store_dwordx4 v[184:187], v0, s[36:39], s64 offen offset:2048 sc1
buffer_store_dwordx4 v[188:191], v0, s[36:39], s64 offen offset:3072 sc1
buffer_store_dwordx4 v[64:67], v0, s[36:39], s66 offen offset:0 sc1
buffer_store_dwordx4 v[68:71], v0, s[36:39], s66 offen offset:1024 sc1
buffer_store_dwordx4 v[72:75], v0, s[36:39], s66 offen offset:2048 sc1
buffer_store_dwordx4 v[76:79], v0, s[36:39], s66 offen offset:3072 sc1
buffer_store_dwordx4 v[80:83], v0, s[36:39], s67 offen offset:0 sc1
buffer_store_dwordx4 v[84:87], v0, s[36:39], s67 offen offset:1024 sc1
buffer_store_dwordx4 v[88:91], v0, s[36:39], s67 offen offset:2048 sc1
buffer_store_dwordx4 v[92:95], v0, s[36:39], s67 offen offset:3072 sc1
buffer_store_dwordx4 v[96:99], v0, s[36:39], s50 offen offset:0 sc1
buffer_store_dwordx4 v[100:103], v0, s[36:39], s50 offen offset:1024 sc1
buffer_store_dwordx4 v[104:107], v0, s[36:39], s50 offen offset:2048 sc1
buffer_store_dwordx4 v[108:111], v0, s[36:39], s50 offen offset:3072 sc1
buffer_store_dwordx4 v[112:115], v0, s[36:39], s51 offen offset:0 sc1
buffer_store_dwordx4 v[116:119], v0, s[36:39], s51 offen offset:1024 sc1
buffer_store_dwordx4 v[120:123], v0, s[36:39], s51 offen offset:2048 sc1
buffer_store_dwordx4 v[124:127], v0, s[36:39], s51 offen offset:3072 sc1
s_add_u32 s48, s45, 0
buffer_load_dwordx4 a[128:131], v2, s[36:39], s48 offen offset:0 sc1
s_add_u32 s48, s46, 0
buffer_load_dwordx4 a[132:135], v2, s[36:39], s48 offen offset:0 sc1
s_add_u32 s48, s45, 0
buffer_load_dwordx4 a[136:139], v2, s[36:39], s48 offen offset:1024 sc1
s_add_u32 s48, s46, 0
buffer_load_dwordx4 a[140:143], v2, s[36:39], s48 offen offset:1024 sc1
s_add_u32 s48, s45, 0
buffer_load_dwordx4 a[144:147], v2, s[36:39], s48 offen offset:2048 sc1
s_add_u32 s48, s46, 0
buffer_load_dwordx4 a[148:151], v2, s[36:39], s48 offen offset:2048 sc1
s_add_u32 s48, s45, 0
buffer_load_dwordx4 a[152:155], v2, s[36:39], s48 offen offset:3072 sc1
s_add_u32 s48, s46, 0
buffer_load_dwordx4 a[156:159], v2, s[36:39], s48 offen offset:3072 sc1
s_add_u32 s48, s45, 8192
buffer_load_dwordx4 a[208:211], v2, s[36:39], s48 offen offset:2048 sc1
s_add_u32 s48, s46, 8192
buffer_load_dwordx4 a[212:215], v2, s[36:39], s48 offen offset:2048 sc1
s_add_u32 s48, s45, 8192
buffer_load_dwordx4 a[216:219], v2, s[36:39], s48 offen offset:3072 sc1
s_add_u32 s48, s46, 8192
buffer_load_dwordx4 a[220:223], v2, s[36:39], s48 offen offset:3072 sc1
s_add_u32 s48, s45, 12288
buffer_load_dwordx4 a[224:227], v2, s[36:39], s48 offen offset:0 sc1
s_add_u32 s48, s46, 12288
buffer_load_dwordx4 a[228:231], v2, s[36:39], s48 offen offset:0 sc1
s_add_u32 s48, s45, 12288
buffer_load_dwordx4 a[232:235], v2, s[36:39], s48 offen offset:1024 sc1
s_add_u32 s48, s46, 12288
buffer_load_dwordx4 a[236:239], v2, s[36:39], s48 offen offset:1024 sc1
s_waitcnt vmcnt(0)
buffer_store_dword v42, v43, s[36:39], s12 offen sc1
s_mov_b32 s61, 536870912
s_mov_b32 s62, 536870912
s_mov_b32 s63, 536870912
s_mov_b32 s64, 536870912
s_mov_b32 s66, 536870912
s_mov_b32 s67, 536870912
s_mov_b32 s50, 536870912
s_mov_b32 s51, 536870912
SEAMZ_%=:
s_mov_b32 s69, 536870912
s_waitcnt vmcnt(22)
s_barrier
s_cmp_eq_u32 s43, 0
s_cbranch_scc1 NOGU_%=
s_getpc_b64 s[70:71]
CALLPC_m0_%=:
s_add_u32 s70, s70, CALLRET_m0_%=-CALLPC_m0_%=
s_addc_u32 s71, s71, 0
s_branch W64F_%=
CALLRET_m0_%=:
s_getpc_b64 s[70:71]
CALLPC_m1_%=:
s_add_u32 s70, s70, CALLRET_m1_%=-CALLPC_m1_%=
s_addc_u32 s71, s71, 0
s_branch GUEPI_%=
CALLRET_m1_%=:
s_lshl_b32 s56, s29, 2
s_add_u32 s56, s56, s28
s_lshl_b32 s56, s56, 6
s_add_u32 s56, s56, 102957056
s_getpc_b64 s[70:71]
CALLPC_m2_%=:
s_add_u32 s70, s70, CALLRET_m2_%=-CALLPC_m2_%=
s_addc_u32 s71, s71, 0
s_branch W64GN_%=
CALLRET_m2_%=:
s_branch M_QE_%=
NOGU_%=:
s_getpc_b64 s[70:71]
CALLPC_m2b_%=:
s_add_u32 s70, s70, CALLRET_m2b_%=-CALLPC_m2b_%=
s_addc_u32 s71, s71, 0
s_branch W64G_%=
CALLRET_m2b_%=:
M_QE_%=:
s_cmp_eq_u32 s40, 0
s_cbranch_scc0 M_QB_%=
s_getpc_b64 s[70:71]
CALLPC_m3_%=:
s_add_u32 s70, s70, CALLRET_m3_%=-CALLPC_m3_%=
s_addc_u32 s71, s71, 0
s_branch QEPIA_%=
CALLRET_m3_%=:
s_lshl_b32 s56, s42, 2
s_add_u32 s56, s56, s28
s_lshl_b32 s56, s56, 6
s_add_u32 s56, s56, 102760448
s_waitcnt vmcnt(16)
s_branch M_FD_%=
M_QB_%=:
s_getpc_b64 s[70:71]
CALLPC_m4_%=:
s_add_u32 s70, s70, CALLRET_m4_%=-CALLPC_m4_%=
s_addc_u32 s71, s71, 0
s_branch QEPIB_%=
CALLRET_m4_%=:
s_waitcnt vmcnt(16)
M_FD_%=:
s_lshl_b32 s58, s42, 2
s_add_u32 s58, s58, s28
s_lshl_b32 s58, s58, 6
s_add_u32 s58, s58, 102760448
s_sub_u32 s44, 15, s31
s_lshl_b32 s59, s30, 4
s_add_u32 s59, s59, s44
s_lshl_b32 s59, s59, 2
s_add_u32 s59, s59, s28
s_lshl_b32 s59, s59, 14
s_add_u32 s59, s59, 35651584
s_getpc_b64 s[70:71]
CALLPC_m5_%=:
s_add_u32 s70, s70, CALLRET_m5_%=-CALLPC_m5_%=
s_addc_u32 s71, s71, 0
s_branch FD_%=
CALLRET_m5_%=:
s_cmp_eq_u32 s40, 0
s_cbranch_scc0 PATHB_%=
s_lshl_b32 s58, s42, 2
s_add_u32 s58, s58, s28
s_lshl_b32 s58, s58, 6
s_add_u32 s58, s58, 102793216
s_mul_i32 s59, s42, 69632
s_mul_i32 s48, s28, 17408
s_add_u32 s59, s59, s48
s_add_u32 s59, s59, 52428800
s_mov_b32 s60, 1
s_sub_u32 s44, 15, s31
s_sub_u32 s34, s68, 1
s_mov_b32 s45, 63
s_getpc_b64 s[70:71]
CALLPC_a6_%=:
s_add_u32 s70, s70, CALLRET_a6_%=-CALLPC_a6_%=
s_addc_u32 s71, s71, 0
s_branch SEG_%=
CALLRET_a6_%=:
v_max_f32 v33, v28, v224
v_sub_f32 v34, v28, v33
v_sub_f32 v35, v224, v33
v_exp_f32 v34, v34
v_exp_f32 v35, v35
s_nop 0
v_mul_f32 v30, v30, v34
v_fmac_f32 v30, v226, v35
v_mov_b32 v36, v30
s_nop 1
v_permlane16_swap_b32 v30, v36
s_nop 1
v_add_f32 v30, v30, v36
v_mov_b32 v36, v30
s_nop 1
v_permlane32_swap_b32 v30, v36
s_nop 1
v_add_f32 v30, v30, v36
v_rcp_f32 v36, v30
s_nop 0
v_mul_f32 v232, v34, v36
v_mul_f32 v234, v35, v36
v_mov_b32 v233, v232
v_mov_b32 v235, v234
v_max_f32 v37, v29, v225
v_sub_f32 v38, v29, v37
v_sub_f32 v39, v225, v37
v_exp_f32 v38, v38
v_exp_f32 v39, v39
s_nop 0
v_mul_f32 v31, v31, v38
v_fmac_f32 v31, v227, v39
v_mov_b32 v40, v31
s_nop 1
v_permlane16_swap_b32 v31, v40
s_nop 1
v_add_f32 v31, v31, v40
v_mov_b32 v40, v31
s_nop 1
v_permlane32_swap_b32 v31, v40
s_nop 1
v_add_f32 v31, v31, v40
v_rcp_f32 v40, v31
s_nop 0
v_mul_f32 v236, v38, v40
v_mul_f32 v238, v39, v40
v_mov_b32 v237, v236
v_mov_b32 v239, v238
v_accvgpr_read_b32 v128, a0
v_accvgpr_read_b32 v129, a1
v_accvgpr_read_b32 v130, a2
v_accvgpr_read_b32 v131, a3
v_accvgpr_read_b32 v132, a8
v_accvgpr_read_b32 v133, a9
v_accvgpr_read_b32 v134, a10
v_accvgpr_read_b32 v135, a11
v_lshlrev_b32 v136, 16, v64
v_and_b32 v137, 0xffff0000, v64
v_lshlrev_b32 v138, 16, v65
v_and_b32 v139, 0xffff0000, v65
v_lshlrev_b32 v140, 16, v66
v_and_b32 v141, 0xffff0000, v66
v_lshlrev_b32 v142, 16, v67
v_and_b32 v143, 0xffff0000, v67
v_pk_mul_f32 v[128:129], v[128:129], v[232:233]
v_pk_mul_f32 v[130:131], v[130:131], v[232:233]
v_pk_mul_f32 v[132:133], v[132:133], v[232:233]
v_pk_mul_f32 v[134:135], v[134:135], v[232:233]
v_pk_fma_f32 v[128:129], v[136:137], v[234:235], v[128:129]
v_pk_fma_f32 v[130:131], v[138:139], v[234:235], v[130:131]
v_pk_fma_f32 v[132:133], v[140:141], v[234:235], v[132:133]
v_pk_fma_f32 v[134:135], v[142:143], v[234:235], v[134:135]
v_cvt_pk_bf16_f32 v64, v128, v129
v_cvt_pk_bf16_f32 v65, v130, v131
v_cvt_pk_bf16_f32 v66, v132, v133
v_cvt_pk_bf16_f32 v67, v134, v135
v_accvgpr_read_b32 v144, a4
v_accvgpr_read_b32 v145, a5
v_accvgpr_read_b32 v146, a6
v_accvgpr_read_b32 v147, a7
v_accvgpr_read_b32 v148, a12
v_accvgpr_read_b32 v149, a13
v_accvgpr_read_b32 v150, a14
v_accvgpr_read_b32 v151, a15
v_lshlrev_b32 v152, 16, v68
v_and_b32 v153, 0xffff0000, v68
v_lshlrev_b32 v154, 16, v69
v_and_b32 v155, 0xffff0000, v69
v_lshlrev_b32 v156, 16, v70
v_and_b32 v157, 0xffff0000, v70
v_lshlrev_b32 v158, 16, v71
v_and_b32 v159, 0xffff0000, v71
v_pk_mul_f32 v[144:145], v[144:145], v[236:237]
v_pk_mul_f32 v[146:147], v[146:147], v[236:237]
v_pk_mul_f32 v[148:149], v[148:149], v[236:237]
v_pk_mul_f32 v[150:151], v[150:151], v[236:237]
v_pk_fma_f32 v[144:145], v[152:153], v[238:239], v[144:145]
v_pk_fma_f32 v[146:147], v[154:155], v[238:239], v[146:147]
v_pk_fma_f32 v[148:149], v[156:157], v[238:239], v[148:149]
v_pk_fma_f32 v[150:151], v[158:159], v[238:239], v[150:151]
v_cvt_pk_bf16_f32 v68, v144, v145
v_cvt_pk_bf16_f32 v69, v146, v147
v_cvt_pk_bf16_f32 v70, v148, v149
v_cvt_pk_bf16_f32 v71, v150, v151
v_accvgpr_read_b32 v160, a16
v_accvgpr_read_b32 v161, a17
v_accvgpr_read_b32 v162, a18
v_accvgpr_read_b32 v163, a19
v_accvgpr_read_b32 v164, a24
v_accvgpr_read_b32 v165, a25
v_accvgpr_read_b32 v166, a26
v_accvgpr_read_b32 v167, a27
v_lshlrev_b32 v168, 16, v72
v_and_b32 v169, 0xffff0000, v72
v_lshlrev_b32 v170, 16, v73
v_and_b32 v171, 0xffff0000, v73
v_lshlrev_b32 v172, 16, v74
v_and_b32 v173, 0xffff0000, v74
v_lshlrev_b32 v174, 16, v75
v_and_b32 v175, 0xffff0000, v75
v_pk_mul_f32 v[160:161], v[160:161], v[232:233]
v_pk_mul_f32 v[162:163], v[162:163], v[232:233]
v_pk_mul_f32 v[164:165], v[164:165], v[232:233]
v_pk_mul_f32 v[166:167], v[166:167], v[232:233]
v_pk_fma_f32 v[160:161], v[168:169], v[234:235], v[160:161]
v_pk_fma_f32 v[162:163], v[170:171], v[234:235], v[162:163]
v_pk_fma_f32 v[164:165], v[172:173], v[234:235], v[164:165]
v_pk_fma_f32 v[166:167], v[174:175], v[234:235], v[166:167]
v_cvt_pk_bf16_f32 v72, v160, v161
v_cvt_pk_bf16_f32 v73, v162, v163
v_cvt_pk_bf16_f32 v74, v164, v165
v_cvt_pk_bf16_f32 v75, v166, v167
v_accvgpr_read_b32 v176, a20
v_accvgpr_read_b32 v177, a21
v_accvgpr_read_b32 v178, a22
v_accvgpr_read_b32 v179, a23
v_accvgpr_read_b32 v180, a28
v_accvgpr_read_b32 v181, a29
v_accvgpr_read_b32 v182, a30
v_accvgpr_read_b32 v183, a31
v_lshlrev_b32 v184, 16, v76
v_and_b32 v185, 0xffff0000, v76
v_lshlrev_b32 v186, 16, v77
v_and_b32 v187, 0xffff0000, v77
v_lshlrev_b32 v188, 16, v78
v_and_b32 v189, 0xffff0000, v78
v_lshlrev_b32 v190, 16, v79
v_and_b32 v191, 0xffff0000, v79
v_pk_mul_f32 v[176:177], v[176:177], v[236:237]
v_pk_mul_f32 v[178:179], v[178:179], v[236:237]
v_pk_mul_f32 v[180:181], v[180:181], v[236:237]
v_pk_mul_f32 v[182:183], v[182:183], v[236:237]
v_pk_fma_f32 v[176:177], v[184:185], v[238:239], v[176:177]
v_pk_fma_f32 v[178:179], v[186:187], v[238:239], v[178:179]
v_pk_fma_f32 v[180:181], v[188:189], v[238:239], v[180:181]
v_pk_fma_f32 v[182:183], v[190:191], v[238:239], v[182:183]
v_cvt_pk_bf16_f32 v76, v176, v177
v_cvt_pk_bf16_f32 v77, v178, v179
v_cvt_pk_bf16_f32 v78, v180, v181
v_cvt_pk_bf16_f32 v79, v182, v183
v_accvgpr_read_b32 v128, a32
v_accvgpr_read_b32 v129, a33
v_accvgpr_read_b32 v130, a34
v_accvgpr_read_b32 v131, a35
v_accvgpr_read_b32 v132, a40
v_accvgpr_read_b32 v133, a41
v_accvgpr_read_b32 v134, a42
v_accvgpr_read_b32 v135, a43
v_lshlrev_b32 v136, 16, v80
v_and_b32 v137, 0xffff0000, v80
v_lshlrev_b32 v138, 16, v81
v_and_b32 v139, 0xffff0000, v81
v_lshlrev_b32 v140, 16, v82
v_and_b32 v141, 0xffff0000, v82
v_lshlrev_b32 v142, 16, v83
v_and_b32 v143, 0xffff0000, v83
v_pk_mul_f32 v[128:129], v[128:129], v[232:233]
v_pk_mul_f32 v[130:131], v[130:131], v[232:233]
v_pk_mul_f32 v[132:133], v[132:133], v[232:233]
v_pk_mul_f32 v[134:135], v[134:135], v[232:233]
v_pk_fma_f32 v[128:129], v[136:137], v[234:235], v[128:129]
v_pk_fma_f32 v[130:131], v[138:139], v[234:235], v[130:131]
v_pk_fma_f32 v[132:133], v[140:141], v[234:235], v[132:133]
v_pk_fma_f32 v[134:135], v[142:143], v[234:235], v[134:135]
v_cvt_pk_bf16_f32 v80, v128, v129
v_cvt_pk_bf16_f32 v81, v130, v131
v_cvt_pk_bf16_f32 v82, v132, v133
v_cvt_pk_bf16_f32 v83, v134, v135
v_accvgpr_read_b32 v144, a36
v_accvgpr_read_b32 v145, a37
v_accvgpr_read_b32 v146, a38
v_accvgpr_read_b32 v147, a39
v_accvgpr_read_b32 v148, a44
v_accvgpr_read_b32 v149, a45
v_accvgpr_read_b32 v150, a46
v_accvgpr_read_b32 v151, a47
v_lshlrev_b32 v152, 16, v84
v_and_b32 v153, 0xffff0000, v84
v_lshlrev_b32 v154, 16, v85
v_and_b32 v155, 0xffff0000, v85
v_lshlrev_b32 v156, 16, v86
v_and_b32 v157, 0xffff0000, v86
v_lshlrev_b32 v158, 16, v87
v_and_b32 v159, 0xffff0000, v87
v_pk_mul_f32 v[144:145], v[144:145], v[236:237]
v_pk_mul_f32 v[146:147], v[146:147], v[236:237]
v_pk_mul_f32 v[148:149], v[148:149], v[236:237]
v_pk_mul_f32 v[150:151], v[150:151], v[236:237]
v_pk_fma_f32 v[144:145], v[152:153], v[238:239], v[144:145]
v_pk_fma_f32 v[146:147], v[154:155], v[238:239], v[146:147]
v_pk_fma_f32 v[148:149], v[156:157], v[238:239], v[148:149]
v_pk_fma_f32 v[150:151], v[158:159], v[238:239], v[150:151]
v_cvt_pk_bf16_f32 v84, v144, v145
v_cvt_pk_bf16_f32 v85, v146, v147
v_cvt_pk_bf16_f32 v86, v148, v149
v_cvt_pk_bf16_f32 v87, v150, v151
v_accvgpr_read_b32 v160, a48
v_accvgpr_read_b32 v161, a49
v_accvgpr_read_b32 v162, a50
v_accvgpr_read_b32 v163, a51
v_accvgpr_read_b32 v164, a56
v_accvgpr_read_b32 v165, a57
v_accvgpr_read_b32 v166, a58
v_accvgpr_read_b32 v167, a59
v_lshlrev_b32 v168, 16, v88
v_and_b32 v169, 0xffff0000, v88
v_lshlrev_b32 v170, 16, v89
v_and_b32 v171, 0xffff0000, v89
v_lshlrev_b32 v172, 16, v90
v_and_b32 v173, 0xffff0000, v90
v_lshlrev_b32 v174, 16, v91
v_and_b32 v175, 0xffff0000, v91
v_pk_mul_f32 v[160:161], v[160:161], v[232:233]
v_pk_mul_f32 v[162:163], v[162:163], v[232:233]
v_pk_mul_f32 v[164:165], v[164:165], v[232:233]
v_pk_mul_f32 v[166:167], v[166:167], v[232:233]
v_pk_fma_f32 v[160:161], v[168:169], v[234:235], v[160:161]
v_pk_fma_f32 v[162:163], v[170:171], v[234:235], v[162:163]
v_pk_fma_f32 v[164:165], v[172:173], v[234:235], v[164:165]
v_pk_fma_f32 v[166:167], v[174:175], v[234:235], v[166:167]
v_cvt_pk_bf16_f32 v88, v160, v161
v_cvt_pk_bf16_f32 v89, v162, v163
v_cvt_pk_bf16_f32 v90, v164, v165
v_cvt_pk_bf16_f32 v91, v166, v167
v_accvgpr_read_b32 v176, a52
v_accvgpr_read_b32 v177, a53
v_accvgpr_read_b32 v178, a54
v_accvgpr_read_b32 v179, a55
v_accvgpr_read_b32 v180, a60
v_accvgpr_read_b32 v181, a61
v_accvgpr_read_b32 v182, a62
v_accvgpr_read_b32 v183, a63
v_lshlrev_b32 v184, 16, v92
v_and_b32 v185, 0xffff0000, v92
v_lshlrev_b32 v186, 16, v93
v_and_b32 v187, 0xffff0000, v93
v_lshlrev_b32 v188, 16, v94
v_and_b32 v189, 0xffff0000, v94
v_lshlrev_b32 v190, 16, v95
v_and_b32 v191, 0xffff0000, v95
v_pk_mul_f32 v[176:177], v[176:177], v[236:237]
v_pk_mul_f32 v[178:179], v[178:179], v[236:237]
v_pk_mul_f32 v[180:181], v[180:181], v[236:237]
v_pk_mul_f32 v[182:183], v[182:183], v[236:237]
v_pk_fma_f32 v[176:177], v[184:185], v[238:239], v[176:177]
v_pk_fma_f32 v[178:179], v[186:187], v[238:239], v[178:179]
v_pk_fma_f32 v[180:181], v[188:189], v[238:239], v[180:181]
v_pk_fma_f32 v[182:183], v[190:191], v[238:239], v[182:183]
v_cvt_pk_bf16_f32 v92, v176, v177
v_cvt_pk_bf16_f32 v93, v178, v179
v_cvt_pk_bf16_f32 v94, v180, v181
v_cvt_pk_bf16_f32 v95, v182, v183
v_accvgpr_read_b32 v128, a64
v_accvgpr_read_b32 v129, a65
v_accvgpr_read_b32 v130, a66
v_accvgpr_read_b32 v131, a67
v_accvgpr_read_b32 v132, a72
v_accvgpr_read_b32 v133, a73
v_accvgpr_read_b32 v134, a74
v_accvgpr_read_b32 v135, a75
v_lshlrev_b32 v136, 16, v96
v_and_b32 v137, 0xffff0000, v96
v_lshlrev_b32 v138, 16, v97
v_and_b32 v139, 0xffff0000, v97
v_lshlrev_b32 v140, 16, v98
v_and_b32 v141, 0xffff0000, v98
v_lshlrev_b32 v142, 16, v99
v_and_b32 v143, 0xffff0000, v99
v_pk_mul_f32 v[128:129], v[128:129], v[232:233]
v_pk_mul_f32 v[130:131], v[130:131], v[232:233]
v_pk_mul_f32 v[132:133], v[132:133], v[232:233]
v_pk_mul_f32 v[134:135], v[134:135], v[232:233]
v_pk_fma_f32 v[128:129], v[136:137], v[234:235], v[128:129]
v_pk_fma_f32 v[130:131], v[138:139], v[234:235], v[130:131]
v_pk_fma_f32 v[132:133], v[140:141], v[234:235], v[132:133]
v_pk_fma_f32 v[134:135], v[142:143], v[234:235], v[134:135]
v_cvt_pk_bf16_f32 v96, v128, v129
v_cvt_pk_bf16_f32 v97, v130, v131
v_cvt_pk_bf16_f32 v98, v132, v133
v_cvt_pk_bf16_f32 v99, v134, v135
v_accvgpr_read_b32 v144, a68
v_accvgpr_read_b32 v145, a69
v_accvgpr_read_b32 v146, a70
v_accvgpr_read_b32 v147, a71
v_accvgpr_read_b32 v148, a76
v_accvgpr_read_b32 v149, a77
v_accvgpr_read_b32 v150, a78
v_accvgpr_read_b32 v151, a79
v_lshlrev_b32 v152, 16, v100
v_and_b32 v153, 0xffff0000, v100
v_lshlrev_b32 v154, 16, v101
v_and_b32 v155, 0xffff0000, v101
v_lshlrev_b32 v156, 16, v102
v_and_b32 v157, 0xffff0000, v102
v_lshlrev_b32 v158, 16, v103
v_and_b32 v159, 0xffff0000, v103
v_pk_mul_f32 v[144:145], v[144:145], v[236:237]
v_pk_mul_f32 v[146:147], v[146:147], v[236:237]
v_pk_mul_f32 v[148:149], v[148:149], v[236:237]
v_pk_mul_f32 v[150:151], v[150:151], v[236:237]
v_pk_fma_f32 v[144:145], v[152:153], v[238:239], v[144:145]
v_pk_fma_f32 v[146:147], v[154:155], v[238:239], v[146:147]
v_pk_fma_f32 v[148:149], v[156:157], v[238:239], v[148:149]
v_pk_fma_f32 v[150:151], v[158:159], v[238:239], v[150:151]
v_cvt_pk_bf16_f32 v100, v144, v145
v_cvt_pk_bf16_f32 v101, v146, v147
v_cvt_pk_bf16_f32 v102, v148, v149
v_cvt_pk_bf16_f32 v103, v150, v151
v_accvgpr_read_b32 v160, a80
v_accvgpr_read_b32 v161, a81
v_accvgpr_read_b32 v162, a82
v_accvgpr_read_b32 v163, a83
v_accvgpr_read_b32 v164, a88
v_accvgpr_read_b32 v165, a89
v_accvgpr_read_b32 v166, a90
v_accvgpr_read_b32 v167, a91
v_lshlrev_b32 v168, 16, v104
v_and_b32 v169, 0xffff0000, v104
v_lshlrev_b32 v170, 16, v105
v_and_b32 v171, 0xffff0000, v105
v_lshlrev_b32 v172, 16, v106
v_and_b32 v173, 0xffff0000, v106
v_lshlrev_b32 v174, 16, v107
v_and_b32 v175, 0xffff0000, v107
v_pk_mul_f32 v[160:161], v[160:161], v[232:233]
v_pk_mul_f32 v[162:163], v[162:163], v[232:233]
v_pk_mul_f32 v[164:165], v[164:165], v[232:233]
v_pk_mul_f32 v[166:167], v[166:167], v[232:233]
v_pk_fma_f32 v[160:161], v[168:169], v[234:235], v[160:161]
v_pk_fma_f32 v[162:163], v[170:171], v[234:235], v[162:163]
v_pk_fma_f32 v[164:165], v[172:173], v[234:235], v[164:165]
v_pk_fma_f32 v[166:167], v[174:175], v[234:235], v[166:167]
v_cvt_pk_bf16_f32 v104, v160, v161
v_cvt_pk_bf16_f32 v105, v162, v163
v_cvt_pk_bf16_f32 v106, v164, v165
v_cvt_pk_bf16_f32 v107, v166, v167
v_accvgpr_read_b32 v176, a84
v_accvgpr_read_b32 v177, a85
v_accvgpr_read_b32 v178, a86
v_accvgpr_read_b32 v179, a87
v_accvgpr_read_b32 v180, a92
v_accvgpr_read_b32 v181, a93
v_accvgpr_read_b32 v182, a94
v_accvgpr_read_b32 v183, a95
v_lshlrev_b32 v184, 16, v108
v_and_b32 v185, 0xffff0000, v108
v_lshlrev_b32 v186, 16, v109
v_and_b32 v187, 0xffff0000, v109
v_lshlrev_b32 v188, 16, v110
v_and_b32 v189, 0xffff0000, v110
v_lshlrev_b32 v190, 16, v111
v_and_b32 v191, 0xffff0000, v111
v_pk_mul_f32 v[176:177], v[176:177], v[236:237]
v_pk_mul_f32 v[178:179], v[178:179], v[236:237]
v_pk_mul_f32 v[180:181], v[180:181], v[236:237]
v_pk_mul_f32 v[182:183], v[182:183], v[236:237]
v_pk_fma_f32 v[176:177], v[184:185], v[238:239], v[176:177]
v_pk_fma_f32 v[178:179], v[186:187], v[238:239], v[178:179]
v_pk_fma_f32 v[180:181], v[188:189], v[238:239], v[180:181]
v_pk_fma_f32 v[182:183], v[190:191], v[238:239], v[182:183]
v_cvt_pk_bf16_f32 v108, v176, v177
v_cvt_pk_bf16_f32 v109, v178, v179
v_cvt_pk_bf16_f32 v110, v180, v181
v_cvt_pk_bf16_f32 v111, v182, v183
v_accvgpr_read_b32 v128, a96
v_accvgpr_read_b32 v129, a97
v_accvgpr_read_b32 v130, a98
v_accvgpr_read_b32 v131, a99
v_accvgpr_read_b32 v132, a104
v_accvgpr_read_b32 v133, a105
v_accvgpr_read_b32 v134, a106
v_accvgpr_read_b32 v135, a107
v_lshlrev_b32 v136, 16, v112
v_and_b32 v137, 0xffff0000, v112
v_lshlrev_b32 v138, 16, v113
v_and_b32 v139, 0xffff0000, v113
v_lshlrev_b32 v140, 16, v114
v_and_b32 v141, 0xffff0000, v114
v_lshlrev_b32 v142, 16, v115
v_and_b32 v143, 0xffff0000, v115
v_pk_mul_f32 v[128:129], v[128:129], v[232:233]
v_pk_mul_f32 v[130:131], v[130:131], v[232:233]
v_pk_mul_f32 v[132:133], v[132:133], v[232:233]
v_pk_mul_f32 v[134:135], v[134:135], v[232:233]
v_pk_fma_f32 v[128:129], v[136:137], v[234:235], v[128:129]
v_pk_fma_f32 v[130:131], v[138:139], v[234:235], v[130:131]
v_pk_fma_f32 v[132:133], v[140:141], v[234:235], v[132:133]
v_pk_fma_f32 v[134:135], v[142:143], v[234:235], v[134:135]
v_cvt_pk_bf16_f32 v112, v128, v129
v_cvt_pk_bf16_f32 v113, v130, v131
v_cvt_pk_bf16_f32 v114, v132, v133
v_cvt_pk_bf16_f32 v115, v134, v135
v_accvgpr_read_b32 v144, a100
v_accvgpr_read_b32 v145, a101
v_accvgpr_read_b32 v146, a102
v_accvgpr_read_b32 v147, a103
v_accvgpr_read_b32 v148, a108
v_accvgpr_read_b32 v149, a109
v_accvgpr_read_b32 v150, a110
v_accvgpr_read_b32 v151, a111
v_lshlrev_b32 v152, 16, v116
v_and_b32 v153, 0xffff0000, v116
v_lshlrev_b32 v154, 16, v117
v_and_b32 v155, 0xffff0000, v117
v_lshlrev_b32 v156, 16, v118
v_and_b32 v157, 0xffff0000, v118
v_lshlrev_b32 v158, 16, v119
v_and_b32 v159, 0xffff0000, v119
v_pk_mul_f32 v[144:145], v[144:145], v[236:237]
v_pk_mul_f32 v[146:147], v[146:147], v[236:237]
v_pk_mul_f32 v[148:149], v[148:149], v[236:237]
v_pk_mul_f32 v[150:151], v[150:151], v[236:237]
v_pk_fma_f32 v[144:145], v[152:153], v[238:239], v[144:145]
v_pk_fma_f32 v[146:147], v[154:155], v[238:239], v[146:147]
v_pk_fma_f32 v[148:149], v[156:157], v[238:239], v[148:149]
v_pk_fma_f32 v[150:151], v[158:159], v[238:239], v[150:151]
v_cvt_pk_bf16_f32 v116, v144, v145
v_cvt_pk_bf16_f32 v117, v146, v147
v_cvt_pk_bf16_f32 v118, v148, v149
v_cvt_pk_bf16_f32 v119, v150, v151
v_accvgpr_read_b32 v160, a112
v_accvgpr_read_b32 v161, a113
v_accvgpr_read_b32 v162, a114
v_accvgpr_read_b32 v163, a115
v_accvgpr_read_b32 v164, a120
v_accvgpr_read_b32 v165, a121
v_accvgpr_read_b32 v166, a122
v_accvgpr_read_b32 v167, a123
v_lshlrev_b32 v168, 16, v120
v_and_b32 v169, 0xffff0000, v120
v_lshlrev_b32 v170, 16, v121
v_and_b32 v171, 0xffff0000, v121
v_lshlrev_b32 v172, 16, v122
v_and_b32 v173, 0xffff0000, v122
v_lshlrev_b32 v174, 16, v123
v_and_b32 v175, 0xffff0000, v123
v_pk_mul_f32 v[160:161], v[160:161], v[232:233]
v_pk_mul_f32 v[162:163], v[162:163], v[232:233]
v_pk_mul_f32 v[164:165], v[164:165], v[232:233]
v_pk_mul_f32 v[166:167], v[166:167], v[232:233]
v_pk_fma_f32 v[160:161], v[168:169], v[234:235], v[160:161]
v_pk_fma_f32 v[162:163], v[170:171], v[234:235], v[162:163]
v_pk_fma_f32 v[164:165], v[172:173], v[234:235], v[164:165]
v_pk_fma_f32 v[166:167], v[174:175], v[234:235], v[166:167]
v_cvt_pk_bf16_f32 v120, v160, v161
v_cvt_pk_bf16_f32 v121, v162, v163
v_cvt_pk_bf16_f32 v122, v164, v165
v_cvt_pk_bf16_f32 v123, v166, v167
v_accvgpr_read_b32 v176, a116
v_accvgpr_read_b32 v177, a117
v_accvgpr_read_b32 v178, a118
v_accvgpr_read_b32 v179, a119
v_accvgpr_read_b32 v180, a124
v_accvgpr_read_b32 v181, a125
v_accvgpr_read_b32 v182, a126
v_accvgpr_read_b32 v183, a127
v_lshlrev_b32 v184, 16, v124
v_and_b32 v185, 0xffff0000, v124
v_lshlrev_b32 v186, 16, v125
v_and_b32 v187, 0xffff0000, v125
v_lshlrev_b32 v188, 16, v126
v_and_b32 v189, 0xffff0000, v126
v_lshlrev_b32 v190, 16, v127
v_and_b32 v191, 0xffff0000, v127
v_pk_mul_f32 v[176:177], v[176:177], v[236:237]
v_pk_mul_f32 v[178:179], v[178:179], v[236:237]
v_pk_mul_f32 v[180:181], v[180:181], v[236:237]
v_pk_mul_f32 v[182:183], v[182:183], v[236:237]
v_pk_fma_f32 v[176:177], v[184:185], v[238:239], v[176:177]
v_pk_fma_f32 v[178:179], v[186:187], v[238:239], v[178:179]
v_pk_fma_f32 v[180:181], v[188:189], v[238:239], v[180:181]
v_pk_fma_f32 v[182:183], v[190:191], v[238:239], v[182:183]
v_cvt_pk_bf16_f32 v124, v176, v177
v_cvt_pk_bf16_f32 v125, v178, v179
v_cvt_pk_bf16_f32 v126, v180, v181
v_cvt_pk_bf16_f32 v127, v182, v183
s_sub_u32 s44, 15, s31
s_branch OPPH_%=
PATHB_%=:
s_mov_b32 s60, 0
s_lshl_b32 s47, s31, 1
s_sub_u32 s34, 31, s47
s_sub_u32 s34, s34, s68
s_lshl_b32 s45, s68, 6
s_add_u32 s45, s45, 63
s_getpc_b64 s[70:71]
CALLPC_b5_%=:
s_add_u32 s70, s70, CALLRET_b5_%=-CALLPC_b5_%=
s_addc_u32 s71, s71, 0
s_branch SEG_%=
CALLRET_b5_%=:
s_mul_i32 s47, s42, 69632
s_mul_i32 s48, s28, 17408
s_add_u32 s47, s47, s48
s_add_u32 s47, s47, 52428800
v_accvgpr_read_b32 v128, a0
v_accvgpr_read_b32 v129, a1
v_accvgpr_read_b32 v130, a2
v_accvgpr_read_b32 v131, a3
v_accvgpr_read_b32 v132, a8
v_accvgpr_read_b32 v133, a9
v_accvgpr_read_b32 v134, a10
v_accvgpr_read_b32 v135, a11
v_cvt_pk_bf16_f32 v136, v128, v129
v_cvt_pk_bf16_f32 v137, v130, v131
v_cvt_pk_bf16_f32 v138, v132, v133
v_cvt_pk_bf16_f32 v139, v134, v135
s_add_u32 s48, s47, 0
buffer_store_dwordx4 v[136:139], v2, s[36:39], s48 offen offset:0 sc1
v_accvgpr_read_b32 v144, a4
v_accvgpr_read_b32 v145, a5
v_accvgpr_read_b32 v146, a6
v_accvgpr_read_b32 v147, a7
v_accvgpr_read_b32 v148, a12
v_accvgpr_read_b32 v149, a13
v_accvgpr_read_b32 v150, a14
v_accvgpr_read_b32 v151, a15
v_cvt_pk_bf16_f32 v152, v144, v145
v_cvt_pk_bf16_f32 v153, v146, v147
v_cvt_pk_bf16_f32 v154, v148, v149
v_cvt_pk_bf16_f32 v155, v150, v151
s_add_u32 s48, s47, 0
buffer_store_dwordx4 v[152:155], v2, s[36:39], s48 offen offset:1024 sc1
v_accvgpr_read_b32 v160, a16
v_accvgpr_read_b32 v161, a17
v_accvgpr_read_b32 v162, a18
v_accvgpr_read_b32 v163, a19
v_accvgpr_read_b32 v164, a24
v_accvgpr_read_b32 v165, a25
v_accvgpr_read_b32 v166, a26
v_accvgpr_read_b32 v167, a27
v_cvt_pk_bf16_f32 v168, v160, v161
v_cvt_pk_bf16_f32 v169, v162, v163
v_cvt_pk_bf16_f32 v170, v164, v165
v_cvt_pk_bf16_f32 v171, v166, v167
s_add_u32 s48, s47, 0
buffer_store_dwordx4 v[168:171], v2, s[36:39], s48 offen offset:2048 sc1
v_accvgpr_read_b32 v176, a20
v_accvgpr_read_b32 v177, a21
v_accvgpr_read_b32 v178, a22
v_accvgpr_read_b32 v179, a23
v_accvgpr_read_b32 v180, a28
v_accvgpr_read_b32 v181, a29
v_accvgpr_read_b32 v182, a30
v_accvgpr_read_b32 v183, a31
v_cvt_pk_bf16_f32 v184, v176, v177
v_cvt_pk_bf16_f32 v185, v178, v179
v_cvt_pk_bf16_f32 v186, v180, v181
v_cvt_pk_bf16_f32 v187, v182, v183
s_add_u32 s48, s47, 0
buffer_store_dwordx4 v[184:187], v2, s[36:39], s48 offen offset:3072 sc1
v_accvgpr_read_b32 v128, a32
v_accvgpr_read_b32 v129, a33
v_accvgpr_read_b32 v130, a34
v_accvgpr_read_b32 v131, a35
v_accvgpr_read_b32 v132, a40
v_accvgpr_read_b32 v133, a41
v_accvgpr_read_b32 v134, a42
v_accvgpr_read_b32 v135, a43
v_cvt_pk_bf16_f32 v136, v128, v129
v_cvt_pk_bf16_f32 v137, v130, v131
v_cvt_pk_bf16_f32 v138, v132, v133
v_cvt_pk_bf16_f32 v139, v134, v135
s_add_u32 s48, s47, 4096
buffer_store_dwordx4 v[136:139], v2, s[36:39], s48 offen offset:0 sc1
v_accvgpr_read_b32 v144, a36
v_accvgpr_read_b32 v145, a37
v_accvgpr_read_b32 v146, a38
v_accvgpr_read_b32 v147, a39
v_accvgpr_read_b32 v148, a44
v_accvgpr_read_b32 v149, a45
v_accvgpr_read_b32 v150, a46
v_accvgpr_read_b32 v151, a47
v_cvt_pk_bf16_f32 v152, v144, v145
v_cvt_pk_bf16_f32 v153, v146, v147
v_cvt_pk_bf16_f32 v154, v148, v149
v_cvt_pk_bf16_f32 v155, v150, v151
s_add_u32 s48, s47, 4096
buffer_store_dwordx4 v[152:155], v2, s[36:39], s48 offen offset:1024 sc1
v_accvgpr_read_b32 v160, a48
v_accvgpr_read_b32 v161, a49
v_accvgpr_read_b32 v162, a50
v_accvgpr_read_b32 v163, a51
v_accvgpr_read_b32 v164, a56
v_accvgpr_read_b32 v165, a57
v_accvgpr_read_b32 v166, a58
v_accvgpr_read_b32 v167, a59
v_cvt_pk_bf16_f32 v168, v160, v161
v_cvt_pk_bf16_f32 v169, v162, v163
v_cvt_pk_bf16_f32 v170, v164, v165
v_cvt_pk_bf16_f32 v171, v166, v167
s_add_u32 s48, s47, 4096
buffer_store_dwordx4 v[168:171], v2, s[36:39], s48 offen offset:2048 sc1
v_accvgpr_read_b32 v176, a52
v_accvgpr_read_b32 v177, a53
v_accvgpr_read_b32 v178, a54
v_accvgpr_read_b32 v179, a55
v_accvgpr_read_b32 v180, a60
v_accvgpr_read_b32 v181, a61
v_accvgpr_read_b32 v182, a62
v_accvgpr_read_b32 v183, a63
v_cvt_pk_bf16_f32 v184, v176, v177
v_cvt_pk_bf16_f32 v185, v178, v179
v_cvt_pk_bf16_f32 v186, v180, v181
v_cvt_pk_bf16_f32 v187, v182, v183
s_add_u32 s48, s47, 4096
buffer_store_dwordx4 v[184:187], v2, s[36:39], s48 offen offset:3072 sc1
v_accvgpr_read_b32 v128, a64
v_accvgpr_read_b32 v129, a65
v_accvgpr_read_b32 v130, a66
v_accvgpr_read_b32 v131, a67
v_accvgpr_read_b32 v132, a72
v_accvgpr_read_b32 v133, a73
v_accvgpr_read_b32 v134, a74
v_accvgpr_read_b32 v135, a75
v_cvt_pk_bf16_f32 v136, v128, v129
v_cvt_pk_bf16_f32 v137, v130, v131
v_cvt_pk_bf16_f32 v138, v132, v133
v_cvt_pk_bf16_f32 v139, v134, v135
s_add_u32 s48, s47, 8192
buffer_store_dwordx4 v[136:139], v2, s[36:39], s48 offen offset:0 sc1
v_accvgpr_read_b32 v144, a68
v_accvgpr_read_b32 v145, a69
v_accvgpr_read_b32 v146, a70
v_accvgpr_read_b32 v147, a71
v_accvgpr_read_b32 v148, a76
v_accvgpr_read_b32 v149, a77
v_accvgpr_read_b32 v150, a78
v_accvgpr_read_b32 v151, a79
v_cvt_pk_bf16_f32 v152, v144, v145
v_cvt_pk_bf16_f32 v153, v146, v147
v_cvt_pk_bf16_f32 v154, v148, v149
v_cvt_pk_bf16_f32 v155, v150, v151
s_add_u32 s48, s47, 8192
buffer_store_dwordx4 v[152:155], v2, s[36:39], s48 offen offset:1024 sc1
v_accvgpr_read_b32 v160, a80
v_accvgpr_read_b32 v161, a81
v_accvgpr_read_b32 v162, a82
v_accvgpr_read_b32 v163, a83
v_accvgpr_read_b32 v164, a88
v_accvgpr_read_b32 v165, a89
v_accvgpr_read_b32 v166, a90
v_accvgpr_read_b32 v167, a91
v_cvt_pk_bf16_f32 v168, v160, v161
v_cvt_pk_bf16_f32 v169, v162, v163
v_cvt_pk_bf16_f32 v170, v164, v165
v_cvt_pk_bf16_f32 v171, v166, v167
s_add_u32 s48, s47, 8192
buffer_store_dwordx4 v[168:171], v2, s[36:39], s48 offen offset:2048 sc1
v_accvgpr_read_b32 v176, a84
v_accvgpr_read_b32 v177, a85
v_accvgpr_read_b32 v178, a86
v_accvgpr_read_b32 v179, a87
v_accvgpr_read_b32 v180, a92
v_accvgpr_read_b32 v181, a93
v_accvgpr_read_b32 v182, a94
v_accvgpr_read_b32 v183, a95
v_cvt_pk_bf16_f32 v184, v176, v177
v_cvt_pk_bf16_f32 v185, v178, v179
v_cvt_pk_bf16_f32 v186, v180, v181
v_cvt_pk_bf16_f32 v187, v182, v183
s_add_u32 s48, s47, 8192
buffer_store_dwordx4 v[184:187], v2, s[36:39], s48 offen offset:3072 sc1
v_accvgpr_read_b32 v128, a96
v_accvgpr_read_b32 v129, a97
v_accvgpr_read_b32 v130, a98
v_accvgpr_read_b32 v131, a99
v_accvgpr_read_b32 v132, a104
v_accvgpr_read_b32 v133, a105
v_accvgpr_read_b32 v134, a106
v_accvgpr_read_b32 v135, a107
v_cvt_pk_bf16_f32 v136, v128, v129
v_cvt_pk_bf16_f32 v137, v130, v131
v_cvt_pk_bf16_f32 v138, v132, v133
v_cvt_pk_bf16_f32 v139, v134, v135
s_add_u32 s48, s47, 12288
buffer_store_dwordx4 v[136:139], v2, s[36:39], s48 offen offset:0 sc1
v_accvgpr_read_b32 v144, a100
v_accvgpr_read_b32 v145, a101
v_accvgpr_read_b32 v146, a102
v_accvgpr_read_b32 v147, a103
v_accvgpr_read_b32 v148, a108
v_accvgpr_read_b32 v149, a109
v_accvgpr_read_b32 v150, a110
v_accvgpr_read_b32 v151, a111
v_cvt_pk_bf16_f32 v152, v144, v145
v_cvt_pk_bf16_f32 v153, v146, v147
v_cvt_pk_bf16_f32 v154, v148, v149
v_cvt_pk_bf16_f32 v155, v150, v151
s_add_u32 s48, s47, 12288
buffer_store_dwordx4 v[152:155], v2, s[36:39], s48 offen offset:1024 sc1
v_accvgpr_read_b32 v160, a112
v_accvgpr_read_b32 v161, a113
v_accvgpr_read_b32 v162, a114
v_accvgpr_read_b32 v163, a115
v_accvgpr_read_b32 v164, a120
v_accvgpr_read_b32 v165, a121
v_accvgpr_read_b32 v166, a122
v_accvgpr_read_b32 v167, a123
v_cvt_pk_bf16_f32 v168, v160, v161
v_cvt_pk_bf16_f32 v169, v162, v163
v_cvt_pk_bf16_f32 v170, v164, v165
v_cvt_pk_bf16_f32 v171, v166, v167
s_add_u32 s48, s47, 12288
buffer_store_dwordx4 v[168:171], v2, s[36:39], s48 offen offset:2048 sc1
v_accvgpr_read_b32 v176, a116
v_accvgpr_read_b32 v177, a117
v_accvgpr_read_b32 v178, a118
v_accvgpr_read_b32 v179, a119
v_accvgpr_read_b32 v180, a124
v_accvgpr_read_b32 v181, a125
v_accvgpr_read_b32 v182, a126
v_accvgpr_read_b32 v183, a127
v_cvt_pk_bf16_f32 v184, v176, v177
v_cvt_pk_bf16_f32 v185, v178, v179
v_cvt_pk_bf16_f32 v186, v180, v181
v_cvt_pk_bf16_f32 v187, v182, v183
s_add_u32 s48, s47, 12288
buffer_store_dwordx4 v[184:187], v2, s[36:39], s48 offen offset:3072 sc1
v_mov_b32 v44, v28
v_mov_b32 v45, v29
v_mov_b32 v46, v30
v_mov_b32 v47, v31
s_add_u32 s48, s47, 16384
buffer_store_dwordx4 v[44:47], v2, s[36:39], s48 offen sc1
s_lshl_b32 s56, s42, 2
s_add_u32 s56, s56, s28
s_lshl_b32 s56, s56, 6
s_add_u32 s56, s56, 102793216
s_mov_b32 s44, s31
s_lshl_b32 s47, s31, 1
s_add_u32 s34, s47, 1
s_mov_b32 s45, 63
s_getpc_b64 s[70:71]
CALLPC_b7_%=:
s_add_u32 s70, s70, CALLRET_b7_%=-CALLPC_b7_%=
s_addc_u32 s71, s71, 0
s_branch SEG_%=
CALLRET_b7_%=:
v_mov_b32 v36, v30
s_nop 1
v_permlane16_swap_b32 v30, v36
s_nop 1
v_add_f32 v30, v30, v36
v_mov_b32 v36, v30
s_nop 1
v_permlane32_swap_b32 v30, v36
s_nop 1
v_add_f32 v30, v30, v36
v_rcp_f32 v224, v30
s_nop 0
v_mov_b32 v225, v224
v_mov_b32 v40, v31
s_nop 1
v_permlane16_swap_b32 v31, v40
s_nop 1
v_add_f32 v31, v31, v40
v_mov_b32 v40, v31
s_nop 1
v_permlane32_swap_b32 v31, v40
s_nop 1
v_add_f32 v31, v31, v40
v_rcp_f32 v226, v31
s_nop 0
v_mov_b32 v227, v226
s_nop 0
v_accvgpr_read_b32 v128, a0
v_accvgpr_read_b32 v129, a1
v_accvgpr_read_b32 v130, a2
v_accvgpr_read_b32 v131, a3
v_accvgpr_read_b32 v132, a8
v_accvgpr_read_b32 v133, a9
v_accvgpr_read_b32 v134, a10
v_accvgpr_read_b32 v135, a11
v_pk_mul_f32 v[128:129], v[128:129], v[224:225]
v_pk_mul_f32 v[130:131], v[130:131], v[224:225]
v_pk_mul_f32 v[132:133], v[132:133], v[224:225]
v_pk_mul_f32 v[134:135], v[134:135], v[224:225]
v_cvt_pk_bf16_f32 v64, v128, v129
v_cvt_pk_bf16_f32 v65, v130, v131
v_cvt_pk_bf16_f32 v66, v132, v133
v_cvt_pk_bf16_f32 v67, v134, v135
v_accvgpr_read_b32 v136, a4
v_accvgpr_read_b32 v137, a5
v_accvgpr_read_b32 v138, a6
v_accvgpr_read_b32 v139, a7
v_accvgpr_read_b32 v140, a12
v_accvgpr_read_b32 v141, a13
v_accvgpr_read_b32 v142, a14
v_accvgpr_read_b32 v143, a15
v_pk_mul_f32 v[136:137], v[136:137], v[226:227]
v_pk_mul_f32 v[138:139], v[138:139], v[226:227]
v_pk_mul_f32 v[140:141], v[140:141], v[226:227]
v_pk_mul_f32 v[142:143], v[142:143], v[226:227]
v_cvt_pk_bf16_f32 v68, v136, v137
v_cvt_pk_bf16_f32 v69, v138, v139
v_cvt_pk_bf16_f32 v70, v140, v141
v_cvt_pk_bf16_f32 v71, v142, v143
v_accvgpr_read_b32 v144, a16
v_accvgpr_read_b32 v145, a17
v_accvgpr_read_b32 v146, a18
v_accvgpr_read_b32 v147, a19
v_accvgpr_read_b32 v148, a24
v_accvgpr_read_b32 v149, a25
v_accvgpr_read_b32 v150, a26
v_accvgpr_read_b32 v151, a27
v_pk_mul_f32 v[144:145], v[144:145], v[224:225]
v_pk_mul_f32 v[146:147], v[146:147], v[224:225]
v_pk_mul_f32 v[148:149], v[148:149], v[224:225]
v_pk_mul_f32 v[150:151], v[150:151], v[224:225]
v_cvt_pk_bf16_f32 v72, v144, v145
v_cvt_pk_bf16_f32 v73, v146, v147
v_cvt_pk_bf16_f32 v74, v148, v149
v_cvt_pk_bf16_f32 v75, v150, v151
v_accvgpr_read_b32 v152, a20
v_accvgpr_read_b32 v153, a21
v_accvgpr_read_b32 v154, a22
v_accvgpr_read_b32 v155, a23
v_accvgpr_read_b32 v156, a28
v_accvgpr_read_b32 v157, a29
v_accvgpr_read_b32 v158, a30
v_accvgpr_read_b32 v159, a31
v_pk_mul_f32 v[152:153], v[152:153], v[226:227]
v_pk_mul_f32 v[154:155], v[154:155], v[226:227]
v_pk_mul_f32 v[156:157], v[156:157], v[226:227]
v_pk_mul_f32 v[158:159], v[158:159], v[226:227]
v_cvt_pk_bf16_f32 v76, v152, v153
v_cvt_pk_bf16_f32 v77, v154, v155
v_cvt_pk_bf16_f32 v78, v156, v157
v_cvt_pk_bf16_f32 v79, v158, v159
v_accvgpr_read_b32 v128, a32
v_accvgpr_read_b32 v129, a33
v_accvgpr_read_b32 v130, a34
v_accvgpr_read_b32 v131, a35
v_accvgpr_read_b32 v132, a40
v_accvgpr_read_b32 v133, a41
v_accvgpr_read_b32 v134, a42
v_accvgpr_read_b32 v135, a43
v_pk_mul_f32 v[128:129], v[128:129], v[224:225]
v_pk_mul_f32 v[130:131], v[130:131], v[224:225]
v_pk_mul_f32 v[132:133], v[132:133], v[224:225]
v_pk_mul_f32 v[134:135], v[134:135], v[224:225]
v_cvt_pk_bf16_f32 v80, v128, v129
v_cvt_pk_bf16_f32 v81, v130, v131
v_cvt_pk_bf16_f32 v82, v132, v133
v_cvt_pk_bf16_f32 v83, v134, v135
v_accvgpr_read_b32 v136, a36
v_accvgpr_read_b32 v137, a37
v_accvgpr_read_b32 v138, a38
v_accvgpr_read_b32 v139, a39
v_accvgpr_read_b32 v140, a44
v_accvgpr_read_b32 v141, a45
v_accvgpr_read_b32 v142, a46
v_accvgpr_read_b32 v143, a47
v_pk_mul_f32 v[136:137], v[136:137], v[226:227]
v_pk_mul_f32 v[138:139], v[138:139], v[226:227]
v_pk_mul_f32 v[140:141], v[140:141], v[226:227]
v_pk_mul_f32 v[142:143], v[142:143], v[226:227]
v_cvt_pk_bf16_f32 v84, v136, v137
v_cvt_pk_bf16_f32 v85, v138, v139
v_cvt_pk_bf16_f32 v86, v140, v141
v_cvt_pk_bf16_f32 v87, v142, v143
v_accvgpr_read_b32 v144, a48
v_accvgpr_read_b32 v145, a49
v_accvgpr_read_b32 v146, a50
v_accvgpr_read_b32 v147, a51
v_accvgpr_read_b32 v148, a56
v_accvgpr_read_b32 v149, a57
v_accvgpr_read_b32 v150, a58
v_accvgpr_read_b32 v151, a59
v_pk_mul_f32 v[144:145], v[144:145], v[224:225]
v_pk_mul_f32 v[146:147], v[146:147], v[224:225]
v_pk_mul_f32 v[148:149], v[148:149], v[224:225]
v_pk_mul_f32 v[150:151], v[150:151], v[224:225]
v_cvt_pk_bf16_f32 v88, v144, v145
v_cvt_pk_bf16_f32 v89, v146, v147
v_cvt_pk_bf16_f32 v90, v148, v149
v_cvt_pk_bf16_f32 v91, v150, v151
v_accvgpr_read_b32 v152, a52
v_accvgpr_read_b32 v153, a53
v_accvgpr_read_b32 v154, a54
v_accvgpr_read_b32 v155, a55
v_accvgpr_read_b32 v156, a60
v_accvgpr_read_b32 v157, a61
v_accvgpr_read_b32 v158, a62
v_accvgpr_read_b32 v159, a63
v_pk_mul_f32 v[152:153], v[152:153], v[226:227]
v_pk_mul_f32 v[154:155], v[154:155], v[226:227]
v_pk_mul_f32 v[156:157], v[156:157], v[226:227]
v_pk_mul_f32 v[158:159], v[158:159], v[226:227]
v_cvt_pk_bf16_f32 v92, v152, v153
v_cvt_pk_bf16_f32 v93, v154, v155
v_cvt_pk_bf16_f32 v94, v156, v157
v_cvt_pk_bf16_f32 v95, v158, v159
v_accvgpr_read_b32 v128, a64
v_accvgpr_read_b32 v129, a65
v_accvgpr_read_b32 v130, a66
v_accvgpr_read_b32 v131, a67
v_accvgpr_read_b32 v132, a72
v_accvgpr_read_b32 v133, a73
v_accvgpr_read_b32 v134, a74
v_accvgpr_read_b32 v135, a75
v_pk_mul_f32 v[128:129], v[128:129], v[224:225]
v_pk_mul_f32 v[130:131], v[130:131], v[224:225]
v_pk_mul_f32 v[132:133], v[132:133], v[224:225]
v_pk_mul_f32 v[134:135], v[134:135], v[224:225]
v_cvt_pk_bf16_f32 v96, v128, v129
v_cvt_pk_bf16_f32 v97, v130, v131
v_cvt_pk_bf16_f32 v98, v132, v133
v_cvt_pk_bf16_f32 v99, v134, v135
v_accvgpr_read_b32 v136, a68
v_accvgpr_read_b32 v137, a69
v_accvgpr_read_b32 v138, a70
v_accvgpr_read_b32 v139, a71
v_accvgpr_read_b32 v140, a76
v_accvgpr_read_b32 v141, a77
v_accvgpr_read_b32 v142, a78
v_accvgpr_read_b32 v143, a79
v_pk_mul_f32 v[136:137], v[136:137], v[226:227]
v_pk_mul_f32 v[138:139], v[138:139], v[226:227]
v_pk_mul_f32 v[140:141], v[140:141], v[226:227]
v_pk_mul_f32 v[142:143], v[142:143], v[226:227]
v_cvt_pk_bf16_f32 v100, v136, v137
v_cvt_pk_bf16_f32 v101, v138, v139
v_cvt_pk_bf16_f32 v102, v140, v141
v_cvt_pk_bf16_f32 v103, v142, v143
v_accvgpr_read_b32 v144, a80
v_accvgpr_read_b32 v145, a81
v_accvgpr_read_b32 v146, a82
v_accvgpr_read_b32 v147, a83
v_accvgpr_read_b32 v148, a88
v_accvgpr_read_b32 v149, a89
v_accvgpr_read_b32 v150, a90
v_accvgpr_read_b32 v151, a91
v_pk_mul_f32 v[144:145], v[144:145], v[224:225]
v_pk_mul_f32 v[146:147], v[146:147], v[224:225]
v_pk_mul_f32 v[148:149], v[148:149], v[224:225]
v_pk_mul_f32 v[150:151], v[150:151], v[224:225]
v_cvt_pk_bf16_f32 v104, v144, v145
v_cvt_pk_bf16_f32 v105, v146, v147
v_cvt_pk_bf16_f32 v106, v148, v149
v_cvt_pk_bf16_f32 v107, v150, v151
v_accvgpr_read_b32 v152, a84
v_accvgpr_read_b32 v153, a85
v_accvgpr_read_b32 v154, a86
v_accvgpr_read_b32 v155, a87
v_accvgpr_read_b32 v156, a92
v_accvgpr_read_b32 v157, a93
v_accvgpr_read_b32 v158, a94
v_accvgpr_read_b32 v159, a95
v_pk_mul_f32 v[152:153], v[152:153], v[226:227]
v_pk_mul_f32 v[154:155], v[154:155], v[226:227]
v_pk_mul_f32 v[156:157], v[156:157], v[226:227]
v_pk_mul_f32 v[158:159], v[158:159], v[226:227]
v_cvt_pk_bf16_f32 v108, v152, v153
v_cvt_pk_bf16_f32 v109, v154, v155
v_cvt_pk_bf16_f32 v110, v156, v157
v_cvt_pk_bf16_f32 v111, v158, v159
v_accvgpr_read_b32 v128, a96
v_accvgpr_read_b32 v129, a97
v_accvgpr_read_b32 v130, a98
v_accvgpr_read_b32 v131, a99
v_accvgpr_read_b32 v132, a104
v_accvgpr_read_b32 v133, a105
v_accvgpr_read_b32 v134, a106
v_accvgpr_read_b32 v135, a107
v_pk_mul_f32 v[128:129], v[128:129], v[224:225]
v_pk_mul_f32 v[130:131], v[130:131], v[224:225]
v_pk_mul_f32 v[132:133], v[132:133], v[224:225]
v_pk_mul_f32 v[134:135], v[134:135], v[224:225]
v_cvt_pk_bf16_f32 v112, v128, v129
v_cvt_pk_bf16_f32 v113, v130, v131
v_cvt_pk_bf16_f32 v114, v132, v133
v_cvt_pk_bf16_f32 v115, v134, v135
v_accvgpr_read_b32 v136, a100
v_accvgpr_read_b32 v137, a101
v_accvgpr_read_b32 v138, a102
v_accvgpr_read_b32 v139, a103
v_accvgpr_read_b32 v140, a108
v_accvgpr_read_b32 v141, a109
v_accvgpr_read_b32 v142, a110
v_accvgpr_read_b32 v143, a111
v_pk_mul_f32 v[136:137], v[136:137], v[226:227]
v_pk_mul_f32 v[138:139], v[138:139], v[226:227]
v_pk_mul_f32 v[140:141], v[140:141], v[226:227]
v_pk_mul_f32 v[142:143], v[142:143], v[226:227]
v_cvt_pk_bf16_f32 v116, v136, v137
v_cvt_pk_bf16_f32 v117, v138, v139
v_cvt_pk_bf16_f32 v118, v140, v141
v_cvt_pk_bf16_f32 v119, v142, v143
v_accvgpr_read_b32 v144, a112
v_accvgpr_read_b32 v145, a113
v_accvgpr_read_b32 v146, a114
v_accvgpr_read_b32 v147, a115
v_accvgpr_read_b32 v148, a120
v_accvgpr_read_b32 v149, a121
v_accvgpr_read_b32 v150, a122
v_accvgpr_read_b32 v151, a123
v_pk_mul_f32 v[144:145], v[144:145], v[224:225]
v_pk_mul_f32 v[146:147], v[146:147], v[224:225]
v_pk_mul_f32 v[148:149], v[148:149], v[224:225]
v_pk_mul_f32 v[150:151], v[150:151], v[224:225]
v_cvt_pk_bf16_f32 v120, v144, v145
v_cvt_pk_bf16_f32 v121, v146, v147
v_cvt_pk_bf16_f32 v122, v148, v149
v_cvt_pk_bf16_f32 v123, v150, v151
v_accvgpr_read_b32 v152, a116
v_accvgpr_read_b32 v153, a117
v_accvgpr_read_b32 v154, a118
v_accvgpr_read_b32 v155, a119
v_accvgpr_read_b32 v156, a124
v_accvgpr_read_b32 v157, a125
v_accvgpr_read_b32 v158, a126
v_accvgpr_read_b32 v159, a127
v_pk_mul_f32 v[152:153], v[152:153], v[226:227]
v_pk_mul_f32 v[154:155], v[154:155], v[226:227]
v_pk_mul_f32 v[156:157], v[156:157], v[226:227]
v_pk_mul_f32 v[158:159], v[158:159], v[226:227]
v_cvt_pk_bf16_f32 v124, v152, v153
v_cvt_pk_bf16_f32 v125, v154, v155
v_cvt_pk_bf16_f32 v126, v156, v157
v_cvt_pk_bf16_f32 v127, v158, v159
OPPH_%=:
s_lshl_b32 s47, s30, 7
s_lshl_b32 s48, s44, 3
s_add_u32 s47, s47, s48
s_lshl_b32 s48, s28, 1
s_add_u32 s47, s47, s48
s_mul_i32 s47, s47, 14336
s_add_u32 s50, s47, 71303168
s_mov_b32 s51, 0x20000000
s_cmp_eq_u32 s40, 0
s_cbranch_scc1 OPNE_%=
v_add_u32 v8, s24, v4
v_add_u32 v9, s24, v5
v_add_u32 v10, s24, v6
v_add_u32 v11, s24, v7
s_nop 0
ds_read_b64_tr_b16 v[192:193], v8 offset:0
ds_read_b64_tr_b16 v[194:195], v8 offset:2048
ds_read_b64_tr_b16 v[196:197], v8 offset:4096
ds_read_b64_tr_b16 v[198:199], v8 offset:6144
ds_read_b64_tr_b16 v[200:201], v8 offset:8192
ds_read_b64_tr_b16 v[202:203], v8 offset:10240
ds_read_b64_tr_b16 v[204:205], v8 offset:12288
ds_read_b64_tr_b16 v[206:207], v8 offset:14336
ds_read_b64_tr_b16 v[208:209], v8 offset:16384
ds_read_b64_tr_b16 v[210:211], v8 offset:18432
ds_read_b64_tr_b16 v[212:213], v8 offset:20480
ds_read_b64_tr_b16 v[214:215], v8 offset:22528
s_waitcnt lgkmcnt(0)
OPNE_%=:
s_mov_b32 s34, 7
OPL_%=:
v_mfma_f32_16x16x32_bf16 v[128:131], v[192:195], v[64:67], 0
s_add_u32 s52, s51, 0
ds_read_b64_tr_b16 v[216:217], v8 offset:24576
v_mfma_f32_16x16x32_bf16 v[132:135], v[192:195], v[68:71], 0
s_sub_u32 s34, s34, 1
ds_read_b64_tr_b16 v[218:219], v8 offset:26624
v_mfma_f32_16x16x32_bf16 v[128:131], v[196:199], v[72:75], v[128:131]
s_add_u32 s20, s20, s21
ds_read_b64_tr_b16 v[220:221], v8 offset:28672
v_mfma_f32_16x16x32_bf16 v[132:135], v[196:199], v[76:79], v[132:135]
s_waitcnt lgkmcnt(7)
ds_read_b64_tr_b16 v[222:223], v8 offset:30720
v_mfma_f32_16x16x32_bf16 v[128:131], v[200:203], v[80:83], v[128:131]
s_add_u32 m0, s23, 2048
ds_read_b64_tr_b16 v[192:193], v9 offset:0
ds_read_b64_tr_b16 v[194:195], v9 offset:2048
v_mfma_f32_16x16x32_bf16 v[132:135], v[200:203], v[84:87], v[132:135]
buffer_load_dwordx4 v14, s[16:19], s20 offen sc1 lds
v_mfma_f32_16x16x32_bf16 v[128:131], v[204:207], v[88:91], v[128:131]
s_add_u32 s53, s51, 12288
ds_read_b64_tr_b16 v[196:197], v9 offset:4096
v_cvt_pk_bf16_f32 v224, v144, v145
v_mfma_f32_16x16x32_bf16 v[132:135], v[204:207], v[92:95], v[132:135]
s_waitcnt lgkmcnt(7)
ds_read_b64_tr_b16 v[198:199], v9 offset:6144
v_cvt_pk_bf16_f32 v225, v146, v147
v_mfma_f32_16x16x32_bf16 v[128:131], v[208:211], v[96:99], v[128:131]
s_add_u32 s54, s50, 0
ds_read_b64_tr_b16 v[200:201], v9 offset:8192
v_cvt_pk_bf16_f32 v226, v152, v153
v_mfma_f32_16x16x32_bf16 v[132:135], v[208:211], v[100:103], v[132:135]
s_add_u32 s55, s50, 12288
ds_read_b64_tr_b16 v[202:203], v9 offset:10240
v_cvt_pk_bf16_f32 v227, v154, v155
v_mfma_f32_16x16x32_bf16 v[128:131], v[212:215], v[104:107], v[128:131]
s_add_u32 s20, s20, s21
ds_read_b64_tr_b16 v[204:205], v9 offset:12288
v_mfma_f32_16x16x32_bf16 v[132:135], v[212:215], v[108:111], v[132:135]
s_waitcnt lgkmcnt(7)
ds_read_b64_tr_b16 v[206:207], v9 offset:14336
buffer_store_dwordx4 v[224:227], v2, s[36:39], s52 offen offset:1024 sc1
v_mfma_f32_16x16x32_bf16 v[128:131], v[216:219], v[112:115], v[128:131]
s_add_u32 m0, s23, 3072
ds_read_b64_tr_b16 v[208:209], v9 offset:16384
ds_read_b64_tr_b16 v[210:211], v9 offset:18432
v_mfma_f32_16x16x32_bf16 v[132:135], v[216:219], v[116:119], v[132:135]
buffer_load_dwordx4 v14, s[16:19], s20 offen sc1 lds
v_mfma_f32_16x16x32_bf16 v[128:131], v[220:223], v[120:123], v[128:131]
s_mov_b32 s51, s50
ds_read_b64_tr_b16 v[212:213], v9 offset:20480
v_cvt_pk_bf16_f32 v228, v148, v149
v_mfma_f32_16x16x32_bf16 v[132:135], v[220:223], v[124:127], v[132:135]
s_waitcnt lgkmcnt(7)
ds_read_b64_tr_b16 v[214:215], v9 offset:22528
v_cvt_pk_bf16_f32 v229, v150, v151
v_mfma_f32_16x16x32_bf16 v[136:139], v[192:195], v[64:67], 0
s_add_u32 s50, s50, 2048
ds_read_b64_tr_b16 v[216:217], v9 offset:24576
v_cvt_pk_bf16_f32 v230, v156, v157
v_mfma_f32_16x16x32_bf16 v[140:143], v[192:195], v[68:71], 0
ds_read_b64_tr_b16 v[218:219], v9 offset:26624
v_cvt_pk_bf16_f32 v231, v158, v159
v_mfma_f32_16x16x32_bf16 v[136:139], v[196:199], v[72:75], v[136:139]
s_add_u32 s20, s20, s21
ds_read_b64_tr_b16 v[220:221], v9 offset:28672
v_mfma_f32_16x16x32_bf16 v[140:143], v[196:199], v[76:79], v[140:143]
s_waitcnt lgkmcnt(7)
ds_read_b64_tr_b16 v[222:223], v9 offset:30720
buffer_store_dwordx4 v[228:231], v2, s[36:39], s53 offen offset:3072 sc1
v_mfma_f32_16x16x32_bf16 v[136:139], v[200:203], v[80:83], v[136:139]
s_add_u32 m0, s23, 4096
ds_read_b64_tr_b16 v[192:193], v10 offset:0
ds_read_b64_tr_b16 v[194:195], v10 offset:2048
v_mfma_f32_16x16x32_bf16 v[140:143], v[200:203], v[84:87], v[140:143]
buffer_load_dwordx4 v14, s[16:19], s20 offen sc1 lds
v_mfma_f32_16x16x32_bf16 v[136:139], v[204:207], v[88:91], v[136:139]
ds_read_b64_tr_b16 v[196:197], v10 offset:4096
v_mfma_f32_16x16x32_bf16 v[140:143], v[204:207], v[92:95], v[140:143]
s_waitcnt lgkmcnt(7)
ds_read_b64_tr_b16 v[198:199], v10 offset:6144
v_mfma_f32_16x16x32_bf16 v[136:139], v[208:211], v[96:99], v[136:139]
ds_read_b64_tr_b16 v[200:201], v10 offset:8192
v_mfma_f32_16x16x32_bf16 v[140:143], v[208:211], v[100:103], v[140:143]
ds_read_b64_tr_b16 v[202:203], v10 offset:10240
v_mfma_f32_16x16x32_bf16 v[136:139], v[212:215], v[104:107], v[136:139]
s_add_u32 s20, s20, s21
ds_read_b64_tr_b16 v[204:205], v10 offset:12288
v_mfma_f32_16x16x32_bf16 v[140:143], v[212:215], v[108:111], v[140:143]
s_waitcnt lgkmcnt(7)
ds_read_b64_tr_b16 v[206:207], v10 offset:14336
v_mfma_f32_16x16x32_bf16 v[136:139], v[216:219], v[112:115], v[136:139]
s_add_u32 m0, s23, 5120
ds_read_b64_tr_b16 v[208:209], v10 offset:16384
ds_read_b64_tr_b16 v[210:211], v10 offset:18432
v_mfma_f32_16x16x32_bf16 v[140:143], v[216:219], v[116:119], v[140:143]
buffer_load_dwordx4 v14, s[16:19], s20 offen sc1 lds
v_mfma_f32_16x16x32_bf16 v[136:139], v[220:223], v[120:123], v[136:139]
ds_read_b64_tr_b16 v[212:213], v10 offset:20480
v_mfma_f32_16x16x32_bf16 v[140:143], v[220:223], v[124:127], v[140:143]
s_waitcnt lgkmcnt(7)
ds_read_b64_tr_b16 v[214:215], v10 offset:22528
v_mfma_f32_16x16x32_bf16 v[144:147], v[192:195], v[64:67], 0
ds_read_b64_tr_b16 v[216:217], v10 offset:24576
v_mfma_f32_16x16x32_bf16 v[148:151], v[192:195], v[68:71], 0
ds_read_b64_tr_b16 v[218:219], v10 offset:26624
v_mfma_f32_16x16x32_bf16 v[144:147], v[196:199], v[72:75], v[144:147]
s_add_u32 s20, s20, s21
ds_read_b64_tr_b16 v[220:221], v10 offset:28672
v_mfma_f32_16x16x32_bf16 v[148:151], v[196:199], v[76:79], v[148:151]
s_waitcnt lgkmcnt(7)
ds_read_b64_tr_b16 v[222:223], v10 offset:30720
v_mfma_f32_16x16x32_bf16 v[144:147], v[200:203], v[80:83], v[144:147]
s_add_u32 m0, s23, 6144
ds_read_b64_tr_b16 v[192:193], v11 offset:0
ds_read_b64_tr_b16 v[194:195], v11 offset:2048
v_mfma_f32_16x16x32_bf16 v[148:151], v[200:203], v[84:87], v[148:151]
buffer_load_dwordx4 v14, s[16:19], s20 offen sc1 lds
v_mfma_f32_16x16x32_bf16 v[144:147], v[204:207], v[88:91], v[144:147]
s_add_u32 s26, s24, 32768
ds_read_b64_tr_b16 v[196:197], v11 offset:4096
v_cvt_pk_bf16_f32 v232, v128, v129
v_mfma_f32_16x16x32_bf16 v[148:151], v[204:207], v[92:95], v[148:151]
s_waitcnt lgkmcnt(7)
ds_read_b64_tr_b16 v[198:199], v11 offset:6144
v_cvt_pk_bf16_f32 v233, v130, v131
v_mfma_f32_16x16x32_bf16 v[144:147], v[208:211], v[96:99], v[144:147]
s_sub_u32 s35, s26, 163840
ds_read_b64_tr_b16 v[200:201], v11 offset:8192
v_cvt_pk_bf16_f32 v234, v136, v137
v_mfma_f32_16x16x32_bf16 v[148:151], v[208:211], v[100:103], v[148:151]
s_cselect_b32 s26, s26, s35
ds_read_b64_tr_b16 v[202:203], v11 offset:10240
v_cvt_pk_bf16_f32 v235, v138, v139
v_mfma_f32_16x16x32_bf16 v[144:147], v[212:215], v[104:107], v[144:147]
s_add_u32 s20, s20, s21
ds_read_b64_tr_b16 v[204:205], v11 offset:12288
v_cvt_pk_bf16_f32 v236, v132, v133
v_mfma_f32_16x16x32_bf16 v[148:151], v[212:215], v[108:111], v[148:151]
s_waitcnt lgkmcnt(7)
ds_read_b64_tr_b16 v[206:207], v11 offset:14336
buffer_store_dwordx4 v[232:235], v2, s[36:39], s54 offen offset:0 sc1
v_cvt_pk_bf16_f32 v237, v134, v135
v_mfma_f32_16x16x32_bf16 v[144:147], v[216:219], v[112:115], v[144:147]
s_add_u32 m0, s23, 7168
ds_read_b64_tr_b16 v[208:209], v11 offset:16384
ds_read_b64_tr_b16 v[210:211], v11 offset:18432
v_mfma_f32_16x16x32_bf16 v[148:151], v[216:219], v[116:119], v[148:151]
buffer_load_dwordx4 v14, s[16:19], s20 offen sc1 lds
v_mfma_f32_16x16x32_bf16 v[144:147], v[220:223], v[120:123], v[144:147]
s_add_u32 s23, s27, s33
ds_read_b64_tr_b16 v[212:213], v11 offset:20480
v_readlane_b32 s16, v16, s22
v_mfma_f32_16x16x32_bf16 v[148:151], v[220:223], v[124:127], v[148:151]
s_waitcnt lgkmcnt(7)
ds_read_b64_tr_b16 v[214:215], v11 offset:22528
v_readlane_b32 s17, v17, s22
v_mfma_f32_16x16x32_bf16 v[152:155], v[192:195], v[64:67], 0
s_waitcnt vmcnt(19)
ds_read_b64_tr_b16 v[216:217], v11 offset:24576
v_readlane_b32 s25, v18, s22
v_mfma_f32_16x16x32_bf16 v[156:159], v[192:195], v[68:71], 0
s_barrier
ds_read_b64_tr_b16 v[218:219], v11 offset:26624
v_add_u32 v8, s26, v4
v_mfma_f32_16x16x32_bf16 v[152:155], v[196:199], v[72:75], v[152:155]
s_mov_b32 s20, 0
ds_read_b64_tr_b16 v[220:221], v11 offset:28672
v_mad_u32_u24 v14, v12, s25, v13
v_mfma_f32_16x16x32_bf16 v[156:159], v[196:199], v[76:79], v[156:159]
s_waitcnt lgkmcnt(7)
ds_read_b64_tr_b16 v[222:223], v11 offset:30720
v_add_u32 v9, s26, v5
v_mfma_f32_16x16x32_bf16 v[152:155], v[200:203], v[80:83], v[152:155]
s_mov_b32 m0, s23
ds_read_b64_tr_b16 v[192:193], v8 offset:0
ds_read_b64_tr_b16 v[194:195], v8 offset:2048
v_add_u32 v10, s26, v6
v_mfma_f32_16x16x32_bf16 v[156:159], v[200:203], v[84:87], v[156:159]
buffer_load_dwordx4 v14, s[16:19], s20 offen sc1 lds
v_mfma_f32_16x16x32_bf16 v[152:155], v[204:207], v[88:91], v[152:155]
s_add_u32 s22, s22, 1
ds_read_b64_tr_b16 v[196:197], v8 offset:4096
v_add_u32 v11, s26, v7
v_mfma_f32_16x16x32_bf16 v[156:159], v[204:207], v[92:95], v[156:159]
s_waitcnt lgkmcnt(7)
ds_read_b64_tr_b16 v[198:199], v8 offset:6144
v_cvt_pk_bf16_f32 v238, v140, v141
v_mfma_f32_16x16x32_bf16 v[152:155], v[208:211], v[96:99], v[152:155]
s_lshl_b32 s21, s25, 3
ds_read_b64_tr_b16 v[200:201], v8 offset:8192
v_cvt_pk_bf16_f32 v239, v142, v143
v_mfma_f32_16x16x32_bf16 v[156:159], v[208:211], v[100:103], v[156:159]
s_mov_b32 s27, s24
ds_read_b64_tr_b16 v[202:203], v8 offset:10240
v_mfma_f32_16x16x32_bf16 v[152:155], v[212:215], v[104:107], v[152:155]
s_add_u32 s20, s20, s21
ds_read_b64_tr_b16 v[204:205], v8 offset:12288
buffer_store_dwordx4 v[236:239], v2, s[36:39], s55 offen offset:2048 sc1
v_mfma_f32_16x16x32_bf16 v[156:159], v[212:215], v[108:111], v[156:159]
s_waitcnt lgkmcnt(7)
ds_read_b64_tr_b16 v[206:207], v8 offset:14336
v_mfma_f32_16x16x32_bf16 v[152:155], v[216:219], v[112:115], v[152:155]
s_add_u32 m0, s23, 1024
ds_read_b64_tr_b16 v[208:209], v8 offset:16384
ds_read_b64_tr_b16 v[210:211], v8 offset:18432
v_mfma_f32_16x16x32_bf16 v[156:159], v[216:219], v[116:119], v[156:159]
buffer_load_dwordx4 v14, s[16:19], s20 offen sc1 lds
v_mfma_f32_16x16x32_bf16 v[152:155], v[220:223], v[120:123], v[152:155]
s_mov_b32 s24, s26
ds_read_b64_tr_b16 v[212:213], v8 offset:20480
v_mfma_f32_16x16x32_bf16 v[156:159], v[220:223], v[124:127], v[156:159]
s_waitcnt lgkmcnt(7)
ds_read_b64_tr_b16 v[214:215], v8 offset:22528
s_cmp_lg_u32 s34, 0
s_cbranch_scc1 OPL_%=
s_nop 7
v_cvt_pk_bf16_f32 v224, v144, v145
v_cvt_pk_bf16_f32 v225, v146, v147
v_cvt_pk_bf16_f32 v226, v152, v153
v_cvt_pk_bf16_f32 v227, v154, v155
s_add_u32 s52, s51, 0
buffer_store_dwordx4 v[224:227], v2, s[36:39], s52 offen offset:1024 sc1
v_cvt_pk_bf16_f32 v228, v148, v149
v_cvt_pk_bf16_f32 v229, v150, v151
v_cvt_pk_bf16_f32 v230, v156, v157
v_cvt_pk_bf16_f32 v231, v158, v159
s_add_u32 s52, s51, 12288
buffer_store_dwordx4 v[228:231], v2, s[36:39], s52 offen offset:3072 sc1
s_lshl_b32 s47, s10, 2
s_add_u32 s47, s47, s28
v_and_b32 v33, 15, v1
s_lshl_b32 s48, s47, 4
v_add_u32 v33, s48, v33
v_mul_u32_u24 v33, 896, v33
v_lshrrev_b32 v34, 4, v1
v_lshl_add_u32 v33, v34, 3, v33
s_lshl_b32 s48, s11, 7
v_add_u32 v33, s48, v33
s_mov_b64 s[16:17], s[76:77]
s_mov_b32 s18, 1835008
s_mov_b64 s[20:21], s[96:97]
s_mov_b32 s22, 1835008
s_mov_b32 s23, 0x20000
s_mul_i32 s49, s47, 14336
s_lshl_b32 s48, s11, 11
s_add_u32 s49, s49, s48
s_add_u32 s49, s49, 71303168
buffer_load_dwordx2 v[224:225], v33, s[16:19], 0 offen offset:0
buffer_load_dwordx2 v[226:227], v33, s[16:19], 0 offen offset:32
buffer_load_dwordx2 v[228:229], v33, s[16:19], 0 offen offset:64
buffer_load_dwordx2 v[230:231], v33, s[16:19], 0 offen offset:96
s_lshr_b32 s50, s10, 1
s_sub_u32 s51, 15, s50
s_cmp_ge_u32 s50, 8
s_cselect_b32 s51, s51, s50
s_cselect_b32 s52, 0, 1
s_lshl_b32 s51, s51, 1
s_add_u32 s51, s51, s52
s_lshl_b32 s51, s51, 4
s_and_b32 s52, s10, 1
s_lshl_b32 s52, s52, 1
s_lshr_b32 s53, s28, 1
s_add_u32 s52, s52, s53
v_and_b32 v35, 15, v1
v_min_u32 v35, 15, v35
v_lshrrev_b32 v38, 1, v35
v_and_b32 v39, 1, v35
v_lshl_add_u32 v38, v39, 3, v38
v_add_u32 v38, s51, v38
v_lshlrev_b32 v38, 2, v38
v_add_u32 v38, s52, v38
v_lshlrev_b32 v38, 6, v38
v_add_u32 v35, 103022592, v38
s_lshl_b32 s56, s29, 2
s_add_u32 s56, s56, s28
s_lshl_b32 s56, s56, 6
s_add_u32 s56, s56, 103022592
v_mov_b32 v208, 0
v_accvgpr_read_b32 v192, a240
v_mov_b32 v209, 0
v_accvgpr_read_b32 v193, a241
v_mov_b32 v210, 0
v_accvgpr_read_b32 v194, a242
v_mov_b32 v211, 0
v_accvgpr_read_b32 v195, a243
v_mov_b32 v212, 0
v_accvgpr_read_b32 v196, a244
v_mov_b32 v213, 0
v_accvgpr_read_b32 v197, a245
v_mov_b32 v214, 0
v_accvgpr_read_b32 v198, a246
v_mov_b32 v215, 0
v_accvgpr_read_b32 v199, a247
v_mov_b32 v216, 0
v_accvgpr_read_b32 v200, a248
v_mov_b32 v217, 0
v_accvgpr_read_b32 v201, a249
v_mov_b32 v218, 0
v_accvgpr_read_b32 v202, a250
v_mov_b32 v219, 0
v_accvgpr_read_b32 v203, a251
v_mov_b32 v220, 0
v_accvgpr_read_b32 v204, a252
v_mov_b32 v221, 0
v_accvgpr_read_b32 v205, a253
v_mov_b32 v222, 0
v_accvgpr_read_b32 v206, a254
v_mov_b32 v223, 0
v_accvgpr_read_b32 v207, a255
v_cvt_pk_bf16_f32 v64, v192, v193
v_cvt_pk_bf16_f32 v65, v194, v195
v_cvt_pk_bf16_f32 v66, v196, v197
v_cvt_pk_bf16_f32 v67, v198, v199
v_cvt_pk_bf16_f32 v68, v200, v201
v_cvt_pk_bf16_f32 v69, v202, v203
v_cvt_pk_bf16_f32 v70, v204, v205
v_cvt_pk_bf16_f32 v71, v206, v207
v_lshlrev_b32 v192, 16, v64
v_and_b32 v193, 0xffff0000, v64
v_lshlrev_b32 v194, 16, v65
v_and_b32 v195, 0xffff0000, v65
v_lshlrev_b32 v196, 16, v66
v_and_b32 v197, 0xffff0000, v66
v_lshlrev_b32 v198, 16, v67
v_and_b32 v199, 0xffff0000, v67
v_lshlrev_b32 v200, 16, v68
v_and_b32 v201, 0xffff0000, v68
v_lshlrev_b32 v202, 16, v69
v_and_b32 v203, 0xffff0000, v69
v_lshlrev_b32 v204, 16, v70
v_and_b32 v205, 0xffff0000, v70
v_lshlrev_b32 v206, 16, v71
v_and_b32 v207, 0xffff0000, v71
v_mul_f32 v64, 0xbfb8aa3b, v192
v_mul_f32 v65, 0xbfb8aa3b, v193
v_mul_f32 v66, 0xbfb8aa3b, v194
v_mul_f32 v67, 0xbfb8aa3b, v195
v_mul_f32 v68, 0xbfb8aa3b, v196
v_mul_f32 v69, 0xbfb8aa3b, v197
v_mul_f32 v70, 0xbfb8aa3b, v198
v_mul_f32 v71, 0xbfb8aa3b, v199
v_mul_f32 v72, 0xbfb8aa3b, v200
v_mul_f32 v73, 0xbfb8aa3b, v201
v_mul_f32 v74, 0xbfb8aa3b, v202
v_mul_f32 v75, 0xbfb8aa3b, v203
v_mul_f32 v76, 0xbfb8aa3b, v204
v_mul_f32 v77, 0xbfb8aa3b, v205
v_mul_f32 v78, 0xbfb8aa3b, v206
v_mul_f32 v79, 0xbfb8aa3b, v207
v_exp_f32 v64, v64
v_exp_f32 v65, v65
v_exp_f32 v66, v66
v_exp_f32 v67, v67
v_exp_f32 v68, v68
v_exp_f32 v69, v69
v_exp_f32 v70, v70
v_exp_f32 v71, v71
v_exp_f32 v72, v72
v_exp_f32 v73, v73
v_exp_f32 v74, v74
v_exp_f32 v75, v75
v_exp_f32 v76, v76
v_exp_f32 v77, v77
v_exp_f32 v78, v78
v_exp_f32 v79, v79
v_add_f32 v64, 1.0, v64
v_add_f32 v65, 1.0, v65
v_add_f32 v66, 1.0, v66
v_add_f32 v67, 1.0, v67
v_add_f32 v68, 1.0, v68
v_add_f32 v69, 1.0, v69
v_add_f32 v70, 1.0, v70
v_add_f32 v71, 1.0, v71
v_add_f32 v72, 1.0, v72
v_add_f32 v73, 1.0, v73
v_add_f32 v74, 1.0, v74
v_add_f32 v75, 1.0, v75
v_add_f32 v76, 1.0, v76
v_add_f32 v77, 1.0, v77
v_add_f32 v78, 1.0, v78
v_add_f32 v79, 1.0, v79
v_rcp_f32 v64, v64
v_rcp_f32 v65, v65
v_rcp_f32 v66, v66
v_rcp_f32 v67, v67
v_rcp_f32 v68, v68
v_rcp_f32 v69, v69
v_rcp_f32 v70, v70
v_rcp_f32 v71, v71
v_rcp_f32 v72, v72
v_rcp_f32 v73, v73
v_rcp_f32 v74, v74
v_rcp_f32 v75, v75
v_rcp_f32 v76, v76
v_rcp_f32 v77, v77
v_rcp_f32 v78, v78
v_rcp_f32 v79, v79
s_nop 0
v_mul_f32 v64, v64, v192
v_mul_f32 v65, v65, v193
v_mul_f32 v66, v66, v194
v_mul_f32 v67, v67, v195
v_mul_f32 v68, v68, v196
v_mul_f32 v69, v69, v197
v_mul_f32 v70, v70, v198
v_mul_f32 v71, v71, v199
v_mul_f32 v72, v72, v200
v_mul_f32 v73, v73, v201
v_mul_f32 v74, v74, v202
v_mul_f32 v75, v75, v203
v_mul_f32 v76, v76, v204
v_mul_f32 v77, v77, v205
v_mul_f32 v78, v78, v206
v_mul_f32 v79, v79, v207
v_cvt_pk_bf16_f32 v80, v64, v65
v_cvt_pk_bf16_f32 v81, v66, v67
v_cvt_pk_bf16_f32 v82, v68, v69
v_cvt_pk_bf16_f32 v83, v70, v71
v_cvt_pk_bf16_f32 v84, v72, v73
v_cvt_pk_bf16_f32 v85, v74, v75
v_cvt_pk_bf16_f32 v86, v76, v77
v_cvt_pk_bf16_f32 v87, v78, v79
v_lshlrev_b32 v192, 16, v80
v_and_b32 v193, 0xffff0000, v80
v_lshlrev_b32 v194, 16, v81
v_and_b32 v195, 0xffff0000, v81
v_lshlrev_b32 v196, 16, v82
v_and_b32 v197, 0xffff0000, v82
v_lshlrev_b32 v198, 16, v83
v_and_b32 v199, 0xffff0000, v83
v_lshlrev_b32 v200, 16, v84
v_and_b32 v201, 0xffff0000, v84
v_lshlrev_b32 v202, 16, v85
v_and_b32 v203, 0xffff0000, v85
v_lshlrev_b32 v204, 16, v86
v_and_b32 v205, 0xffff0000, v86
v_lshlrev_b32 v206, 16, v87
v_and_b32 v207, 0xffff0000, v87
s_mov_b32 s54, 0x00003f80
s_mov_b32 s55, 0x3f800000
s_waitcnt vmcnt(0)
buffer_store_dword v42, v43, s[36:39], s56 offen sc1
s_or_b32 s47, s29, s28
s_cmp_eq_u32 s47, 0
s_cbranch_scc0 EPDONE_%=
v_lshlrev_b32 v37, 6, v1
s_mov_b32 s52, 0
EPL_%=:
s_mov_b32 s48, 102825984
buffer_load_dword v64, v37, s[36:39], s48 offen sc1
s_mov_b32 s48, 102830080
buffer_load_dword v65, v37, s[36:39], s48 offen sc1
s_mov_b32 s48, 102834176
buffer_load_dword v66, v37, s[36:39], s48 offen sc1
s_mov_b32 s48, 102838272
buffer_load_dword v67, v37, s[36:39], s48 offen sc1
s_mov_b32 s48, 102842368
buffer_load_dword v68, v37, s[36:39], s48 offen sc1
s_mov_b32 s48, 102846464
buffer_load_dword v69, v37, s[36:39], s48 offen sc1
s_mov_b32 s48, 102850560
buffer_load_dword v70, v37, s[36:39], s48 offen sc1
s_mov_b32 s48, 102854656
buffer_load_dword v71, v37, s[36:39], s48 offen sc1
s_mov_b32 s48, 102858752
buffer_load_dword v72, v37, s[36:39], s48 offen sc1
s_mov_b32 s48, 102862848
buffer_load_dword v73, v37, s[36:39], s48 offen sc1
s_mov_b32 s48, 102866944
buffer_load_dword v74, v37, s[36:39], s48 offen sc1
s_mov_b32 s48, 102871040
buffer_load_dword v75, v37, s[36:39], s48 offen sc1
s_mov_b32 s48, 102875136
buffer_load_dword v76, v37, s[36:39], s48 offen sc1
s_mov_b32 s48, 102879232
buffer_load_dword v77, v37, s[36:39], s48 offen sc1
s_mov_b32 s48, 102883328
buffer_load_dword v78, v37, s[36:39], s48 offen sc1
s_mov_b32 s48, 102887424
buffer_load_dword v79, v37, s[36:39], s48 offen sc1
s_waitcnt vmcnt(0)
s_mov_b64 s[50:51], -1
v_cmp_eq_u32 vcc, s98, v64
s_nop 4
s_and_b64 s[50:51], s[50:51], vcc
v_cmp_eq_u32 vcc, s98, v65
s_nop 4
s_and_b64 s[50:51], s[50:51], vcc
v_cmp_eq_u32 vcc, s98, v66
s_nop 4
s_and_b64 s[50:51], s[50:51], vcc
v_cmp_eq_u32 vcc, s98, v67
s_nop 4
s_and_b64 s[50:51], s[50:51], vcc
v_cmp_eq_u32 vcc, s98, v68
s_nop 4
s_and_b64 s[50:51], s[50:51], vcc
v_cmp_eq_u32 vcc, s98, v69
s_nop 4
s_and_b64 s[50:51], s[50:51], vcc
v_cmp_eq_u32 vcc, s98, v70
s_nop 4
s_and_b64 s[50:51], s[50:51], vcc
v_cmp_eq_u32 vcc, s98, v71
s_nop 4
s_and_b64 s[50:51], s[50:51], vcc
v_cmp_eq_u32 vcc, s98, v72
s_nop 4
s_and_b64 s[50:51], s[50:51], vcc
v_cmp_eq_u32 vcc, s98, v73
s_nop 4
s_and_b64 s[50:51], s[50:51], vcc
v_cmp_eq_u32 vcc, s98, v74
s_nop 4
s_and_b64 s[50:51], s[50:51], vcc
v_cmp_eq_u32 vcc, s98, v75
s_nop 4
s_and_b64 s[50:51], s[50:51], vcc
v_cmp_eq_u32 vcc, s98, v76
s_nop 4
s_and_b64 s[50:51], s[50:51], vcc
v_cmp_eq_u32 vcc, s98, v77
s_nop 4
s_and_b64 s[50:51], s[50:51], vcc
v_cmp_eq_u32 vcc, s98, v78
s_nop 4
s_and_b64 s[50:51], s[50:51], vcc
v_cmp_eq_u32 vcc, s98, v79
s_nop 4
s_and_b64 s[50:51], s[50:51], vcc
s_add_u32 s52, s52, 1
s_cmp_eq_u64 s[50:51], -1
s_cbranch_scc1 EPOK_%=
s_cmp_lt_u32 s52, 0x4000
s_cbranch_scc1 EPL_%=
EPOK_%=:
v_mov_b32 v38, s98
s_mov_b32 s48, 103153664
buffer_store_dword v38, v43, s[36:39], s48 offen sc1
EPDONE_%=:
s_cmp_lt_u32 s29, 224
s_cbranch_scc0 FINALD_%=
s_mov_b32 s47, 0
s_mov_b32 s50, 0
s_mov_b32 s51, 0
v_mov_b32 v37, 0x7fffffff
FINP_%=:
buffer_load_dword v36, v35, s[36:39], 0 offen sc1
s_waitcnt vmcnt(0)
v_cmp_eq_u32 vcc, s98, v36
s_andn2_b32 s52, s50, s51
s_mov_b32 s51, s50
s_bitcmp1_b32 s52, 0
s_cbranch_scc0 FINA0_%=
v_dot2c_f32_bf16 v208, s54, v64
v_dot2c_f32_bf16 v209, s55, v64
v_dot2c_f32_bf16 v210, s54, v65
v_dot2c_f32_bf16 v211, s55, v65
v_dot2c_f32_bf16 v212, s54, v66
v_dot2c_f32_bf16 v213, s55, v66
v_dot2c_f32_bf16 v214, s54, v67
v_dot2c_f32_bf16 v215, s55, v67
v_dot2c_f32_bf16 v216, s54, v68
v_dot2c_f32_bf16 v217, s55, v68
v_dot2c_f32_bf16 v218, s54, v69
v_dot2c_f32_bf16 v219, s55, v69
v_dot2c_f32_bf16 v220, s54, v70
v_dot2c_f32_bf16 v221, s55, v70
v_dot2c_f32_bf16 v222, s54, v71
v_dot2c_f32_bf16 v223, s55, v71
FINA0_%=:
s_bitcmp1_b32 s52, 1
s_cbranch_scc0 FINA1_%=
v_dot2c_f32_bf16 v208, s54, v72
v_dot2c_f32_bf16 v209, s55, v72
v_dot2c_f32_bf16 v210, s54, v73
v_dot2c_f32_bf16 v211, s55, v73
v_dot2c_f32_bf16 v212, s54, v74
v_dot2c_f32_bf16 v213, s55, v74
v_dot2c_f32_bf16 v214, s54, v75
v_dot2c_f32_bf16 v215, s55, v75
v_dot2c_f32_bf16 v216, s54, v76
v_dot2c_f32_bf16 v217, s55, v76
v_dot2c_f32_bf16 v218, s54, v77
v_dot2c_f32_bf16 v219, s55, v77
v_dot2c_f32_bf16 v220, s54, v78
v_dot2c_f32_bf16 v221, s55, v78
v_dot2c_f32_bf16 v222, s54, v79
v_dot2c_f32_bf16 v223, s55, v79
FINA1_%=:
s_bitcmp1_b32 s52, 2
s_cbranch_scc0 FINA2_%=
v_dot2c_f32_bf16 v208, s54, v80
v_dot2c_f32_bf16 v209, s55, v80
v_dot2c_f32_bf16 v210, s54, v81
v_dot2c_f32_bf16 v211, s55, v81
v_dot2c_f32_bf16 v212, s54, v82
v_dot2c_f32_bf16 v213, s55, v82
v_dot2c_f32_bf16 v214, s54, v83
v_dot2c_f32_bf16 v215, s55, v83
v_dot2c_f32_bf16 v216, s54, v84
v_dot2c_f32_bf16 v217, s55, v84
v_dot2c_f32_bf16 v218, s54, v85
v_dot2c_f32_bf16 v219, s55, v85
v_dot2c_f32_bf16 v220, s54, v86
v_dot2c_f32_bf16 v221, s55, v86
v_dot2c_f32_bf16 v222, s54, v87
v_dot2c_f32_bf16 v223, s55, v87
FINA2_%=:
s_bitcmp1_b32 s52, 3
s_cbranch_scc0 FINA3_%=
v_dot2c_f32_bf16 v208, s54, v88
v_dot2c_f32_bf16 v209, s55, v88
v_dot2c_f32_bf16 v210, s54, v89
v_dot2c_f32_bf16 v211, s55, v89
v_dot2c_f32_bf16 v212, s54, v90
v_dot2c_f32_bf16 v213, s55, v90
v_dot2c_f32_bf16 v214, s54, v91
v_dot2c_f32_bf16 v215, s55, v91
v_dot2c_f32_bf16 v216, s54, v92
v_dot2c_f32_bf16 v217, s55, v92
v_dot2c_f32_bf16 v218, s54, v93
v_dot2c_f32_bf16 v219, s55, v93
v_dot2c_f32_bf16 v220, s54, v94
v_dot2c_f32_bf16 v221, s55, v94
v_dot2c_f32_bf16 v222, s54, v95
v_dot2c_f32_bf16 v223, s55, v95
FINA3_%=:
s_bitcmp1_b32 s52, 4
s_cbranch_scc0 FINA4_%=
v_dot2c_f32_bf16 v208, s54, v96
v_dot2c_f32_bf16 v209, s55, v96
v_dot2c_f32_bf16 v210, s54, v97
v_dot2c_f32_bf16 v211, s55, v97
v_dot2c_f32_bf16 v212, s54, v98
v_dot2c_f32_bf16 v213, s55, v98
v_dot2c_f32_bf16 v214, s54, v99
v_dot2c_f32_bf16 v215, s55, v99
v_dot2c_f32_bf16 v216, s54, v100
v_dot2c_f32_bf16 v217, s55, v100
v_dot2c_f32_bf16 v218, s54, v101
v_dot2c_f32_bf16 v219, s55, v101
v_dot2c_f32_bf16 v220, s54, v102
v_dot2c_f32_bf16 v221, s55, v102
v_dot2c_f32_bf16 v222, s54, v103
v_dot2c_f32_bf16 v223, s55, v103
FINA4_%=:
s_bitcmp1_b32 s52, 5
s_cbranch_scc0 FINA5_%=
v_dot2c_f32_bf16 v208, s54, v104
v_dot2c_f32_bf16 v209, s55, v104
v_dot2c_f32_bf16 v210, s54, v105
v_dot2c_f32_bf16 v211, s55, v105
v_dot2c_f32_bf16 v212, s54, v106
v_dot2c_f32_bf16 v213, s55, v106
v_dot2c_f32_bf16 v214, s54, v107
v_dot2c_f32_bf16 v215, s55, v107
v_dot2c_f32_bf16 v216, s54, v108
v_dot2c_f32_bf16 v217, s55, v108
v_dot2c_f32_bf16 v218, s54, v109
v_dot2c_f32_bf16 v219, s55, v109
v_dot2c_f32_bf16 v220, s54, v110
v_dot2c_f32_bf16 v221, s55, v110
v_dot2c_f32_bf16 v222, s54, v111
v_dot2c_f32_bf16 v223, s55, v111
FINA5_%=:
s_bitcmp1_b32 s52, 6
s_cbranch_scc0 FINA6_%=
v_dot2c_f32_bf16 v208, s54, v112
v_dot2c_f32_bf16 v209, s55, v112
v_dot2c_f32_bf16 v210, s54, v113
v_dot2c_f32_bf16 v211, s55, v113
v_dot2c_f32_bf16 v212, s54, v114
v_dot2c_f32_bf16 v213, s55, v114
v_dot2c_f32_bf16 v214, s54, v115
v_dot2c_f32_bf16 v215, s55, v115
v_dot2c_f32_bf16 v216, s54, v116
v_dot2c_f32_bf16 v217, s55, v116
v_dot2c_f32_bf16 v218, s54, v117
v_dot2c_f32_bf16 v219, s55, v117
v_dot2c_f32_bf16 v220, s54, v118
v_dot2c_f32_bf16 v221, s55, v118
v_dot2c_f32_bf16 v222, s54, v119
v_dot2c_f32_bf16 v223, s55, v119
FINA6_%=:
s_bitcmp1_b32 s52, 7
s_cbranch_scc0 FINA7_%=
v_dot2c_f32_bf16 v208, s54, v120
v_dot2c_f32_bf16 v209, s55, v120
v_dot2c_f32_bf16 v210, s54, v121
v_dot2c_f32_bf16 v211, s55, v121
v_dot2c_f32_bf16 v212, s54, v122
v_dot2c_f32_bf16 v213, s55, v122
v_dot2c_f32_bf16 v214, s54, v123
v_dot2c_f32_bf16 v215, s55, v123
v_dot2c_f32_bf16 v216, s54, v124
v_dot2c_f32_bf16 v217, s55, v124
v_dot2c_f32_bf16 v218, s54, v125
v_dot2c_f32_bf16 v219, s55, v125
v_dot2c_f32_bf16 v220, s54, v126
v_dot2c_f32_bf16 v221, s55, v126
v_dot2c_f32_bf16 v222, s54, v127
v_dot2c_f32_bf16 v223, s55, v127
FINA7_%=:
s_bitcmp1_b32 s52, 8
s_cbranch_scc0 FINA8_%=
v_dot2c_f32_bf16 v208, s54, v128
v_dot2c_f32_bf16 v209, s55, v128
v_dot2c_f32_bf16 v210, s54, v129
v_dot2c_f32_bf16 v211, s55, v129
v_dot2c_f32_bf16 v212, s54, v130
v_dot2c_f32_bf16 v213, s55, v130
v_dot2c_f32_bf16 v214, s54, v131
v_dot2c_f32_bf16 v215, s55, v131
v_dot2c_f32_bf16 v216, s54, v132
v_dot2c_f32_bf16 v217, s55, v132
v_dot2c_f32_bf16 v218, s54, v133
v_dot2c_f32_bf16 v219, s55, v133
v_dot2c_f32_bf16 v220, s54, v134
v_dot2c_f32_bf16 v221, s55, v134
v_dot2c_f32_bf16 v222, s54, v135
v_dot2c_f32_bf16 v223, s55, v135
FINA8_%=:
s_bitcmp1_b32 s52, 9
s_cbranch_scc0 FINA9_%=
v_dot2c_f32_bf16 v208, s54, v136
v_dot2c_f32_bf16 v209, s55, v136
v_dot2c_f32_bf16 v210, s54, v137
v_dot2c_f32_bf16 v211, s55, v137
v_dot2c_f32_bf16 v212, s54, v138
v_dot2c_f32_bf16 v213, s55, v138
v_dot2c_f32_bf16 v214, s54, v139
v_dot2c_f32_bf16 v215, s55, v139
v_dot2c_f32_bf16 v216, s54, v140
v_dot2c_f32_bf16 v217, s55, v140
v_dot2c_f32_bf16 v218, s54, v141
v_dot2c_f32_bf16 v219, s55, v141
v_dot2c_f32_bf16 v220, s54, v142
v_dot2c_f32_bf16 v221, s55, v142
v_dot2c_f32_bf16 v222, s54, v143
v_dot2c_f32_bf16 v223, s55, v143
FINA9_%=:
s_bitcmp1_b32 s52, 10
s_cbranch_scc0 FINA10_%=
v_dot2c_f32_bf16 v208, s54, v144
v_dot2c_f32_bf16 v209, s55, v144
v_dot2c_f32_bf16 v210, s54, v145
v_dot2c_f32_bf16 v211, s55, v145
v_dot2c_f32_bf16 v212, s54, v146
v_dot2c_f32_bf16 v213, s55, v146
v_dot2c_f32_bf16 v214, s54, v147
v_dot2c_f32_bf16 v215, s55, v147
v_dot2c_f32_bf16 v216, s54, v148
v_dot2c_f32_bf16 v217, s55, v148
v_dot2c_f32_bf16 v218, s54, v149
v_dot2c_f32_bf16 v219, s55, v149
v_dot2c_f32_bf16 v220, s54, v150
v_dot2c_f32_bf16 v221, s55, v150
v_dot2c_f32_bf16 v222, s54, v151
v_dot2c_f32_bf16 v223, s55, v151
FINA10_%=:
s_bitcmp1_b32 s52, 11
s_cbranch_scc0 FINA11_%=
v_dot2c_f32_bf16 v208, s54, v152
v_dot2c_f32_bf16 v209, s55, v152
v_dot2c_f32_bf16 v210, s54, v153
v_dot2c_f32_bf16 v211, s55, v153
v_dot2c_f32_bf16 v212, s54, v154
v_dot2c_f32_bf16 v213, s55, v154
v_dot2c_f32_bf16 v214, s54, v155
v_dot2c_f32_bf16 v215, s55, v155
v_dot2c_f32_bf16 v216, s54, v156
v_dot2c_f32_bf16 v217, s55, v156
v_dot2c_f32_bf16 v218, s54, v157
v_dot2c_f32_bf16 v219, s55, v157
v_dot2c_f32_bf16 v220, s54, v158
v_dot2c_f32_bf16 v221, s55, v158
v_dot2c_f32_bf16 v222, s54, v159
v_dot2c_f32_bf16 v223, s55, v159
FINA11_%=:
s_bitcmp1_b32 s52, 12
s_cbranch_scc0 FINA12_%=
v_dot2c_f32_bf16 v208, s54, v160
v_dot2c_f32_bf16 v209, s55, v160
v_dot2c_f32_bf16 v210, s54, v161
v_dot2c_f32_bf16 v211, s55, v161
v_dot2c_f32_bf16 v212, s54, v162
v_dot2c_f32_bf16 v213, s55, v162
v_dot2c_f32_bf16 v214, s54, v163
v_dot2c_f32_bf16 v215, s55, v163
v_dot2c_f32_bf16 v216, s54, v164
v_dot2c_f32_bf16 v217, s55, v164
v_dot2c_f32_bf16 v218, s54, v165
v_dot2c_f32_bf16 v219, s55, v165
v_dot2c_f32_bf16 v220, s54, v166
v_dot2c_f32_bf16 v221, s55, v166
v_dot2c_f32_bf16 v222, s54, v167
v_dot2c_f32_bf16 v223, s55, v167
FINA12_%=:
s_bitcmp1_b32 s52, 13
s_cbranch_scc0 FINA13_%=
v_dot2c_f32_bf16 v208, s54, v168
v_dot2c_f32_bf16 v209, s55, v168
v_dot2c_f32_bf16 v210, s54, v169
v_dot2c_f32_bf16 v211, s55, v169
v_dot2c_f32_bf16 v212, s54, v170
v_dot2c_f32_bf16 v213, s55, v170
v_dot2c_f32_bf16 v214, s54, v171
v_dot2c_f32_bf16 v215, s55, v171
v_dot2c_f32_bf16 v216, s54, v172
v_dot2c_f32_bf16 v217, s55, v172
v_dot2c_f32_bf16 v218, s54, v173
v_dot2c_f32_bf16 v219, s55, v173
v_dot2c_f32_bf16 v220, s54, v174
v_dot2c_f32_bf16 v221, s55, v174
v_dot2c_f32_bf16 v222, s54, v175
v_dot2c_f32_bf16 v223, s55, v175
FINA13_%=:
s_bitcmp1_b32 s52, 14
s_cbranch_scc0 FINA14_%=
v_dot2c_f32_bf16 v208, s54, v176
v_dot2c_f32_bf16 v209, s55, v176
v_dot2c_f32_bf16 v210, s54, v177
v_dot2c_f32_bf16 v211, s55, v177
v_dot2c_f32_bf16 v212, s54, v178
v_dot2c_f32_bf16 v213, s55, v178
v_dot2c_f32_bf16 v214, s54, v179
v_dot2c_f32_bf16 v215, s55, v179
v_dot2c_f32_bf16 v216, s54, v180
v_dot2c_f32_bf16 v217, s55, v180
v_dot2c_f32_bf16 v218, s54, v181
v_dot2c_f32_bf16 v219, s55, v181
v_dot2c_f32_bf16 v220, s54, v182
v_dot2c_f32_bf16 v221, s55, v182
v_dot2c_f32_bf16 v222, s54, v183
v_dot2c_f32_bf16 v223, s55, v183
FINA14_%=:
s_bitcmp1_b32 s52, 15
s_cbranch_scc0 FINA15_%=
v_dot2c_f32_bf16 v208, s54, v184
v_dot2c_f32_bf16 v209, s55, v184
v_dot2c_f32_bf16 v210, s54, v185
v_dot2c_f32_bf16 v211, s55, v185
v_dot2c_f32_bf16 v212, s54, v186
v_dot2c_f32_bf16 v213, s55, v186
v_dot2c_f32_bf16 v214, s54, v187
v_dot2c_f32_bf16 v215, s55, v187
v_dot2c_f32_bf16 v216, s54, v188
v_dot2c_f32_bf16 v217, s55, v188
v_dot2c_f32_bf16 v218, s54, v189
v_dot2c_f32_bf16 v219, s55, v189
v_dot2c_f32_bf16 v220, s54, v190
v_dot2c_f32_bf16 v221, s55, v190
v_dot2c_f32_bf16 v222, s54, v191
v_dot2c_f32_bf16 v223, s55, v191
FINA15_%=:
s_cmp_eq_u32 s51, 0xffff
s_cbranch_scc1 FINOK_%=
s_and_b32 s53, vcc_lo, 0xffff
v_cndmask_b32 v35, v35, v37, vcc
s_add_u32 s47, s47, 1
s_cmp_ge_u32 s47, 0x40000
s_cselect_b32 s53, 0xffff, s53
s_andn2_b32 s52, s53, s50
s_or_b32 s50, s50, s52
s_bitcmp1_b32 s52, 0
s_cbranch_scc0 FINL0_%=
s_add_u32 s53, s49, 0
buffer_load_dwordx4 v[64:67], v2, s[36:39], s53 offen offset:0 sc1
buffer_load_dwordx4 v[68:71], v2, s[36:39], s53 offen offset:1024 sc1
FINL0_%=:
s_bitcmp1_b32 s52, 1
s_cbranch_scc0 FINL1_%=
s_add_u32 s53, s49, 1835008
buffer_load_dwordx4 v[72:75], v2, s[36:39], s53 offen offset:0 sc1
buffer_load_dwordx4 v[76:79], v2, s[36:39], s53 offen offset:1024 sc1
FINL1_%=:
s_bitcmp1_b32 s52, 2
s_cbranch_scc0 FINL2_%=
s_add_u32 s53, s49, 3670016
buffer_load_dwordx4 v[80:83], v2, s[36:39], s53 offen offset:0 sc1
buffer_load_dwordx4 v[84:87], v2, s[36:39], s53 offen offset:1024 sc1
FINL2_%=:
s_bitcmp1_b32 s52, 3
s_cbranch_scc0 FINL3_%=
s_add_u32 s53, s49, 5505024
buffer_load_dwordx4 v[88:91], v2, s[36:39], s53 offen offset:0 sc1
buffer_load_dwordx4 v[92:95], v2, s[36:39], s53 offen offset:1024 sc1
FINL3_%=:
s_bitcmp1_b32 s52, 4
s_cbranch_scc0 FINL4_%=
s_add_u32 s53, s49, 7340032
buffer_load_dwordx4 v[96:99], v2, s[36:39], s53 offen offset:0 sc1
buffer_load_dwordx4 v[100:103], v2, s[36:39], s53 offen offset:1024 sc1
FINL4_%=:
s_bitcmp1_b32 s52, 5
s_cbranch_scc0 FINL5_%=
s_add_u32 s53, s49, 9175040
buffer_load_dwordx4 v[104:107], v2, s[36:39], s53 offen offset:0 sc1
buffer_load_dwordx4 v[108:111], v2, s[36:39], s53 offen offset:1024 sc1
FINL5_%=:
s_bitcmp1_b32 s52, 6
s_cbranch_scc0 FINL6_%=
s_add_u32 s53, s49, 11010048
buffer_load_dwordx4 v[112:115], v2, s[36:39], s53 offen offset:0 sc1
buffer_load_dwordx4 v[116:119], v2, s[36:39], s53 offen offset:1024 sc1
FINL6_%=:
s_bitcmp1_b32 s52, 7
s_cbranch_scc0 FINL7_%=
s_add_u32 s53, s49, 12845056
buffer_load_dwordx4 v[120:123], v2, s[36:39], s53 offen offset:0 sc1
buffer_load_dwordx4 v[124:127], v2, s[36:39], s53 offen offset:1024 sc1
FINL7_%=:
s_bitcmp1_b32 s52, 8
s_cbranch_scc0 FINL8_%=
s_add_u32 s53, s49, 14680064
buffer_load_dwordx4 v[128:131], v2, s[36:39], s53 offen offset:0 sc1
buffer_load_dwordx4 v[132:135], v2, s[36:39], s53 offen offset:1024 sc1
FINL8_%=:
s_bitcmp1_b32 s52, 9
s_cbranch_scc0 FINL9_%=
s_add_u32 s53, s49, 16515072
buffer_load_dwordx4 v[136:139], v2, s[36:39], s53 offen offset:0 sc1
buffer_load_dwordx4 v[140:143], v2, s[36:39], s53 offen offset:1024 sc1
FINL9_%=:
s_bitcmp1_b32 s52, 10
s_cbranch_scc0 FINL10_%=
s_add_u32 s53, s49, 18350080
buffer_load_dwordx4 v[144:147], v2, s[36:39], s53 offen offset:0 sc1
buffer_load_dwordx4 v[148:151], v2, s[36:39], s53 offen offset:1024 sc1
FINL10_%=:
s_bitcmp1_b32 s52, 11
s_cbranch_scc0 FINL11_%=
s_add_u32 s53, s49, 20185088
buffer_load_dwordx4 v[152:155], v2, s[36:39], s53 offen offset:0 sc1
buffer_load_dwordx4 v[156:159], v2, s[36:39], s53 offen offset:1024 sc1
FINL11_%=:
s_bitcmp1_b32 s52, 12
s_cbranch_scc0 FINL12_%=
s_add_u32 s53, s49, 22020096
buffer_load_dwordx4 v[160:163], v2, s[36:39], s53 offen offset:0 sc1
buffer_load_dwordx4 v[164:167], v2, s[36:39], s53 offen offset:1024 sc1
FINL12_%=:
s_bitcmp1_b32 s52, 13
s_cbranch_scc0 FINL13_%=
s_add_u32 s53, s49, 23855104
buffer_load_dwordx4 v[168:171], v2, s[36:39], s53 offen offset:0 sc1
buffer_load_dwordx4 v[172:175], v2, s[36:39], s53 offen offset:1024 sc1
FINL13_%=:
s_bitcmp1_b32 s52, 14
s_cbranch_scc0 FINL14_%=
s_add_u32 s53, s49, 25690112
buffer_load_dwordx4 v[176:179], v2, s[36:39], s53 offen offset:0 sc1
buffer_load_dwordx4 v[180:183], v2, s[36:39], s53 offen offset:1024 sc1
FINL14_%=:
s_bitcmp1_b32 s52, 15
s_cbranch_scc0 FINL15_%=
s_add_u32 s53, s49, 27525120
buffer_load_dwordx4 v[184:187], v2, s[36:39], s53 offen offset:0 sc1
buffer_load_dwordx4 v[188:191], v2, s[36:39], s53 offen offset:1024 sc1
FINL15_%=:
s_cmp_lg_u32 s52, 0
s_cbranch_scc1 FINP_%=
s_sleep 4
s_branch FINP_%=
FINOK_%=:
s_nop 3
v_cvt_pk_bf16_f32 v64, v208, v209
v_cvt_pk_bf16_f32 v65, v210, v211
v_cvt_pk_bf16_f32 v66, v212, v213
v_cvt_pk_bf16_f32 v67, v214, v215
v_cvt_pk_bf16_f32 v68, v216, v217
v_cvt_pk_bf16_f32 v69, v218, v219
v_cvt_pk_bf16_f32 v70, v220, v221
v_cvt_pk_bf16_f32 v71, v222, v223
v_lshlrev_b32 v208, 16, v64
v_and_b32 v209, 0xffff0000, v64
v_lshlrev_b32 v210, 16, v65
v_and_b32 v211, 0xffff0000, v65
v_lshlrev_b32 v212, 16, v66
v_and_b32 v213, 0xffff0000, v66
v_lshlrev_b32 v214, 16, v67
v_and_b32 v215, 0xffff0000, v67
v_lshlrev_b32 v216, 16, v68
v_and_b32 v217, 0xffff0000, v68
v_lshlrev_b32 v218, 16, v69
v_and_b32 v219, 0xffff0000, v69
v_lshlrev_b32 v220, 16, v70
v_and_b32 v221, 0xffff0000, v70
v_lshlrev_b32 v222, 16, v71
v_and_b32 v223, 0xffff0000, v71
v_mul_f32 v208, v208, v192
v_mul_f32 v209, v209, v193
v_mul_f32 v210, v210, v194
v_mul_f32 v211, v211, v195
v_mul_f32 v212, v212, v196
v_mul_f32 v213, v213, v197
v_mul_f32 v214, v214, v198
v_mul_f32 v215, v215, v199
v_mul_f32 v216, v216, v200
v_mul_f32 v217, v217, v201
v_mul_f32 v218, v218, v202
v_mul_f32 v219, v219, v203
v_mul_f32 v220, v220, v204
v_mul_f32 v221, v221, v205
v_mul_f32 v222, v222, v206
v_mul_f32 v223, v223, v207
v_cvt_pk_bf16_f32 v64, v208, v209
v_cvt_pk_bf16_f32 v65, v210, v211
v_cvt_pk_bf16_f32 v66, v212, v213
v_cvt_pk_bf16_f32 v67, v214, v215
v_cvt_pk_bf16_f32 v68, v216, v217
v_cvt_pk_bf16_f32 v69, v218, v219
v_cvt_pk_bf16_f32 v70, v220, v221
v_cvt_pk_bf16_f32 v71, v222, v223
v_lshlrev_b32 v208, 16, v64
v_and_b32 v209, 0xffff0000, v64
v_lshlrev_b32 v192, 16, v224
v_and_b32 v193, 0xffff0000, v224
v_lshlrev_b32 v210, 16, v65
v_and_b32 v211, 0xffff0000, v65
v_lshlrev_b32 v194, 16, v225
v_and_b32 v195, 0xffff0000, v225
v_lshlrev_b32 v212, 16, v66
v_and_b32 v213, 0xffff0000, v66
v_lshlrev_b32 v196, 16, v226
v_and_b32 v197, 0xffff0000, v226
v_lshlrev_b32 v214, 16, v67
v_and_b32 v215, 0xffff0000, v67
v_lshlrev_b32 v198, 16, v227
v_and_b32 v199, 0xffff0000, v227
v_lshlrev_b32 v216, 16, v68
v_and_b32 v217, 0xffff0000, v68
v_lshlrev_b32 v200, 16, v228
v_and_b32 v201, 0xffff0000, v228
v_lshlrev_b32 v218, 16, v69
v_and_b32 v219, 0xffff0000, v69
v_lshlrev_b32 v202, 16, v229
v_and_b32 v203, 0xffff0000, v229
v_lshlrev_b32 v220, 16, v70
v_and_b32 v221, 0xffff0000, v70
v_lshlrev_b32 v204, 16, v230
v_and_b32 v205, 0xffff0000, v230
v_lshlrev_b32 v222, 16, v71
v_and_b32 v223, 0xffff0000, v71
v_lshlrev_b32 v206, 16, v231
v_and_b32 v207, 0xffff0000, v231
v_add_f32 v208, v208, v192
v_add_f32 v209, v209, v193
v_add_f32 v210, v210, v194
v_add_f32 v211, v211, v195
v_add_f32 v212, v212, v196
v_add_f32 v213, v213, v197
v_add_f32 v214, v214, v198
v_add_f32 v215, v215, v199
v_add_f32 v216, v216, v200
v_add_f32 v217, v217, v201
v_add_f32 v218, v218, v202
v_add_f32 v219, v219, v203
v_add_f32 v220, v220, v204
v_add_f32 v221, v221, v205
v_add_f32 v222, v222, v206
v_add_f32 v223, v223, v207
v_cvt_pk_bf16_f32 v64, v208, v209
v_cvt_pk_bf16_f32 v65, v210, v211
v_cvt_pk_bf16_f32 v66, v212, v213
v_cvt_pk_bf16_f32 v67, v214, v215
v_cvt_pk_bf16_f32 v68, v216, v217
v_cvt_pk_bf16_f32 v69, v218, v219
v_cvt_pk_bf16_f32 v70, v220, v221
v_cvt_pk_bf16_f32 v71, v222, v223
buffer_store_dwordx2 v[64:65], v33, s[20:23], 0 offen offset:0
buffer_store_dwordx2 v[66:67], v33, s[20:23], 0 offen offset:32
buffer_store_dwordx2 v[68:69], v33, s[20:23], 0 offen offset:64
buffer_store_dwordx2 v[70:71], v33, s[20:23], 0 offen offset:96
FINALD_%=:
s_waitcnt vmcnt(0)
s_endpgm
W64F_%=:
s_add_u32 s54, s45, 10240
s_add_u32 s55, s46, 10240
v_add_u32 v8, s24, v4
v_add_u32 v9, s24, v5
v_add_u32 v10, s24, v6
v_add_u32 v11, s24, v7
s_nop 0
ds_read_b64_tr_b16 v[192:193], v8 offset:0
ds_read_b64_tr_b16 v[194:195], v8 offset:2048
ds_read_b64_tr_b16 v[196:197], v9 offset:0
ds_read_b64_tr_b16 v[198:199], v9 offset:2048
ds_read_b64_tr_b16 v[200:201], v10 offset:0
ds_read_b64_tr_b16 v[202:203], v10 offset:2048
ds_read_b64_tr_b16 v[204:205], v11 offset:0
ds_read_b64_tr_b16 v[206:207], v11 offset:2048
ds_read_b64_tr_b16 v[208:209], v8 offset:8192
ds_read_b64_tr_b16 v[210:211], v8 offset:10240
ds_read_b64_tr_b16 v[212:213], v9 offset:8192
ds_read_b64_tr_b16 v[214:215], v9 offset:10240
s_waitcnt lgkmcnt(0)
v_mfma_f32_16x16x32_bf16 a[0:3], v[192:195], v[20:23], 0
ds_read_b64_tr_b16 v[216:217], v10 offset:8192
v_mfma_f32_16x16x32_bf16 a[4:7], v[192:195], v[24:27], 0
ds_read_b64_tr_b16 v[218:219], v10 offset:10240
buffer_load_dwordx4 a[208:211], v2, s[36:39], s54 offen offset:0 sc1
v_mfma_f32_16x16x32_bf16 a[12:15], v[196:199], v[24:27], 0
s_add_u32 s20, s20, s21
ds_read_b64_tr_b16 v[220:221], v11 offset:8192
v_mfma_f32_16x16x32_bf16 a[8:11], v[196:199], v[20:23], 0
s_waitcnt lgkmcnt(7)
ds_read_b64_tr_b16 v[222:223], v11 offset:10240
v_mfma_f32_16x16x32_bf16 a[16:19], v[200:203], v[20:23], 0
s_add_u32 m0, s23, 2048
ds_read_b64_tr_b16 v[192:193], v8 offset:16384
ds_read_b64_tr_b16 v[194:195], v8 offset:18432
v_mfma_f32_16x16x32_bf16 a[20:23], v[200:203], v[24:27], 0
buffer_load_dwordx4 v14, s[16:19], s20 offen sc1 lds
v_mfma_f32_16x16x32_bf16 a[28:31], v[204:207], v[24:27], 0
ds_read_b64_tr_b16 v[196:197], v9 offset:16384
v_mfma_f32_16x16x32_bf16 a[24:27], v[204:207], v[20:23], 0
s_waitcnt lgkmcnt(7)
ds_read_b64_tr_b16 v[198:199], v9 offset:18432
v_mfma_f32_16x16x32_bf16 a[32:35], v[208:211], v[20:23], 0
ds_read_b64_tr_b16 v[200:201], v10 offset:16384
v_mfma_f32_16x16x32_bf16 a[36:39], v[208:211], v[24:27], 0
ds_read_b64_tr_b16 v[202:203], v10 offset:18432
buffer_load_dwordx4 a[212:215], v2, s[36:39], s55 offen offset:0 sc1
v_mfma_f32_16x16x32_bf16 a[44:47], v[212:215], v[24:27], 0
s_add_u32 s20, s20, s21
ds_read_b64_tr_b16 v[204:205], v11 offset:16384
v_mfma_f32_16x16x32_bf16 a[40:43], v[212:215], v[20:23], 0
s_waitcnt lgkmcnt(7)
ds_read_b64_tr_b16 v[206:207], v11 offset:18432
v_mfma_f32_16x16x32_bf16 a[48:51], v[216:219], v[20:23], 0
s_add_u32 m0, s23, 3072
ds_read_b64_tr_b16 v[208:209], v8 offset:24576
ds_read_b64_tr_b16 v[210:211], v8 offset:26624
v_mfma_f32_16x16x32_bf16 a[52:55], v[216:219], v[24:27], 0
buffer_load_dwordx4 v14, s[16:19], s20 offen sc1 lds
v_mfma_f32_16x16x32_bf16 a[60:63], v[220:223], v[24:27], 0
ds_read_b64_tr_b16 v[212:213], v9 offset:24576
v_mfma_f32_16x16x32_bf16 a[56:59], v[220:223], v[20:23], 0
s_waitcnt lgkmcnt(7)
ds_read_b64_tr_b16 v[214:215], v9 offset:26624
v_mfma_f32_16x16x32_bf16 a[64:67], v[192:195], v[20:23], 0
ds_read_b64_tr_b16 v[216:217], v10 offset:24576
v_mfma_f32_16x16x32_bf16 a[68:71], v[192:195], v[24:27], 0
ds_read_b64_tr_b16 v[218:219], v10 offset:26624
buffer_load_dwordx4 a[216:219], v2, s[36:39], s54 offen offset:1024 sc1
v_mfma_f32_16x16x32_bf16 a[76:79], v[196:199], v[24:27], 0
s_add_u32 s20, s20, s21
ds_read_b64_tr_b16 v[220:221], v11 offset:24576
v_mfma_f32_16x16x32_bf16 a[72:75], v[196:199], v[20:23], 0
s_waitcnt lgkmcnt(7)
ds_read_b64_tr_b16 v[222:223], v11 offset:26624
v_mfma_f32_16x16x32_bf16 a[80:83], v[200:203], v[20:23], 0
s_add_u32 m0, s23, 4096
ds_read_b64_tr_b16 v[192:193], v8 offset:4096
ds_read_b64_tr_b16 v[194:195], v8 offset:6144
v_mfma_f32_16x16x32_bf16 a[84:87], v[200:203], v[24:27], 0
buffer_load_dwordx4 v14, s[16:19], s20 offen sc1 lds
v_mfma_f32_16x16x32_bf16 a[92:95], v[204:207], v[24:27], 0
ds_read_b64_tr_b16 v[196:197], v9 offset:4096
v_mfma_f32_16x16x32_bf16 a[88:91], v[204:207], v[20:23], 0
s_waitcnt lgkmcnt(7)
ds_read_b64_tr_b16 v[198:199], v9 offset:6144
v_mfma_f32_16x16x32_bf16 a[96:99], v[208:211], v[20:23], 0
ds_read_b64_tr_b16 v[200:201], v10 offset:4096
v_mfma_f32_16x16x32_bf16 a[100:103], v[208:211], v[24:27], 0
ds_read_b64_tr_b16 v[202:203], v10 offset:6144
buffer_load_dwordx4 a[220:223], v2, s[36:39], s55 offen offset:1024 sc1
v_mfma_f32_16x16x32_bf16 a[108:111], v[212:215], v[24:27], 0
s_add_u32 s20, s20, s21
ds_read_b64_tr_b16 v[204:205], v11 offset:4096
v_mfma_f32_16x16x32_bf16 a[104:107], v[212:215], v[20:23], 0
s_waitcnt lgkmcnt(7)
ds_read_b64_tr_b16 v[206:207], v11 offset:6144
v_mfma_f32_16x16x32_bf16 a[112:115], v[216:219], v[20:23], 0
s_add_u32 m0, s23, 5120
ds_read_b64_tr_b16 v[208:209], v8 offset:12288
ds_read_b64_tr_b16 v[210:211], v8 offset:14336
v_mfma_f32_16x16x32_bf16 a[116:119], v[216:219], v[24:27], 0
buffer_load_dwordx4 v14, s[16:19], s20 offen sc1 lds
v_mfma_f32_16x16x32_bf16 a[124:127], v[220:223], v[24:27], 0
ds_read_b64_tr_b16 v[212:213], v9 offset:12288
v_mfma_f32_16x16x32_bf16 a[120:123], v[220:223], v[20:23], 0
s_waitcnt lgkmcnt(7)
ds_read_b64_tr_b16 v[214:215], v9 offset:14336
v_mfma_f32_16x16x32_bf16 a[0:3], v[192:195], v[28:31], a[0:3]
ds_read_b64_tr_b16 v[216:217], v10 offset:12288
v_mfma_f32_16x16x32_bf16 a[4:7], v[192:195], v[32:35], a[4:7]
ds_read_b64_tr_b16 v[218:219], v10 offset:14336
buffer_load_dwordx4 a[224:227], v2, s[36:39], s54 offen offset:2048 sc1
v_mfma_f32_16x16x32_bf16 a[12:15], v[196:199], v[32:35], a[12:15]
s_add_u32 s20, s20, s21
ds_read_b64_tr_b16 v[220:221], v11 offset:12288
v_mfma_f32_16x16x32_bf16 a[8:11], v[196:199], v[28:31], a[8:11]
s_waitcnt lgkmcnt(7)
ds_read_b64_tr_b16 v[222:223], v11 offset:14336
v_mfma_f32_16x16x32_bf16 a[16:19], v[200:203], v[28:31], a[16:19]
s_add_u32 m0, s23, 6144
ds_read_b64_tr_b16 v[192:193], v8 offset:20480
ds_read_b64_tr_b16 v[194:195], v8 offset:22528
v_mfma_f32_16x16x32_bf16 a[20:23], v[200:203], v[32:35], a[20:23]
buffer_load_dwordx4 v14, s[16:19], s20 offen sc1 lds
v_mfma_f32_16x16x32_bf16 a[28:31], v[204:207], v[32:35], a[28:31]
s_add_u32 s26, s24, 32768
ds_read_b64_tr_b16 v[196:197], v9 offset:20480
v_mfma_f32_16x16x32_bf16 a[24:27], v[204:207], v[28:31], a[24:27]
s_waitcnt lgkmcnt(7)
ds_read_b64_tr_b16 v[198:199], v9 offset:22528
v_mfma_f32_16x16x32_bf16 a[32:35], v[208:211], v[28:31], a[32:35]
s_sub_u32 s35, s26, 163840
ds_read_b64_tr_b16 v[200:201], v10 offset:20480
v_mfma_f32_16x16x32_bf16 a[36:39], v[208:211], v[32:35], a[36:39]
s_cselect_b32 s26, s26, s35
ds_read_b64_tr_b16 v[202:203], v10 offset:22528
buffer_load_dwordx4 a[228:231], v2, s[36:39], s55 offen offset:2048 sc1
v_mfma_f32_16x16x32_bf16 a[44:47], v[212:215], v[32:35], a[44:47]
s_add_u32 s20, s20, s21
ds_read_b64_tr_b16 v[204:205], v11 offset:20480
v_mfma_f32_16x16x32_bf16 a[40:43], v[212:215], v[28:31], a[40:43]
s_waitcnt lgkmcnt(7)
ds_read_b64_tr_b16 v[206:207], v11 offset:22528
v_mfma_f32_16x16x32_bf16 a[48:51], v[216:219], v[28:31], a[48:51]
s_add_u32 m0, s23, 7168
ds_read_b64_tr_b16 v[208:209], v8 offset:28672
ds_read_b64_tr_b16 v[210:211], v8 offset:30720
v_mfma_f32_16x16x32_bf16 a[52:55], v[216:219], v[32:35], a[52:55]
buffer_load_dwordx4 v14, s[16:19], s20 offen sc1 lds
v_mfma_f32_16x16x32_bf16 a[60:63], v[220:223], v[32:35], a[60:63]
s_add_u32 s23, s27, s33
ds_read_b64_tr_b16 v[212:213], v9 offset:28672
v_readlane_b32 s16, v16, s22
v_mfma_f32_16x16x32_bf16 a[56:59], v[220:223], v[28:31], a[56:59]
s_waitcnt lgkmcnt(7)
ds_read_b64_tr_b16 v[214:215], v9 offset:30720
v_readlane_b32 s17, v17, s22
v_mfma_f32_16x16x32_bf16 a[64:67], v[192:195], v[28:31], a[64:67]
s_waitcnt vmcnt(63)
ds_read_b64_tr_b16 v[216:217], v10 offset:28672
v_readlane_b32 s25, v18, s22
v_mfma_f32_16x16x32_bf16 a[68:71], v[192:195], v[32:35], a[68:71]
s_barrier
ds_read_b64_tr_b16 v[218:219], v10 offset:30720
buffer_load_dwordx4 a[232:235], v2, s[36:39], s54 offen offset:3072 sc1
v_add_u32 v8, s26, v4
v_mfma_f32_16x16x32_bf16 a[76:79], v[196:199], v[32:35], a[76:79]
s_mov_b32 s20, 0
ds_read_b64_tr_b16 v[220:221], v11 offset:28672
v_mad_u32_u24 v14, v12, s25, v13
v_mfma_f32_16x16x32_bf16 a[72:75], v[196:199], v[28:31], a[72:75]
s_waitcnt lgkmcnt(7)
ds_read_b64_tr_b16 v[222:223], v11 offset:30720
v_add_u32 v9, s26, v5
v_mfma_f32_16x16x32_bf16 a[80:83], v[200:203], v[28:31], a[80:83]
s_mov_b32 m0, s23
ds_read_b64_tr_b16 v[192:193], v8 offset:0
ds_read_b64_tr_b16 v[194:195], v8 offset:2048
v_add_u32 v10, s26, v6
v_mfma_f32_16x16x32_bf16 a[84:87], v[200:203], v[32:35], a[84:87]
buffer_load_dwordx4 v14, s[16:19], s20 offen sc1 lds
v_mfma_f32_16x16x32_bf16 a[92:95], v[204:207], v[32:35], a[92:95]
s_add_u32 s22, s22, 1
ds_read_b64_tr_b16 v[196:197], v9 offset:0
v_add_u32 v11, s26, v7
v_mfma_f32_16x16x32_bf16 a[88:91], v[204:207], v[28:31], a[88:91]
s_waitcnt lgkmcnt(7)
ds_read_b64_tr_b16 v[198:199], v9 offset:2048
v_mfma_f32_16x16x32_bf16 a[96:99], v[208:211], v[28:31], a[96:99]
s_lshl_b32 s21, s25, 3
ds_read_b64_tr_b16 v[200:201], v10 offset:0
v_mfma_f32_16x16x32_bf16 a[100:103], v[208:211], v[32:35], a[100:103]
s_mov_b32 s27, s24
ds_read_b64_tr_b16 v[202:203], v10 offset:2048
buffer_load_dwordx4 a[236:239], v2, s[36:39], s55 offen offset:3072 sc1
v_mfma_f32_16x16x32_bf16 a[108:111], v[212:215], v[32:35], a[108:111]
s_add_u32 s20, s20, s21
ds_read_b64_tr_b16 v[204:205], v11 offset:0
v_mfma_f32_16x16x32_bf16 a[104:107], v[212:215], v[28:31], a[104:107]
s_waitcnt lgkmcnt(7)
ds_read_b64_tr_b16 v[206:207], v11 offset:2048
v_mfma_f32_16x16x32_bf16 a[112:115], v[216:219], v[28:31], a[112:115]
s_add_u32 m0, s23, 1024
ds_read_b64_tr_b16 v[208:209], v8 offset:8192
ds_read_b64_tr_b16 v[210:211], v8 offset:10240
v_mfma_f32_16x16x32_bf16 a[116:119], v[216:219], v[32:35], a[116:119]
buffer_load_dwordx4 v14, s[16:19], s20 offen sc1 lds
v_mfma_f32_16x16x32_bf16 a[124:127], v[220:223], v[32:35], a[124:127]
s_mov_b32 s24, s26
ds_read_b64_tr_b16 v[212:213], v9 offset:8192
v_mfma_f32_16x16x32_bf16 a[120:123], v[220:223], v[28:31], a[120:123]
s_waitcnt lgkmcnt(7)
ds_read_b64_tr_b16 v[214:215], v9 offset:10240
v_mfma_f32_16x16x32_bf16 a[0:3], v[192:195], v[36:39], a[0:3]
ds_read_b64_tr_b16 v[216:217], v10 offset:8192
v_mfma_f32_16x16x32_bf16 a[4:7], v[192:195], v[48:51], a[4:7]
ds_read_b64_tr_b16 v[218:219], v10 offset:10240
v_mfma_f32_16x16x32_bf16 a[12:15], v[196:199], v[48:51], a[12:15]
s_add_u32 s20, s20, s21
ds_read_b64_tr_b16 v[220:221], v11 offset:8192
v_mfma_f32_16x16x32_bf16 a[8:11], v[196:199], v[36:39], a[8:11]
s_waitcnt lgkmcnt(7)
ds_read_b64_tr_b16 v[222:223], v11 offset:10240
v_mfma_f32_16x16x32_bf16 a[16:19], v[200:203], v[36:39], a[16:19]
s_add_u32 m0, s23, 2048
ds_read_b64_tr_b16 v[192:193], v8 offset:16384
ds_read_b64_tr_b16 v[194:195], v8 offset:18432
v_mfma_f32_16x16x32_bf16 a[20:23], v[200:203], v[48:51], a[20:23]
buffer_load_dwordx4 v14, s[16:19], s20 offen sc1 lds
v_mfma_f32_16x16x32_bf16 a[28:31], v[204:207], v[48:51], a[28:31]
ds_read_b64_tr_b16 v[196:197], v9 offset:16384
v_mfma_f32_16x16x32_bf16 a[24:27], v[204:207], v[36:39], a[24:27]
s_waitcnt lgkmcnt(7)
ds_read_b64_tr_b16 v[198:199], v9 offset:18432
v_mfma_f32_16x16x32_bf16 a[32:35], v[208:211], v[36:39], a[32:35]
ds_read_b64_tr_b16 v[200:201], v10 offset:16384
v_mfma_f32_16x16x32_bf16 a[36:39], v[208:211], v[48:51], a[36:39]
ds_read_b64_tr_b16 v[202:203], v10 offset:18432
buffer_store_dwordx4 v[128:131], v0, s[36:39], s61 offen offset:0 sc1
v_mfma_f32_16x16x32_bf16 a[44:47], v[212:215], v[48:51], a[44:47]
s_add_u32 s20, s20, s21
ds_read_b64_tr_b16 v[204:205], v11 offset:16384
v_mfma_f32_16x16x32_bf16 a[40:43], v[212:215], v[36:39], a[40:43]
s_waitcnt lgkmcnt(7)
ds_read_b64_tr_b16 v[206:207], v11 offset:18432
v_mfma_f32_16x16x32_bf16 a[48:51], v[216:219], v[36:39], a[48:51]
s_add_u32 m0, s23, 3072
ds_read_b64_tr_b16 v[208:209], v8 offset:24576
ds_read_b64_tr_b16 v[210:211], v8 offset:26624
v_mfma_f32_16x16x32_bf16 a[52:55], v[216:219], v[48:51], a[52:55]
buffer_load_dwordx4 v14, s[16:19], s20 offen sc1 lds
v_mfma_f32_16x16x32_bf16 a[60:63], v[220:223], v[48:51], a[60:63]
ds_read_b64_tr_b16 v[212:213], v9 offset:24576
v_mfma_f32_16x16x32_bf16 a[56:59], v[220:223], v[36:39], a[56:59]
s_waitcnt lgkmcnt(7)
ds_read_b64_tr_b16 v[214:215], v9 offset:26624
v_mfma_f32_16x16x32_bf16 a[64:67], v[192:195], v[36:39], a[64:67]
ds_read_b64_tr_b16 v[216:217], v10 offset:24576
v_mfma_f32_16x16x32_bf16 a[68:71], v[192:195], v[48:51], a[68:71]
ds_read_b64_tr_b16 v[218:219], v10 offset:26624
v_mfma_f32_16x16x32_bf16 a[76:79], v[196:199], v[48:51], a[76:79]
s_add_u32 s20, s20, s21
ds_read_b64_tr_b16 v[220:221], v11 offset:24576
v_mfma_f32_16x16x32_bf16 a[72:75], v[196:199], v[36:39], a[72:75]
s_waitcnt lgkmcnt(7)
ds_read_b64_tr_b16 v[222:223], v11 offset:26624
v_mfma_f32_16x16x32_bf16 a[80:83], v[200:203], v[36:39], a[80:83]
s_add_u32 m0, s23, 4096
ds_read_b64_tr_b16 v[192:193], v8 offset:4096
ds_read_b64_tr_b16 v[194:195], v8 offset:6144
v_mfma_f32_16x16x32_bf16 a[84:87], v[200:203], v[48:51], a[84:87]
buffer_load_dwordx4 v14, s[16:19], s20 offen sc1 lds
v_mfma_f32_16x16x32_bf16 a[92:95], v[204:207], v[48:51], a[92:95]
ds_read_b64_tr_b16 v[196:197], v9 offset:4096
v_mfma_f32_16x16x32_bf16 a[88:91], v[204:207], v[36:39], a[88:91]
s_waitcnt lgkmcnt(7)
ds_read_b64_tr_b16 v[198:199], v9 offset:6144
v_mfma_f32_16x16x32_bf16 a[96:99], v[208:211], v[36:39], a[96:99]
ds_read_b64_tr_b16 v[200:201], v10 offset:4096
v_mfma_f32_16x16x32_bf16 a[100:103], v[208:211], v[48:51], a[100:103]
ds_read_b64_tr_b16 v[202:203], v10 offset:6144
buffer_store_dwordx4 v[132:135], v0, s[36:39], s61 offen offset:1024 sc1
v_mfma_f32_16x16x32_bf16 a[108:111], v[212:215], v[48:51], a[108:111]
s_add_u32 s20, s20, s21
ds_read_b64_tr_b16 v[204:205], v11 offset:4096
v_mfma_f32_16x16x32_bf16 a[104:107], v[212:215], v[36:39], a[104:107]
s_waitcnt lgkmcnt(7)
ds_read_b64_tr_b16 v[206:207], v11 offset:6144
v_mfma_f32_16x16x32_bf16 a[112:115], v[216:219], v[36:39], a[112:115]
s_add_u32 m0, s23, 5120
ds_read_b64_tr_b16 v[208:209], v8 offset:12288
ds_read_b64_tr_b16 v[210:211], v8 offset:14336
v_mfma_f32_16x16x32_bf16 a[116:119], v[216:219], v[48:51], a[116:119]
buffer_load_dwordx4 v14, s[16:19], s20 offen sc1 lds
v_mfma_f32_16x16x32_bf16 a[124:127], v[220:223], v[48:51], a[124:127]
ds_read_b64_tr_b16 v[212:213], v9 offset:12288
v_mfma_f32_16x16x32_bf16 a[120:123], v[220:223], v[36:39], a[120:123]
s_waitcnt lgkmcnt(7)
ds_read_b64_tr_b16 v[214:215], v9 offset:14336
v_mfma_f32_16x16x32_bf16 a[0:3], v[192:195], v[52:55], a[0:3]
ds_read_b64_tr_b16 v[216:217], v10 offset:12288
v_mfma_f32_16x16x32_bf16 a[4:7], v[192:195], v[56:59], a[4:7]
ds_read_b64_tr_b16 v[218:219], v10 offset:14336
v_mfma_f32_16x16x32_bf16 a[12:15], v[196:199], v[56:59], a[12:15]
s_add_u32 s20, s20, s21
ds_read_b64_tr_b16 v[220:221], v11 offset:12288
v_mfma_f32_16x16x32_bf16 a[8:11], v[196:199], v[52:55], a[8:11]
s_waitcnt lgkmcnt(7)
ds_read_b64_tr_b16 v[222:223], v11 offset:14336
v_mfma_f32_16x16x32_bf16 a[16:19], v[200:203], v[52:55], a[16:19]
s_add_u32 m0, s23, 6144
ds_read_b64_tr_b16 v[192:193], v8 offset:20480
ds_read_b64_tr_b16 v[194:195], v8 offset:22528
v_mfma_f32_16x16x32_bf16 a[20:23], v[200:203], v[56:59], a[20:23]
buffer_load_dwordx4 v14, s[16:19], s20 offen sc1 lds
v_mfma_f32_16x16x32_bf16 a[28:31], v[204:207], v[56:59], a[28:31]
s_add_u32 s26, s24, 32768
ds_read_b64_tr_b16 v[196:197], v9 offset:20480
v_mfma_f32_16x16x32_bf16 a[24:27], v[204:207], v[52:55], a[24:27]
s_waitcnt lgkmcnt(7)
ds_read_b64_tr_b16 v[198:199], v9 offset:22528
v_mfma_f32_16x16x32_bf16 a[32:35], v[208:211], v[52:55], a[32:35]
s_sub_u32 s35, s26, 163840
ds_read_b64_tr_b16 v[200:201], v10 offset:20480
v_mfma_f32_16x16x32_bf16 a[36:39], v[208:211], v[56:59], a[36:39]
s_cselect_b32 s26, s26, s35
ds_read_b64_tr_b16 v[202:203], v10 offset:22528
buffer_store_dwordx4 v[136:139], v0, s[36:39], s61 offen offset:2048 sc1
v_mfma_f32_16x16x32_bf16 a[44:47], v[212:215], v[56:59], a[44:47]
s_add_u32 s20, s20, s21
ds_read_b64_tr_b16 v[204:205], v11 offset:20480
v_mfma_f32_16x16x32_bf16 a[40:43], v[212:215], v[52:55], a[40:43]
s_waitcnt lgkmcnt(7)
ds_read_b64_tr_b16 v[206:207], v11 offset:22528
v_mfma_f32_16x16x32_bf16 a[48:51], v[216:219], v[52:55], a[48:51]
s_add_u32 m0, s23, 7168
ds_read_b64_tr_b16 v[208:209], v8 offset:28672
ds_read_b64_tr_b16 v[210:211], v8 offset:30720
v_mfma_f32_16x16x32_bf16 a[52:55], v[216:219], v[56:59], a[52:55]
buffer_load_dwordx4 v14, s[16:19], s20 offen sc1 lds
v_mfma_f32_16x16x32_bf16 a[60:63], v[220:223], v[56:59], a[60:63]
s_add_u32 s23, s27, s33
ds_read_b64_tr_b16 v[212:213], v9 offset:28672
v_readlane_b32 s16, v16, s22
v_mfma_f32_16x16x32_bf16 a[56:59], v[220:223], v[52:55], a[56:59]
s_waitcnt lgkmcnt(7)
ds_read_b64_tr_b16 v[214:215], v9 offset:30720
v_readlane_b32 s17, v17, s22
v_mfma_f32_16x16x32_bf16 a[64:67], v[192:195], v[52:55], a[64:67]
s_waitcnt vmcnt(33)
ds_read_b64_tr_b16 v[216:217], v10 offset:28672
v_readlane_b32 s25, v18, s22
v_mfma_f32_16x16x32_bf16 a[68:71], v[192:195], v[56:59], a[68:71]
s_barrier
ds_read_b64_tr_b16 v[218:219], v10 offset:30720
v_add_u32 v8, s26, v4
v_mfma_f32_16x16x32_bf16 a[76:79], v[196:199], v[56:59], a[76:79]
s_mov_b32 s20, 0
ds_read_b64_tr_b16 v[220:221], v11 offset:28672
v_mad_u32_u24 v14, v12, s25, v13
v_mfma_f32_16x16x32_bf16 a[72:75], v[196:199], v[52:55], a[72:75]
s_waitcnt lgkmcnt(7)
ds_read_b64_tr_b16 v[222:223], v11 offset:30720
v_add_u32 v9, s26, v5
v_mfma_f32_16x16x32_bf16 a[80:83], v[200:203], v[52:55], a[80:83]
s_mov_b32 m0, s23
ds_read_b64_tr_b16 v[192:193], v8 offset:0
ds_read_b64_tr_b16 v[194:195], v8 offset:2048
v_add_u32 v10, s26, v6
v_mfma_f32_16x16x32_bf16 a[84:87], v[200:203], v[56:59], a[84:87]
buffer_load_dwordx4 v14, s[16:19], s20 offen sc1 lds
v_mfma_f32_16x16x32_bf16 a[92:95], v[204:207], v[56:59], a[92:95]
s_add_u32 s22, s22, 1
ds_read_b64_tr_b16 v[196:197], v9 offset:0
v_add_u32 v11, s26, v7
v_mfma_f32_16x16x32_bf16 a[88:91], v[204:207], v[52:55], a[88:91]
s_waitcnt lgkmcnt(7)
ds_read_b64_tr_b16 v[198:199], v9 offset:2048
v_mfma_f32_16x16x32_bf16 a[96:99], v[208:211], v[52:55], a[96:99]
s_lshl_b32 s21, s25, 3
ds_read_b64_tr_b16 v[200:201], v10 offset:0
v_mfma_f32_16x16x32_bf16 a[100:103], v[208:211], v[56:59], a[100:103]
s_mov_b32 s27, s24
ds_read_b64_tr_b16 v[202:203], v10 offset:2048
v_mfma_f32_16x16x32_bf16 a[108:111], v[212:215], v[56:59], a[108:111]
s_add_u32 s20, s20, s21
ds_read_b64_tr_b16 v[204:205], v11 offset:0
v_mfma_f32_16x16x32_bf16 a[104:107], v[212:215], v[52:55], a[104:107]
s_waitcnt lgkmcnt(7)
ds_read_b64_tr_b16 v[206:207], v11 offset:2048
v_mfma_f32_16x16x32_bf16 a[112:115], v[216:219], v[52:55], a[112:115]
s_add_u32 m0, s23, 1024
ds_read_b64_tr_b16 v[208:209], v8 offset:8192
ds_read_b64_tr_b16 v[210:211], v8 offset:10240
v_mfma_f32_16x16x32_bf16 a[116:119], v[216:219], v[56:59], a[116:119]
buffer_load_dwordx4 v14, s[16:19], s20 offen sc1 lds
v_mfma_f32_16x16x32_bf16 a[124:127], v[220:223], v[56:59], a[124:127]
s_mov_b32 s24, s26
ds_read_b64_tr_b16 v[212:213], v9 offset:8192
v_mfma_f32_16x16x32_bf16 a[120:123], v[220:223], v[52:55], a[120:123]
s_waitcnt lgkmcnt(7)
ds_read_b64_tr_b16 v[214:215], v9 offset:10240
v_mfma_f32_16x16x32_bf16 a[0:3], v[192:195], a[160:163], a[0:3]
ds_read_b64_tr_b16 v[216:217], v10 offset:8192
v_accvgpr_write_b32 a128, v20
v_mfma_f32_16x16x32_bf16 a[4:7], v[192:195], a[164:167], a[4:7]
ds_read_b64_tr_b16 v[218:219], v10 offset:10240
v_mfma_f32_16x16x32_bf16 a[12:15], v[196:199], a[164:167], a[12:15]
s_add_u32 s20, s20, s21
ds_read_b64_tr_b16 v[220:221], v11 offset:8192
v_accvgpr_write_b32 a129, v21
v_mfma_f32_16x16x32_bf16 a[8:11], v[196:199], a[160:163], a[8:11]
s_waitcnt lgkmcnt(7)
ds_read_b64_tr_b16 v[222:223], v11 offset:10240
v_accvgpr_write_b32 a130, v22
v_mfma_f32_16x16x32_bf16 a[16:19], v[200:203], a[160:163], a[16:19]
s_add_u32 m0, s23, 2048
ds_read_b64_tr_b16 v[192:193], v8 offset:16384
ds_read_b64_tr_b16 v[194:195], v8 offset:18432
v_mfma_f32_16x16x32_bf16 a[20:23], v[200:203], a[164:167], a[20:23]
buffer_load_dwordx4 v14, s[16:19], s20 offen sc1 lds
v_mfma_f32_16x16x32_bf16 a[28:31], v[204:207], a[164:167], a[28:31]
ds_read_b64_tr_b16 v[196:197], v9 offset:16384
v_accvgpr_write_b32 a131, v23
v_mfma_f32_16x16x32_bf16 a[24:27], v[204:207], a[160:163], a[24:27]
s_waitcnt lgkmcnt(7)
ds_read_b64_tr_b16 v[198:199], v9 offset:18432
v_accvgpr_write_b32 a132, v24
v_mfma_f32_16x16x32_bf16 a[32:35], v[208:211], a[160:163], a[32:35]
ds_read_b64_tr_b16 v[200:201], v10 offset:16384
v_accvgpr_write_b32 a133, v25
v_mfma_f32_16x16x32_bf16 a[36:39], v[208:211], a[164:167], a[36:39]
ds_read_b64_tr_b16 v[202:203], v10 offset:18432
buffer_store_dwordx4 v[140:143], v0, s[36:39], s61 offen offset:3072 sc1
v_mfma_f32_16x16x32_bf16 a[44:47], v[212:215], a[164:167], a[44:47]
s_add_u32 s20, s20, s21
ds_read_b64_tr_b16 v[204:205], v11 offset:16384
v_accvgpr_write_b32 a134, v26
v_mfma_f32_16x16x32_bf16 a[40:43], v[212:215], a[160:163], a[40:43]
s_waitcnt lgkmcnt(7)
ds_read_b64_tr_b16 v[206:207], v11 offset:18432
v_accvgpr_write_b32 a135, v27
v_mfma_f32_16x16x32_bf16 a[48:51], v[216:219], a[160:163], a[48:51]
s_add_u32 m0, s23, 3072
ds_read_b64_tr_b16 v[208:209], v8 offset:24576
ds_read_b64_tr_b16 v[210:211], v8 offset:26624
v_mfma_f32_16x16x32_bf16 a[52:55], v[216:219], a[164:167], a[52:55]
buffer_load_dwordx4 v14, s[16:19], s20 offen sc1 lds
v_mfma_f32_16x16x32_bf16 a[60:63], v[220:223], a[164:167], a[60:63]
ds_read_b64_tr_b16 v[212:213], v9 offset:24576
v_accvgpr_write_b32 a136, v28
v_mfma_f32_16x16x32_bf16 a[56:59], v[220:223], a[160:163], a[56:59]
s_waitcnt lgkmcnt(7)
ds_read_b64_tr_b16 v[214:215], v9 offset:26624
v_accvgpr_write_b32 a137, v29
v_mfma_f32_16x16x32_bf16 a[64:67], v[192:195], a[160:163], a[64:67]
ds_read_b64_tr_b16 v[216:217], v10 offset:24576
v_accvgpr_write_b32 a138, v30
v_mfma_f32_16x16x32_bf16 a[68:71], v[192:195], a[164:167], a[68:71]
ds_read_b64_tr_b16 v[218:219], v10 offset:26624
v_mfma_f32_16x16x32_bf16 a[76:79], v[196:199], a[164:167], a[76:79]
s_add_u32 s20, s20, s21
ds_read_b64_tr_b16 v[220:221], v11 offset:24576
v_accvgpr_write_b32 a139, v31
v_mfma_f32_16x16x32_bf16 a[72:75], v[196:199], a[160:163], a[72:75]
s_waitcnt lgkmcnt(7)
ds_read_b64_tr_b16 v[222:223], v11 offset:26624
v_accvgpr_write_b32 a140, v32
v_mfma_f32_16x16x32_bf16 a[80:83], v[200:203], a[160:163], a[80:83]
s_add_u32 m0, s23, 4096
ds_read_b64_tr_b16 v[192:193], v8 offset:4096
ds_read_b64_tr_b16 v[194:195], v8 offset:6144
v_mfma_f32_16x16x32_bf16 a[84:87], v[200:203], a[164:167], a[84:87]
buffer_load_dwordx4 v14, s[16:19], s20 offen sc1 lds
v_mfma_f32_16x16x32_bf16 a[92:95], v[204:207], a[164:167], a[92:95]
ds_read_b64_tr_b16 v[196:197], v9 offset:4096
v_accvgpr_write_b32 a141, v33
v_mfma_f32_16x16x32_bf16 a[88:91], v[204:207], a[160:163], a[88:91]
s_waitcnt lgkmcnt(7)
ds_read_b64_tr_b16 v[198:199], v9 offset:6144
v_accvgpr_write_b32 a142, v34
v_mfma_f32_16x16x32_bf16 a[96:99], v[208:211], a[160:163], a[96:99]
ds_read_b64_tr_b16 v[200:201], v10 offset:4096
v_accvgpr_write_b32 a143, v35
v_mfma_f32_16x16x32_bf16 a[100:103], v[208:211], a[164:167], a[100:103]
ds_read_b64_tr_b16 v[202:203], v10 offset:6144
buffer_store_dwordx4 v[144:147], v0, s[36:39], s62 offen offset:0 sc1
v_mfma_f32_16x16x32_bf16 a[108:111], v[212:215], a[164:167], a[108:111]
s_add_u32 s20, s20, s21
ds_read_b64_tr_b16 v[204:205], v11 offset:4096
v_accvgpr_write_b32 a144, v36
v_mfma_f32_16x16x32_bf16 a[104:107], v[212:215], a[160:163], a[104:107]
s_waitcnt lgkmcnt(7)
ds_read_b64_tr_b16 v[206:207], v11 offset:6144
v_accvgpr_write_b32 a145, v37
v_mfma_f32_16x16x32_bf16 a[112:115], v[216:219], a[160:163], a[112:115]
s_add_u32 m0, s23, 5120
ds_read_b64_tr_b16 v[208:209], v8 offset:12288
ds_read_b64_tr_b16 v[210:211], v8 offset:14336
v_mfma_f32_16x16x32_bf16 a[116:119], v[216:219], a[164:167], a[116:119]
buffer_load_dwordx4 v14, s[16:19], s20 offen sc1 lds
v_mfma_f32_16x16x32_bf16 a[124:127], v[220:223], a[164:167], a[124:127]
ds_read_b64_tr_b16 v[212:213], v9 offset:12288
v_accvgpr_write_b32 a146, v38
v_mfma_f32_16x16x32_bf16 a[120:123], v[220:223], a[160:163], a[120:123]
s_waitcnt lgkmcnt(7)
ds_read_b64_tr_b16 v[214:215], v9 offset:14336
v_accvgpr_write_b32 a147, v39
v_mfma_f32_16x16x32_bf16 a[0:3], v[192:195], a[168:171], a[0:3]
ds_read_b64_tr_b16 v[216:217], v10 offset:12288
v_accvgpr_write_b32 a148, v48
v_mfma_f32_16x16x32_bf16 a[4:7], v[192:195], a[172:175], a[4:7]
ds_read_b64_tr_b16 v[218:219], v10 offset:14336
v_mfma_f32_16x16x32_bf16 a[12:15], v[196:199], a[172:175], a[12:15]
s_add_u32 s20, s20, s21
ds_read_b64_tr_b16 v[220:221], v11 offset:12288
v_accvgpr_write_b32 a149, v49
v_mfma_f32_16x16x32_bf16 a[8:11], v[196:199], a[168:171], a[8:11]
s_waitcnt lgkmcnt(7)
ds_read_b64_tr_b16 v[222:223], v11 offset:14336
v_accvgpr_write_b32 a150, v50
v_mfma_f32_16x16x32_bf16 a[16:19], v[200:203], a[168:171], a[16:19]
s_add_u32 m0, s23, 6144
ds_read_b64_tr_b16 v[192:193], v8 offset:20480
ds_read_b64_tr_b16 v[194:195], v8 offset:22528
v_mfma_f32_16x16x32_bf16 a[20:23], v[200:203], a[172:175], a[20:23]
buffer_load_dwordx4 v14, s[16:19], s20 offen sc1 lds
v_mfma_f32_16x16x32_bf16 a[28:31], v[204:207], a[172:175], a[28:31]
s_add_u32 s26, s24, 32768
ds_read_b64_tr_b16 v[196:197], v9 offset:20480
v_accvgpr_write_b32 a151, v51
v_mfma_f32_16x16x32_bf16 a[24:27], v[204:207], a[168:171], a[24:27]
s_waitcnt lgkmcnt(7)
ds_read_b64_tr_b16 v[198:199], v9 offset:22528
v_accvgpr_write_b32 a152, v52
v_mfma_f32_16x16x32_bf16 a[32:35], v[208:211], a[168:171], a[32:35]
s_sub_u32 s35, s26, 163840
ds_read_b64_tr_b16 v[200:201], v10 offset:20480
v_accvgpr_write_b32 a153, v53
v_mfma_f32_16x16x32_bf16 a[36:39], v[208:211], a[172:175], a[36:39]
s_cselect_b32 s26, s26, s35
ds_read_b64_tr_b16 v[202:203], v10 offset:22528
buffer_store_dwordx4 v[148:151], v0, s[36:39], s62 offen offset:1024 sc1
v_mfma_f32_16x16x32_bf16 a[44:47], v[212:215], a[172:175], a[44:47]
s_add_u32 s20, s20, s21
ds_read_b64_tr_b16 v[204:205], v11 offset:20480
v_accvgpr_write_b32 a154, v54
v_mfma_f32_16x16x32_bf16 a[40:43], v[212:215], a[168:171], a[40:43]
s_waitcnt lgkmcnt(7)
ds_read_b64_tr_b16 v[206:207], v11 offset:22528
v_accvgpr_write_b32 a155, v55
v_mfma_f32_16x16x32_bf16 a[48:51], v[216:219], a[168:171], a[48:51]
s_add_u32 m0, s23, 7168
ds_read_b64_tr_b16 v[208:209], v8 offset:28672
ds_read_b64_tr_b16 v[210:211], v8 offset:30720
v_mfma_f32_16x16x32_bf16 a[52:55], v[216:219], a[172:175], a[52:55]
buffer_load_dwordx4 v14, s[16:19], s20 offen sc1 lds
v_mfma_f32_16x16x32_bf16 a[60:63], v[220:223], a[172:175], a[60:63]
s_add_u32 s23, s27, s33
ds_read_b64_tr_b16 v[212:213], v9 offset:28672
v_readlane_b32 s16, v16, s22
v_mfma_f32_16x16x32_bf16 a[56:59], v[220:223], a[168:171], a[56:59]
s_waitcnt lgkmcnt(7)
ds_read_b64_tr_b16 v[214:215], v9 offset:30720
v_readlane_b32 s17, v17, s22
v_mfma_f32_16x16x32_bf16 a[64:67], v[192:195], a[168:171], a[64:67]
s_waitcnt vmcnt(24)
ds_read_b64_tr_b16 v[216:217], v10 offset:28672
v_readlane_b32 s25, v18, s22
v_mfma_f32_16x16x32_bf16 a[68:71], v[192:195], a[172:175], a[68:71]
s_barrier
ds_read_b64_tr_b16 v[218:219], v10 offset:30720
v_add_u32 v8, s26, v4
v_mfma_f32_16x16x32_bf16 a[76:79], v[196:199], a[172:175], a[76:79]
s_mov_b32 s20, 0
ds_read_b64_tr_b16 v[220:221], v11 offset:28672
v_mad_u32_u24 v14, v12, s25, v13
v_mfma_f32_16x16x32_bf16 a[72:75], v[196:199], a[168:171], a[72:75]
s_waitcnt lgkmcnt(7)
ds_read_b64_tr_b16 v[222:223], v11 offset:30720
v_add_u32 v9, s26, v5
v_mfma_f32_16x16x32_bf16 a[80:83], v[200:203], a[168:171], a[80:83]
s_mov_b32 m0, s23
ds_read_b64_tr_b16 v[192:193], v8 offset:0
ds_read_b64_tr_b16 v[194:195], v8 offset:2048
v_add_u32 v10, s26, v6
v_mfma_f32_16x16x32_bf16 a[84:87], v[200:203], a[172:175], a[84:87]
buffer_load_dwordx4 v14, s[16:19], s20 offen sc1 lds
v_mfma_f32_16x16x32_bf16 a[92:95], v[204:207], a[172:175], a[92:95]
s_add_u32 s22, s22, 1
ds_read_b64_tr_b16 v[196:197], v9 offset:0
v_add_u32 v11, s26, v7
v_mfma_f32_16x16x32_bf16 a[88:91], v[204:207], a[168:171], a[88:91]
s_waitcnt lgkmcnt(7)
ds_read_b64_tr_b16 v[198:199], v9 offset:2048
v_accvgpr_write_b32 a156, v56
v_mfma_f32_16x16x32_bf16 a[96:99], v[208:211], a[168:171], a[96:99]
s_lshl_b32 s21, s25, 3
ds_read_b64_tr_b16 v[200:201], v10 offset:0
v_accvgpr_write_b32 a157, v57
v_mfma_f32_16x16x32_bf16 a[100:103], v[208:211], a[172:175], a[100:103]
s_mov_b32 s27, s24
ds_read_b64_tr_b16 v[202:203], v10 offset:2048
v_mfma_f32_16x16x32_bf16 a[108:111], v[212:215], a[172:175], a[108:111]
s_add_u32 s20, s20, s21
ds_read_b64_tr_b16 v[204:205], v11 offset:0
v_accvgpr_write_b32 a158, v58
v_mfma_f32_16x16x32_bf16 a[104:107], v[212:215], a[168:171], a[104:107]
s_waitcnt lgkmcnt(7)
ds_read_b64_tr_b16 v[206:207], v11 offset:2048
v_accvgpr_write_b32 a159, v59
v_mfma_f32_16x16x32_bf16 a[112:115], v[216:219], a[168:171], a[112:115]
s_add_u32 m0, s23, 1024
ds_read_b64_tr_b16 v[208:209], v8 offset:8192
ds_read_b64_tr_b16 v[210:211], v8 offset:10240
v_mfma_f32_16x16x32_bf16 a[116:119], v[216:219], a[172:175], a[116:119]
buffer_load_dwordx4 v14, s[16:19], s20 offen sc1 lds
v_mfma_f32_16x16x32_bf16 a[124:127], v[220:223], a[172:175], a[124:127]
s_mov_b32 s24, s26
ds_read_b64_tr_b16 v[212:213], v9 offset:8192
v_mfma_f32_16x16x32_bf16 a[120:123], v[220:223], a[168:171], a[120:123]
s_waitcnt lgkmcnt(7)
ds_read_b64_tr_b16 v[214:215], v9 offset:10240
v_mfma_f32_16x16x32_bf16 a[0:3], v[192:195], a[176:179], a[0:3]
ds_read_b64_tr_b16 v[216:217], v10 offset:8192
v_mfma_f32_16x16x32_bf16 a[4:7], v[192:195], a[180:183], a[4:7]
ds_read_b64_tr_b16 v[218:219], v10 offset:10240
v_mfma_f32_16x16x32_bf16 a[12:15], v[196:199], a[180:183], a[12:15]
s_add_u32 s20, s20, s21
ds_read_b64_tr_b16 v[220:221], v11 offset:8192
v_mfma_f32_16x16x32_bf16 a[8:11], v[196:199], a[176:179], a[8:11]
s_waitcnt lgkmcnt(7)
ds_read_b64_tr_b16 v[222:223], v11 offset:10240
v_mfma_f32_16x16x32_bf16 a[16:19], v[200:203], a[176:179], a[16:19]
s_add_u32 m0, s23, 2048
ds_read_b64_tr_b16 v[192:193], v8 offset:16384
ds_read_b64_tr_b16 v[194:195], v8 offset:18432
v_mfma_f32_16x16x32_bf16 a[20:23], v[200:203], a[180:183], a[20:23]
buffer_load_dwordx4 v14, s[16:19], s20 offen sc1 lds
v_mfma_f32_16x16x32_bf16 a[28:31], v[204:207], a[180:183], a[28:31]
ds_read_b64_tr_b16 v[196:197], v9 offset:16384
v_mfma_f32_16x16x32_bf16 a[24:27], v[204:207], a[176:179], a[24:27]
s_waitcnt lgkmcnt(7)
ds_read_b64_tr_b16 v[198:199], v9 offset:18432
v_mfma_f32_16x16x32_bf16 a[32:35], v[208:211], a[176:179], a[32:35]
ds_read_b64_tr_b16 v[200:201], v10 offset:16384
v_mfma_f32_16x16x32_bf16 a[36:39], v[208:211], a[180:183], a[36:39]
ds_read_b64_tr_b16 v[202:203], v10 offset:18432
buffer_store_dwordx4 v[152:155], v0, s[36:39], s62 offen offset:2048 sc1
v_mfma_f32_16x16x32_bf16 a[44:47], v[212:215], a[180:183], a[44:47]
s_add_u32 s20, s20, s21
ds_read_b64_tr_b16 v[204:205], v11 offset:16384
v_mfma_f32_16x16x32_bf16 a[40:43], v[212:215], a[176:179], a[40:43]
s_waitcnt lgkmcnt(7)
ds_read_b64_tr_b16 v[206:207], v11 offset:18432
v_mfma_f32_16x16x32_bf16 a[48:51], v[216:219], a[176:179], a[48:51]
s_add_u32 m0, s23, 3072
ds_read_b64_tr_b16 v[208:209], v8 offset:24576
ds_read_b64_tr_b16 v[210:211], v8 offset:26624
v_mfma_f32_16x16x32_bf16 a[52:55], v[216:219], a[180:183], a[52:55]
buffer_load_dwordx4 v14, s[16:19], s20 offen sc1 lds
v_mfma_f32_16x16x32_bf16 a[60:63], v[220:223], a[180:183], a[60:63]
ds_read_b64_tr_b16 v[212:213], v9 offset:24576
v_mfma_f32_16x16x32_bf16 a[56:59], v[220:223], a[176:179], a[56:59]
s_waitcnt lgkmcnt(7)
ds_read_b64_tr_b16 v[214:215], v9 offset:26624
v_mfma_f32_16x16x32_bf16 a[64:67], v[192:195], a[176:179], a[64:67]
ds_read_b64_tr_b16 v[216:217], v10 offset:24576
v_mfma_f32_16x16x32_bf16 a[68:71], v[192:195], a[180:183], a[68:71]
ds_read_b64_tr_b16 v[218:219], v10 offset:26624
v_mfma_f32_16x16x32_bf16 a[76:79], v[196:199], a[180:183], a[76:79]
s_add_u32 s20, s20, s21
ds_read_b64_tr_b16 v[220:221], v11 offset:24576
v_mfma_f32_16x16x32_bf16 a[72:75], v[196:199], a[176:179], a[72:75]
s_waitcnt lgkmcnt(7)
ds_read_b64_tr_b16 v[222:223], v11 offset:26624
v_mfma_f32_16x16x32_bf16 a[80:83], v[200:203], a[176:179], a[80:83]
s_add_u32 m0, s23, 4096
ds_read_b64_tr_b16 v[192:193], v8 offset:4096
ds_read_b64_tr_b16 v[194:195], v8 offset:6144
v_mfma_f32_16x16x32_bf16 a[84:87], v[200:203], a[180:183], a[84:87]
buffer_load_dwordx4 v14, s[16:19], s20 offen sc1 lds
v_mfma_f32_16x16x32_bf16 a[92:95], v[204:207], a[180:183], a[92:95]
ds_read_b64_tr_b16 v[196:197], v9 offset:4096
v_mfma_f32_16x16x32_bf16 a[88:91], v[204:207], a[176:179], a[88:91]
s_waitcnt lgkmcnt(7)
ds_read_b64_tr_b16 v[198:199], v9 offset:6144
v_mfma_f32_16x16x32_bf16 a[96:99], v[208:211], a[176:179], a[96:99]
ds_read_b64_tr_b16 v[200:201], v10 offset:4096
v_mfma_f32_16x16x32_bf16 a[100:103], v[208:211], a[180:183], a[100:103]
ds_read_b64_tr_b16 v[202:203], v10 offset:6144
buffer_store_dwordx4 v[156:159], v0, s[36:39], s62 offen offset:3072 sc1
v_mfma_f32_16x16x32_bf16 a[108:111], v[212:215], a[180:183], a[108:111]
s_add_u32 s20, s20, s21
ds_read_b64_tr_b16 v[204:205], v11 offset:4096
v_mfma_f32_16x16x32_bf16 a[104:107], v[212:215], a[176:179], a[104:107]
s_waitcnt lgkmcnt(7)
ds_read_b64_tr_b16 v[206:207], v11 offset:6144
v_mfma_f32_16x16x32_bf16 a[112:115], v[216:219], a[176:179], a[112:115]
s_add_u32 m0, s23, 5120
ds_read_b64_tr_b16 v[208:209], v8 offset:12288
ds_read_b64_tr_b16 v[210:211], v8 offset:14336
v_mfma_f32_16x16x32_bf16 a[116:119], v[216:219], a[180:183], a[116:119]
buffer_load_dwordx4 v14, s[16:19], s20 offen sc1 lds
v_mfma_f32_16x16x32_bf16 a[124:127], v[220:223], a[180:183], a[124:127]
ds_read_b64_tr_b16 v[212:213], v9 offset:12288
v_mfma_f32_16x16x32_bf16 a[120:123], v[220:223], a[176:179], a[120:123]
s_waitcnt lgkmcnt(7)
ds_read_b64_tr_b16 v[214:215], v9 offset:14336
v_mfma_f32_16x16x32_bf16 a[0:3], v[192:195], a[184:187], a[0:3]
ds_read_b64_tr_b16 v[216:217], v10 offset:12288
v_mfma_f32_16x16x32_bf16 a[4:7], v[192:195], a[188:191], a[4:7]
ds_read_b64_tr_b16 v[218:219], v10 offset:14336
v_mfma_f32_16x16x32_bf16 a[12:15], v[196:199], a[188:191], a[12:15]
s_add_u32 s20, s20, s21
ds_read_b64_tr_b16 v[220:221], v11 offset:12288
v_mfma_f32_16x16x32_bf16 a[8:11], v[196:199], a[184:187], a[8:11]
s_waitcnt lgkmcnt(7)
ds_read_b64_tr_b16 v[222:223], v11 offset:14336
v_mfma_f32_16x16x32_bf16 a[16:19], v[200:203], a[184:187], a[16:19]
s_add_u32 m0, s23, 6144
ds_read_b64_tr_b16 v[192:193], v8 offset:20480
ds_read_b64_tr_b16 v[194:195], v8 offset:22528
v_mfma_f32_16x16x32_bf16 a[20:23], v[200:203], a[188:191], a[20:23]
buffer_load_dwordx4 v14, s[16:19], s20 offen sc1 lds
v_mfma_f32_16x16x32_bf16 a[28:31], v[204:207], a[188:191], a[28:31]
s_add_u32 s26, s24, 32768
ds_read_b64_tr_b16 v[196:197], v9 offset:20480
v_mfma_f32_16x16x32_bf16 a[24:27], v[204:207], a[184:187], a[24:27]
s_waitcnt lgkmcnt(7)
ds_read_b64_tr_b16 v[198:199], v9 offset:22528
v_mfma_f32_16x16x32_bf16 a[32:35], v[208:211], a[184:187], a[32:35]
s_sub_u32 s35, s26, 163840
ds_read_b64_tr_b16 v[200:201], v10 offset:20480
v_mfma_f32_16x16x32_bf16 a[36:39], v[208:211], a[188:191], a[36:39]
s_cselect_b32 s26, s26, s35
ds_read_b64_tr_b16 v[202:203], v10 offset:22528
buffer_store_dwordx4 v[160:163], v0, s[36:39], s63 offen offset:0 sc1
v_mfma_f32_16x16x32_bf16 a[44:47], v[212:215], a[188:191], a[44:47]
s_add_u32 s20, s20, s21
ds_read_b64_tr_b16 v[204:205], v11 offset:20480
v_mfma_f32_16x16x32_bf16 a[40:43], v[212:215], a[184:187], a[40:43]
s_waitcnt lgkmcnt(7)
ds_read_b64_tr_b16 v[206:207], v11 offset:22528
v_mfma_f32_16x16x32_bf16 a[48:51], v[216:219], a[184:187], a[48:51]
s_add_u32 m0, s23, 7168
ds_read_b64_tr_b16 v[208:209], v8 offset:28672
ds_read_b64_tr_b16 v[210:211], v8 offset:30720
v_mfma_f32_16x16x32_bf16 a[52:55], v[216:219], a[188:191], a[52:55]
buffer_load_dwordx4 v14, s[16:19], s20 offen sc1 lds
v_mfma_f32_16x16x32_bf16 a[60:63], v[220:223], a[188:191], a[60:63]
s_add_u32 s23, s27, s33
ds_read_b64_tr_b16 v[212:213], v9 offset:28672
v_readlane_b32 s16, v16, s22
v_mfma_f32_16x16x32_bf16 a[56:59], v[220:223], a[184:187], a[56:59]
s_waitcnt lgkmcnt(7)
ds_read_b64_tr_b16 v[214:215], v9 offset:30720
v_readlane_b32 s17, v17, s22
v_mfma_f32_16x16x32_bf16 a[64:67], v[192:195], a[184:187], a[64:67]
s_waitcnt vmcnt(22)
ds_read_b64_tr_b16 v[216:217], v10 offset:28672
v_readlane_b32 s25, v18, s22
v_mfma_f32_16x16x32_bf16 a[68:71], v[192:195], a[188:191], a[68:71]
s_barrier
ds_read_b64_tr_b16 v[218:219], v10 offset:30720
v_add_u32 v8, s26, v4
v_mfma_f32_16x16x32_bf16 a[76:79], v[196:199], a[188:191], a[76:79]
s_mov_b32 s20, 0
ds_read_b64_tr_b16 v[220:221], v11 offset:28672
v_mad_u32_u24 v14, v12, s25, v13
v_mfma_f32_16x16x32_bf16 a[72:75], v[196:199], a[184:187], a[72:75]
s_waitcnt lgkmcnt(7)
ds_read_b64_tr_b16 v[222:223], v11 offset:30720
v_add_u32 v9, s26, v5
v_mfma_f32_16x16x32_bf16 a[80:83], v[200:203], a[184:187], a[80:83]
s_mov_b32 m0, s23
ds_read_b64_tr_b16 v[192:193], v8 offset:0
ds_read_b64_tr_b16 v[194:195], v8 offset:2048
v_add_u32 v10, s26, v6
v_mfma_f32_16x16x32_bf16 a[84:87], v[200:203], a[188:191], a[84:87]
buffer_load_dwordx4 v14, s[16:19], s20 offen sc1 lds
v_mfma_f32_16x16x32_bf16 a[92:95], v[204:207], a[188:191], a[92:95]
s_add_u32 s22, s22, 1
ds_read_b64_tr_b16 v[196:197], v9 offset:0
v_add_u32 v11, s26, v7
v_mfma_f32_16x16x32_bf16 a[88:91], v[204:207], a[184:187], a[88:91]
s_waitcnt lgkmcnt(7)
ds_read_b64_tr_b16 v[198:199], v9 offset:2048
v_mfma_f32_16x16x32_bf16 a[96:99], v[208:211], a[184:187], a[96:99]
s_lshl_b32 s21, s25, 3
ds_read_b64_tr_b16 v[200:201], v10 offset:0
v_mfma_f32_16x16x32_bf16 a[100:103], v[208:211], a[188:191], a[100:103]
s_mov_b32 s27, s24
ds_read_b64_tr_b16 v[202:203], v10 offset:2048
v_mfma_f32_16x16x32_bf16 a[108:111], v[212:215], a[188:191], a[108:111]
s_add_u32 s20, s20, s21
ds_read_b64_tr_b16 v[204:205], v11 offset:0
v_mfma_f32_16x16x32_bf16 a[104:107], v[212:215], a[184:187], a[104:107]
s_waitcnt lgkmcnt(7)
ds_read_b64_tr_b16 v[206:207], v11 offset:2048
v_mfma_f32_16x16x32_bf16 a[112:115], v[216:219], a[184:187], a[112:115]
s_add_u32 m0, s23, 1024
ds_read_b64_tr_b16 v[208:209], v8 offset:8192
ds_read_b64_tr_b16 v[210:211], v8 offset:10240
v_mfma_f32_16x16x32_bf16 a[116:119], v[216:219], a[188:191], a[116:119]
buffer_load_dwordx4 v14, s[16:19], s20 offen sc1 lds
v_mfma_f32_16x16x32_bf16 a[124:127], v[220:223], a[188:191], a[124:127]
s_mov_b32 s24, s26
ds_read_b64_tr_b16 v[212:213], v9 offset:8192
v_mfma_f32_16x16x32_bf16 a[120:123], v[220:223], a[184:187], a[120:123]
s_waitcnt lgkmcnt(7)
ds_read_b64_tr_b16 v[214:215], v9 offset:10240
v_mfma_f32_16x16x32_bf16 a[0:3], v[192:195], a[192:195], a[0:3]
ds_read_b64_tr_b16 v[216:217], v10 offset:8192
v_mfma_f32_16x16x32_bf16 a[4:7], v[192:195], a[196:199], a[4:7]
ds_read_b64_tr_b16 v[218:219], v10 offset:10240
v_mfma_f32_16x16x32_bf16 a[12:15], v[196:199], a[196:199], a[12:15]
s_add_u32 s20, s20, s21
ds_read_b64_tr_b16 v[220:221], v11 offset:8192
v_mfma_f32_16x16x32_bf16 a[8:11], v[196:199], a[192:195], a[8:11]
s_waitcnt lgkmcnt(7)
ds_read_b64_tr_b16 v[222:223], v11 offset:10240
v_mfma_f32_16x16x32_bf16 a[16:19], v[200:203], a[192:195], a[16:19]
s_add_u32 m0, s23, 2048
ds_read_b64_tr_b16 v[192:193], v8 offset:16384
ds_read_b64_tr_b16 v[194:195], v8 offset:18432
v_mfma_f32_16x16x32_bf16 a[20:23], v[200:203], a[196:199], a[20:23]
buffer_load_dwordx4 v14, s[16:19], s20 offen sc1 lds
v_mfma_f32_16x16x32_bf16 a[28:31], v[204:207], a[196:199], a[28:31]
ds_read_b64_tr_b16 v[196:197], v9 offset:16384
v_mfma_f32_16x16x32_bf16 a[24:27], v[204:207], a[192:195], a[24:27]
s_waitcnt lgkmcnt(7)
ds_read_b64_tr_b16 v[198:199], v9 offset:18432
v_mfma_f32_16x16x32_bf16 a[32:35], v[208:211], a[192:195], a[32:35]
ds_read_b64_tr_b16 v[200:201], v10 offset:16384
v_mfma_f32_16x16x32_bf16 a[36:39], v[208:211], a[196:199], a[36:39]
ds_read_b64_tr_b16 v[202:203], v10 offset:18432
buffer_store_dwordx4 v[164:167], v0, s[36:39], s63 offen offset:1024 sc1
v_mfma_f32_16x16x32_bf16 a[44:47], v[212:215], a[196:199], a[44:47]
s_add_u32 s20, s20, s21
ds_read_b64_tr_b16 v[204:205], v11 offset:16384
v_mfma_f32_16x16x32_bf16 a[40:43], v[212:215], a[192:195], a[40:43]
s_waitcnt lgkmcnt(7)
ds_read_b64_tr_b16 v[206:207], v11 offset:18432
v_mfma_f32_16x16x32_bf16 a[48:51], v[216:219], a[192:195], a[48:51]
s_add_u32 m0, s23, 3072
ds_read_b64_tr_b16 v[208:209], v8 offset:24576
ds_read_b64_tr_b16 v[210:211], v8 offset:26624
v_mfma_f32_16x16x32_bf16 a[52:55], v[216:219], a[196:199], a[52:55]
buffer_load_dwordx4 v14, s[16:19], s20 offen sc1 lds
v_mfma_f32_16x16x32_bf16 a[60:63], v[220:223], a[196:199], a[60:63]
ds_read_b64_tr_b16 v[212:213], v9 offset:24576
v_mfma_f32_16x16x32_bf16 a[56:59], v[220:223], a[192:195], a[56:59]
s_waitcnt lgkmcnt(7)
ds_read_b64_tr_b16 v[214:215], v9 offset:26624
v_mfma_f32_16x16x32_bf16 a[64:67], v[192:195], a[192:195], a[64:67]
ds_read_b64_tr_b16 v[216:217], v10 offset:24576
v_mfma_f32_16x16x32_bf16 a[68:71], v[192:195], a[196:199], a[68:71]
ds_read_b64_tr_b16 v[218:219], v10 offset:26624
v_mfma_f32_16x16x32_bf16 a[76:79], v[196:199], a[196:199], a[76:79]
s_add_u32 s20, s20, s21
ds_read_b64_tr_b16 v[220:221], v11 offset:24576
v_mfma_f32_16x16x32_bf16 a[72:75], v[196:199], a[192:195], a[72:75]
s_waitcnt lgkmcnt(7)
ds_read_b64_tr_b16 v[222:223], v11 offset:26624
v_mfma_f32_16x16x32_bf16 a[80:83], v[200:203], a[192:195], a[80:83]
s_add_u32 m0, s23, 4096
ds_read_b64_tr_b16 v[192:193], v8 offset:4096
ds_read_b64_tr_b16 v[194:195], v8 offset:6144
v_mfma_f32_16x16x32_bf16 a[84:87], v[200:203], a[196:199], a[84:87]
buffer_load_dwordx4 v14, s[16:19], s20 offen sc1 lds
v_mfma_f32_16x16x32_bf16 a[92:95], v[204:207], a[196:199], a[92:95]
ds_read_b64_tr_b16 v[196:197], v9 offset:4096
v_mfma_f32_16x16x32_bf16 a[88:91], v[204:207], a[192:195], a[88:91]
s_waitcnt lgkmcnt(7)
ds_read_b64_tr_b16 v[198:199], v9 offset:6144
v_mfma_f32_16x16x32_bf16 a[96:99], v[208:211], a[192:195], a[96:99]
ds_read_b64_tr_b16 v[200:201], v10 offset:4096
v_mfma_f32_16x16x32_bf16 a[100:103], v[208:211], a[196:199], a[100:103]
ds_read_b64_tr_b16 v[202:203], v10 offset:6144
buffer_store_dwordx4 v[168:171], v0, s[36:39], s63 offen offset:2048 sc1
v_mfma_f32_16x16x32_bf16 a[108:111], v[212:215], a[196:199], a[108:111]
s_add_u32 s20, s20, s21
ds_read_b64_tr_b16 v[204:205], v11 offset:4096
v_mfma_f32_16x16x32_bf16 a[104:107], v[212:215], a[192:195], a[104:107]
s_waitcnt lgkmcnt(7)
ds_read_b64_tr_b16 v[206:207], v11 offset:6144
v_mfma_f32_16x16x32_bf16 a[112:115], v[216:219], a[192:195], a[112:115]
s_add_u32 m0, s23, 5120
ds_read_b64_tr_b16 v[208:209], v8 offset:12288
ds_read_b64_tr_b16 v[210:211], v8 offset:14336
v_mfma_f32_16x16x32_bf16 a[116:119], v[216:219], a[196:199], a[116:119]
buffer_load_dwordx4 v14, s[16:19], s20 offen sc1 lds
v_mfma_f32_16x16x32_bf16 a[124:127], v[220:223], a[196:199], a[124:127]
ds_read_b64_tr_b16 v[212:213], v9 offset:12288
v_mfma_f32_16x16x32_bf16 a[120:123], v[220:223], a[192:195], a[120:123]
s_waitcnt lgkmcnt(7)
ds_read_b64_tr_b16 v[214:215], v9 offset:14336
v_mfma_f32_16x16x32_bf16 a[0:3], v[192:195], a[200:203], a[0:3]
ds_read_b64_tr_b16 v[216:217], v10 offset:12288
v_mfma_f32_16x16x32_bf16 a[4:7], v[192:195], a[204:207], a[4:7]
ds_read_b64_tr_b16 v[218:219], v10 offset:14336
v_mfma_f32_16x16x32_bf16 a[12:15], v[196:199], a[204:207], a[12:15]
s_add_u32 s20, s20, s21
ds_read_b64_tr_b16 v[220:221], v11 offset:12288
v_mfma_f32_16x16x32_bf16 a[8:11], v[196:199], a[200:203], a[8:11]
s_waitcnt lgkmcnt(7)
ds_read_b64_tr_b16 v[222:223], v11 offset:14336
v_mfma_f32_16x16x32_bf16 a[16:19], v[200:203], a[200:203], a[16:19]
s_add_u32 m0, s23, 6144
ds_read_b64_tr_b16 v[192:193], v8 offset:20480
ds_read_b64_tr_b16 v[194:195], v8 offset:22528
v_mfma_f32_16x16x32_bf16 a[20:23], v[200:203], a[204:207], a[20:23]
buffer_load_dwordx4 v14, s[16:19], s20 offen sc1 lds
v_mfma_f32_16x16x32_bf16 a[28:31], v[204:207], a[204:207], a[28:31]
s_add_u32 s26, s24, 32768
ds_read_b64_tr_b16 v[196:197], v9 offset:20480
v_mfma_f32_16x16x32_bf16 a[24:27], v[204:207], a[200:203], a[24:27]
s_waitcnt lgkmcnt(7)
ds_read_b64_tr_b16 v[198:199], v9 offset:22528
v_mfma_f32_16x16x32_bf16 a[32:35], v[208:211], a[200:203], a[32:35]
s_sub_u32 s35, s26, 163840
ds_read_b64_tr_b16 v[200:201], v10 offset:20480
v_mfma_f32_16x16x32_bf16 a[36:39], v[208:211], a[204:207], a[36:39]
s_cselect_b32 s26, s26, s35
ds_read_b64_tr_b16 v[202:203], v10 offset:22528
buffer_store_dwordx4 v[172:175], v0, s[36:39], s63 offen offset:3072 sc1
v_mfma_f32_16x16x32_bf16 a[44:47], v[212:215], a[204:207], a[44:47]
s_add_u32 s20, s20, s21
ds_read_b64_tr_b16 v[204:205], v11 offset:20480
v_mfma_f32_16x16x32_bf16 a[40:43], v[212:215], a[200:203], a[40:43]
s_waitcnt lgkmcnt(7)
ds_read_b64_tr_b16 v[206:207], v11 offset:22528
v_mfma_f32_16x16x32_bf16 a[48:51], v[216:219], a[200:203], a[48:51]
s_add_u32 m0, s23, 7168
ds_read_b64_tr_b16 v[208:209], v8 offset:28672
ds_read_b64_tr_b16 v[210:211], v8 offset:30720
v_mfma_f32_16x16x32_bf16 a[52:55], v[216:219], a[204:207], a[52:55]
buffer_load_dwordx4 v14, s[16:19], s20 offen sc1 lds
v_mfma_f32_16x16x32_bf16 a[60:63], v[220:223], a[204:207], a[60:63]
s_add_u32 s23, s27, s33
ds_read_b64_tr_b16 v[212:213], v9 offset:28672
v_readlane_b32 s16, v16, s22
v_mfma_f32_16x16x32_bf16 a[56:59], v[220:223], a[200:203], a[56:59]
s_waitcnt lgkmcnt(7)
ds_read_b64_tr_b16 v[214:215], v9 offset:30720
v_readlane_b32 s17, v17, s22
v_mfma_f32_16x16x32_bf16 a[64:67], v[192:195], a[200:203], a[64:67]
s_waitcnt vmcnt(22)
ds_read_b64_tr_b16 v[216:217], v10 offset:28672
v_readlane_b32 s25, v18, s22
v_mfma_f32_16x16x32_bf16 a[68:71], v[192:195], a[204:207], a[68:71]
s_barrier
ds_read_b64_tr_b16 v[218:219], v10 offset:30720
v_add_u32 v8, s26, v4
v_mfma_f32_16x16x32_bf16 a[76:79], v[196:199], a[204:207], a[76:79]
s_mov_b32 s20, 0
ds_read_b64_tr_b16 v[220:221], v11 offset:28672
v_mad_u32_u24 v14, v12, s25, v13
v_mfma_f32_16x16x32_bf16 a[72:75], v[196:199], a[200:203], a[72:75]
s_waitcnt lgkmcnt(7)
ds_read_b64_tr_b16 v[222:223], v11 offset:30720
v_add_u32 v9, s26, v5
v_mfma_f32_16x16x32_bf16 a[80:83], v[200:203], a[200:203], a[80:83]
s_mov_b32 m0, s23
ds_read_b64_tr_b16 v[192:193], v8 offset:0
ds_read_b64_tr_b16 v[194:195], v8 offset:2048
v_add_u32 v10, s26, v6
v_mfma_f32_16x16x32_bf16 a[84:87], v[200:203], a[204:207], a[84:87]
buffer_load_dwordx4 v14, s[16:19], s20 offen sc1 lds
v_mfma_f32_16x16x32_bf16 a[92:95], v[204:207], a[204:207], a[92:95]
s_add_u32 s22, s22, 1
ds_read_b64_tr_b16 v[196:197], v9 offset:0
v_add_u32 v11, s26, v7
v_mfma_f32_16x16x32_bf16 a[88:91], v[204:207], a[200:203], a[88:91]
s_waitcnt lgkmcnt(7)
ds_read_b64_tr_b16 v[198:199], v9 offset:2048
v_mfma_f32_16x16x32_bf16 a[96:99], v[208:211], a[200:203], a[96:99]
s_lshl_b32 s21, s25, 3
ds_read_b64_tr_b16 v[200:201], v10 offset:0
v_mfma_f32_16x16x32_bf16 a[100:103], v[208:211], a[204:207], a[100:103]
s_mov_b32 s27, s24
ds_read_b64_tr_b16 v[202:203], v10 offset:2048
v_mfma_f32_16x16x32_bf16 a[108:111], v[212:215], a[204:207], a[108:111]
s_add_u32 s20, s20, s21
ds_read_b64_tr_b16 v[204:205], v11 offset:0
v_mfma_f32_16x16x32_bf16 a[104:107], v[212:215], a[200:203], a[104:107]
s_waitcnt lgkmcnt(7)
ds_read_b64_tr_b16 v[206:207], v11 offset:2048
v_mfma_f32_16x16x32_bf16 a[112:115], v[216:219], a[200:203], a[112:115]
s_add_u32 m0, s23, 1024
ds_read_b64_tr_b16 v[208:209], v8 offset:8192
ds_read_b64_tr_b16 v[210:211], v8 offset:10240
v_mfma_f32_16x16x32_bf16 a[116:119], v[216:219], a[204:207], a[116:119]
buffer_load_dwordx4 v14, s[16:19], s20 offen sc1 lds
v_mfma_f32_16x16x32_bf16 a[124:127], v[220:223], a[204:207], a[124:127]
s_mov_b32 s24, s26
ds_read_b64_tr_b16 v[212:213], v9 offset:8192
v_mfma_f32_16x16x32_bf16 a[120:123], v[220:223], a[200:203], a[120:123]
s_waitcnt lgkmcnt(7)
ds_read_b64_tr_b16 v[214:215], v9 offset:10240
v_mfma_f32_16x16x32_bf16 a[0:3], v[192:195], a[208:211], a[0:3]
ds_read_b64_tr_b16 v[216:217], v10 offset:8192
v_mfma_f32_16x16x32_bf16 a[4:7], v[192:195], a[212:215], a[4:7]
ds_read_b64_tr_b16 v[218:219], v10 offset:10240
v_mfma_f32_16x16x32_bf16 a[12:15], v[196:199], a[212:215], a[12:15]
s_add_u32 s20, s20, s21
ds_read_b64_tr_b16 v[220:221], v11 offset:8192
v_mfma_f32_16x16x32_bf16 a[8:11], v[196:199], a[208:211], a[8:11]
s_waitcnt lgkmcnt(7)
ds_read_b64_tr_b16 v[222:223], v11 offset:10240
v_mfma_f32_16x16x32_bf16 a[16:19], v[200:203], a[208:211], a[16:19]
s_add_u32 m0, s23, 2048
ds_read_b64_tr_b16 v[192:193], v8 offset:16384
ds_read_b64_tr_b16 v[194:195], v8 offset:18432
v_mfma_f32_16x16x32_bf16 a[20:23], v[200:203], a[212:215], a[20:23]
buffer_load_dwordx4 v14, s[16:19], s20 offen sc1 lds
v_mfma_f32_16x16x32_bf16 a[28:31], v[204:207], a[212:215], a[28:31]
ds_read_b64_tr_b16 v[196:197], v9 offset:16384
v_mfma_f32_16x16x32_bf16 a[24:27], v[204:207], a[208:211], a[24:27]
s_waitcnt lgkmcnt(7)
ds_read_b64_tr_b16 v[198:199], v9 offset:18432
v_mfma_f32_16x16x32_bf16 a[32:35], v[208:211], a[208:211], a[32:35]
ds_read_b64_tr_b16 v[200:201], v10 offset:16384
v_mfma_f32_16x16x32_bf16 a[36:39], v[208:211], a[212:215], a[36:39]
ds_read_b64_tr_b16 v[202:203], v10 offset:18432
buffer_store_dwordx4 v[176:179], v0, s[36:39], s64 offen offset:0 sc1
v_mfma_f32_16x16x32_bf16 a[44:47], v[212:215], a[212:215], a[44:47]
s_add_u32 s20, s20, s21
ds_read_b64_tr_b16 v[204:205], v11 offset:16384
v_mfma_f32_16x16x32_bf16 a[40:43], v[212:215], a[208:211], a[40:43]
s_waitcnt lgkmcnt(7)
ds_read_b64_tr_b16 v[206:207], v11 offset:18432
v_mfma_f32_16x16x32_bf16 a[48:51], v[216:219], a[208:211], a[48:51]
s_add_u32 m0, s23, 3072
ds_read_b64_tr_b16 v[208:209], v8 offset:24576
ds_read_b64_tr_b16 v[210:211], v8 offset:26624
v_mfma_f32_16x16x32_bf16 a[52:55], v[216:219], a[212:215], a[52:55]
buffer_load_dwordx4 v14, s[16:19], s20 offen sc1 lds
v_mfma_f32_16x16x32_bf16 a[60:63], v[220:223], a[212:215], a[60:63]
ds_read_b64_tr_b16 v[212:213], v9 offset:24576
v_mfma_f32_16x16x32_bf16 a[56:59], v[220:223], a[208:211], a[56:59]
s_waitcnt lgkmcnt(7)
ds_read_b64_tr_b16 v[214:215], v9 offset:26624
v_mfma_f32_16x16x32_bf16 a[64:67], v[192:195], a[208:211], a[64:67]
ds_read_b64_tr_b16 v[216:217], v10 offset:24576
v_mfma_f32_16x16x32_bf16 a[68:71], v[192:195], a[212:215], a[68:71]
ds_read_b64_tr_b16 v[218:219], v10 offset:26624
v_mfma_f32_16x16x32_bf16 a[76:79], v[196:199], a[212:215], a[76:79]
s_add_u32 s20, s20, s21
ds_read_b64_tr_b16 v[220:221], v11 offset:24576
v_mfma_f32_16x16x32_bf16 a[72:75], v[196:199], a[208:211], a[72:75]
s_waitcnt lgkmcnt(7)
ds_read_b64_tr_b16 v[222:223], v11 offset:26624
v_mfma_f32_16x16x32_bf16 a[80:83], v[200:203], a[208:211], a[80:83]
s_add_u32 m0, s23, 4096
ds_read_b64_tr_b16 v[192:193], v8 offset:4096
ds_read_b64_tr_b16 v[194:195], v8 offset:6144
v_mfma_f32_16x16x32_bf16 a[84:87], v[200:203], a[212:215], a[84:87]
buffer_load_dwordx4 v14, s[16:19], s20 offen sc1 lds
v_mfma_f32_16x16x32_bf16 a[92:95], v[204:207], a[212:215], a[92:95]
ds_read_b64_tr_b16 v[196:197], v9 offset:4096
v_mfma_f32_16x16x32_bf16 a[88:91], v[204:207], a[208:211], a[88:91]
s_waitcnt lgkmcnt(7)
ds_read_b64_tr_b16 v[198:199], v9 offset:6144
v_mfma_f32_16x16x32_bf16 a[96:99], v[208:211], a[208:211], a[96:99]
ds_read_b64_tr_b16 v[200:201], v10 offset:4096
v_mfma_f32_16x16x32_bf16 a[100:103], v[208:211], a[212:215], a[100:103]
ds_read_b64_tr_b16 v[202:203], v10 offset:6144
v_mfma_f32_16x16x32_bf16 a[108:111], v[212:215], a[212:215], a[108:111]
s_add_u32 s20, s20, s21
ds_read_b64_tr_b16 v[204:205], v11 offset:4096
v_mfma_f32_16x16x32_bf16 a[104:107], v[212:215], a[208:211], a[104:107]
s_waitcnt lgkmcnt(7)
ds_read_b64_tr_b16 v[206:207], v11 offset:6144
v_mfma_f32_16x16x32_bf16 a[112:115], v[216:219], a[208:211], a[112:115]
s_add_u32 m0, s23, 5120
ds_read_b64_tr_b16 v[208:209], v8 offset:12288
ds_read_b64_tr_b16 v[210:211], v8 offset:14336
v_mfma_f32_16x16x32_bf16 a[116:119], v[216:219], a[212:215], a[116:119]
buffer_load_dwordx4 v14, s[16:19], s20 offen sc1 lds
v_mfma_f32_16x16x32_bf16 a[124:127], v[220:223], a[212:215], a[124:127]
ds_read_b64_tr_b16 v[212:213], v9 offset:12288
v_mfma_f32_16x16x32_bf16 a[120:123], v[220:223], a[208:211], a[120:123]
s_waitcnt lgkmcnt(7)
ds_read_b64_tr_b16 v[214:215], v9 offset:14336
v_mfma_f32_16x16x32_bf16 a[0:3], v[192:195], a[216:219], a[0:3]
ds_read_b64_tr_b16 v[216:217], v10 offset:12288
v_mfma_f32_16x16x32_bf16 a[4:7], v[192:195], a[220:223], a[4:7]
ds_read_b64_tr_b16 v[218:219], v10 offset:14336
v_mfma_f32_16x16x32_bf16 a[12:15], v[196:199], a[220:223], a[12:15]
s_add_u32 s20, s20, s21
ds_read_b64_tr_b16 v[220:221], v11 offset:12288
v_mfma_f32_16x16x32_bf16 a[8:11], v[196:199], a[216:219], a[8:11]
s_waitcnt lgkmcnt(7)
ds_read_b64_tr_b16 v[222:223], v11 offset:14336
v_mfma_f32_16x16x32_bf16 a[16:19], v[200:203], a[216:219], a[16:19]
s_add_u32 m0, s23, 6144
ds_read_b64_tr_b16 v[192:193], v8 offset:20480
ds_read_b64_tr_b16 v[194:195], v8 offset:22528
v_mfma_f32_16x16x32_bf16 a[20:23], v[200:203], a[220:223], a[20:23]
buffer_load_dwordx4 v14, s[16:19], s20 offen sc1 lds
v_mfma_f32_16x16x32_bf16 a[28:31], v[204:207], a[220:223], a[28:31]
s_add_u32 s26, s24, 32768
ds_read_b64_tr_b16 v[196:197], v9 offset:20480
v_mfma_f32_16x16x32_bf16 a[24:27], v[204:207], a[216:219], a[24:27]
s_waitcnt lgkmcnt(7)
ds_read_b64_tr_b16 v[198:199], v9 offset:22528
v_mfma_f32_16x16x32_bf16 a[32:35], v[208:211], a[216:219], a[32:35]
s_sub_u32 s35, s26, 163840
ds_read_b64_tr_b16 v[200:201], v10 offset:20480
v_mfma_f32_16x16x32_bf16 a[36:39], v[208:211], a[220:223], a[36:39]
s_cselect_b32 s26, s26, s35
ds_read_b64_tr_b16 v[202:203], v10 offset:22528
buffer_store_dwordx4 v[180:183], v0, s[36:39], s64 offen offset:1024 sc1
v_mfma_f32_16x16x32_bf16 a[44:47], v[212:215], a[220:223], a[44:47]
s_add_u32 s20, s20, s21
ds_read_b64_tr_b16 v[204:205], v11 offset:20480
v_mfma_f32_16x16x32_bf16 a[40:43], v[212:215], a[216:219], a[40:43]
s_waitcnt lgkmcnt(7)
ds_read_b64_tr_b16 v[206:207], v11 offset:22528
v_mfma_f32_16x16x32_bf16 a[48:51], v[216:219], a[216:219], a[48:51]
s_add_u32 m0, s23, 7168
ds_read_b64_tr_b16 v[208:209], v8 offset:28672
ds_read_b64_tr_b16 v[210:211], v8 offset:30720
v_mfma_f32_16x16x32_bf16 a[52:55], v[216:219], a[220:223], a[52:55]
buffer_load_dwordx4 v14, s[16:19], s20 offen sc1 lds
v_mfma_f32_16x16x32_bf16 a[60:63], v[220:223], a[220:223], a[60:63]
s_add_u32 s23, s27, s33
ds_read_b64_tr_b16 v[212:213], v9 offset:28672
v_readlane_b32 s16, v16, s22
v_mfma_f32_16x16x32_bf16 a[56:59], v[220:223], a[216:219], a[56:59]
s_waitcnt lgkmcnt(7)
ds_read_b64_tr_b16 v[214:215], v9 offset:30720
v_readlane_b32 s17, v17, s22
v_mfma_f32_16x16x32_bf16 a[64:67], v[192:195], a[216:219], a[64:67]
s_waitcnt vmcnt(21)
ds_read_b64_tr_b16 v[216:217], v10 offset:28672
v_readlane_b32 s25, v18, s22
v_mfma_f32_16x16x32_bf16 a[68:71], v[192:195], a[220:223], a[68:71]
s_barrier
ds_read_b64_tr_b16 v[218:219], v10 offset:30720
v_add_u32 v8, s26, v4
v_mfma_f32_16x16x32_bf16 a[76:79], v[196:199], a[220:223], a[76:79]
s_mov_b32 s20, 0
ds_read_b64_tr_b16 v[220:221], v11 offset:28672
v_mad_u32_u24 v14, v12, s25, v13
v_mfma_f32_16x16x32_bf16 a[72:75], v[196:199], a[216:219], a[72:75]
s_waitcnt lgkmcnt(7)
ds_read_b64_tr_b16 v[222:223], v11 offset:30720
v_add_u32 v9, s26, v5
v_mfma_f32_16x16x32_bf16 a[80:83], v[200:203], a[216:219], a[80:83]
s_mov_b32 m0, s23
ds_read_b64_tr_b16 v[192:193], v8 offset:0
ds_read_b64_tr_b16 v[194:195], v8 offset:2048
v_add_u32 v10, s26, v6
v_mfma_f32_16x16x32_bf16 a[84:87], v[200:203], a[220:223], a[84:87]
buffer_load_dwordx4 v14, s[16:19], s20 offen sc1 lds
v_mfma_f32_16x16x32_bf16 a[92:95], v[204:207], a[220:223], a[92:95]
s_add_u32 s22, s22, 1
ds_read_b64_tr_b16 v[196:197], v9 offset:0
v_add_u32 v11, s26, v7
v_mfma_f32_16x16x32_bf16 a[88:91], v[204:207], a[216:219], a[88:91]
s_waitcnt lgkmcnt(7)
ds_read_b64_tr_b16 v[198:199], v9 offset:2048
v_mfma_f32_16x16x32_bf16 a[96:99], v[208:211], a[216:219], a[96:99]
s_lshl_b32 s21, s25, 3
ds_read_b64_tr_b16 v[200:201], v10 offset:0
v_mfma_f32_16x16x32_bf16 a[100:103], v[208:211], a[220:223], a[100:103]
s_mov_b32 s27, s24
ds_read_b64_tr_b16 v[202:203], v10 offset:2048
v_mfma_f32_16x16x32_bf16 a[108:111], v[212:215], a[220:223], a[108:111]
s_add_u32 s20, s20, s21
ds_read_b64_tr_b16 v[204:205], v11 offset:0
v_mfma_f32_16x16x32_bf16 a[104:107], v[212:215], a[216:219], a[104:107]
s_waitcnt lgkmcnt(7)
ds_read_b64_tr_b16 v[206:207], v11 offset:2048
v_mfma_f32_16x16x32_bf16 a[112:115], v[216:219], a[216:219], a[112:115]
s_add_u32 m0, s23, 1024
ds_read_b64_tr_b16 v[208:209], v8 offset:8192
ds_read_b64_tr_b16 v[210:211], v8 offset:10240
v_mfma_f32_16x16x32_bf16 a[116:119], v[216:219], a[220:223], a[116:119]
buffer_load_dwordx4 v14, s[16:19], s20 offen sc1 lds
v_mfma_f32_16x16x32_bf16 a[124:127], v[220:223], a[220:223], a[124:127]
s_mov_b32 s24, s26
ds_read_b64_tr_b16 v[212:213], v9 offset:8192
v_mfma_f32_16x16x32_bf16 a[120:123], v[220:223], a[216:219], a[120:123]
s_waitcnt lgkmcnt(7)
ds_read_b64_tr_b16 v[214:215], v9 offset:10240
v_mfma_f32_16x16x32_bf16 a[0:3], v[192:195], a[224:227], a[0:3]
ds_read_b64_tr_b16 v[216:217], v10 offset:8192
v_mfma_f32_16x16x32_bf16 a[4:7], v[192:195], a[228:231], a[4:7]
ds_read_b64_tr_b16 v[218:219], v10 offset:10240
v_mfma_f32_16x16x32_bf16 a[12:15], v[196:199], a[228:231], a[12:15]
s_add_u32 s20, s20, s21
ds_read_b64_tr_b16 v[220:221], v11 offset:8192
v_mfma_f32_16x16x32_bf16 a[8:11], v[196:199], a[224:227], a[8:11]
s_waitcnt lgkmcnt(7)
ds_read_b64_tr_b16 v[222:223], v11 offset:10240
v_mfma_f32_16x16x32_bf16 a[16:19], v[200:203], a[224:227], a[16:19]
s_add_u32 m0, s23, 2048
ds_read_b64_tr_b16 v[192:193], v8 offset:16384
ds_read_b64_tr_b16 v[194:195], v8 offset:18432
v_mfma_f32_16x16x32_bf16 a[20:23], v[200:203], a[228:231], a[20:23]
buffer_load_dwordx4 v14, s[16:19], s20 offen sc1 lds
v_mfma_f32_16x16x32_bf16 a[28:31], v[204:207], a[228:231], a[28:31]
ds_read_b64_tr_b16 v[196:197], v9 offset:16384
v_mfma_f32_16x16x32_bf16 a[24:27], v[204:207], a[224:227], a[24:27]
s_waitcnt lgkmcnt(7)
ds_read_b64_tr_b16 v[198:199], v9 offset:18432
v_mfma_f32_16x16x32_bf16 a[32:35], v[208:211], a[224:227], a[32:35]
ds_read_b64_tr_b16 v[200:201], v10 offset:16384
v_mfma_f32_16x16x32_bf16 a[36:39], v[208:211], a[228:231], a[36:39]
ds_read_b64_tr_b16 v[202:203], v10 offset:18432
buffer_store_dwordx4 v[184:187], v0, s[36:39], s64 offen offset:2048 sc1
v_mfma_f32_16x16x32_bf16 a[44:47], v[212:215], a[228:231], a[44:47]
s_add_u32 s20, s20, s21
ds_read_b64_tr_b16 v[204:205], v11 offset:16384
v_mfma_f32_16x16x32_bf16 a[40:43], v[212:215], a[224:227], a[40:43]
s_waitcnt lgkmcnt(7)
ds_read_b64_tr_b16 v[206:207], v11 offset:18432
v_mfma_f32_16x16x32_bf16 a[48:51], v[216:219], a[224:227], a[48:51]
s_add_u32 m0, s23, 3072
ds_read_b64_tr_b16 v[208:209], v8 offset:24576
ds_read_b64_tr_b16 v[210:211], v8 offset:26624
v_mfma_f32_16x16x32_bf16 a[52:55], v[216:219], a[228:231], a[52:55]
buffer_load_dwordx4 v14, s[16:19], s20 offen sc1 lds
v_mfma_f32_16x16x32_bf16 a[60:63], v[220:223], a[228:231], a[60:63]
ds_read_b64_tr_b16 v[212:213], v9 offset:24576
v_mfma_f32_16x16x32_bf16 a[56:59], v[220:223], a[224:227], a[56:59]
s_waitcnt lgkmcnt(7)
ds_read_b64_tr_b16 v[214:215], v9 offset:26624
v_mfma_f32_16x16x32_bf16 a[64:67], v[192:195], a[224:227], a[64:67]
ds_read_b64_tr_b16 v[216:217], v10 offset:24576
v_mfma_f32_16x16x32_bf16 a[68:71], v[192:195], a[228:231], a[68:71]
ds_read_b64_tr_b16 v[218:219], v10 offset:26624
v_mfma_f32_16x16x32_bf16 a[76:79], v[196:199], a[228:231], a[76:79]
s_add_u32 s20, s20, s21
ds_read_b64_tr_b16 v[220:221], v11 offset:24576
v_mfma_f32_16x16x32_bf16 a[72:75], v[196:199], a[224:227], a[72:75]
s_waitcnt lgkmcnt(7)
ds_read_b64_tr_b16 v[222:223], v11 offset:26624
v_mfma_f32_16x16x32_bf16 a[80:83], v[200:203], a[224:227], a[80:83]
s_add_u32 m0, s23, 4096
ds_read_b64_tr_b16 v[192:193], v8 offset:4096
ds_read_b64_tr_b16 v[194:195], v8 offset:6144
v_mfma_f32_16x16x32_bf16 a[84:87], v[200:203], a[228:231], a[84:87]
buffer_load_dwordx4 v14, s[16:19], s20 offen sc1 lds
v_mfma_f32_16x16x32_bf16 a[92:95], v[204:207], a[228:231], a[92:95]
ds_read_b64_tr_b16 v[196:197], v9 offset:4096
v_mfma_f32_16x16x32_bf16 a[88:91], v[204:207], a[224:227], a[88:91]
s_waitcnt lgkmcnt(7)
ds_read_b64_tr_b16 v[198:199], v9 offset:6144
v_mfma_f32_16x16x32_bf16 a[96:99], v[208:211], a[224:227], a[96:99]
ds_read_b64_tr_b16 v[200:201], v10 offset:4096
v_mfma_f32_16x16x32_bf16 a[100:103], v[208:211], a[228:231], a[100:103]
ds_read_b64_tr_b16 v[202:203], v10 offset:6144
v_mfma_f32_16x16x32_bf16 a[108:111], v[212:215], a[228:231], a[108:111]
s_add_u32 s20, s20, s21
ds_read_b64_tr_b16 v[204:205], v11 offset:4096
v_mfma_f32_16x16x32_bf16 a[104:107], v[212:215], a[224:227], a[104:107]
s_waitcnt lgkmcnt(7)
ds_read_b64_tr_b16 v[206:207], v11 offset:6144
v_mfma_f32_16x16x32_bf16 a[112:115], v[216:219], a[224:227], a[112:115]
s_add_u32 m0, s23, 5120
ds_read_b64_tr_b16 v[208:209], v8 offset:12288
ds_read_b64_tr_b16 v[210:211], v8 offset:14336
v_mfma_f32_16x16x32_bf16 a[116:119], v[216:219], a[228:231], a[116:119]
buffer_load_dwordx4 v14, s[16:19], s20 offen sc1 lds
v_mfma_f32_16x16x32_bf16 a[124:127], v[220:223], a[228:231], a[124:127]
ds_read_b64_tr_b16 v[212:213], v9 offset:12288
v_mfma_f32_16x16x32_bf16 a[120:123], v[220:223], a[224:227], a[120:123]
s_waitcnt lgkmcnt(7)
ds_read_b64_tr_b16 v[214:215], v9 offset:14336
v_mfma_f32_16x16x32_bf16 a[0:3], v[192:195], a[232:235], a[0:3]
ds_read_b64_tr_b16 v[216:217], v10 offset:12288
v_mfma_f32_16x16x32_bf16 a[4:7], v[192:195], a[236:239], a[4:7]
ds_read_b64_tr_b16 v[218:219], v10 offset:14336
v_mfma_f32_16x16x32_bf16 a[12:15], v[196:199], a[236:239], a[12:15]
s_add_u32 s20, s20, s21
ds_read_b64_tr_b16 v[220:221], v11 offset:12288
v_mfma_f32_16x16x32_bf16 a[8:11], v[196:199], a[232:235], a[8:11]
s_waitcnt lgkmcnt(7)
ds_read_b64_tr_b16 v[222:223], v11 offset:14336
v_mfma_f32_16x16x32_bf16 a[16:19], v[200:203], a[232:235], a[16:19]
s_add_u32 m0, s23, 6144
ds_read_b64_tr_b16 v[192:193], v8 offset:20480
ds_read_b64_tr_b16 v[194:195], v8 offset:22528
v_mfma_f32_16x16x32_bf16 a[20:23], v[200:203], a[236:239], a[20:23]
buffer_load_dwordx4 v14, s[16:19], s20 offen sc1 lds
v_mfma_f32_16x16x32_bf16 a[28:31], v[204:207], a[236:239], a[28:31]
s_add_u32 s26, s24, 32768
ds_read_b64_tr_b16 v[196:197], v9 offset:20480
v_mfma_f32_16x16x32_bf16 a[24:27], v[204:207], a[232:235], a[24:27]
s_waitcnt lgkmcnt(7)
ds_read_b64_tr_b16 v[198:199], v9 offset:22528
v_mfma_f32_16x16x32_bf16 a[32:35], v[208:211], a[232:235], a[32:35]
s_sub_u32 s35, s26, 163840
ds_read_b64_tr_b16 v[200:201], v10 offset:20480
v_mfma_f32_16x16x32_bf16 a[36:39], v[208:211], a[236:239], a[36:39]
s_cselect_b32 s26, s26, s35
ds_read_b64_tr_b16 v[202:203], v10 offset:22528
buffer_store_dwordx4 v[188:191], v0, s[36:39], s64 offen offset:3072 sc1
v_mfma_f32_16x16x32_bf16 a[44:47], v[212:215], a[236:239], a[44:47]
s_add_u32 s20, s20, s21
ds_read_b64_tr_b16 v[204:205], v11 offset:20480
v_mfma_f32_16x16x32_bf16 a[40:43], v[212:215], a[232:235], a[40:43]
s_waitcnt lgkmcnt(7)
ds_read_b64_tr_b16 v[206:207], v11 offset:22528
v_mfma_f32_16x16x32_bf16 a[48:51], v[216:219], a[232:235], a[48:51]
s_add_u32 m0, s23, 7168
ds_read_b64_tr_b16 v[208:209], v8 offset:28672
ds_read_b64_tr_b16 v[210:211], v8 offset:30720
v_mfma_f32_16x16x32_bf16 a[52:55], v[216:219], a[236:239], a[52:55]
buffer_load_dwordx4 v14, s[16:19], s20 offen sc1 lds
v_mfma_f32_16x16x32_bf16 a[60:63], v[220:223], a[236:239], a[60:63]
s_add_u32 s23, s27, s33
ds_read_b64_tr_b16 v[212:213], v9 offset:28672
v_readlane_b32 s16, v16, s22
v_mfma_f32_16x16x32_bf16 a[56:59], v[220:223], a[232:235], a[56:59]
s_waitcnt lgkmcnt(7)
ds_read_b64_tr_b16 v[214:215], v9 offset:30720
v_readlane_b32 s17, v17, s22
v_mfma_f32_16x16x32_bf16 a[64:67], v[192:195], a[232:235], a[64:67]
s_waitcnt vmcnt(20)
ds_read_b64_tr_b16 v[216:217], v10 offset:28672
v_readlane_b32 s25, v18, s22
v_mfma_f32_16x16x32_bf16 a[68:71], v[192:195], a[236:239], a[68:71]
s_barrier
ds_read_b64_tr_b16 v[218:219], v10 offset:30720
v_add_u32 v8, s26, v4
v_mfma_f32_16x16x32_bf16 a[76:79], v[196:199], a[236:239], a[76:79]
s_mov_b32 s20, 0
ds_read_b64_tr_b16 v[220:221], v11 offset:28672
v_mad_u32_u24 v14, v12, s25, v13
v_mfma_f32_16x16x32_bf16 a[72:75], v[196:199], a[232:235], a[72:75]
s_waitcnt lgkmcnt(7)
ds_read_b64_tr_b16 v[222:223], v11 offset:30720
v_add_u32 v9, s26, v5
v_mfma_f32_16x16x32_bf16 a[80:83], v[200:203], a[232:235], a[80:83]
s_mov_b32 m0, s23
ds_read_b64_tr_b16 v[192:193], v8 offset:0
ds_read_b64_tr_b16 v[194:195], v8 offset:2048
v_add_u32 v10, s26, v6
v_mfma_f32_16x16x32_bf16 a[84:87], v[200:203], a[236:239], a[84:87]
buffer_load_dwordx4 v14, s[16:19], s20 offen sc1 lds
v_mfma_f32_16x16x32_bf16 a[92:95], v[204:207], a[236:239], a[92:95]
s_add_u32 s22, s22, 1
ds_read_b64_tr_b16 v[196:197], v9 offset:0
v_add_u32 v11, s26, v7
v_mfma_f32_16x16x32_bf16 a[88:91], v[204:207], a[232:235], a[88:91]
s_waitcnt lgkmcnt(7)
ds_read_b64_tr_b16 v[198:199], v9 offset:2048
v_mfma_f32_16x16x32_bf16 a[96:99], v[208:211], a[232:235], a[96:99]
s_lshl_b32 s21, s25, 3
ds_read_b64_tr_b16 v[200:201], v10 offset:0
v_mfma_f32_16x16x32_bf16 a[100:103], v[208:211], a[236:239], a[100:103]
s_mov_b32 s27, s24
ds_read_b64_tr_b16 v[202:203], v10 offset:2048
v_mfma_f32_16x16x32_bf16 a[108:111], v[212:215], a[236:239], a[108:111]
s_add_u32 s20, s20, s21
ds_read_b64_tr_b16 v[204:205], v11 offset:0
v_mfma_f32_16x16x32_bf16 a[104:107], v[212:215], a[232:235], a[104:107]
s_waitcnt lgkmcnt(7)
ds_read_b64_tr_b16 v[206:207], v11 offset:2048
v_mfma_f32_16x16x32_bf16 a[112:115], v[216:219], a[232:235], a[112:115]
s_add_u32 m0, s23, 1024
ds_read_b64_tr_b16 v[208:209], v8 offset:8192
ds_read_b64_tr_b16 v[210:211], v8 offset:10240
v_mfma_f32_16x16x32_bf16 a[116:119], v[216:219], a[236:239], a[116:119]
buffer_load_dwordx4 v14, s[16:19], s20 offen sc1 lds
v_mfma_f32_16x16x32_bf16 a[124:127], v[220:223], a[236:239], a[124:127]
s_mov_b32 s24, s26
ds_read_b64_tr_b16 v[212:213], v9 offset:8192
v_mfma_f32_16x16x32_bf16 a[120:123], v[220:223], a[232:235], a[120:123]
s_waitcnt lgkmcnt(7)
ds_read_b64_tr_b16 v[214:215], v9 offset:10240
s_nop 7
s_setpc_b64 s[70:71]
W64G_%=:
v_add_u32 v8, s24, v4
v_add_u32 v9, s24, v5
v_add_u32 v10, s24, v6
v_add_u32 v11, s24, v7
s_nop 0
ds_read_b64_tr_b16 v[192:193], v8 offset:0
ds_read_b64_tr_b16 v[194:195], v8 offset:2048
ds_read_b64_tr_b16 v[196:197], v9 offset:0
ds_read_b64_tr_b16 v[198:199], v9 offset:2048
ds_read_b64_tr_b16 v[200:201], v10 offset:0
ds_read_b64_tr_b16 v[202:203], v10 offset:2048
ds_read_b64_tr_b16 v[204:205], v11 offset:0
ds_read_b64_tr_b16 v[206:207], v11 offset:2048
ds_read_b64_tr_b16 v[208:209], v8 offset:8192
ds_read_b64_tr_b16 v[210:211], v8 offset:10240
ds_read_b64_tr_b16 v[212:213], v9 offset:8192
ds_read_b64_tr_b16 v[214:215], v9 offset:10240
s_waitcnt lgkmcnt(0)
W64GN_%=:
v_mfma_f32_16x16x32_bf16 a[0:3], v[192:195], a[128:131], 0
ds_read_b64_tr_b16 v[216:217], v10 offset:8192
v_mfma_f32_16x16x32_bf16 a[4:7], v[192:195], a[132:135], 0
ds_read_b64_tr_b16 v[218:219], v10 offset:10240
v_mfma_f32_16x16x32_bf16 a[12:15], v[196:199], a[132:135], 0
s_add_u32 s20, s20, s21
ds_read_b64_tr_b16 v[220:221], v11 offset:8192
v_mfma_f32_16x16x32_bf16 a[8:11], v[196:199], a[128:131], 0
s_waitcnt lgkmcnt(7)
ds_read_b64_tr_b16 v[222:223], v11 offset:10240
v_mfma_f32_16x16x32_bf16 a[16:19], v[200:203], a[128:131], 0
s_add_u32 m0, s23, 2048
ds_read_b64_tr_b16 v[192:193], v8 offset:16384
ds_read_b64_tr_b16 v[194:195], v8 offset:18432
v_mfma_f32_16x16x32_bf16 a[20:23], v[200:203], a[132:135], 0
buffer_load_dwordx4 v14, s[16:19], s20 offen sc1 lds
v_mfma_f32_16x16x32_bf16 a[28:31], v[204:207], a[132:135], 0
ds_read_b64_tr_b16 v[196:197], v9 offset:16384
v_mfma_f32_16x16x32_bf16 a[24:27], v[204:207], a[128:131], 0
s_waitcnt lgkmcnt(7)
ds_read_b64_tr_b16 v[198:199], v9 offset:18432
v_mfma_f32_16x16x32_bf16 a[32:35], v[208:211], a[128:131], 0
ds_read_b64_tr_b16 v[200:201], v10 offset:16384
v_mfma_f32_16x16x32_bf16 a[36:39], v[208:211], a[132:135], 0
ds_read_b64_tr_b16 v[202:203], v10 offset:18432
buffer_store_dwordx4 v[64:67], v0, s[36:39], s66 offen offset:0 sc1
v_mfma_f32_16x16x32_bf16 a[44:47], v[212:215], a[132:135], 0
s_add_u32 s20, s20, s21
ds_read_b64_tr_b16 v[204:205], v11 offset:16384
v_mfma_f32_16x16x32_bf16 a[40:43], v[212:215], a[128:131], 0
s_waitcnt lgkmcnt(7)
ds_read_b64_tr_b16 v[206:207], v11 offset:18432
v_mfma_f32_16x16x32_bf16 a[48:51], v[216:219], a[128:131], 0
s_add_u32 m0, s23, 3072
ds_read_b64_tr_b16 v[208:209], v8 offset:24576
ds_read_b64_tr_b16 v[210:211], v8 offset:26624
v_mfma_f32_16x16x32_bf16 a[52:55], v[216:219], a[132:135], 0
buffer_load_dwordx4 v14, s[16:19], s20 offen sc1 lds
v_mfma_f32_16x16x32_bf16 a[60:63], v[220:223], a[132:135], 0
ds_read_b64_tr_b16 v[212:213], v9 offset:24576
v_mfma_f32_16x16x32_bf16 a[56:59], v[220:223], a[128:131], 0
s_waitcnt lgkmcnt(7)
ds_read_b64_tr_b16 v[214:215], v9 offset:26624
v_mfma_f32_16x16x32_bf16 a[64:67], v[192:195], a[128:131], 0
ds_read_b64_tr_b16 v[216:217], v10 offset:24576
v_mfma_f32_16x16x32_bf16 a[68:71], v[192:195], a[132:135], 0
ds_read_b64_tr_b16 v[218:219], v10 offset:26624
v_mfma_f32_16x16x32_bf16 a[76:79], v[196:199], a[132:135], 0
s_add_u32 s20, s20, s21
ds_read_b64_tr_b16 v[220:221], v11 offset:24576
v_mfma_f32_16x16x32_bf16 a[72:75], v[196:199], a[128:131], 0
s_waitcnt lgkmcnt(7)
ds_read_b64_tr_b16 v[222:223], v11 offset:26624
v_mfma_f32_16x16x32_bf16 a[80:83], v[200:203], a[128:131], 0
s_add_u32 m0, s23, 4096
ds_read_b64_tr_b16 v[192:193], v8 offset:4096
ds_read_b64_tr_b16 v[194:195], v8 offset:6144
v_mfma_f32_16x16x32_bf16 a[84:87], v[200:203], a[132:135], 0
buffer_load_dwordx4 v14, s[16:19], s20 offen sc1 lds
v_mfma_f32_16x16x32_bf16 a[92:95], v[204:207], a[132:135], 0
ds_read_b64_tr_b16 v[196:197], v9 offset:4096
v_mfma_f32_16x16x32_bf16 a[88:91], v[204:207], a[128:131], 0
s_waitcnt lgkmcnt(7)
ds_read_b64_tr_b16 v[198:199], v9 offset:6144
v_mfma_f32_16x16x32_bf16 a[96:99], v[208:211], a[128:131], 0
ds_read_b64_tr_b16 v[200:201], v10 offset:4096
v_mfma_f32_16x16x32_bf16 a[100:103], v[208:211], a[132:135], 0
ds_read_b64_tr_b16 v[202:203], v10 offset:6144
buffer_store_dwordx4 v[68:71], v0, s[36:39], s66 offen offset:1024 sc1
v_mfma_f32_16x16x32_bf16 a[108:111], v[212:215], a[132:135], 0
s_add_u32 s20, s20, s21
ds_read_b64_tr_b16 v[204:205], v11 offset:4096
v_mfma_f32_16x16x32_bf16 a[104:107], v[212:215], a[128:131], 0
s_waitcnt lgkmcnt(7)
ds_read_b64_tr_b16 v[206:207], v11 offset:6144
v_mfma_f32_16x16x32_bf16 a[112:115], v[216:219], a[128:131], 0
s_add_u32 m0, s23, 5120
ds_read_b64_tr_b16 v[208:209], v8 offset:12288
ds_read_b64_tr_b16 v[210:211], v8 offset:14336
v_mfma_f32_16x16x32_bf16 a[116:119], v[216:219], a[132:135], 0
buffer_load_dwordx4 v14, s[16:19], s20 offen sc1 lds
v_mfma_f32_16x16x32_bf16 a[124:127], v[220:223], a[132:135], 0
ds_read_b64_tr_b16 v[212:213], v9 offset:12288
v_mfma_f32_16x16x32_bf16 a[120:123], v[220:223], a[128:131], 0
s_waitcnt lgkmcnt(7)
ds_read_b64_tr_b16 v[214:215], v9 offset:14336
v_mfma_f32_16x16x32_bf16 a[0:3], v[192:195], a[136:139], a[0:3]
ds_read_b64_tr_b16 v[216:217], v10 offset:12288
v_mfma_f32_16x16x32_bf16 a[4:7], v[192:195], a[140:143], a[4:7]
ds_read_b64_tr_b16 v[218:219], v10 offset:14336
v_mfma_f32_16x16x32_bf16 a[12:15], v[196:199], a[140:143], a[12:15]
s_add_u32 s20, s20, s21
ds_read_b64_tr_b16 v[220:221], v11 offset:12288
v_mfma_f32_16x16x32_bf16 a[8:11], v[196:199], a[136:139], a[8:11]
s_waitcnt lgkmcnt(7)
ds_read_b64_tr_b16 v[222:223], v11 offset:14336
v_mfma_f32_16x16x32_bf16 a[16:19], v[200:203], a[136:139], a[16:19]
s_add_u32 m0, s23, 6144
ds_read_b64_tr_b16 v[192:193], v8 offset:20480
ds_read_b64_tr_b16 v[194:195], v8 offset:22528
v_mfma_f32_16x16x32_bf16 a[20:23], v[200:203], a[140:143], a[20:23]
buffer_load_dwordx4 v14, s[16:19], s20 offen sc1 lds
v_mfma_f32_16x16x32_bf16 a[28:31], v[204:207], a[140:143], a[28:31]
s_add_u32 s26, s24, 32768
ds_read_b64_tr_b16 v[196:197], v9 offset:20480
v_mfma_f32_16x16x32_bf16 a[24:27], v[204:207], a[136:139], a[24:27]
s_waitcnt lgkmcnt(7)
ds_read_b64_tr_b16 v[198:199], v9 offset:22528
v_mfma_f32_16x16x32_bf16 a[32:35], v[208:211], a[136:139], a[32:35]
s_sub_u32 s35, s26, 163840
ds_read_b64_tr_b16 v[200:201], v10 offset:20480
v_mfma_f32_16x16x32_bf16 a[36:39], v[208:211], a[140:143], a[36:39]
s_cselect_b32 s26, s26, s35
ds_read_b64_tr_b16 v[202:203], v10 offset:22528
buffer_store_dwordx4 v[72:75], v0, s[36:39], s66 offen offset:2048 sc1
v_mfma_f32_16x16x32_bf16 a[44:47], v[212:215], a[140:143], a[44:47]
s_add_u32 s20, s20, s21
ds_read_b64_tr_b16 v[204:205], v11 offset:20480
v_mfma_f32_16x16x32_bf16 a[40:43], v[212:215], a[136:139], a[40:43]
s_waitcnt lgkmcnt(7)
ds_read_b64_tr_b16 v[206:207], v11 offset:22528
v_mfma_f32_16x16x32_bf16 a[48:51], v[216:219], a[136:139], a[48:51]
s_add_u32 m0, s23, 7168
ds_read_b64_tr_b16 v[208:209], v8 offset:28672
ds_read_b64_tr_b16 v[210:211], v8 offset:30720
v_mfma_f32_16x16x32_bf16 a[52:55], v[216:219], a[140:143], a[52:55]
buffer_load_dwordx4 v14, s[16:19], s20 offen sc1 lds
v_mfma_f32_16x16x32_bf16 a[60:63], v[220:223], a[140:143], a[60:63]
s_add_u32 s23, s27, s33
ds_read_b64_tr_b16 v[212:213], v9 offset:28672
v_readlane_b32 s16, v16, s22
v_mfma_f32_16x16x32_bf16 a[56:59], v[220:223], a[136:139], a[56:59]
s_waitcnt lgkmcnt(7)
ds_read_b64_tr_b16 v[214:215], v9 offset:30720
v_readlane_b32 s17, v17, s22
v_mfma_f32_16x16x32_bf16 a[64:67], v[192:195], a[136:139], a[64:67]
s_waitcnt vmcnt(19)
ds_read_b64_tr_b16 v[216:217], v10 offset:28672
v_readlane_b32 s25, v18, s22
v_mfma_f32_16x16x32_bf16 a[68:71], v[192:195], a[140:143], a[68:71]
s_barrier
ds_read_b64_tr_b16 v[218:219], v10 offset:30720
v_add_u32 v8, s26, v4
v_mfma_f32_16x16x32_bf16 a[76:79], v[196:199], a[140:143], a[76:79]
s_mov_b32 s20, 0
ds_read_b64_tr_b16 v[220:221], v11 offset:28672
v_mad_u32_u24 v14, v12, s25, v13
v_mfma_f32_16x16x32_bf16 a[72:75], v[196:199], a[136:139], a[72:75]
s_waitcnt lgkmcnt(7)
ds_read_b64_tr_b16 v[222:223], v11 offset:30720
v_add_u32 v9, s26, v5
v_mfma_f32_16x16x32_bf16 a[80:83], v[200:203], a[136:139], a[80:83]
s_mov_b32 m0, s23
ds_read_b64_tr_b16 v[192:193], v8 offset:0
ds_read_b64_tr_b16 v[194:195], v8 offset:2048
v_add_u32 v10, s26, v6
v_mfma_f32_16x16x32_bf16 a[84:87], v[200:203], a[140:143], a[84:87]
buffer_load_dwordx4 v14, s[16:19], s20 offen sc1 lds
v_mfma_f32_16x16x32_bf16 a[92:95], v[204:207], a[140:143], a[92:95]
s_add_u32 s22, s22, 1
ds_read_b64_tr_b16 v[196:197], v9 offset:0
v_add_u32 v11, s26, v7
v_mfma_f32_16x16x32_bf16 a[88:91], v[204:207], a[136:139], a[88:91]
s_waitcnt lgkmcnt(7)
ds_read_b64_tr_b16 v[198:199], v9 offset:2048
v_mfma_f32_16x16x32_bf16 a[96:99], v[208:211], a[136:139], a[96:99]
s_lshl_b32 s21, s25, 3
ds_read_b64_tr_b16 v[200:201], v10 offset:0
v_mfma_f32_16x16x32_bf16 a[100:103], v[208:211], a[140:143], a[100:103]
s_mov_b32 s27, s24
ds_read_b64_tr_b16 v[202:203], v10 offset:2048
v_mfma_f32_16x16x32_bf16 a[108:111], v[212:215], a[140:143], a[108:111]
s_add_u32 s20, s20, s21
ds_read_b64_tr_b16 v[204:205], v11 offset:0
v_mfma_f32_16x16x32_bf16 a[104:107], v[212:215], a[136:139], a[104:107]
s_waitcnt lgkmcnt(7)
ds_read_b64_tr_b16 v[206:207], v11 offset:2048
v_mfma_f32_16x16x32_bf16 a[112:115], v[216:219], a[136:139], a[112:115]
s_add_u32 m0, s23, 1024
ds_read_b64_tr_b16 v[208:209], v8 offset:8192
ds_read_b64_tr_b16 v[210:211], v8 offset:10240
v_mfma_f32_16x16x32_bf16 a[116:119], v[216:219], a[140:143], a[116:119]
buffer_load_dwordx4 v14, s[16:19], s20 offen sc1 lds
v_mfma_f32_16x16x32_bf16 a[124:127], v[220:223], a[140:143], a[124:127]
s_mov_b32 s24, s26
ds_read_b64_tr_b16 v[212:213], v9 offset:8192
v_mfma_f32_16x16x32_bf16 a[120:123], v[220:223], a[136:139], a[120:123]
s_waitcnt lgkmcnt(7)
ds_read_b64_tr_b16 v[214:215], v9 offset:10240
v_mfma_f32_16x16x32_bf16 a[0:3], v[192:195], a[144:147], a[0:3]
ds_read_b64_tr_b16 v[216:217], v10 offset:8192
v_mfma_f32_16x16x32_bf16 a[4:7], v[192:195], a[148:151], a[4:7]
ds_read_b64_tr_b16 v[218:219], v10 offset:10240
v_mfma_f32_16x16x32_bf16 a[12:15], v[196:199], a[148:151], a[12:15]
s_add_u32 s20, s20, s21
ds_read_b64_tr_b16 v[220:221], v11 offset:8192
v_mfma_f32_16x16x32_bf16 a[8:11], v[196:199], a[144:147], a[8:11]
s_waitcnt lgkmcnt(7)
ds_read_b64_tr_b16 v[222:223], v11 offset:10240
v_mfma_f32_16x16x32_bf16 a[16:19], v[200:203], a[144:147], a[16:19]
s_add_u32 m0, s23, 2048
ds_read_b64_tr_b16 v[192:193], v8 offset:16384
ds_read_b64_tr_b16 v[194:195], v8 offset:18432
v_mfma_f32_16x16x32_bf16 a[20:23], v[200:203], a[148:151], a[20:23]
buffer_load_dwordx4 v14, s[16:19], s20 offen sc1 lds
v_mfma_f32_16x16x32_bf16 a[28:31], v[204:207], a[148:151], a[28:31]
ds_read_b64_tr_b16 v[196:197], v9 offset:16384
v_mfma_f32_16x16x32_bf16 a[24:27], v[204:207], a[144:147], a[24:27]
s_waitcnt lgkmcnt(7)
ds_read_b64_tr_b16 v[198:199], v9 offset:18432
v_mfma_f32_16x16x32_bf16 a[32:35], v[208:211], a[144:147], a[32:35]
ds_read_b64_tr_b16 v[200:201], v10 offset:16384
v_mfma_f32_16x16x32_bf16 a[36:39], v[208:211], a[148:151], a[36:39]
ds_read_b64_tr_b16 v[202:203], v10 offset:18432
buffer_store_dwordx4 v[76:79], v0, s[36:39], s66 offen offset:3072 sc1
v_mfma_f32_16x16x32_bf16 a[44:47], v[212:215], a[148:151], a[44:47]
s_add_u32 s20, s20, s21
ds_read_b64_tr_b16 v[204:205], v11 offset:16384
v_mfma_f32_16x16x32_bf16 a[40:43], v[212:215], a[144:147], a[40:43]
s_waitcnt lgkmcnt(7)
ds_read_b64_tr_b16 v[206:207], v11 offset:18432
v_mfma_f32_16x16x32_bf16 a[48:51], v[216:219], a[144:147], a[48:51]
s_add_u32 m0, s23, 3072
ds_read_b64_tr_b16 v[208:209], v8 offset:24576
ds_read_b64_tr_b16 v[210:211], v8 offset:26624
v_mfma_f32_16x16x32_bf16 a[52:55], v[216:219], a[148:151], a[52:55]
buffer_load_dwordx4 v14, s[16:19], s20 offen sc1 lds
v_mfma_f32_16x16x32_bf16 a[60:63], v[220:223], a[148:151], a[60:63]
ds_read_b64_tr_b16 v[212:213], v9 offset:24576
v_mfma_f32_16x16x32_bf16 a[56:59], v[220:223], a[144:147], a[56:59]
s_waitcnt lgkmcnt(7)
ds_read_b64_tr_b16 v[214:215], v9 offset:26624
v_mfma_f32_16x16x32_bf16 a[64:67], v[192:195], a[144:147], a[64:67]
ds_read_b64_tr_b16 v[216:217], v10 offset:24576
v_mfma_f32_16x16x32_bf16 a[68:71], v[192:195], a[148:151], a[68:71]
ds_read_b64_tr_b16 v[218:219], v10 offset:26624
v_mfma_f32_16x16x32_bf16 a[76:79], v[196:199], a[148:151], a[76:79]
s_add_u32 s20, s20, s21
ds_read_b64_tr_b16 v[220:221], v11 offset:24576
v_mfma_f32_16x16x32_bf16 a[72:75], v[196:199], a[144:147], a[72:75]
s_waitcnt lgkmcnt(7)
ds_read_b64_tr_b16 v[222:223], v11 offset:26624
v_mfma_f32_16x16x32_bf16 a[80:83], v[200:203], a[144:147], a[80:83]
s_add_u32 m0, s23, 4096
ds_read_b64_tr_b16 v[192:193], v8 offset:4096
ds_read_b64_tr_b16 v[194:195], v8 offset:6144
v_mfma_f32_16x16x32_bf16 a[84:87], v[200:203], a[148:151], a[84:87]
buffer_load_dwordx4 v14, s[16:19], s20 offen sc1 lds
v_mfma_f32_16x16x32_bf16 a[92:95], v[204:207], a[148:151], a[92:95]
ds_read_b64_tr_b16 v[196:197], v9 offset:4096
v_mfma_f32_16x16x32_bf16 a[88:91], v[204:207], a[144:147], a[88:91]
s_waitcnt lgkmcnt(7)
ds_read_b64_tr_b16 v[198:199], v9 offset:6144
v_mfma_f32_16x16x32_bf16 a[96:99], v[208:211], a[144:147], a[96:99]
ds_read_b64_tr_b16 v[200:201], v10 offset:4096
v_mfma_f32_16x16x32_bf16 a[100:103], v[208:211], a[148:151], a[100:103]
ds_read_b64_tr_b16 v[202:203], v10 offset:6144
buffer_store_dwordx4 v[80:83], v0, s[36:39], s67 offen offset:0 sc1
v_mfma_f32_16x16x32_bf16 a[108:111], v[212:215], a[148:151], a[108:111]
s_add_u32 s20, s20, s21
ds_read_b64_tr_b16 v[204:205], v11 offset:4096
v_mfma_f32_16x16x32_bf16 a[104:107], v[212:215], a[144:147], a[104:107]
s_waitcnt lgkmcnt(7)
ds_read_b64_tr_b16 v[206:207], v11 offset:6144
v_mfma_f32_16x16x32_bf16 a[112:115], v[216:219], a[144:147], a[112:115]
s_add_u32 m0, s23, 5120
ds_read_b64_tr_b16 v[208:209], v8 offset:12288
ds_read_b64_tr_b16 v[210:211], v8 offset:14336
v_mfma_f32_16x16x32_bf16 a[116:119], v[216:219], a[148:151], a[116:119]
buffer_load_dwordx4 v14, s[16:19], s20 offen sc1 lds
v_mfma_f32_16x16x32_bf16 a[124:127], v[220:223], a[148:151], a[124:127]
ds_read_b64_tr_b16 v[212:213], v9 offset:12288
v_mfma_f32_16x16x32_bf16 a[120:123], v[220:223], a[144:147], a[120:123]
s_waitcnt lgkmcnt(7)
ds_read_b64_tr_b16 v[214:215], v9 offset:14336
v_mfma_f32_16x16x32_bf16 a[0:3], v[192:195], a[152:155], a[0:3]
ds_read_b64_tr_b16 v[216:217], v10 offset:12288
v_mfma_f32_16x16x32_bf16 a[4:7], v[192:195], a[156:159], a[4:7]
ds_read_b64_tr_b16 v[218:219], v10 offset:14336
v_mfma_f32_16x16x32_bf16 a[12:15], v[196:199], a[156:159], a[12:15]
s_add_u32 s20, s20, s21
ds_read_b64_tr_b16 v[220:221], v11 offset:12288
v_mfma_f32_16x16x32_bf16 a[8:11], v[196:199], a[152:155], a[8:11]
s_waitcnt lgkmcnt(7)
ds_read_b64_tr_b16 v[222:223], v11 offset:14336
v_mfma_f32_16x16x32_bf16 a[16:19], v[200:203], a[152:155], a[16:19]
s_add_u32 m0, s23, 6144
ds_read_b64_tr_b16 v[192:193], v8 offset:20480
ds_read_b64_tr_b16 v[194:195], v8 offset:22528
v_mfma_f32_16x16x32_bf16 a[20:23], v[200:203], a[156:159], a[20:23]
buffer_load_dwordx4 v14, s[16:19], s20 offen sc1 lds
v_mfma_f32_16x16x32_bf16 a[28:31], v[204:207], a[156:159], a[28:31]
s_add_u32 s26, s24, 32768
ds_read_b64_tr_b16 v[196:197], v9 offset:20480
v_mfma_f32_16x16x32_bf16 a[24:27], v[204:207], a[152:155], a[24:27]
s_waitcnt lgkmcnt(7)
ds_read_b64_tr_b16 v[198:199], v9 offset:22528
v_mfma_f32_16x16x32_bf16 a[32:35], v[208:211], a[152:155], a[32:35]
s_sub_u32 s35, s26, 163840
ds_read_b64_tr_b16 v[200:201], v10 offset:20480
v_mfma_f32_16x16x32_bf16 a[36:39], v[208:211], a[156:159], a[36:39]
s_cselect_b32 s26, s26, s35
ds_read_b64_tr_b16 v[202:203], v10 offset:22528
buffer_store_dwordx4 v[84:87], v0, s[36:39], s67 offen offset:1024 sc1
v_mfma_f32_16x16x32_bf16 a[44:47], v[212:215], a[156:159], a[44:47]
s_add_u32 s20, s20, s21
ds_read_b64_tr_b16 v[204:205], v11 offset:20480
v_mfma_f32_16x16x32_bf16 a[40:43], v[212:215], a[152:155], a[40:43]
s_waitcnt lgkmcnt(7)
ds_read_b64_tr_b16 v[206:207], v11 offset:22528
v_mfma_f32_16x16x32_bf16 a[48:51], v[216:219], a[152:155], a[48:51]
s_add_u32 m0, s23, 7168
ds_read_b64_tr_b16 v[208:209], v8 offset:28672
ds_read_b64_tr_b16 v[210:211], v8 offset:30720
v_mfma_f32_16x16x32_bf16 a[52:55], v[216:219], a[156:159], a[52:55]
buffer_load_dwordx4 v14, s[16:19], s20 offen sc1 lds
v_mfma_f32_16x16x32_bf16 a[60:63], v[220:223], a[156:159], a[60:63]
s_add_u32 s23, s27, s33
ds_read_b64_tr_b16 v[212:213], v9 offset:28672
v_readlane_b32 s16, v16, s22
v_mfma_f32_16x16x32_bf16 a[56:59], v[220:223], a[152:155], a[56:59]
s_waitcnt lgkmcnt(7)
ds_read_b64_tr_b16 v[214:215], v9 offset:30720
v_readlane_b32 s17, v17, s22
v_mfma_f32_16x16x32_bf16 a[64:67], v[192:195], a[152:155], a[64:67]
s_waitcnt vmcnt(20)
ds_read_b64_tr_b16 v[216:217], v10 offset:28672
v_readlane_b32 s25, v18, s22
v_mfma_f32_16x16x32_bf16 a[68:71], v[192:195], a[156:159], a[68:71]
s_barrier
ds_read_b64_tr_b16 v[218:219], v10 offset:30720
v_add_u32 v8, s26, v4
v_mfma_f32_16x16x32_bf16 a[76:79], v[196:199], a[156:159], a[76:79]
s_mov_b32 s20, 0
ds_read_b64_tr_b16 v[220:221], v11 offset:28672
v_mad_u32_u24 v14, v12, s25, v13
v_mfma_f32_16x16x32_bf16 a[72:75], v[196:199], a[152:155], a[72:75]
s_waitcnt lgkmcnt(7)
ds_read_b64_tr_b16 v[222:223], v11 offset:30720
v_add_u32 v9, s26, v5
v_mfma_f32_16x16x32_bf16 a[80:83], v[200:203], a[152:155], a[80:83]
s_mov_b32 m0, s23
ds_read_b64_tr_b16 v[192:193], v8 offset:0
ds_read_b64_tr_b16 v[194:195], v8 offset:2048
v_add_u32 v10, s26, v6
v_mfma_f32_16x16x32_bf16 a[84:87], v[200:203], a[156:159], a[84:87]
buffer_load_dwordx4 v14, s[16:19], s20 offen sc1 lds
v_mfma_f32_16x16x32_bf16 a[92:95], v[204:207], a[156:159], a[92:95]
s_add_u32 s22, s22, 1
ds_read_b64_tr_b16 v[196:197], v9 offset:0
v_add_u32 v11, s26, v7
v_mfma_f32_16x16x32_bf16 a[88:91], v[204:207], a[152:155], a[88:91]
s_waitcnt lgkmcnt(7)
ds_read_b64_tr_b16 v[198:199], v9 offset:2048
buffer_store_dword v42, v43, s[36:39], s56 offen sc1
v_mfma_f32_16x16x32_bf16 a[96:99], v[208:211], a[152:155], a[96:99]
s_lshl_b32 s21, s25, 3
ds_read_b64_tr_b16 v[200:201], v10 offset:0
v_mfma_f32_16x16x32_bf16 a[100:103], v[208:211], a[156:159], a[100:103]
s_mov_b32 s27, s24
ds_read_b64_tr_b16 v[202:203], v10 offset:2048
v_mfma_f32_16x16x32_bf16 a[108:111], v[212:215], a[156:159], a[108:111]
s_add_u32 s20, s20, s21
ds_read_b64_tr_b16 v[204:205], v11 offset:0
v_mfma_f32_16x16x32_bf16 a[104:107], v[212:215], a[152:155], a[104:107]
s_waitcnt lgkmcnt(7)
ds_read_b64_tr_b16 v[206:207], v11 offset:2048
v_mfma_f32_16x16x32_bf16 a[112:115], v[216:219], a[152:155], a[112:115]
s_add_u32 m0, s23, 1024
ds_read_b64_tr_b16 v[208:209], v8 offset:8192
ds_read_b64_tr_b16 v[210:211], v8 offset:10240
v_mfma_f32_16x16x32_bf16 a[116:119], v[216:219], a[156:159], a[116:119]
buffer_load_dwordx4 v14, s[16:19], s20 offen sc1 lds
v_mfma_f32_16x16x32_bf16 a[124:127], v[220:223], a[156:159], a[124:127]
s_mov_b32 s24, s26
ds_read_b64_tr_b16 v[212:213], v9 offset:8192
v_mfma_f32_16x16x32_bf16 a[120:123], v[220:223], a[152:155], a[120:123]
s_waitcnt lgkmcnt(7)
ds_read_b64_tr_b16 v[214:215], v9 offset:10240
v_mfma_f32_16x16x32_bf16 a[0:3], v[192:195], a[160:163], a[0:3]
ds_read_b64_tr_b16 v[216:217], v10 offset:8192
v_mfma_f32_16x16x32_bf16 a[4:7], v[192:195], a[164:167], a[4:7]
ds_read_b64_tr_b16 v[218:219], v10 offset:10240
v_mfma_f32_16x16x32_bf16 a[12:15], v[196:199], a[164:167], a[12:15]
s_add_u32 s20, s20, s21
ds_read_b64_tr_b16 v[220:221], v11 offset:8192
v_mfma_f32_16x16x32_bf16 a[8:11], v[196:199], a[160:163], a[8:11]
s_waitcnt lgkmcnt(7)
ds_read_b64_tr_b16 v[222:223], v11 offset:10240
v_mfma_f32_16x16x32_bf16 a[16:19], v[200:203], a[160:163], a[16:19]
s_add_u32 m0, s23, 2048
ds_read_b64_tr_b16 v[192:193], v8 offset:16384
ds_read_b64_tr_b16 v[194:195], v8 offset:18432
v_mfma_f32_16x16x32_bf16 a[20:23], v[200:203], a[164:167], a[20:23]
buffer_load_dwordx4 v14, s[16:19], s20 offen sc1 lds
v_mfma_f32_16x16x32_bf16 a[28:31], v[204:207], a[164:167], a[28:31]
ds_read_b64_tr_b16 v[196:197], v9 offset:16384
v_mfma_f32_16x16x32_bf16 a[24:27], v[204:207], a[160:163], a[24:27]
s_waitcnt lgkmcnt(7)
ds_read_b64_tr_b16 v[198:199], v9 offset:18432
v_mfma_f32_16x16x32_bf16 a[32:35], v[208:211], a[160:163], a[32:35]
ds_read_b64_tr_b16 v[200:201], v10 offset:16384
v_mfma_f32_16x16x32_bf16 a[36:39], v[208:211], a[164:167], a[36:39]
ds_read_b64_tr_b16 v[202:203], v10 offset:18432
buffer_store_dwordx4 v[88:91], v0, s[36:39], s67 offen offset:2048 sc1
v_mfma_f32_16x16x32_bf16 a[44:47], v[212:215], a[164:167], a[44:47]
s_add_u32 s20, s20, s21
ds_read_b64_tr_b16 v[204:205], v11 offset:16384
v_mfma_f32_16x16x32_bf16 a[40:43], v[212:215], a[160:163], a[40:43]
s_waitcnt lgkmcnt(7)
ds_read_b64_tr_b16 v[206:207], v11 offset:18432
v_mfma_f32_16x16x32_bf16 a[48:51], v[216:219], a[160:163], a[48:51]
s_add_u32 m0, s23, 3072
ds_read_b64_tr_b16 v[208:209], v8 offset:24576
ds_read_b64_tr_b16 v[210:211], v8 offset:26624
v_mfma_f32_16x16x32_bf16 a[52:55], v[216:219], a[164:167], a[52:55]
buffer_load_dwordx4 v14, s[16:19], s20 offen sc1 lds
v_mfma_f32_16x16x32_bf16 a[60:63], v[220:223], a[164:167], a[60:63]
ds_read_b64_tr_b16 v[212:213], v9 offset:24576
v_mfma_f32_16x16x32_bf16 a[56:59], v[220:223], a[160:163], a[56:59]
s_waitcnt lgkmcnt(7)
ds_read_b64_tr_b16 v[214:215], v9 offset:26624
v_mfma_f32_16x16x32_bf16 a[64:67], v[192:195], a[160:163], a[64:67]
ds_read_b64_tr_b16 v[216:217], v10 offset:24576
v_mfma_f32_16x16x32_bf16 a[68:71], v[192:195], a[164:167], a[68:71]
ds_read_b64_tr_b16 v[218:219], v10 offset:26624
v_mfma_f32_16x16x32_bf16 a[76:79], v[196:199], a[164:167], a[76:79]
s_add_u32 s20, s20, s21
ds_read_b64_tr_b16 v[220:221], v11 offset:24576
v_mfma_f32_16x16x32_bf16 a[72:75], v[196:199], a[160:163], a[72:75]
s_waitcnt lgkmcnt(7)
ds_read_b64_tr_b16 v[222:223], v11 offset:26624
v_mfma_f32_16x16x32_bf16 a[80:83], v[200:203], a[160:163], a[80:83]
s_add_u32 m0, s23, 4096
ds_read_b64_tr_b16 v[192:193], v8 offset:4096
ds_read_b64_tr_b16 v[194:195], v8 offset:6144
v_mfma_f32_16x16x32_bf16 a[84:87], v[200:203], a[164:167], a[84:87]
buffer_load_dwordx4 v14, s[16:19], s20 offen sc1 lds
v_mfma_f32_16x16x32_bf16 a[92:95], v[204:207], a[164:167], a[92:95]
ds_read_b64_tr_b16 v[196:197], v9 offset:4096
v_mfma_f32_16x16x32_bf16 a[88:91], v[204:207], a[160:163], a[88:91]
s_waitcnt lgkmcnt(7)
ds_read_b64_tr_b16 v[198:199], v9 offset:6144
v_mfma_f32_16x16x32_bf16 a[96:99], v[208:211], a[160:163], a[96:99]
ds_read_b64_tr_b16 v[200:201], v10 offset:4096
v_mfma_f32_16x16x32_bf16 a[100:103], v[208:211], a[164:167], a[100:103]
ds_read_b64_tr_b16 v[202:203], v10 offset:6144
buffer_store_dwordx4 v[92:95], v0, s[36:39], s67 offen offset:3072 sc1
v_mfma_f32_16x16x32_bf16 a[108:111], v[212:215], a[164:167], a[108:111]
s_add_u32 s20, s20, s21
ds_read_b64_tr_b16 v[204:205], v11 offset:4096
v_mfma_f32_16x16x32_bf16 a[104:107], v[212:215], a[160:163], a[104:107]
s_waitcnt lgkmcnt(7)
ds_read_b64_tr_b16 v[206:207], v11 offset:6144
v_mfma_f32_16x16x32_bf16 a[112:115], v[216:219], a[160:163], a[112:115]
s_add_u32 m0, s23, 5120
ds_read_b64_tr_b16 v[208:209], v8 offset:12288
ds_read_b64_tr_b16 v[210:211], v8 offset:14336
v_mfma_f32_16x16x32_bf16 a[116:119], v[216:219], a[164:167], a[116:119]
buffer_load_dwordx4 v14, s[16:19], s20 offen sc1 lds
v_mfma_f32_16x16x32_bf16 a[124:127], v[220:223], a[164:167], a[124:127]
ds_read_b64_tr_b16 v[212:213], v9 offset:12288
v_mfma_f32_16x16x32_bf16 a[120:123], v[220:223], a[160:163], a[120:123]
s_waitcnt lgkmcnt(7)
ds_read_b64_tr_b16 v[214:215], v9 offset:14336
v_mfma_f32_16x16x32_bf16 a[0:3], v[192:195], a[168:171], a[0:3]
ds_read_b64_tr_b16 v[216:217], v10 offset:12288
v_mfma_f32_16x16x32_bf16 a[4:7], v[192:195], a[172:175], a[4:7]
ds_read_b64_tr_b16 v[218:219], v10 offset:14336
v_mfma_f32_16x16x32_bf16 a[12:15], v[196:199], a[172:175], a[12:15]
s_add_u32 s20, s20, s21
ds_read_b64_tr_b16 v[220:221], v11 offset:12288
v_mfma_f32_16x16x32_bf16 a[8:11], v[196:199], a[168:171], a[8:11]
s_waitcnt lgkmcnt(7)
ds_read_b64_tr_b16 v[222:223], v11 offset:14336
v_mfma_f32_16x16x32_bf16 a[16:19], v[200:203], a[168:171], a[16:19]
s_add_u32 m0, s23, 6144
ds_read_b64_tr_b16 v[192:193], v8 offset:20480
ds_read_b64_tr_b16 v[194:195], v8 offset:22528
v_mfma_f32_16x16x32_bf16 a[20:23], v[200:203], a[172:175], a[20:23]
buffer_load_dwordx4 v14, s[16:19], s20 offen sc1 lds
v_mfma_f32_16x16x32_bf16 a[28:31], v[204:207], a[172:175], a[28:31]
s_add_u32 s26, s24, 32768
ds_read_b64_tr_b16 v[196:197], v9 offset:20480
v_mfma_f32_16x16x32_bf16 a[24:27], v[204:207], a[168:171], a[24:27]
s_waitcnt lgkmcnt(7)
ds_read_b64_tr_b16 v[198:199], v9 offset:22528
v_mfma_f32_16x16x32_bf16 a[32:35], v[208:211], a[168:171], a[32:35]
s_sub_u32 s35, s26, 163840
ds_read_b64_tr_b16 v[200:201], v10 offset:20480
v_mfma_f32_16x16x32_bf16 a[36:39], v[208:211], a[172:175], a[36:39]
s_cselect_b32 s26, s26, s35
ds_read_b64_tr_b16 v[202:203], v10 offset:22528
buffer_store_dwordx4 v[96:99], v0, s[36:39], s50 offen offset:0 sc1
v_mfma_f32_16x16x32_bf16 a[44:47], v[212:215], a[172:175], a[44:47]
s_add_u32 s20, s20, s21
ds_read_b64_tr_b16 v[204:205], v11 offset:20480
v_mfma_f32_16x16x32_bf16 a[40:43], v[212:215], a[168:171], a[40:43]
s_waitcnt lgkmcnt(7)
ds_read_b64_tr_b16 v[206:207], v11 offset:22528
v_mfma_f32_16x16x32_bf16 a[48:51], v[216:219], a[168:171], a[48:51]
s_add_u32 m0, s23, 7168
ds_read_b64_tr_b16 v[208:209], v8 offset:28672
ds_read_b64_tr_b16 v[210:211], v8 offset:30720
v_mfma_f32_16x16x32_bf16 a[52:55], v[216:219], a[172:175], a[52:55]
buffer_load_dwordx4 v14, s[16:19], s20 offen sc1 lds
v_mfma_f32_16x16x32_bf16 a[60:63], v[220:223], a[172:175], a[60:63]
s_add_u32 s23, s27, s33
ds_read_b64_tr_b16 v[212:213], v9 offset:28672
v_readlane_b32 s16, v16, s22
v_mfma_f32_16x16x32_bf16 a[56:59], v[220:223], a[168:171], a[56:59]
s_waitcnt lgkmcnt(7)
ds_read_b64_tr_b16 v[214:215], v9 offset:30720
v_readlane_b32 s17, v17, s22
v_mfma_f32_16x16x32_bf16 a[64:67], v[192:195], a[168:171], a[64:67]
s_waitcnt vmcnt(23)
ds_read_b64_tr_b16 v[216:217], v10 offset:28672
v_readlane_b32 s25, v18, s22
v_mfma_f32_16x16x32_bf16 a[68:71], v[192:195], a[172:175], a[68:71]
s_barrier
ds_read_b64_tr_b16 v[218:219], v10 offset:30720
v_add_u32 v8, s26, v4
v_mfma_f32_16x16x32_bf16 a[76:79], v[196:199], a[172:175], a[76:79]
s_mov_b32 s20, 0
ds_read_b64_tr_b16 v[220:221], v11 offset:28672
v_mad_u32_u24 v14, v12, s25, v13
v_mfma_f32_16x16x32_bf16 a[72:75], v[196:199], a[168:171], a[72:75]
s_waitcnt lgkmcnt(7)
ds_read_b64_tr_b16 v[222:223], v11 offset:30720
v_add_u32 v9, s26, v5
v_mfma_f32_16x16x32_bf16 a[80:83], v[200:203], a[168:171], a[80:83]
s_mov_b32 m0, s23
ds_read_b64_tr_b16 v[192:193], v8 offset:0
ds_read_b64_tr_b16 v[194:195], v8 offset:2048
v_add_u32 v10, s26, v6
v_mfma_f32_16x16x32_bf16 a[84:87], v[200:203], a[172:175], a[84:87]
buffer_load_dwordx4 v14, s[16:19], s20 offen sc1 lds
v_mfma_f32_16x16x32_bf16 a[92:95], v[204:207], a[172:175], a[92:95]
s_add_u32 s22, s22, 1
ds_read_b64_tr_b16 v[196:197], v9 offset:0
v_add_u32 v11, s26, v7
v_mfma_f32_16x16x32_bf16 a[88:91], v[204:207], a[168:171], a[88:91]
s_waitcnt lgkmcnt(7)
ds_read_b64_tr_b16 v[198:199], v9 offset:2048
v_mfma_f32_16x16x32_bf16 a[96:99], v[208:211], a[168:171], a[96:99]
s_lshl_b32 s21, s25, 3
ds_read_b64_tr_b16 v[200:201], v10 offset:0
v_mfma_f32_16x16x32_bf16 a[100:103], v[208:211], a[172:175], a[100:103]
s_mov_b32 s27, s24
ds_read_b64_tr_b16 v[202:203], v10 offset:2048
v_mfma_f32_16x16x32_bf16 a[108:111], v[212:215], a[172:175], a[108:111]
s_add_u32 s20, s20, s21
ds_read_b64_tr_b16 v[204:205], v11 offset:0
v_mfma_f32_16x16x32_bf16 a[104:107], v[212:215], a[168:171], a[104:107]
s_waitcnt lgkmcnt(7)
ds_read_b64_tr_b16 v[206:207], v11 offset:2048
v_mfma_f32_16x16x32_bf16 a[112:115], v[216:219], a[168:171], a[112:115]
s_add_u32 m0, s23, 1024
ds_read_b64_tr_b16 v[208:209], v8 offset:8192
ds_read_b64_tr_b16 v[210:211], v8 offset:10240
v_mfma_f32_16x16x32_bf16 a[116:119], v[216:219], a[172:175], a[116:119]
buffer_load_dwordx4 v14, s[16:19], s20 offen sc1 lds
v_mfma_f32_16x16x32_bf16 a[124:127], v[220:223], a[172:175], a[124:127]
s_mov_b32 s24, s26
ds_read_b64_tr_b16 v[212:213], v9 offset:8192
v_mfma_f32_16x16x32_bf16 a[120:123], v[220:223], a[168:171], a[120:123]
s_waitcnt lgkmcnt(7)
ds_read_b64_tr_b16 v[214:215], v9 offset:10240
v_mfma_f32_16x16x32_bf16 a[0:3], v[192:195], a[176:179], a[0:3]
ds_read_b64_tr_b16 v[216:217], v10 offset:8192
v_mfma_f32_16x16x32_bf16 a[4:7], v[192:195], a[180:183], a[4:7]
ds_read_b64_tr_b16 v[218:219], v10 offset:10240
v_mfma_f32_16x16x32_bf16 a[12:15], v[196:199], a[180:183], a[12:15]
s_add_u32 s20, s20, s21
ds_read_b64_tr_b16 v[220:221], v11 offset:8192
v_mfma_f32_16x16x32_bf16 a[8:11], v[196:199], a[176:179], a[8:11]
s_waitcnt lgkmcnt(7)
ds_read_b64_tr_b16 v[222:223], v11 offset:10240
v_mfma_f32_16x16x32_bf16 a[16:19], v[200:203], a[176:179], a[16:19]
s_add_u32 m0, s23, 2048
ds_read_b64_tr_b16 v[192:193], v8 offset:16384
ds_read_b64_tr_b16 v[194:195], v8 offset:18432
v_mfma_f32_16x16x32_bf16 a[20:23], v[200:203], a[180:183], a[20:23]
buffer_load_dwordx4 v14, s[16:19], s20 offen sc1 lds
v_mfma_f32_16x16x32_bf16 a[28:31], v[204:207], a[180:183], a[28:31]
ds_read_b64_tr_b16 v[196:197], v9 offset:16384
v_mfma_f32_16x16x32_bf16 a[24:27], v[204:207], a[176:179], a[24:27]
s_waitcnt lgkmcnt(7)
ds_read_b64_tr_b16 v[198:199], v9 offset:18432
v_mfma_f32_16x16x32_bf16 a[32:35], v[208:211], a[176:179], a[32:35]
ds_read_b64_tr_b16 v[200:201], v10 offset:16384
v_mfma_f32_16x16x32_bf16 a[36:39], v[208:211], a[180:183], a[36:39]
ds_read_b64_tr_b16 v[202:203], v10 offset:18432
buffer_store_dwordx4 v[100:103], v0, s[36:39], s50 offen offset:1024 sc1
v_mfma_f32_16x16x32_bf16 a[44:47], v[212:215], a[180:183], a[44:47]
s_add_u32 s20, s20, s21
ds_read_b64_tr_b16 v[204:205], v11 offset:16384
v_mfma_f32_16x16x32_bf16 a[40:43], v[212:215], a[176:179], a[40:43]
s_waitcnt lgkmcnt(7)
ds_read_b64_tr_b16 v[206:207], v11 offset:18432
v_mfma_f32_16x16x32_bf16 a[48:51], v[216:219], a[176:179], a[48:51]
s_add_u32 m0, s23, 3072
ds_read_b64_tr_b16 v[208:209], v8 offset:24576
ds_read_b64_tr_b16 v[210:211], v8 offset:26624
v_mfma_f32_16x16x32_bf16 a[52:55], v[216:219], a[180:183], a[52:55]
buffer_load_dwordx4 v14, s[16:19], s20 offen sc1 lds
v_mfma_f32_16x16x32_bf16 a[60:63], v[220:223], a[180:183], a[60:63]
ds_read_b64_tr_b16 v[212:213], v9 offset:24576
v_mfma_f32_16x16x32_bf16 a[56:59], v[220:223], a[176:179], a[56:59]
s_waitcnt lgkmcnt(7)
ds_read_b64_tr_b16 v[214:215], v9 offset:26624
v_mfma_f32_16x16x32_bf16 a[64:67], v[192:195], a[176:179], a[64:67]
ds_read_b64_tr_b16 v[216:217], v10 offset:24576
v_mfma_f32_16x16x32_bf16 a[68:71], v[192:195], a[180:183], a[68:71]
ds_read_b64_tr_b16 v[218:219], v10 offset:26624
v_mfma_f32_16x16x32_bf16 a[76:79], v[196:199], a[180:183], a[76:79]
s_add_u32 s20, s20, s21
ds_read_b64_tr_b16 v[220:221], v11 offset:24576
v_mfma_f32_16x16x32_bf16 a[72:75], v[196:199], a[176:179], a[72:75]
s_waitcnt lgkmcnt(7)
ds_read_b64_tr_b16 v[222:223], v11 offset:26624
v_mfma_f32_16x16x32_bf16 a[80:83], v[200:203], a[176:179], a[80:83]
s_add_u32 m0, s23, 4096
ds_read_b64_tr_b16 v[192:193], v8 offset:4096
ds_read_b64_tr_b16 v[194:195], v8 offset:6144
v_mfma_f32_16x16x32_bf16 a[84:87], v[200:203], a[180:183], a[84:87]
buffer_load_dwordx4 v14, s[16:19], s20 offen sc1 lds
v_mfma_f32_16x16x32_bf16 a[92:95], v[204:207], a[180:183], a[92:95]
ds_read_b64_tr_b16 v[196:197], v9 offset:4096
v_mfma_f32_16x16x32_bf16 a[88:91], v[204:207], a[176:179], a[88:91]
s_waitcnt lgkmcnt(7)
ds_read_b64_tr_b16 v[198:199], v9 offset:6144
v_mfma_f32_16x16x32_bf16 a[96:99], v[208:211], a[176:179], a[96:99]
ds_read_b64_tr_b16 v[200:201], v10 offset:4096
v_mfma_f32_16x16x32_bf16 a[100:103], v[208:211], a[180:183], a[100:103]
ds_read_b64_tr_b16 v[202:203], v10 offset:6144
buffer_store_dwordx4 v[104:107], v0, s[36:39], s50 offen offset:2048 sc1
v_mfma_f32_16x16x32_bf16 a[108:111], v[212:215], a[180:183], a[108:111]
s_add_u32 s20, s20, s21
ds_read_b64_tr_b16 v[204:205], v11 offset:4096
v_mfma_f32_16x16x32_bf16 a[104:107], v[212:215], a[176:179], a[104:107]
s_waitcnt lgkmcnt(7)
ds_read_b64_tr_b16 v[206:207], v11 offset:6144
v_mfma_f32_16x16x32_bf16 a[112:115], v[216:219], a[176:179], a[112:115]
s_add_u32 m0, s23, 5120
ds_read_b64_tr_b16 v[208:209], v8 offset:12288
ds_read_b64_tr_b16 v[210:211], v8 offset:14336
v_mfma_f32_16x16x32_bf16 a[116:119], v[216:219], a[180:183], a[116:119]
buffer_load_dwordx4 v14, s[16:19], s20 offen sc1 lds
v_mfma_f32_16x16x32_bf16 a[124:127], v[220:223], a[180:183], a[124:127]
ds_read_b64_tr_b16 v[212:213], v9 offset:12288
v_mfma_f32_16x16x32_bf16 a[120:123], v[220:223], a[176:179], a[120:123]
s_waitcnt lgkmcnt(7)
ds_read_b64_tr_b16 v[214:215], v9 offset:14336
v_mfma_f32_16x16x32_bf16 a[0:3], v[192:195], a[184:187], a[0:3]
ds_read_b64_tr_b16 v[216:217], v10 offset:12288
v_mfma_f32_16x16x32_bf16 a[4:7], v[192:195], a[188:191], a[4:7]
ds_read_b64_tr_b16 v[218:219], v10 offset:14336
v_mfma_f32_16x16x32_bf16 a[12:15], v[196:199], a[188:191], a[12:15]
s_add_u32 s20, s20, s21
ds_read_b64_tr_b16 v[220:221], v11 offset:12288
v_mfma_f32_16x16x32_bf16 a[8:11], v[196:199], a[184:187], a[8:11]
s_waitcnt lgkmcnt(7)
ds_read_b64_tr_b16 v[222:223], v11 offset:14336
v_mfma_f32_16x16x32_bf16 a[16:19], v[200:203], a[184:187], a[16:19]
s_add_u32 m0, s23, 6144
ds_read_b64_tr_b16 v[192:193], v8 offset:20480
ds_read_b64_tr_b16 v[194:195], v8 offset:22528
v_mfma_f32_16x16x32_bf16 a[20:23], v[200:203], a[188:191], a[20:23]
buffer_load_dwordx4 v14, s[16:19], s20 offen sc1 lds
v_mfma_f32_16x16x32_bf16 a[28:31], v[204:207], a[188:191], a[28:31]
s_add_u32 s26, s24, 32768
ds_read_b64_tr_b16 v[196:197], v9 offset:20480
v_mfma_f32_16x16x32_bf16 a[24:27], v[204:207], a[184:187], a[24:27]
s_waitcnt lgkmcnt(7)
ds_read_b64_tr_b16 v[198:199], v9 offset:22528
v_mfma_f32_16x16x32_bf16 a[32:35], v[208:211], a[184:187], a[32:35]
s_sub_u32 s35, s26, 163840
ds_read_b64_tr_b16 v[200:201], v10 offset:20480
v_mfma_f32_16x16x32_bf16 a[36:39], v[208:211], a[188:191], a[36:39]
s_cselect_b32 s26, s26, s35
ds_read_b64_tr_b16 v[202:203], v10 offset:22528
buffer_store_dwordx4 v[108:111], v0, s[36:39], s50 offen offset:3072 sc1
v_mfma_f32_16x16x32_bf16 a[44:47], v[212:215], a[188:191], a[44:47]
s_add_u32 s20, s20, s21
ds_read_b64_tr_b16 v[204:205], v11 offset:20480
v_mfma_f32_16x16x32_bf16 a[40:43], v[212:215], a[184:187], a[40:43]
s_waitcnt lgkmcnt(7)
ds_read_b64_tr_b16 v[206:207], v11 offset:22528
v_mfma_f32_16x16x32_bf16 a[48:51], v[216:219], a[184:187], a[48:51]
s_add_u32 m0, s23, 7168
ds_read_b64_tr_b16 v[208:209], v8 offset:28672
ds_read_b64_tr_b16 v[210:211], v8 offset:30720
v_mfma_f32_16x16x32_bf16 a[52:55], v[216:219], a[188:191], a[52:55]
buffer_load_dwordx4 v14, s[16:19], s20 offen sc1 lds
v_mfma_f32_16x16x32_bf16 a[60:63], v[220:223], a[188:191], a[60:63]
s_add_u32 s23, s27, s33
ds_read_b64_tr_b16 v[212:213], v9 offset:28672
v_readlane_b32 s16, v16, s22
v_mfma_f32_16x16x32_bf16 a[56:59], v[220:223], a[184:187], a[56:59]
s_waitcnt lgkmcnt(7)
ds_read_b64_tr_b16 v[214:215], v9 offset:30720
v_readlane_b32 s17, v17, s22
v_mfma_f32_16x16x32_bf16 a[64:67], v[192:195], a[184:187], a[64:67]
s_waitcnt vmcnt(23)
ds_read_b64_tr_b16 v[216:217], v10 offset:28672
v_readlane_b32 s25, v18, s22
v_mfma_f32_16x16x32_bf16 a[68:71], v[192:195], a[188:191], a[68:71]
s_barrier
ds_read_b64_tr_b16 v[218:219], v10 offset:30720
v_add_u32 v8, s26, v4
v_mfma_f32_16x16x32_bf16 a[76:79], v[196:199], a[188:191], a[76:79]
s_mov_b32 s20, 0
ds_read_b64_tr_b16 v[220:221], v11 offset:28672
v_mad_u32_u24 v14, v12, s25, v13
v_mfma_f32_16x16x32_bf16 a[72:75], v[196:199], a[184:187], a[72:75]
s_waitcnt lgkmcnt(7)
ds_read_b64_tr_b16 v[222:223], v11 offset:30720
v_add_u32 v9, s26, v5
v_mfma_f32_16x16x32_bf16 a[80:83], v[200:203], a[184:187], a[80:83]
s_mov_b32 m0, s23
ds_read_b64_tr_b16 v[192:193], v8 offset:0
ds_read_b64_tr_b16 v[194:195], v8 offset:2048
v_add_u32 v10, s26, v6
v_mfma_f32_16x16x32_bf16 a[84:87], v[200:203], a[188:191], a[84:87]
buffer_load_dwordx4 v14, s[16:19], s20 offen sc1 lds
v_mfma_f32_16x16x32_bf16 a[92:95], v[204:207], a[188:191], a[92:95]
s_add_u32 s22, s22, 1
ds_read_b64_tr_b16 v[196:197], v9 offset:0
v_add_u32 v11, s26, v7
v_mfma_f32_16x16x32_bf16 a[88:91], v[204:207], a[184:187], a[88:91]
s_waitcnt lgkmcnt(7)
ds_read_b64_tr_b16 v[198:199], v9 offset:2048
v_mfma_f32_16x16x32_bf16 a[96:99], v[208:211], a[184:187], a[96:99]
s_lshl_b32 s21, s25, 3
ds_read_b64_tr_b16 v[200:201], v10 offset:0
v_mfma_f32_16x16x32_bf16 a[100:103], v[208:211], a[188:191], a[100:103]
s_mov_b32 s27, s24
ds_read_b64_tr_b16 v[202:203], v10 offset:2048
v_mfma_f32_16x16x32_bf16 a[108:111], v[212:215], a[188:191], a[108:111]
s_add_u32 s20, s20, s21
ds_read_b64_tr_b16 v[204:205], v11 offset:0
v_mfma_f32_16x16x32_bf16 a[104:107], v[212:215], a[184:187], a[104:107]
s_waitcnt lgkmcnt(7)
ds_read_b64_tr_b16 v[206:207], v11 offset:2048
v_mfma_f32_16x16x32_bf16 a[112:115], v[216:219], a[184:187], a[112:115]
s_add_u32 m0, s23, 1024
ds_read_b64_tr_b16 v[208:209], v8 offset:8192
ds_read_b64_tr_b16 v[210:211], v8 offset:10240
v_mfma_f32_16x16x32_bf16 a[116:119], v[216:219], a[188:191], a[116:119]
buffer_load_dwordx4 v14, s[16:19], s20 offen sc1 lds
v_mfma_f32_16x16x32_bf16 a[124:127], v[220:223], a[188:191], a[124:127]
s_mov_b32 s24, s26
ds_read_b64_tr_b16 v[212:213], v9 offset:8192
v_mfma_f32_16x16x32_bf16 a[120:123], v[220:223], a[184:187], a[120:123]
s_waitcnt lgkmcnt(7)
ds_read_b64_tr_b16 v[214:215], v9 offset:10240
v_mfma_f32_16x16x32_bf16 a[0:3], v[192:195], a[192:195], a[0:3]
ds_read_b64_tr_b16 v[216:217], v10 offset:8192
v_mfma_f32_16x16x32_bf16 a[4:7], v[192:195], a[196:199], a[4:7]
ds_read_b64_tr_b16 v[218:219], v10 offset:10240
v_mfma_f32_16x16x32_bf16 a[12:15], v[196:199], a[196:199], a[12:15]
s_add_u32 s20, s20, s21
ds_read_b64_tr_b16 v[220:221], v11 offset:8192
v_mfma_f32_16x16x32_bf16 a[8:11], v[196:199], a[192:195], a[8:11]
s_waitcnt lgkmcnt(7)
ds_read_b64_tr_b16 v[222:223], v11 offset:10240
v_mfma_f32_16x16x32_bf16 a[16:19], v[200:203], a[192:195], a[16:19]
s_add_u32 m0, s23, 2048
ds_read_b64_tr_b16 v[192:193], v8 offset:16384
ds_read_b64_tr_b16 v[194:195], v8 offset:18432
v_mfma_f32_16x16x32_bf16 a[20:23], v[200:203], a[196:199], a[20:23]
buffer_load_dwordx4 v14, s[16:19], s20 offen sc1 lds
v_mfma_f32_16x16x32_bf16 a[28:31], v[204:207], a[196:199], a[28:31]
ds_read_b64_tr_b16 v[196:197], v9 offset:16384
v_mfma_f32_16x16x32_bf16 a[24:27], v[204:207], a[192:195], a[24:27]
s_waitcnt lgkmcnt(7)
ds_read_b64_tr_b16 v[198:199], v9 offset:18432
v_mfma_f32_16x16x32_bf16 a[32:35], v[208:211], a[192:195], a[32:35]
ds_read_b64_tr_b16 v[200:201], v10 offset:16384
v_mfma_f32_16x16x32_bf16 a[36:39], v[208:211], a[196:199], a[36:39]
ds_read_b64_tr_b16 v[202:203], v10 offset:18432
buffer_store_dwordx4 v[112:115], v0, s[36:39], s51 offen offset:0 sc1
v_mfma_f32_16x16x32_bf16 a[44:47], v[212:215], a[196:199], a[44:47]
s_add_u32 s20, s20, s21
ds_read_b64_tr_b16 v[204:205], v11 offset:16384
v_mfma_f32_16x16x32_bf16 a[40:43], v[212:215], a[192:195], a[40:43]
s_waitcnt lgkmcnt(7)
ds_read_b64_tr_b16 v[206:207], v11 offset:18432
v_mfma_f32_16x16x32_bf16 a[48:51], v[216:219], a[192:195], a[48:51]
s_add_u32 m0, s23, 3072
ds_read_b64_tr_b16 v[208:209], v8 offset:24576
ds_read_b64_tr_b16 v[210:211], v8 offset:26624
v_mfma_f32_16x16x32_bf16 a[52:55], v[216:219], a[196:199], a[52:55]
buffer_load_dwordx4 v14, s[16:19], s20 offen sc1 lds
v_mfma_f32_16x16x32_bf16 a[60:63], v[220:223], a[196:199], a[60:63]
ds_read_b64_tr_b16 v[212:213], v9 offset:24576
v_mfma_f32_16x16x32_bf16 a[56:59], v[220:223], a[192:195], a[56:59]
s_waitcnt lgkmcnt(7)
ds_read_b64_tr_b16 v[214:215], v9 offset:26624
v_mfma_f32_16x16x32_bf16 a[64:67], v[192:195], a[192:195], a[64:67]
ds_read_b64_tr_b16 v[216:217], v10 offset:24576
v_mfma_f32_16x16x32_bf16 a[68:71], v[192:195], a[196:199], a[68:71]
ds_read_b64_tr_b16 v[218:219], v10 offset:26624
v_mfma_f32_16x16x32_bf16 a[76:79], v[196:199], a[196:199], a[76:79]
s_add_u32 s20, s20, s21
ds_read_b64_tr_b16 v[220:221], v11 offset:24576
v_mfma_f32_16x16x32_bf16 a[72:75], v[196:199], a[192:195], a[72:75]
s_waitcnt lgkmcnt(7)
ds_read_b64_tr_b16 v[222:223], v11 offset:26624
v_mfma_f32_16x16x32_bf16 a[80:83], v[200:203], a[192:195], a[80:83]
s_add_u32 m0, s23, 4096
ds_read_b64_tr_b16 v[192:193], v8 offset:4096
ds_read_b64_tr_b16 v[194:195], v8 offset:6144
v_mfma_f32_16x16x32_bf16 a[84:87], v[200:203], a[196:199], a[84:87]
buffer_load_dwordx4 v14, s[16:19], s20 offen sc1 lds
v_mfma_f32_16x16x32_bf16 a[92:95], v[204:207], a[196:199], a[92:95]
ds_read_b64_tr_b16 v[196:197], v9 offset:4096
v_mfma_f32_16x16x32_bf16 a[88:91], v[204:207], a[192:195], a[88:91]
s_waitcnt lgkmcnt(7)
ds_read_b64_tr_b16 v[198:199], v9 offset:6144
v_mfma_f32_16x16x32_bf16 a[96:99], v[208:211], a[192:195], a[96:99]
ds_read_b64_tr_b16 v[200:201], v10 offset:4096
v_mfma_f32_16x16x32_bf16 a[100:103], v[208:211], a[196:199], a[100:103]
ds_read_b64_tr_b16 v[202:203], v10 offset:6144
v_mfma_f32_16x16x32_bf16 a[108:111], v[212:215], a[196:199], a[108:111]
s_add_u32 s20, s20, s21
ds_read_b64_tr_b16 v[204:205], v11 offset:4096
v_mfma_f32_16x16x32_bf16 a[104:107], v[212:215], a[192:195], a[104:107]
s_waitcnt lgkmcnt(7)
ds_read_b64_tr_b16 v[206:207], v11 offset:6144
v_mfma_f32_16x16x32_bf16 a[112:115], v[216:219], a[192:195], a[112:115]
s_add_u32 m0, s23, 5120
ds_read_b64_tr_b16 v[208:209], v8 offset:12288
ds_read_b64_tr_b16 v[210:211], v8 offset:14336
v_mfma_f32_16x16x32_bf16 a[116:119], v[216:219], a[196:199], a[116:119]
buffer_load_dwordx4 v14, s[16:19], s20 offen sc1 lds
v_mfma_f32_16x16x32_bf16 a[124:127], v[220:223], a[196:199], a[124:127]
ds_read_b64_tr_b16 v[212:213], v9 offset:12288
v_mfma_f32_16x16x32_bf16 a[120:123], v[220:223], a[192:195], a[120:123]
s_waitcnt lgkmcnt(7)
ds_read_b64_tr_b16 v[214:215], v9 offset:14336
v_mfma_f32_16x16x32_bf16 a[0:3], v[192:195], a[200:203], a[0:3]
ds_read_b64_tr_b16 v[216:217], v10 offset:12288
v_mfma_f32_16x16x32_bf16 a[4:7], v[192:195], a[204:207], a[4:7]
ds_read_b64_tr_b16 v[218:219], v10 offset:14336
v_mfma_f32_16x16x32_bf16 a[12:15], v[196:199], a[204:207], a[12:15]
s_add_u32 s20, s20, s21
ds_read_b64_tr_b16 v[220:221], v11 offset:12288
v_mfma_f32_16x16x32_bf16 a[8:11], v[196:199], a[200:203], a[8:11]
s_waitcnt lgkmcnt(7)
ds_read_b64_tr_b16 v[222:223], v11 offset:14336
v_mfma_f32_16x16x32_bf16 a[16:19], v[200:203], a[200:203], a[16:19]
s_add_u32 m0, s23, 6144
ds_read_b64_tr_b16 v[192:193], v8 offset:20480
ds_read_b64_tr_b16 v[194:195], v8 offset:22528
v_mfma_f32_16x16x32_bf16 a[20:23], v[200:203], a[204:207], a[20:23]
buffer_load_dwordx4 v14, s[16:19], s20 offen sc1 lds
v_mfma_f32_16x16x32_bf16 a[28:31], v[204:207], a[204:207], a[28:31]
s_add_u32 s26, s24, 32768
ds_read_b64_tr_b16 v[196:197], v9 offset:20480
v_mfma_f32_16x16x32_bf16 a[24:27], v[204:207], a[200:203], a[24:27]
s_waitcnt lgkmcnt(7)
ds_read_b64_tr_b16 v[198:199], v9 offset:22528
v_mfma_f32_16x16x32_bf16 a[32:35], v[208:211], a[200:203], a[32:35]
s_sub_u32 s35, s26, 163840
ds_read_b64_tr_b16 v[200:201], v10 offset:20480
v_mfma_f32_16x16x32_bf16 a[36:39], v[208:211], a[204:207], a[36:39]
s_cselect_b32 s26, s26, s35
ds_read_b64_tr_b16 v[202:203], v10 offset:22528
buffer_store_dwordx4 v[116:119], v0, s[36:39], s51 offen offset:1024 sc1
v_mfma_f32_16x16x32_bf16 a[44:47], v[212:215], a[204:207], a[44:47]
s_add_u32 s20, s20, s21
ds_read_b64_tr_b16 v[204:205], v11 offset:20480
v_mfma_f32_16x16x32_bf16 a[40:43], v[212:215], a[200:203], a[40:43]
s_waitcnt lgkmcnt(7)
ds_read_b64_tr_b16 v[206:207], v11 offset:22528
v_mfma_f32_16x16x32_bf16 a[48:51], v[216:219], a[200:203], a[48:51]
s_add_u32 m0, s23, 7168
ds_read_b64_tr_b16 v[208:209], v8 offset:28672
ds_read_b64_tr_b16 v[210:211], v8 offset:30720
v_mfma_f32_16x16x32_bf16 a[52:55], v[216:219], a[204:207], a[52:55]
buffer_load_dwordx4 v14, s[16:19], s20 offen sc1 lds
v_mfma_f32_16x16x32_bf16 a[60:63], v[220:223], a[204:207], a[60:63]
s_add_u32 s23, s27, s33
ds_read_b64_tr_b16 v[212:213], v9 offset:28672
v_readlane_b32 s16, v16, s22
v_mfma_f32_16x16x32_bf16 a[56:59], v[220:223], a[200:203], a[56:59]
s_waitcnt lgkmcnt(7)
ds_read_b64_tr_b16 v[214:215], v9 offset:30720
v_readlane_b32 s17, v17, s22
v_mfma_f32_16x16x32_bf16 a[64:67], v[192:195], a[200:203], a[64:67]
s_waitcnt vmcnt(21)
ds_read_b64_tr_b16 v[216:217], v10 offset:28672
v_readlane_b32 s25, v18, s22
v_mfma_f32_16x16x32_bf16 a[68:71], v[192:195], a[204:207], a[68:71]
s_barrier
ds_read_b64_tr_b16 v[218:219], v10 offset:30720
v_add_u32 v8, s26, v4
v_mfma_f32_16x16x32_bf16 a[76:79], v[196:199], a[204:207], a[76:79]
s_mov_b32 s20, 0
ds_read_b64_tr_b16 v[220:221], v11 offset:28672
v_mad_u32_u24 v14, v12, s25, v13
v_mfma_f32_16x16x32_bf16 a[72:75], v[196:199], a[200:203], a[72:75]
s_waitcnt lgkmcnt(7)
ds_read_b64_tr_b16 v[222:223], v11 offset:30720
v_add_u32 v9, s26, v5
v_mfma_f32_16x16x32_bf16 a[80:83], v[200:203], a[200:203], a[80:83]
s_mov_b32 m0, s23
ds_read_b64_tr_b16 v[192:193], v8 offset:0
ds_read_b64_tr_b16 v[194:195], v8 offset:2048
v_add_u32 v10, s26, v6
v_mfma_f32_16x16x32_bf16 a[84:87], v[200:203], a[204:207], a[84:87]
buffer_load_dwordx4 v14, s[16:19], s20 offen sc1 lds
v_mfma_f32_16x16x32_bf16 a[92:95], v[204:207], a[204:207], a[92:95]
s_add_u32 s22, s22, 1
ds_read_b64_tr_b16 v[196:197], v9 offset:0
v_add_u32 v11, s26, v7
v_mfma_f32_16x16x32_bf16 a[88:91], v[204:207], a[200:203], a[88:91]
s_waitcnt lgkmcnt(7)
ds_read_b64_tr_b16 v[198:199], v9 offset:2048
v_mfma_f32_16x16x32_bf16 a[96:99], v[208:211], a[200:203], a[96:99]
s_lshl_b32 s21, s25, 3
ds_read_b64_tr_b16 v[200:201], v10 offset:0
v_mfma_f32_16x16x32_bf16 a[100:103], v[208:211], a[204:207], a[100:103]
s_mov_b32 s27, s24
ds_read_b64_tr_b16 v[202:203], v10 offset:2048
v_mfma_f32_16x16x32_bf16 a[108:111], v[212:215], a[204:207], a[108:111]
s_add_u32 s20, s20, s21
ds_read_b64_tr_b16 v[204:205], v11 offset:0
v_mfma_f32_16x16x32_bf16 a[104:107], v[212:215], a[200:203], a[104:107]
s_waitcnt lgkmcnt(7)
ds_read_b64_tr_b16 v[206:207], v11 offset:2048
v_mfma_f32_16x16x32_bf16 a[112:115], v[216:219], a[200:203], a[112:115]
s_add_u32 m0, s23, 1024
ds_read_b64_tr_b16 v[208:209], v8 offset:8192
ds_read_b64_tr_b16 v[210:211], v8 offset:10240
v_mfma_f32_16x16x32_bf16 a[116:119], v[216:219], a[204:207], a[116:119]
buffer_load_dwordx4 v14, s[16:19], s20 offen sc1 lds
v_mfma_f32_16x16x32_bf16 a[124:127], v[220:223], a[204:207], a[124:127]
s_mov_b32 s24, s26
ds_read_b64_tr_b16 v[212:213], v9 offset:8192
v_mfma_f32_16x16x32_bf16 a[120:123], v[220:223], a[200:203], a[120:123]
s_waitcnt lgkmcnt(7)
ds_read_b64_tr_b16 v[214:215], v9 offset:10240
v_mfma_f32_16x16x32_bf16 a[0:3], v[192:195], a[208:211], a[0:3]
ds_read_b64_tr_b16 v[216:217], v10 offset:8192
v_mfma_f32_16x16x32_bf16 a[4:7], v[192:195], a[212:215], a[4:7]
ds_read_b64_tr_b16 v[218:219], v10 offset:10240
v_mfma_f32_16x16x32_bf16 a[12:15], v[196:199], a[212:215], a[12:15]
s_add_u32 s20, s20, s21
ds_read_b64_tr_b16 v[220:221], v11 offset:8192
v_mfma_f32_16x16x32_bf16 a[8:11], v[196:199], a[208:211], a[8:11]
s_waitcnt lgkmcnt(7)
ds_read_b64_tr_b16 v[222:223], v11 offset:10240
buffer_load_dword v45, v46, s[36:39], 0 offen sc1
v_mfma_f32_16x16x32_bf16 a[16:19], v[200:203], a[208:211], a[16:19]
s_add_u32 m0, s23, 2048
ds_read_b64_tr_b16 v[192:193], v8 offset:16384
ds_read_b64_tr_b16 v[194:195], v8 offset:18432
v_mfma_f32_16x16x32_bf16 a[20:23], v[200:203], a[212:215], a[20:23]
buffer_load_dwordx4 v14, s[16:19], s20 offen sc1 lds
v_mfma_f32_16x16x32_bf16 a[28:31], v[204:207], a[212:215], a[28:31]
ds_read_b64_tr_b16 v[196:197], v9 offset:16384
v_mfma_f32_16x16x32_bf16 a[24:27], v[204:207], a[208:211], a[24:27]
s_waitcnt lgkmcnt(7)
ds_read_b64_tr_b16 v[198:199], v9 offset:18432
v_mfma_f32_16x16x32_bf16 a[32:35], v[208:211], a[208:211], a[32:35]
ds_read_b64_tr_b16 v[200:201], v10 offset:16384
v_mfma_f32_16x16x32_bf16 a[36:39], v[208:211], a[212:215], a[36:39]
ds_read_b64_tr_b16 v[202:203], v10 offset:18432
buffer_store_dwordx4 v[120:123], v0, s[36:39], s51 offen offset:2048 sc1
v_mfma_f32_16x16x32_bf16 a[44:47], v[212:215], a[212:215], a[44:47]
s_add_u32 s20, s20, s21
ds_read_b64_tr_b16 v[204:205], v11 offset:16384
v_mfma_f32_16x16x32_bf16 a[40:43], v[212:215], a[208:211], a[40:43]
s_waitcnt lgkmcnt(7)
ds_read_b64_tr_b16 v[206:207], v11 offset:18432
v_mfma_f32_16x16x32_bf16 a[48:51], v[216:219], a[208:211], a[48:51]
s_add_u32 m0, s23, 3072
ds_read_b64_tr_b16 v[208:209], v8 offset:24576
ds_read_b64_tr_b16 v[210:211], v8 offset:26624
v_mfma_f32_16x16x32_bf16 a[52:55], v[216:219], a[212:215], a[52:55]
buffer_load_dwordx4 v14, s[16:19], s20 offen sc1 lds
v_mfma_f32_16x16x32_bf16 a[60:63], v[220:223], a[212:215], a[60:63]
ds_read_b64_tr_b16 v[212:213], v9 offset:24576
v_mfma_f32_16x16x32_bf16 a[56:59], v[220:223], a[208:211], a[56:59]
s_waitcnt lgkmcnt(7)
ds_read_b64_tr_b16 v[214:215], v9 offset:26624
v_mfma_f32_16x16x32_bf16 a[64:67], v[192:195], a[208:211], a[64:67]
ds_read_b64_tr_b16 v[216:217], v10 offset:24576
v_mfma_f32_16x16x32_bf16 a[68:71], v[192:195], a[212:215], a[68:71]
ds_read_b64_tr_b16 v[218:219], v10 offset:26624
v_mfma_f32_16x16x32_bf16 a[76:79], v[196:199], a[212:215], a[76:79]
s_add_u32 s20, s20, s21
ds_read_b64_tr_b16 v[220:221], v11 offset:24576
v_mfma_f32_16x16x32_bf16 a[72:75], v[196:199], a[208:211], a[72:75]
s_waitcnt lgkmcnt(7)
ds_read_b64_tr_b16 v[222:223], v11 offset:26624
v_mfma_f32_16x16x32_bf16 a[80:83], v[200:203], a[208:211], a[80:83]
s_add_u32 m0, s23, 4096
ds_read_b64_tr_b16 v[192:193], v8 offset:4096
ds_read_b64_tr_b16 v[194:195], v8 offset:6144
v_mfma_f32_16x16x32_bf16 a[84:87], v[200:203], a[212:215], a[84:87]
buffer_load_dwordx4 v14, s[16:19], s20 offen sc1 lds
v_mfma_f32_16x16x32_bf16 a[92:95], v[204:207], a[212:215], a[92:95]
ds_read_b64_tr_b16 v[196:197], v9 offset:4096
v_mfma_f32_16x16x32_bf16 a[88:91], v[204:207], a[208:211], a[88:91]
s_waitcnt lgkmcnt(7)
ds_read_b64_tr_b16 v[198:199], v9 offset:6144
v_mfma_f32_16x16x32_bf16 a[96:99], v[208:211], a[208:211], a[96:99]
ds_read_b64_tr_b16 v[200:201], v10 offset:4096
v_mfma_f32_16x16x32_bf16 a[100:103], v[208:211], a[212:215], a[100:103]
ds_read_b64_tr_b16 v[202:203], v10 offset:6144
v_mfma_f32_16x16x32_bf16 a[108:111], v[212:215], a[212:215], a[108:111]
s_add_u32 s20, s20, s21
ds_read_b64_tr_b16 v[204:205], v11 offset:4096
v_mfma_f32_16x16x32_bf16 a[104:107], v[212:215], a[208:211], a[104:107]
s_waitcnt lgkmcnt(7)
ds_read_b64_tr_b16 v[206:207], v11 offset:6144
v_mfma_f32_16x16x32_bf16 a[112:115], v[216:219], a[208:211], a[112:115]
s_add_u32 m0, s23, 5120
ds_read_b64_tr_b16 v[208:209], v8 offset:12288
ds_read_b64_tr_b16 v[210:211], v8 offset:14336
v_mfma_f32_16x16x32_bf16 a[116:119], v[216:219], a[212:215], a[116:119]
buffer_load_dwordx4 v14, s[16:19], s20 offen sc1 lds
v_mfma_f32_16x16x32_bf16 a[124:127], v[220:223], a[212:215], a[124:127]
ds_read_b64_tr_b16 v[212:213], v9 offset:12288
v_mfma_f32_16x16x32_bf16 a[120:123], v[220:223], a[208:211], a[120:123]
s_waitcnt lgkmcnt(7)
ds_read_b64_tr_b16 v[214:215], v9 offset:14336
v_mfma_f32_16x16x32_bf16 a[0:3], v[192:195], a[216:219], a[0:3]
ds_read_b64_tr_b16 v[216:217], v10 offset:12288
v_mfma_f32_16x16x32_bf16 a[4:7], v[192:195], a[220:223], a[4:7]
ds_read_b64_tr_b16 v[218:219], v10 offset:14336
v_mfma_f32_16x16x32_bf16 a[12:15], v[196:199], a[220:223], a[12:15]
s_add_u32 s20, s20, s21
ds_read_b64_tr_b16 v[220:221], v11 offset:12288
v_mfma_f32_16x16x32_bf16 a[8:11], v[196:199], a[216:219], a[8:11]
s_waitcnt lgkmcnt(7)
ds_read_b64_tr_b16 v[222:223], v11 offset:14336
v_mfma_f32_16x16x32_bf16 a[16:19], v[200:203], a[216:219], a[16:19]
s_add_u32 m0, s23, 6144
ds_read_b64_tr_b16 v[192:193], v8 offset:20480
ds_read_b64_tr_b16 v[194:195], v8 offset:22528
v_mfma_f32_16x16x32_bf16 a[20:23], v[200:203], a[220:223], a[20:23]
buffer_load_dwordx4 v14, s[16:19], s20 offen sc1 lds
v_mfma_f32_16x16x32_bf16 a[28:31], v[204:207], a[220:223], a[28:31]
s_add_u32 s26, s24, 32768
ds_read_b64_tr_b16 v[196:197], v9 offset:20480
v_mfma_f32_16x16x32_bf16 a[24:27], v[204:207], a[216:219], a[24:27]
s_waitcnt lgkmcnt(7)
ds_read_b64_tr_b16 v[198:199], v9 offset:22528
v_mfma_f32_16x16x32_bf16 a[32:35], v[208:211], a[216:219], a[32:35]
s_sub_u32 s35, s26, 163840
ds_read_b64_tr_b16 v[200:201], v10 offset:20480
v_mfma_f32_16x16x32_bf16 a[36:39], v[208:211], a[220:223], a[36:39]
s_cselect_b32 s26, s26, s35
ds_read_b64_tr_b16 v[202:203], v10 offset:22528
buffer_store_dwordx4 v[124:127], v0, s[36:39], s51 offen offset:3072 sc1
v_mfma_f32_16x16x32_bf16 a[44:47], v[212:215], a[220:223], a[44:47]
s_add_u32 s20, s20, s21
ds_read_b64_tr_b16 v[204:205], v11 offset:20480
v_mfma_f32_16x16x32_bf16 a[40:43], v[212:215], a[216:219], a[40:43]
s_waitcnt lgkmcnt(7)
ds_read_b64_tr_b16 v[206:207], v11 offset:22528
v_mfma_f32_16x16x32_bf16 a[48:51], v[216:219], a[216:219], a[48:51]
s_add_u32 m0, s23, 7168
ds_read_b64_tr_b16 v[208:209], v8 offset:28672
ds_read_b64_tr_b16 v[210:211], v8 offset:30720
v_mfma_f32_16x16x32_bf16 a[52:55], v[216:219], a[220:223], a[52:55]
buffer_load_dwordx4 v14, s[16:19], s20 offen sc1 lds
v_mfma_f32_16x16x32_bf16 a[60:63], v[220:223], a[220:223], a[60:63]
s_add_u32 s23, s27, s33
ds_read_b64_tr_b16 v[212:213], v9 offset:28672
v_readlane_b32 s16, v16, s22
v_mfma_f32_16x16x32_bf16 a[56:59], v[220:223], a[216:219], a[56:59]
s_waitcnt lgkmcnt(7)
ds_read_b64_tr_b16 v[214:215], v9 offset:30720
v_readlane_b32 s17, v17, s22
v_mfma_f32_16x16x32_bf16 a[64:67], v[192:195], a[216:219], a[64:67]
s_waitcnt vmcnt(21)
ds_read_b64_tr_b16 v[216:217], v10 offset:28672
v_readlane_b32 s25, v18, s22
v_mfma_f32_16x16x32_bf16 a[68:71], v[192:195], a[220:223], a[68:71]
s_barrier
ds_read_b64_tr_b16 v[218:219], v10 offset:30720
v_add_u32 v8, s26, v4
v_mfma_f32_16x16x32_bf16 a[76:79], v[196:199], a[220:223], a[76:79]
s_mov_b32 s20, 0
ds_read_b64_tr_b16 v[220:221], v11 offset:28672
v_mad_u32_u24 v14, v12, s25, v13
v_mfma_f32_16x16x32_bf16 a[72:75], v[196:199], a[216:219], a[72:75]
s_waitcnt lgkmcnt(7)
ds_read_b64_tr_b16 v[222:223], v11 offset:30720
v_add_u32 v9, s26, v5
v_mfma_f32_16x16x32_bf16 a[80:83], v[200:203], a[216:219], a[80:83]
s_mov_b32 m0, s23
ds_read_b64_tr_b16 v[192:193], v8 offset:0
ds_read_b64_tr_b16 v[194:195], v8 offset:2048
v_add_u32 v10, s26, v6
v_mfma_f32_16x16x32_bf16 a[84:87], v[200:203], a[220:223], a[84:87]
buffer_load_dwordx4 v14, s[16:19], s20 offen sc1 lds
v_mfma_f32_16x16x32_bf16 a[92:95], v[204:207], a[220:223], a[92:95]
s_add_u32 s22, s22, 1
ds_read_b64_tr_b16 v[196:197], v9 offset:0
v_add_u32 v11, s26, v7
v_mfma_f32_16x16x32_bf16 a[88:91], v[204:207], a[216:219], a[88:91]
s_waitcnt lgkmcnt(7)
ds_read_b64_tr_b16 v[198:199], v9 offset:2048
v_mfma_f32_16x16x32_bf16 a[96:99], v[208:211], a[216:219], a[96:99]
s_lshl_b32 s21, s25, 3
ds_read_b64_tr_b16 v[200:201], v10 offset:0
v_mfma_f32_16x16x32_bf16 a[100:103], v[208:211], a[220:223], a[100:103]
s_mov_b32 s27, s24
ds_read_b64_tr_b16 v[202:203], v10 offset:2048
v_mfma_f32_16x16x32_bf16 a[108:111], v[212:215], a[220:223], a[108:111]
s_add_u32 s20, s20, s21
ds_read_b64_tr_b16 v[204:205], v11 offset:0
v_mfma_f32_16x16x32_bf16 a[104:107], v[212:215], a[216:219], a[104:107]
s_waitcnt lgkmcnt(7)
ds_read_b64_tr_b16 v[206:207], v11 offset:2048
v_mfma_f32_16x16x32_bf16 a[112:115], v[216:219], a[216:219], a[112:115]
s_add_u32 m0, s23, 1024
ds_read_b64_tr_b16 v[208:209], v8 offset:8192
ds_read_b64_tr_b16 v[210:211], v8 offset:10240
v_mfma_f32_16x16x32_bf16 a[116:119], v[216:219], a[220:223], a[116:119]
buffer_load_dwordx4 v14, s[16:19], s20 offen sc1 lds
v_mfma_f32_16x16x32_bf16 a[124:127], v[220:223], a[220:223], a[124:127]
s_mov_b32 s24, s26
ds_read_b64_tr_b16 v[212:213], v9 offset:8192
v_mfma_f32_16x16x32_bf16 a[120:123], v[220:223], a[216:219], a[120:123]
s_waitcnt lgkmcnt(7)
ds_read_b64_tr_b16 v[214:215], v9 offset:10240
s_waitcnt vmcnt(10)
v_cmp_eq_u32 vcc, s98, v45
s_nop 4
s_cmp_eq_u64 vcc, -1
s_cbranch_scc1 ALLOKhf_%=
s_waitcnt vmcnt(0)
buffer_store_dword v42, v43, s[36:39], s56 offen sc1
buffer_store_dword v42, v43, s[36:39], s12 offen sc1
s_mov_b32 s47, 0
ALLPhf_%=:
buffer_load_dword v45, v46, s[36:39], 0 offen sc1
s_waitcnt vmcnt(0)
v_cmp_eq_u32 vcc, s98, v45
s_add_u32 s47, s47, 1
s_nop 4
s_cmp_eq_u64 vcc, -1
s_cbranch_scc1 ALLOKhf_%=
s_sleep 1
s_cmp_lt_u32 s47, 0x40000
s_cbranch_scc1 ALLPhf_%=
ALLOKhf_%=:
v_mfma_f32_16x16x32_bf16 a[0:3], v[192:195], a[224:227], a[0:3]
ds_read_b64_tr_b16 v[216:217], v10 offset:8192
v_mfma_f32_16x16x32_bf16 a[4:7], v[192:195], a[228:231], a[4:7]
ds_read_b64_tr_b16 v[218:219], v10 offset:10240
buffer_load_dwordx4 a[128:131], v47, s[36:39], s13 offen offset:0 sc1
v_mfma_f32_16x16x32_bf16 a[12:15], v[196:199], a[228:231], a[12:15]
s_add_u32 s20, s20, s21
ds_read_b64_tr_b16 v[220:221], v11 offset:8192
v_mfma_f32_16x16x32_bf16 a[8:11], v[196:199], a[224:227], a[8:11]
s_waitcnt lgkmcnt(7)
ds_read_b64_tr_b16 v[222:223], v11 offset:10240
v_mfma_f32_16x16x32_bf16 a[16:19], v[200:203], a[224:227], a[16:19]
s_add_u32 m0, s23, 2048
ds_read_b64_tr_b16 v[192:193], v8 offset:16384
ds_read_b64_tr_b16 v[194:195], v8 offset:18432
v_mfma_f32_16x16x32_bf16 a[20:23], v[200:203], a[228:231], a[20:23]
buffer_load_dwordx4 v14, s[16:19], s20 offen sc1 lds
v_mfma_f32_16x16x32_bf16 a[28:31], v[204:207], a[228:231], a[28:31]
ds_read_b64_tr_b16 v[196:197], v9 offset:16384
v_mfma_f32_16x16x32_bf16 a[24:27], v[204:207], a[224:227], a[24:27]
s_waitcnt lgkmcnt(7)
ds_read_b64_tr_b16 v[198:199], v9 offset:18432
v_mfma_f32_16x16x32_bf16 a[32:35], v[208:211], a[224:227], a[32:35]
ds_read_b64_tr_b16 v[200:201], v10 offset:16384
v_mfma_f32_16x16x32_bf16 a[36:39], v[208:211], a[228:231], a[36:39]
ds_read_b64_tr_b16 v[202:203], v10 offset:18432
buffer_load_dwordx4 a[132:135], v47, s[36:39], s13 offen offset:1024 sc1
v_mfma_f32_16x16x32_bf16 a[44:47], v[212:215], a[228:231], a[44:47]
s_add_u32 s20, s20, s21
ds_read_b64_tr_b16 v[204:205], v11 offset:16384
v_mfma_f32_16x16x32_bf16 a[40:43], v[212:215], a[224:227], a[40:43]
s_waitcnt lgkmcnt(7)
ds_read_b64_tr_b16 v[206:207], v11 offset:18432
v_mfma_f32_16x16x32_bf16 a[48:51], v[216:219], a[224:227], a[48:51]
s_add_u32 m0, s23, 3072
ds_read_b64_tr_b16 v[208:209], v8 offset:24576
ds_read_b64_tr_b16 v[210:211], v8 offset:26624
v_mfma_f32_16x16x32_bf16 a[52:55], v[216:219], a[228:231], a[52:55]
buffer_load_dwordx4 v14, s[16:19], s20 offen sc1 lds
v_mfma_f32_16x16x32_bf16 a[60:63], v[220:223], a[228:231], a[60:63]
ds_read_b64_tr_b16 v[212:213], v9 offset:24576
v_mfma_f32_16x16x32_bf16 a[56:59], v[220:223], a[224:227], a[56:59]
s_waitcnt lgkmcnt(7)
ds_read_b64_tr_b16 v[214:215], v9 offset:26624
v_mfma_f32_16x16x32_bf16 a[64:67], v[192:195], a[224:227], a[64:67]
ds_read_b64_tr_b16 v[216:217], v10 offset:24576
v_mfma_f32_16x16x32_bf16 a[68:71], v[192:195], a[228:231], a[68:71]
ds_read_b64_tr_b16 v[218:219], v10 offset:26624
buffer_load_dwordx4 a[136:139], v47, s[36:39], s13 offen offset:2048 sc1
v_mfma_f32_16x16x32_bf16 a[76:79], v[196:199], a[228:231], a[76:79]
s_add_u32 s20, s20, s21
ds_read_b64_tr_b16 v[220:221], v11 offset:24576
v_mfma_f32_16x16x32_bf16 a[72:75], v[196:199], a[224:227], a[72:75]
s_waitcnt lgkmcnt(7)
ds_read_b64_tr_b16 v[222:223], v11 offset:26624
v_mfma_f32_16x16x32_bf16 a[80:83], v[200:203], a[224:227], a[80:83]
s_add_u32 m0, s23, 4096
ds_read_b64_tr_b16 v[192:193], v8 offset:4096
ds_read_b64_tr_b16 v[194:195], v8 offset:6144
v_mfma_f32_16x16x32_bf16 a[84:87], v[200:203], a[228:231], a[84:87]
buffer_load_dwordx4 v14, s[16:19], s20 offen sc1 lds
v_mfma_f32_16x16x32_bf16 a[92:95], v[204:207], a[228:231], a[92:95]
ds_read_b64_tr_b16 v[196:197], v9 offset:4096
v_mfma_f32_16x16x32_bf16 a[88:91], v[204:207], a[224:227], a[88:91]
s_waitcnt lgkmcnt(7)
ds_read_b64_tr_b16 v[198:199], v9 offset:6144
v_mfma_f32_16x16x32_bf16 a[96:99], v[208:211], a[224:227], a[96:99]
ds_read_b64_tr_b16 v[200:201], v10 offset:4096
v_mfma_f32_16x16x32_bf16 a[100:103], v[208:211], a[228:231], a[100:103]
ds_read_b64_tr_b16 v[202:203], v10 offset:6144
buffer_load_dwordx4 a[140:143], v47, s[36:39], s13 offen offset:3072 sc1
v_mfma_f32_16x16x32_bf16 a[108:111], v[212:215], a[228:231], a[108:111]
s_add_u32 s20, s20, s21
ds_read_b64_tr_b16 v[204:205], v11 offset:4096
v_mfma_f32_16x16x32_bf16 a[104:107], v[212:215], a[224:227], a[104:107]
s_waitcnt lgkmcnt(7)
ds_read_b64_tr_b16 v[206:207], v11 offset:6144
v_add_u32 v47, 4096, v47
v_mfma_f32_16x16x32_bf16 a[112:115], v[216:219], a[224:227], a[112:115]
s_add_u32 m0, s23, 5120
ds_read_b64_tr_b16 v[208:209], v8 offset:12288
ds_read_b64_tr_b16 v[210:211], v8 offset:14336
v_mfma_f32_16x16x32_bf16 a[116:119], v[216:219], a[228:231], a[116:119]
buffer_load_dwordx4 v14, s[16:19], s20 offen sc1 lds
v_mfma_f32_16x16x32_bf16 a[124:127], v[220:223], a[228:231], a[124:127]
ds_read_b64_tr_b16 v[212:213], v9 offset:12288
v_mfma_f32_16x16x32_bf16 a[120:123], v[220:223], a[224:227], a[120:123]
s_waitcnt lgkmcnt(7)
ds_read_b64_tr_b16 v[214:215], v9 offset:14336
v_mfma_f32_16x16x32_bf16 a[0:3], v[192:195], a[232:235], a[0:3]
ds_read_b64_tr_b16 v[216:217], v10 offset:12288
v_mfma_f32_16x16x32_bf16 a[4:7], v[192:195], a[236:239], a[4:7]
ds_read_b64_tr_b16 v[218:219], v10 offset:14336
buffer_load_dwordx4 a[144:147], v47, s[36:39], s13 offen offset:0 sc1
v_mfma_f32_16x16x32_bf16 a[12:15], v[196:199], a[236:239], a[12:15]
s_add_u32 s20, s20, s21
ds_read_b64_tr_b16 v[220:221], v11 offset:12288
v_mfma_f32_16x16x32_bf16 a[8:11], v[196:199], a[232:235], a[8:11]
s_waitcnt lgkmcnt(7)
ds_read_b64_tr_b16 v[222:223], v11 offset:14336
v_mfma_f32_16x16x32_bf16 a[16:19], v[200:203], a[232:235], a[16:19]
s_add_u32 m0, s23, 6144
ds_read_b64_tr_b16 v[192:193], v8 offset:20480
ds_read_b64_tr_b16 v[194:195], v8 offset:22528
v_mfma_f32_16x16x32_bf16 a[20:23], v[200:203], a[236:239], a[20:23]
buffer_load_dwordx4 v14, s[16:19], s20 offen sc1 lds
v_mfma_f32_16x16x32_bf16 a[28:31], v[204:207], a[236:239], a[28:31]
s_add_u32 s26, s24, 32768
ds_read_b64_tr_b16 v[196:197], v9 offset:20480
v_mfma_f32_16x16x32_bf16 a[24:27], v[204:207], a[232:235], a[24:27]
s_waitcnt lgkmcnt(7)
ds_read_b64_tr_b16 v[198:199], v9 offset:22528
v_mfma_f32_16x16x32_bf16 a[32:35], v[208:211], a[232:235], a[32:35]
s_sub_u32 s35, s26, 163840
ds_read_b64_tr_b16 v[200:201], v10 offset:20480
v_mfma_f32_16x16x32_bf16 a[36:39], v[208:211], a[236:239], a[36:39]
s_cselect_b32 s26, s26, s35
ds_read_b64_tr_b16 v[202:203], v10 offset:22528
buffer_load_dwordx4 a[148:151], v47, s[36:39], s13 offen offset:1024 sc1
v_mfma_f32_16x16x32_bf16 a[44:47], v[212:215], a[236:239], a[44:47]
s_add_u32 s20, s20, s21
ds_read_b64_tr_b16 v[204:205], v11 offset:20480
v_mfma_f32_16x16x32_bf16 a[40:43], v[212:215], a[232:235], a[40:43]
s_waitcnt lgkmcnt(7)
ds_read_b64_tr_b16 v[206:207], v11 offset:22528
v_mfma_f32_16x16x32_bf16 a[48:51], v[216:219], a[232:235], a[48:51]
s_add_u32 m0, s23, 7168
ds_read_b64_tr_b16 v[208:209], v8 offset:28672
ds_read_b64_tr_b16 v[210:211], v8 offset:30720
v_mfma_f32_16x16x32_bf16 a[52:55], v[216:219], a[236:239], a[52:55]
buffer_load_dwordx4 v14, s[16:19], s20 offen sc1 lds
v_mfma_f32_16x16x32_bf16 a[60:63], v[220:223], a[236:239], a[60:63]
s_add_u32 s23, s27, s33
ds_read_b64_tr_b16 v[212:213], v9 offset:28672
v_readlane_b32 s16, v16, s22
v_mfma_f32_16x16x32_bf16 a[56:59], v[220:223], a[232:235], a[56:59]
s_waitcnt lgkmcnt(7)
ds_read_b64_tr_b16 v[214:215], v9 offset:30720
v_readlane_b32 s17, v17, s22
v_mfma_f32_16x16x32_bf16 a[64:67], v[192:195], a[232:235], a[64:67]
s_waitcnt vmcnt(25)
ds_read_b64_tr_b16 v[216:217], v10 offset:28672
v_readlane_b32 s25, v18, s22
v_mfma_f32_16x16x32_bf16 a[68:71], v[192:195], a[236:239], a[68:71]
s_barrier
ds_read_b64_tr_b16 v[218:219], v10 offset:30720
buffer_load_dwordx4 a[152:155], v47, s[36:39], s13 offen offset:2048 sc1
v_add_u32 v8, s26, v4
v_mfma_f32_16x16x32_bf16 a[76:79], v[196:199], a[236:239], a[76:79]
s_mov_b32 s20, 0
ds_read_b64_tr_b16 v[220:221], v11 offset:28672
v_mad_u32_u24 v14, v12, s25, v13
v_mfma_f32_16x16x32_bf16 a[72:75], v[196:199], a[232:235], a[72:75]
s_waitcnt lgkmcnt(7)
ds_read_b64_tr_b16 v[222:223], v11 offset:30720
v_add_u32 v9, s26, v5
v_mfma_f32_16x16x32_bf16 a[80:83], v[200:203], a[232:235], a[80:83]
s_mov_b32 m0, s23
ds_read_b64_tr_b16 v[192:193], v8 offset:0
ds_read_b64_tr_b16 v[194:195], v8 offset:2048
v_add_u32 v10, s26, v6
v_mfma_f32_16x16x32_bf16 a[84:87], v[200:203], a[236:239], a[84:87]
buffer_load_dwordx4 v14, s[16:19], s20 offen sc1 lds
v_mfma_f32_16x16x32_bf16 a[92:95], v[204:207], a[236:239], a[92:95]
s_add_u32 s22, s22, 1
ds_read_b64_tr_b16 v[196:197], v8 offset:4096
v_add_u32 v11, s26, v7
v_mfma_f32_16x16x32_bf16 a[88:91], v[204:207], a[232:235], a[88:91]
s_waitcnt lgkmcnt(7)
ds_read_b64_tr_b16 v[198:199], v8 offset:6144
v_mfma_f32_16x16x32_bf16 a[96:99], v[208:211], a[232:235], a[96:99]
s_lshl_b32 s21, s25, 3
ds_read_b64_tr_b16 v[200:201], v8 offset:8192
v_mfma_f32_16x16x32_bf16 a[100:103], v[208:211], a[236:239], a[100:103]
s_mov_b32 s27, s24
ds_read_b64_tr_b16 v[202:203], v8 offset:10240
buffer_load_dwordx4 a[156:159], v47, s[36:39], s13 offen offset:3072 sc1
v_mfma_f32_16x16x32_bf16 a[108:111], v[212:215], a[236:239], a[108:111]
s_add_u32 s20, s20, s21
ds_read_b64_tr_b16 v[204:205], v8 offset:12288
v_mfma_f32_16x16x32_bf16 a[104:107], v[212:215], a[232:235], a[104:107]
s_waitcnt lgkmcnt(7)
ds_read_b64_tr_b16 v[206:207], v8 offset:14336
v_add_u32 v47, 4096, v47
v_mfma_f32_16x16x32_bf16 a[112:115], v[216:219], a[232:235], a[112:115]
s_add_u32 m0, s23, 1024
ds_read_b64_tr_b16 v[208:209], v8 offset:16384
ds_read_b64_tr_b16 v[210:211], v8 offset:18432
v_mfma_f32_16x16x32_bf16 a[116:119], v[216:219], a[236:239], a[116:119]
buffer_load_dwordx4 v14, s[16:19], s20 offen sc1 lds
v_mfma_f32_16x16x32_bf16 a[124:127], v[220:223], a[236:239], a[124:127]
s_mov_b32 s24, s26
ds_read_b64_tr_b16 v[212:213], v8 offset:20480
v_mfma_f32_16x16x32_bf16 a[120:123], v[220:223], a[232:235], a[120:123]
s_waitcnt lgkmcnt(7)
ds_read_b64_tr_b16 v[214:215], v8 offset:22528
s_nop 7
s_setpc_b64 s[70:71]
FD_%=:
v_mfma_f32_16x16x32_bf16 a[240:243], v[192:195], a[128:131], 0
ds_read_b64_tr_b16 v[216:217], v8 offset:24576
ds_read_b64_tr_b16 v[218:219], v8 offset:26624
buffer_load_dwordx4 a[64:67], v47, s[36:39], s13 offen offset:0 sc1
v_mfma_f32_16x16x32_bf16 a[240:243], v[196:199], a[132:135], a[240:243]
s_add_u32 s20, s20, s21
ds_read_b64_tr_b16 v[220:221], v8 offset:28672
s_waitcnt lgkmcnt(7)
ds_read_b64_tr_b16 v[222:223], v8 offset:30720
buffer_store_dwordx4 v[64:67], v2, s[36:39], s50 offen offset:0 sc1
v_mfma_f32_16x16x32_bf16 a[240:243], v[200:203], a[136:139], a[240:243]
s_add_u32 m0, s23, 2048
ds_read_b64_tr_b16 v[192:193], v9 offset:0
ds_read_b64_tr_b16 v[194:195], v9 offset:2048
buffer_load_dwordx4 v14, s[16:19], s20 offen sc1 lds
v_mfma_f32_16x16x32_bf16 a[240:243], v[204:207], a[140:143], a[240:243]
ds_read_b64_tr_b16 v[196:197], v9 offset:4096
s_waitcnt lgkmcnt(7)
ds_read_b64_tr_b16 v[198:199], v9 offset:6144
v_mfma_f32_16x16x32_bf16 a[240:243], v[208:211], a[144:147], a[240:243]
ds_read_b64_tr_b16 v[200:201], v9 offset:8192
ds_read_b64_tr_b16 v[202:203], v9 offset:10240
buffer_load_dwordx4 a[68:71], v47, s[36:39], s13 offen offset:1024 sc1
v_mfma_f32_16x16x32_bf16 a[240:243], v[212:215], a[148:151], a[240:243]
s_add_u32 s20, s20, s21
ds_read_b64_tr_b16 v[204:205], v9 offset:12288
s_waitcnt lgkmcnt(7)
ds_read_b64_tr_b16 v[206:207], v9 offset:14336
buffer_store_dwordx4 v[68:71], v2, s[36:39], s50 offen offset:1024 sc1
v_mfma_f32_16x16x32_bf16 a[240:243], v[216:219], a[152:155], a[240:243]
s_add_u32 m0, s23, 3072
ds_read_b64_tr_b16 v[208:209], v9 offset:16384
ds_read_b64_tr_b16 v[210:211], v9 offset:18432
buffer_load_dwordx4 v14, s[16:19], s20 offen sc1 lds
v_mfma_f32_16x16x32_bf16 a[240:243], v[220:223], a[156:159], a[240:243]
ds_read_b64_tr_b16 v[212:213], v9 offset:20480
s_waitcnt lgkmcnt(7)
ds_read_b64_tr_b16 v[214:215], v9 offset:22528
v_mfma_f32_16x16x32_bf16 a[244:247], v[192:195], a[128:131], 0
ds_read_b64_tr_b16 v[216:217], v9 offset:24576
ds_read_b64_tr_b16 v[218:219], v9 offset:26624
buffer_load_dwordx4 a[72:75], v47, s[36:39], s13 offen offset:2048 sc1
v_mfma_f32_16x16x32_bf16 a[244:247], v[196:199], a[132:135], a[244:247]
s_add_u32 s20, s20, s21
ds_read_b64_tr_b16 v[220:221], v9 offset:28672
s_waitcnt lgkmcnt(7)
ds_read_b64_tr_b16 v[222:223], v9 offset:30720
buffer_store_dwordx4 v[72:75], v2, s[36:39], s50 offen offset:2048 sc1
v_mfma_f32_16x16x32_bf16 a[244:247], v[200:203], a[136:139], a[244:247]
s_add_u32 m0, s23, 4096
ds_read_b64_tr_b16 v[192:193], v10 offset:0
ds_read_b64_tr_b16 v[194:195], v10 offset:2048
buffer_load_dwordx4 v14, s[16:19], s20 offen sc1 lds
v_mfma_f32_16x16x32_bf16 a[244:247], v[204:207], a[140:143], a[244:247]
ds_read_b64_tr_b16 v[196:197], v10 offset:4096
s_waitcnt lgkmcnt(7)
ds_read_b64_tr_b16 v[198:199], v10 offset:6144
v_mfma_f32_16x16x32_bf16 a[244:247], v[208:211], a[144:147], a[244:247]
ds_read_b64_tr_b16 v[200:201], v10 offset:8192
ds_read_b64_tr_b16 v[202:203], v10 offset:10240
buffer_load_dwordx4 a[76:79], v47, s[36:39], s13 offen offset:3072 sc1
v_mfma_f32_16x16x32_bf16 a[244:247], v[212:215], a[148:151], a[244:247]
s_add_u32 s20, s20, s21
ds_read_b64_tr_b16 v[204:205], v10 offset:12288
s_waitcnt lgkmcnt(7)
ds_read_b64_tr_b16 v[206:207], v10 offset:14336
buffer_store_dwordx4 v[76:79], v2, s[36:39], s50 offen offset:3072 sc1
v_add_u32 v47, 4096, v47
v_mfma_f32_16x16x32_bf16 a[244:247], v[216:219], a[152:155], a[244:247]
s_add_u32 m0, s23, 5120
ds_read_b64_tr_b16 v[208:209], v10 offset:16384
ds_read_b64_tr_b16 v[210:211], v10 offset:18432
buffer_load_dwordx4 v14, s[16:19], s20 offen sc1 lds
v_mfma_f32_16x16x32_bf16 a[244:247], v[220:223], a[156:159], a[244:247]
ds_read_b64_tr_b16 v[212:213], v10 offset:20480
s_waitcnt lgkmcnt(7)
ds_read_b64_tr_b16 v[214:215], v10 offset:22528
v_mfma_f32_16x16x32_bf16 a[248:251], v[192:195], a[128:131], 0
ds_read_b64_tr_b16 v[216:217], v10 offset:24576
ds_read_b64_tr_b16 v[218:219], v10 offset:26624
buffer_load_dwordx4 a[80:83], v47, s[36:39], s13 offen offset:0 sc1
v_mfma_f32_16x16x32_bf16 a[248:251], v[196:199], a[132:135], a[248:251]
s_add_u32 s20, s20, s21
ds_read_b64_tr_b16 v[220:221], v10 offset:28672
s_waitcnt lgkmcnt(7)
ds_read_b64_tr_b16 v[222:223], v10 offset:30720
buffer_store_dwordx4 v[80:83], v2, s[36:39], s51 offen offset:0 sc1
v_mfma_f32_16x16x32_bf16 a[248:251], v[200:203], a[136:139], a[248:251]
s_add_u32 m0, s23, 6144
ds_read_b64_tr_b16 v[192:193], v11 offset:0
ds_read_b64_tr_b16 v[194:195], v11 offset:2048
buffer_load_dwordx4 v14, s[16:19], s20 offen sc1 lds
v_mfma_f32_16x16x32_bf16 a[248:251], v[204:207], a[140:143], a[248:251]
s_add_u32 s26, s24, 32768
ds_read_b64_tr_b16 v[196:197], v11 offset:4096
s_waitcnt lgkmcnt(7)
ds_read_b64_tr_b16 v[198:199], v11 offset:6144
v_mfma_f32_16x16x32_bf16 a[248:251], v[208:211], a[144:147], a[248:251]
s_sub_u32 s35, s26, 163840
ds_read_b64_tr_b16 v[200:201], v11 offset:8192
s_cselect_b32 s26, s26, s35
ds_read_b64_tr_b16 v[202:203], v11 offset:10240
buffer_load_dwordx4 a[84:87], v47, s[36:39], s13 offen offset:1024 sc1
v_mfma_f32_16x16x32_bf16 a[248:251], v[212:215], a[148:151], a[248:251]
s_add_u32 s20, s20, s21
ds_read_b64_tr_b16 v[204:205], v11 offset:12288
s_waitcnt lgkmcnt(7)
ds_read_b64_tr_b16 v[206:207], v11 offset:14336
buffer_store_dwordx4 v[84:87], v2, s[36:39], s51 offen offset:1024 sc1
v_mfma_f32_16x16x32_bf16 a[248:251], v[216:219], a[152:155], a[248:251]
s_add_u32 m0, s23, 7168
ds_read_b64_tr_b16 v[208:209], v11 offset:16384
ds_read_b64_tr_b16 v[210:211], v11 offset:18432
buffer_load_dwordx4 v14, s[16:19], s20 offen sc1 lds
v_mfma_f32_16x16x32_bf16 a[248:251], v[220:223], a[156:159], a[248:251]
s_add_u32 s23, s27, s33
ds_read_b64_tr_b16 v[212:213], v11 offset:20480
v_readlane_b32 s16, v16, s22
s_waitcnt lgkmcnt(7)
ds_read_b64_tr_b16 v[214:215], v11 offset:22528
v_readlane_b32 s17, v17, s22
v_mfma_f32_16x16x32_bf16 a[252:255], v[192:195], a[128:131], 0
s_waitcnt vmcnt(26)
ds_read_b64_tr_b16 v[216:217], v11 offset:24576
v_readlane_b32 s25, v18, s22
s_barrier
ds_read_b64_tr_b16 v[218:219], v11 offset:26624
buffer_load_dwordx4 a[88:91], v47, s[36:39], s13 offen offset:2048 sc1
v_add_u32 v8, s26, v4
v_mfma_f32_16x16x32_bf16 a[252:255], v[196:199], a[132:135], a[252:255]
s_mov_b32 s20, 0
ds_read_b64_tr_b16 v[220:221], v11 offset:28672
v_mad_u32_u24 v14, v12, s25, v13
s_waitcnt lgkmcnt(7)
ds_read_b64_tr_b16 v[222:223], v11 offset:30720
buffer_store_dwordx4 v[88:91], v2, s[36:39], s51 offen offset:2048 sc1
v_add_u32 v9, s26, v5
v_mfma_f32_16x16x32_bf16 a[252:255], v[200:203], a[136:139], a[252:255]
s_mov_b32 m0, s23
ds_read_b64_tr_b16 v[192:193], v8 offset:0
ds_read_b64_tr_b16 v[194:195], v8 offset:2048
v_add_u32 v10, s26, v6
buffer_load_dwordx4 v14, s[16:19], s20 offen sc1 lds
v_mfma_f32_16x16x32_bf16 a[252:255], v[204:207], a[140:143], a[252:255]
s_add_u32 s22, s22, 1
ds_read_b64_tr_b16 v[196:197], v8 offset:4096
v_add_u32 v11, s26, v7
s_waitcnt lgkmcnt(7)
ds_read_b64_tr_b16 v[198:199], v8 offset:6144
buffer_store_dword v42, v43, s[36:39], s12 offen sc1
v_mfma_f32_16x16x32_bf16 a[252:255], v[208:211], a[144:147], a[252:255]
s_lshl_b32 s21, s25, 3
ds_read_b64_tr_b16 v[200:201], v8 offset:8192
s_mov_b32 s27, s24
ds_read_b64_tr_b16 v[202:203], v8 offset:10240
buffer_load_dwordx4 a[92:95], v47, s[36:39], s13 offen offset:3072 sc1
v_mfma_f32_16x16x32_bf16 a[252:255], v[212:215], a[148:151], a[252:255]
s_add_u32 s20, s20, s21
ds_read_b64_tr_b16 v[204:205], v8 offset:12288
s_waitcnt lgkmcnt(7)
ds_read_b64_tr_b16 v[206:207], v8 offset:14336
buffer_store_dwordx4 v[92:95], v2, s[36:39], s51 offen offset:3072 sc1
v_add_u32 v47, 4096, v47
v_mfma_f32_16x16x32_bf16 a[252:255], v[216:219], a[152:155], a[252:255]
s_add_u32 m0, s23, 1024
ds_read_b64_tr_b16 v[208:209], v8 offset:16384
ds_read_b64_tr_b16 v[210:211], v8 offset:18432
buffer_load_dwordx4 v14, s[16:19], s20 offen sc1 lds
v_mfma_f32_16x16x32_bf16 a[252:255], v[220:223], a[156:159], a[252:255]
s_mov_b32 s24, s26
ds_read_b64_tr_b16 v[212:213], v8 offset:20480
s_waitcnt lgkmcnt(7)
ds_read_b64_tr_b16 v[214:215], v8 offset:22528
v_mfma_f32_16x16x32_bf16 a[240:243], v[192:195], a[0:3], a[240:243]
ds_read_b64_tr_b16 v[216:217], v8 offset:24576
ds_read_b64_tr_b16 v[218:219], v8 offset:26624
buffer_load_dwordx4 a[96:99], v47, s[36:39], s13 offen offset:0 sc1
v_mfma_f32_16x16x32_bf16 a[240:243], v[196:199], a[4:7], a[240:243]
s_add_u32 s20, s20, s21
ds_read_b64_tr_b16 v[220:221], v8 offset:28672
s_waitcnt lgkmcnt(7)
ds_read_b64_tr_b16 v[222:223], v8 offset:30720
buffer_store_dwordx4 v[96:99], v2, s[36:39], s52 offen offset:0 sc1
v_mfma_f32_16x16x32_bf16 a[240:243], v[200:203], a[8:11], a[240:243]
s_add_u32 m0, s23, 2048
ds_read_b64_tr_b16 v[192:193], v9 offset:0
ds_read_b64_tr_b16 v[194:195], v9 offset:2048
buffer_load_dwordx4 v14, s[16:19], s20 offen sc1 lds
v_mfma_f32_16x16x32_bf16 a[240:243], v[204:207], a[12:15], a[240:243]
ds_read_b64_tr_b16 v[196:197], v9 offset:4096
s_waitcnt lgkmcnt(7)
ds_read_b64_tr_b16 v[198:199], v9 offset:6144
v_mfma_f32_16x16x32_bf16 a[240:243], v[208:211], a[16:19], a[240:243]
ds_read_b64_tr_b16 v[200:201], v9 offset:8192
ds_read_b64_tr_b16 v[202:203], v9 offset:10240
buffer_load_dwordx4 a[100:103], v47, s[36:39], s13 offen offset:1024 sc1
v_mfma_f32_16x16x32_bf16 a[240:243], v[212:215], a[20:23], a[240:243]
s_add_u32 s20, s20, s21
ds_read_b64_tr_b16 v[204:205], v9 offset:12288
s_waitcnt lgkmcnt(7)
ds_read_b64_tr_b16 v[206:207], v9 offset:14336
buffer_store_dwordx4 v[100:103], v2, s[36:39], s52 offen offset:1024 sc1
v_mfma_f32_16x16x32_bf16 a[240:243], v[216:219], a[24:27], a[240:243]
s_add_u32 m0, s23, 3072
ds_read_b64_tr_b16 v[208:209], v9 offset:16384
ds_read_b64_tr_b16 v[210:211], v9 offset:18432
buffer_load_dwordx4 v14, s[16:19], s20 offen sc1 lds
v_mfma_f32_16x16x32_bf16 a[240:243], v[220:223], a[28:31], a[240:243]
ds_read_b64_tr_b16 v[212:213], v9 offset:20480
s_waitcnt lgkmcnt(7)
ds_read_b64_tr_b16 v[214:215], v9 offset:22528
v_mfma_f32_16x16x32_bf16 a[244:247], v[192:195], a[0:3], a[244:247]
ds_read_b64_tr_b16 v[216:217], v9 offset:24576
ds_read_b64_tr_b16 v[218:219], v9 offset:26624
buffer_load_dwordx4 a[104:107], v47, s[36:39], s13 offen offset:2048 sc1
v_mfma_f32_16x16x32_bf16 a[244:247], v[196:199], a[4:7], a[244:247]
s_add_u32 s20, s20, s21
ds_read_b64_tr_b16 v[220:221], v9 offset:28672
s_waitcnt lgkmcnt(7)
ds_read_b64_tr_b16 v[222:223], v9 offset:30720
buffer_store_dwordx4 v[104:107], v2, s[36:39], s52 offen offset:2048 sc1
v_mfma_f32_16x16x32_bf16 a[244:247], v[200:203], a[8:11], a[244:247]
s_add_u32 m0, s23, 4096
ds_read_b64_tr_b16 v[192:193], v10 offset:0
ds_read_b64_tr_b16 v[194:195], v10 offset:2048
buffer_load_dwordx4 v14, s[16:19], s20 offen sc1 lds
v_mfma_f32_16x16x32_bf16 a[244:247], v[204:207], a[12:15], a[244:247]
ds_read_b64_tr_b16 v[196:197], v10 offset:4096
s_waitcnt lgkmcnt(7)
ds_read_b64_tr_b16 v[198:199], v10 offset:6144
v_mfma_f32_16x16x32_bf16 a[244:247], v[208:211], a[16:19], a[244:247]
ds_read_b64_tr_b16 v[200:201], v10 offset:8192
ds_read_b64_tr_b16 v[202:203], v10 offset:10240
buffer_load_dwordx4 a[108:111], v47, s[36:39], s13 offen offset:3072 sc1
v_mfma_f32_16x16x32_bf16 a[244:247], v[212:215], a[20:23], a[244:247]
s_add_u32 s20, s20, s21
ds_read_b64_tr_b16 v[204:205], v10 offset:12288
s_waitcnt lgkmcnt(7)
ds_read_b64_tr_b16 v[206:207], v10 offset:14336
buffer_store_dwordx4 v[108:111], v2, s[36:39], s52 offen offset:3072 sc1
v_add_u32 v47, 4096, v47
v_mfma_f32_16x16x32_bf16 a[244:247], v[216:219], a[24:27], a[244:247]
s_add_u32 m0, s23, 5120
ds_read_b64_tr_b16 v[208:209], v10 offset:16384
ds_read_b64_tr_b16 v[210:211], v10 offset:18432
buffer_load_dwordx4 v14, s[16:19], s20 offen sc1 lds
v_mfma_f32_16x16x32_bf16 a[244:247], v[220:223], a[28:31], a[244:247]
ds_read_b64_tr_b16 v[212:213], v10 offset:20480
s_waitcnt lgkmcnt(7)
ds_read_b64_tr_b16 v[214:215], v10 offset:22528
v_mfma_f32_16x16x32_bf16 a[248:251], v[192:195], a[0:3], a[248:251]
ds_read_b64_tr_b16 v[216:217], v10 offset:24576
ds_read_b64_tr_b16 v[218:219], v10 offset:26624
buffer_load_dwordx4 a[112:115], v47, s[36:39], s13 offen offset:0 sc1
v_mfma_f32_16x16x32_bf16 a[248:251], v[196:199], a[4:7], a[248:251]
s_add_u32 s20, s20, s21
ds_read_b64_tr_b16 v[220:221], v10 offset:28672
s_waitcnt lgkmcnt(7)
ds_read_b64_tr_b16 v[222:223], v10 offset:30720
buffer_store_dwordx4 v[112:115], v2, s[36:39], s53 offen offset:0 sc1
v_mfma_f32_16x16x32_bf16 a[248:251], v[200:203], a[8:11], a[248:251]
s_add_u32 m0, s23, 6144
ds_read_b64_tr_b16 v[192:193], v11 offset:0
ds_read_b64_tr_b16 v[194:195], v11 offset:2048
buffer_load_dwordx4 v14, s[16:19], s20 offen sc1 lds
v_mfma_f32_16x16x32_bf16 a[248:251], v[204:207], a[12:15], a[248:251]
s_add_u32 s26, s24, 32768
ds_read_b64_tr_b16 v[196:197], v11 offset:4096
s_waitcnt lgkmcnt(7)
ds_read_b64_tr_b16 v[198:199], v11 offset:6144
v_mfma_f32_16x16x32_bf16 a[248:251], v[208:211], a[16:19], a[248:251]
s_sub_u32 s35, s26, 163840
ds_read_b64_tr_b16 v[200:201], v11 offset:8192
s_cselect_b32 s26, s26, s35
ds_read_b64_tr_b16 v[202:203], v11 offset:10240
buffer_load_dwordx4 a[116:119], v47, s[36:39], s13 offen offset:1024 sc1
v_mfma_f32_16x16x32_bf16 a[248:251], v[212:215], a[20:23], a[248:251]
s_add_u32 s20, s20, s21
ds_read_b64_tr_b16 v[204:205], v11 offset:12288
s_waitcnt lgkmcnt(7)
ds_read_b64_tr_b16 v[206:207], v11 offset:14336
buffer_store_dwordx4 v[116:119], v2, s[36:39], s53 offen offset:1024 sc1
v_mfma_f32_16x16x32_bf16 a[248:251], v[216:219], a[24:27], a[248:251]
s_add_u32 m0, s23, 7168
ds_read_b64_tr_b16 v[208:209], v11 offset:16384
ds_read_b64_tr_b16 v[210:211], v11 offset:18432
buffer_load_dwordx4 v14, s[16:19], s20 offen sc1 lds
v_mfma_f32_16x16x32_bf16 a[248:251], v[220:223], a[28:31], a[248:251]
s_add_u32 s23, s27, s33
ds_read_b64_tr_b16 v[212:213], v11 offset:20480
v_readlane_b32 s16, v16, s22
s_waitcnt lgkmcnt(7)
ds_read_b64_tr_b16 v[214:215], v11 offset:22528
v_readlane_b32 s17, v17, s22
v_mfma_f32_16x16x32_bf16 a[252:255], v[192:195], a[0:3], a[252:255]
s_waitcnt vmcnt(43)
ds_read_b64_tr_b16 v[216:217], v11 offset:24576
v_readlane_b32 s25, v18, s22
s_barrier
ds_read_b64_tr_b16 v[218:219], v11 offset:26624
buffer_load_dwordx4 a[120:123], v47, s[36:39], s13 offen offset:2048 sc1
v_add_u32 v8, s26, v4
v_mfma_f32_16x16x32_bf16 a[252:255], v[196:199], a[4:7], a[252:255]
s_mov_b32 s20, 0
ds_read_b64_tr_b16 v[220:221], v11 offset:28672
v_mad_u32_u24 v14, v12, s25, v13
s_waitcnt lgkmcnt(7)
ds_read_b64_tr_b16 v[222:223], v11 offset:30720
buffer_store_dwordx4 v[120:123], v2, s[36:39], s53 offen offset:2048 sc1
v_add_u32 v9, s26, v5
v_mfma_f32_16x16x32_bf16 a[252:255], v[200:203], a[8:11], a[252:255]
s_mov_b32 m0, s23
ds_read_b64_tr_b16 v[192:193], v8 offset:0
ds_read_b64_tr_b16 v[194:195], v8 offset:2048
v_add_u32 v10, s26, v6
buffer_load_dwordx4 v14, s[16:19], s20 offen sc1 lds
v_mfma_f32_16x16x32_bf16 a[252:255], v[204:207], a[12:15], a[252:255]
s_add_u32 s22, s22, 1
ds_read_b64_tr_b16 v[196:197], v8 offset:4096
v_add_u32 v11, s26, v7
s_waitcnt lgkmcnt(7)
ds_read_b64_tr_b16 v[198:199], v8 offset:6144
v_mfma_f32_16x16x32_bf16 a[252:255], v[208:211], a[16:19], a[252:255]
s_lshl_b32 s21, s25, 3
ds_read_b64_tr_b16 v[200:201], v8 offset:8192
s_mov_b32 s27, s24
ds_read_b64_tr_b16 v[202:203], v8 offset:10240
buffer_load_dwordx4 a[124:127], v47, s[36:39], s13 offen offset:3072 sc1
v_mfma_f32_16x16x32_bf16 a[252:255], v[212:215], a[20:23], a[252:255]
s_add_u32 s20, s20, s21
ds_read_b64_tr_b16 v[204:205], v8 offset:12288
s_waitcnt lgkmcnt(7)
ds_read_b64_tr_b16 v[206:207], v8 offset:14336
buffer_store_dwordx4 v[124:127], v2, s[36:39], s53 offen offset:3072 sc1
v_add_u32 v47, 4096, v47
v_mfma_f32_16x16x32_bf16 a[252:255], v[216:219], a[24:27], a[252:255]
s_add_u32 m0, s23, 1024
ds_read_b64_tr_b16 v[208:209], v8 offset:16384
ds_read_b64_tr_b16 v[210:211], v8 offset:18432
buffer_load_dwordx4 v14, s[16:19], s20 offen sc1 lds
v_mfma_f32_16x16x32_bf16 a[252:255], v[220:223], a[28:31], a[252:255]
s_mov_b32 s24, s26
ds_read_b64_tr_b16 v[212:213], v8 offset:20480
s_waitcnt lgkmcnt(7)
ds_read_b64_tr_b16 v[214:215], v8 offset:22528
v_mfma_f32_16x16x32_bf16 a[240:243], v[192:195], a[32:35], a[240:243]
ds_read_b64_tr_b16 v[216:217], v8 offset:24576
ds_read_b64_tr_b16 v[218:219], v8 offset:26624
buffer_load_dwordx4 a[0:3], v47, s[36:39], s13 offen offset:0 sc1
v_mfma_f32_16x16x32_bf16 a[240:243], v[196:199], a[36:39], a[240:243]
s_add_u32 s20, s20, s21
ds_read_b64_tr_b16 v[220:221], v8 offset:28672
s_waitcnt lgkmcnt(7)
ds_read_b64_tr_b16 v[222:223], v8 offset:30720
v_mfma_f32_16x16x32_bf16 a[240:243], v[200:203], a[40:43], a[240:243]
s_add_u32 m0, s23, 2048
ds_read_b64_tr_b16 v[192:193], v9 offset:0
ds_read_b64_tr_b16 v[194:195], v9 offset:2048
buffer_load_dwordx4 v14, s[16:19], s20 offen sc1 lds
v_mfma_f32_16x16x32_bf16 a[240:243], v[204:207], a[44:47], a[240:243]
ds_read_b64_tr_b16 v[196:197], v9 offset:4096
s_waitcnt lgkmcnt(7)
ds_read_b64_tr_b16 v[198:199], v9 offset:6144
v_mfma_f32_16x16x32_bf16 a[240:243], v[208:211], a[48:51], a[240:243]
ds_read_b64_tr_b16 v[200:201], v9 offset:8192
ds_read_b64_tr_b16 v[202:203], v9 offset:10240
buffer_load_dwordx4 a[4:7], v47, s[36:39], s13 offen offset:1024 sc1
v_mfma_f32_16x16x32_bf16 a[240:243], v[212:215], a[52:55], a[240:243]
s_add_u32 s20, s20, s21
ds_read_b64_tr_b16 v[204:205], v9 offset:12288
s_waitcnt lgkmcnt(7)
ds_read_b64_tr_b16 v[206:207], v9 offset:14336
v_mfma_f32_16x16x32_bf16 a[240:243], v[216:219], a[56:59], a[240:243]
s_add_u32 m0, s23, 3072
ds_read_b64_tr_b16 v[208:209], v9 offset:16384
ds_read_b64_tr_b16 v[210:211], v9 offset:18432
buffer_load_dwordx4 v14, s[16:19], s20 offen sc1 lds
v_mfma_f32_16x16x32_bf16 a[240:243], v[220:223], a[60:63], a[240:243]
ds_read_b64_tr_b16 v[212:213], v9 offset:20480
s_waitcnt lgkmcnt(7)
ds_read_b64_tr_b16 v[214:215], v9 offset:22528
v_mfma_f32_16x16x32_bf16 a[244:247], v[192:195], a[32:35], a[244:247]
ds_read_b64_tr_b16 v[216:217], v9 offset:24576
ds_read_b64_tr_b16 v[218:219], v9 offset:26624
buffer_load_dwordx4 a[8:11], v47, s[36:39], s13 offen offset:2048 sc1
v_mfma_f32_16x16x32_bf16 a[244:247], v[196:199], a[36:39], a[244:247]
s_add_u32 s20, s20, s21
ds_read_b64_tr_b16 v[220:221], v9 offset:28672
s_waitcnt lgkmcnt(7)
ds_read_b64_tr_b16 v[222:223], v9 offset:30720
v_mfma_f32_16x16x32_bf16 a[244:247], v[200:203], a[40:43], a[244:247]
s_add_u32 m0, s23, 4096
ds_read_b64_tr_b16 v[192:193], v10 offset:0
ds_read_b64_tr_b16 v[194:195], v10 offset:2048
buffer_load_dwordx4 v14, s[16:19], s20 offen sc1 lds
v_mfma_f32_16x16x32_bf16 a[244:247], v[204:207], a[44:47], a[244:247]
ds_read_b64_tr_b16 v[196:197], v10 offset:4096
s_waitcnt lgkmcnt(7)
ds_read_b64_tr_b16 v[198:199], v10 offset:6144
v_mfma_f32_16x16x32_bf16 a[244:247], v[208:211], a[48:51], a[244:247]
ds_read_b64_tr_b16 v[200:201], v10 offset:8192
ds_read_b64_tr_b16 v[202:203], v10 offset:10240
buffer_load_dwordx4 a[12:15], v47, s[36:39], s13 offen offset:3072 sc1
v_mfma_f32_16x16x32_bf16 a[244:247], v[212:215], a[52:55], a[244:247]
s_add_u32 s20, s20, s21
ds_read_b64_tr_b16 v[204:205], v10 offset:12288
s_waitcnt lgkmcnt(7)
ds_read_b64_tr_b16 v[206:207], v10 offset:14336
v_add_u32 v47, 4096, v47
v_mfma_f32_16x16x32_bf16 a[244:247], v[216:219], a[56:59], a[244:247]
s_add_u32 m0, s23, 5120
ds_read_b64_tr_b16 v[208:209], v10 offset:16384
ds_read_b64_tr_b16 v[210:211], v10 offset:18432
buffer_load_dwordx4 v14, s[16:19], s20 offen sc1 lds
v_mfma_f32_16x16x32_bf16 a[244:247], v[220:223], a[60:63], a[244:247]
ds_read_b64_tr_b16 v[212:213], v10 offset:20480
s_waitcnt lgkmcnt(7)
ds_read_b64_tr_b16 v[214:215], v10 offset:22528
v_mfma_f32_16x16x32_bf16 a[248:251], v[192:195], a[32:35], a[248:251]
ds_read_b64_tr_b16 v[216:217], v10 offset:24576
ds_read_b64_tr_b16 v[218:219], v10 offset:26624
buffer_load_dwordx4 a[16:19], v47, s[36:39], s13 offen offset:0 sc1
v_mfma_f32_16x16x32_bf16 a[248:251], v[196:199], a[36:39], a[248:251]
s_add_u32 s20, s20, s21
ds_read_b64_tr_b16 v[220:221], v10 offset:28672
s_waitcnt lgkmcnt(7)
ds_read_b64_tr_b16 v[222:223], v10 offset:30720
buffer_load_dword v45, v44, s[36:39], 0 offen sc1
v_mfma_f32_16x16x32_bf16 a[248:251], v[200:203], a[40:43], a[248:251]
s_add_u32 m0, s23, 6144
ds_read_b64_tr_b16 v[192:193], v11 offset:0
ds_read_b64_tr_b16 v[194:195], v11 offset:2048
buffer_load_dwordx4 v14, s[16:19], s20 offen sc1 lds
v_mfma_f32_16x16x32_bf16 a[248:251], v[204:207], a[44:47], a[248:251]
s_add_u32 s26, s24, 32768
ds_read_b64_tr_b16 v[196:197], v11 offset:4096
s_waitcnt lgkmcnt(7)
ds_read_b64_tr_b16 v[198:199], v11 offset:6144
v_mfma_f32_16x16x32_bf16 a[248:251], v[208:211], a[48:51], a[248:251]
s_sub_u32 s35, s26, 163840
ds_read_b64_tr_b16 v[200:201], v11 offset:8192
s_cselect_b32 s26, s26, s35
ds_read_b64_tr_b16 v[202:203], v11 offset:10240
buffer_load_dwordx4 a[20:23], v47, s[36:39], s13 offen offset:1024 sc1
v_mfma_f32_16x16x32_bf16 a[248:251], v[212:215], a[52:55], a[248:251]
s_add_u32 s20, s20, s21
ds_read_b64_tr_b16 v[204:205], v11 offset:12288
s_waitcnt lgkmcnt(7)
ds_read_b64_tr_b16 v[206:207], v11 offset:14336
v_mfma_f32_16x16x32_bf16 a[248:251], v[216:219], a[56:59], a[248:251]
s_add_u32 m0, s23, 7168
ds_read_b64_tr_b16 v[208:209], v11 offset:16384
ds_read_b64_tr_b16 v[210:211], v11 offset:18432
buffer_load_dwordx4 v14, s[16:19], s20 offen sc1 lds
v_mfma_f32_16x16x32_bf16 a[248:251], v[220:223], a[60:63], a[248:251]
s_add_u32 s23, s27, s33
ds_read_b64_tr_b16 v[212:213], v11 offset:20480
v_readlane_b32 s16, v16, s22
s_waitcnt lgkmcnt(7)
ds_read_b64_tr_b16 v[214:215], v11 offset:22528
v_readlane_b32 s17, v17, s22
v_mfma_f32_16x16x32_bf16 a[252:255], v[192:195], a[32:35], a[252:255]
s_waitcnt vmcnt(39)
ds_read_b64_tr_b16 v[216:217], v11 offset:24576
v_readlane_b32 s25, v18, s22
s_barrier
ds_read_b64_tr_b16 v[218:219], v11 offset:26624
buffer_load_dwordx4 a[24:27], v47, s[36:39], s13 offen offset:2048 sc1
v_add_u32 v8, s26, v4
v_mfma_f32_16x16x32_bf16 a[252:255], v[196:199], a[36:39], a[252:255]
s_mov_b32 s20, 0
ds_read_b64_tr_b16 v[220:221], v11 offset:28672
v_mad_u32_u24 v14, v12, s25, v13
s_waitcnt lgkmcnt(7)
ds_read_b64_tr_b16 v[222:223], v11 offset:30720
v_add_u32 v9, s26, v5
v_mfma_f32_16x16x32_bf16 a[252:255], v[200:203], a[40:43], a[252:255]
s_mov_b32 m0, s23
ds_read_b64_tr_b16 v[192:193], v8 offset:0
ds_read_b64_tr_b16 v[194:195], v8 offset:2048
v_add_u32 v10, s26, v6
buffer_load_dwordx4 v14, s[16:19], s20 offen sc1 lds
v_mfma_f32_16x16x32_bf16 a[252:255], v[204:207], a[44:47], a[252:255]
s_add_u32 s22, s22, 1
ds_read_b64_tr_b16 v[196:197], v8 offset:4096
v_add_u32 v11, s26, v7
s_waitcnt lgkmcnt(7)
ds_read_b64_tr_b16 v[198:199], v8 offset:6144
v_mfma_f32_16x16x32_bf16 a[252:255], v[208:211], a[48:51], a[252:255]
s_lshl_b32 s21, s25, 3
ds_read_b64_tr_b16 v[200:201], v8 offset:8192
s_mov_b32 s27, s24
ds_read_b64_tr_b16 v[202:203], v8 offset:10240
buffer_load_dwordx4 a[28:31], v47, s[36:39], s13 offen offset:3072 sc1
v_mfma_f32_16x16x32_bf16 a[252:255], v[212:215], a[52:55], a[252:255]
s_add_u32 s20, s20, s21
ds_read_b64_tr_b16 v[204:205], v8 offset:12288
s_waitcnt lgkmcnt(7)
ds_read_b64_tr_b16 v[206:207], v8 offset:14336
v_add_u32 v47, 4096, v47
v_mfma_f32_16x16x32_bf16 a[252:255], v[216:219], a[56:59], a[252:255]
s_add_u32 m0, s23, 1024
ds_read_b64_tr_b16 v[208:209], v8 offset:16384
ds_read_b64_tr_b16 v[210:211], v8 offset:18432
buffer_load_dwordx4 v14, s[16:19], s20 offen sc1 lds
v_mfma_f32_16x16x32_bf16 a[252:255], v[220:223], a[60:63], a[252:255]
s_mov_b32 s24, s26
ds_read_b64_tr_b16 v[212:213], v8 offset:20480
s_waitcnt lgkmcnt(7)
ds_read_b64_tr_b16 v[214:215], v8 offset:22528
v_mfma_f32_16x16x32_bf16 a[240:243], v[192:195], a[64:67], a[240:243]
ds_read_b64_tr_b16 v[216:217], v8 offset:24576
ds_read_b64_tr_b16 v[218:219], v8 offset:26624
buffer_load_dwordx4 a[32:35], v47, s[36:39], s13 offen offset:0 sc1
v_mfma_f32_16x16x32_bf16 a[240:243], v[196:199], a[68:71], a[240:243]
s_add_u32 s20, s20, s21
ds_read_b64_tr_b16 v[220:221], v8 offset:28672
s_waitcnt lgkmcnt(7)
ds_read_b64_tr_b16 v[222:223], v8 offset:30720
v_mfma_f32_16x16x32_bf16 a[240:243], v[200:203], a[72:75], a[240:243]
s_add_u32 m0, s23, 2048
ds_read_b64_tr_b16 v[192:193], v9 offset:0
ds_read_b64_tr_b16 v[194:195], v9 offset:2048
buffer_load_dwordx4 v14, s[16:19], s20 offen sc1 lds
v_mfma_f32_16x16x32_bf16 a[240:243], v[204:207], a[76:79], a[240:243]
ds_read_b64_tr_b16 v[196:197], v9 offset:4096
s_waitcnt lgkmcnt(7)
ds_read_b64_tr_b16 v[198:199], v9 offset:6144
v_mfma_f32_16x16x32_bf16 a[240:243], v[208:211], a[80:83], a[240:243]
ds_read_b64_tr_b16 v[200:201], v9 offset:8192
ds_read_b64_tr_b16 v[202:203], v9 offset:10240
buffer_load_dwordx4 a[36:39], v47, s[36:39], s13 offen offset:1024 sc1
v_mfma_f32_16x16x32_bf16 a[240:243], v[212:215], a[84:87], a[240:243]
s_add_u32 s20, s20, s21
ds_read_b64_tr_b16 v[204:205], v9 offset:12288
s_waitcnt lgkmcnt(7)
ds_read_b64_tr_b16 v[206:207], v9 offset:14336
v_mfma_f32_16x16x32_bf16 a[240:243], v[216:219], a[88:91], a[240:243]
s_add_u32 m0, s23, 3072
ds_read_b64_tr_b16 v[208:209], v9 offset:16384
ds_read_b64_tr_b16 v[210:211], v9 offset:18432
buffer_load_dwordx4 v14, s[16:19], s20 offen sc1 lds
v_mfma_f32_16x16x32_bf16 a[240:243], v[220:223], a[92:95], a[240:243]
ds_read_b64_tr_b16 v[212:213], v9 offset:20480
s_waitcnt lgkmcnt(7)
ds_read_b64_tr_b16 v[214:215], v9 offset:22528
v_mfma_f32_16x16x32_bf16 a[244:247], v[192:195], a[64:67], a[244:247]
ds_read_b64_tr_b16 v[216:217], v9 offset:24576
ds_read_b64_tr_b16 v[218:219], v9 offset:26624
buffer_load_dwordx4 a[40:43], v47, s[36:39], s13 offen offset:2048 sc1
v_mfma_f32_16x16x32_bf16 a[244:247], v[196:199], a[68:71], a[244:247]
s_add_u32 s20, s20, s21
ds_read_b64_tr_b16 v[220:221], v9 offset:28672
s_waitcnt lgkmcnt(7)
ds_read_b64_tr_b16 v[222:223], v9 offset:30720
v_mfma_f32_16x16x32_bf16 a[244:247], v[200:203], a[72:75], a[244:247]
s_add_u32 m0, s23, 4096
ds_read_b64_tr_b16 v[192:193], v10 offset:0
ds_read_b64_tr_b16 v[194:195], v10 offset:2048
buffer_load_dwordx4 v14, s[16:19], s20 offen sc1 lds
v_mfma_f32_16x16x32_bf16 a[244:247], v[204:207], a[76:79], a[244:247]
ds_read_b64_tr_b16 v[196:197], v10 offset:4096
s_waitcnt lgkmcnt(7)
ds_read_b64_tr_b16 v[198:199], v10 offset:6144
v_mfma_f32_16x16x32_bf16 a[244:247], v[208:211], a[80:83], a[244:247]
ds_read_b64_tr_b16 v[200:201], v10 offset:8192
ds_read_b64_tr_b16 v[202:203], v10 offset:10240
buffer_load_dwordx4 a[44:47], v47, s[36:39], s13 offen offset:3072 sc1
v_mfma_f32_16x16x32_bf16 a[244:247], v[212:215], a[84:87], a[244:247]
s_add_u32 s20, s20, s21
ds_read_b64_tr_b16 v[204:205], v10 offset:12288
s_waitcnt lgkmcnt(7)
ds_read_b64_tr_b16 v[206:207], v10 offset:14336
v_add_u32 v47, 4096, v47
v_mfma_f32_16x16x32_bf16 a[244:247], v[216:219], a[88:91], a[244:247]
s_add_u32 m0, s23, 5120
ds_read_b64_tr_b16 v[208:209], v10 offset:16384
ds_read_b64_tr_b16 v[210:211], v10 offset:18432
buffer_load_dwordx4 v14, s[16:19], s20 offen sc1 lds
v_mfma_f32_16x16x32_bf16 a[244:247], v[220:223], a[92:95], a[244:247]
ds_read_b64_tr_b16 v[212:213], v10 offset:20480
s_waitcnt lgkmcnt(7)
ds_read_b64_tr_b16 v[214:215], v10 offset:22528
v_mfma_f32_16x16x32_bf16 a[248:251], v[192:195], a[64:67], a[248:251]
ds_read_b64_tr_b16 v[216:217], v10 offset:24576
ds_read_b64_tr_b16 v[218:219], v10 offset:26624
buffer_load_dwordx4 a[48:51], v47, s[36:39], s13 offen offset:0 sc1
v_mfma_f32_16x16x32_bf16 a[248:251], v[196:199], a[68:71], a[248:251]
s_add_u32 s20, s20, s21
ds_read_b64_tr_b16 v[220:221], v10 offset:28672
s_waitcnt lgkmcnt(7)
ds_read_b64_tr_b16 v[222:223], v10 offset:30720
v_mfma_f32_16x16x32_bf16 a[248:251], v[200:203], a[72:75], a[248:251]
s_add_u32 m0, s23, 6144
ds_read_b64_tr_b16 v[192:193], v11 offset:0
ds_read_b64_tr_b16 v[194:195], v11 offset:2048
buffer_load_dwordx4 v14, s[16:19], s20 offen sc1 lds
v_mfma_f32_16x16x32_bf16 a[248:251], v[204:207], a[76:79], a[248:251]
s_add_u32 s26, s24, 32768
ds_read_b64_tr_b16 v[196:197], v11 offset:4096
s_waitcnt lgkmcnt(7)
ds_read_b64_tr_b16 v[198:199], v11 offset:6144
v_mfma_f32_16x16x32_bf16 a[248:251], v[208:211], a[80:83], a[248:251]
s_sub_u32 s35, s26, 163840
ds_read_b64_tr_b16 v[200:201], v11 offset:8192
s_cselect_b32 s26, s26, s35
ds_read_b64_tr_b16 v[202:203], v11 offset:10240
buffer_load_dwordx4 a[52:55], v47, s[36:39], s13 offen offset:1024 sc1
v_mfma_f32_16x16x32_bf16 a[248:251], v[212:215], a[84:87], a[248:251]
s_add_u32 s20, s20, s21
ds_read_b64_tr_b16 v[204:205], v11 offset:12288
s_waitcnt lgkmcnt(7)
ds_read_b64_tr_b16 v[206:207], v11 offset:14336
v_mfma_f32_16x16x32_bf16 a[248:251], v[216:219], a[88:91], a[248:251]
s_add_u32 m0, s23, 7168
ds_read_b64_tr_b16 v[208:209], v11 offset:16384
ds_read_b64_tr_b16 v[210:211], v11 offset:18432
buffer_load_dwordx4 v14, s[16:19], s20 offen sc1 lds
v_mfma_f32_16x16x32_bf16 a[248:251], v[220:223], a[92:95], a[248:251]
s_add_u32 s23, s27, s33
ds_read_b64_tr_b16 v[212:213], v11 offset:20480
v_readlane_b32 s16, v16, s22
s_waitcnt lgkmcnt(7)
ds_read_b64_tr_b16 v[214:215], v11 offset:22528
v_readlane_b32 s17, v17, s22
v_mfma_f32_16x16x32_bf16 a[252:255], v[192:195], a[64:67], a[252:255]
s_waitcnt vmcnt(30)
ds_read_b64_tr_b16 v[216:217], v11 offset:24576
v_readlane_b32 s25, v18, s22
s_barrier
ds_read_b64_tr_b16 v[218:219], v11 offset:26624
buffer_load_dwordx4 a[56:59], v47, s[36:39], s13 offen offset:2048 sc1
v_add_u32 v8, s26, v4
v_mfma_f32_16x16x32_bf16 a[252:255], v[196:199], a[68:71], a[252:255]
s_mov_b32 s20, 0
ds_read_b64_tr_b16 v[220:221], v11 offset:28672
v_mad_u32_u24 v14, v12, s25, v13
s_waitcnt lgkmcnt(7)
ds_read_b64_tr_b16 v[222:223], v11 offset:30720
v_add_u32 v9, s26, v5
s_waitcnt vmcnt(20)
v_cmp_eq_u32 vcc, s98, v45
s_nop 4
s_cmp_eq_u64 vcc, -1
s_cbranch_scc1 ALLOKkv_%=
s_waitcnt vmcnt(0)
buffer_store_dword v42, v43, s[36:39], s56 offen sc1
buffer_store_dword v42, v43, s[36:39], s12 offen sc1
s_mov_b32 s47, 0
ALLPkv_%=:
buffer_load_dword v45, v44, s[36:39], 0 offen sc1
s_waitcnt vmcnt(0)
v_cmp_eq_u32 vcc, s98, v45
s_add_u32 s47, s47, 1
s_nop 4
s_cmp_eq_u64 vcc, -1
s_cbranch_scc1 ALLOKkv_%=
s_sleep 1
s_cmp_lt_u32 s47, 0x40000
s_cbranch_scc1 ALLPkv_%=
ALLOKkv_%=:
v_mfma_f32_16x16x32_bf16 a[252:255], v[200:203], a[72:75], a[252:255]
s_mov_b32 m0, s23
ds_read_b64_tr_b16 v[192:193], v8 offset:0
ds_read_b64_tr_b16 v[194:195], v8 offset:2048
v_add_u32 v10, s26, v6
buffer_load_dwordx4 v14, s[16:19], s20 offen sc1 lds
v_mfma_f32_16x16x32_bf16 a[252:255], v[204:207], a[76:79], a[252:255]
s_add_u32 s22, s22, 1
ds_read_b64_tr_b16 v[196:197], v8 offset:4096
v_add_u32 v11, s26, v7
s_waitcnt lgkmcnt(7)
ds_read_b64_tr_b16 v[198:199], v8 offset:6144
buffer_store_dword v42, v43, s[36:39], s56 offen sc1
v_mfma_f32_16x16x32_bf16 a[252:255], v[208:211], a[80:83], a[252:255]
s_lshl_b32 s21, s25, 3
ds_read_b64_tr_b16 v[200:201], v8 offset:8192
s_mov_b32 s27, s24
ds_read_b64_tr_b16 v[202:203], v8 offset:10240
buffer_load_dwordx4 a[60:63], v47, s[36:39], s13 offen offset:3072 sc1
v_mfma_f32_16x16x32_bf16 a[252:255], v[212:215], a[84:87], a[252:255]
s_add_u32 s20, s20, s21
ds_read_b64_tr_b16 v[204:205], v8 offset:12288
s_waitcnt lgkmcnt(7)
ds_read_b64_tr_b16 v[206:207], v8 offset:14336
v_add_u32 v47, 4096, v47
v_mfma_f32_16x16x32_bf16 a[252:255], v[216:219], a[88:91], a[252:255]
s_add_u32 m0, s23, 1024
ds_read_b64_tr_b16 v[208:209], v8 offset:16384
ds_read_b64_tr_b16 v[210:211], v8 offset:18432
buffer_load_dwordx4 v14, s[16:19], s20 offen sc1 lds
v_mfma_f32_16x16x32_bf16 a[252:255], v[220:223], a[92:95], a[252:255]
s_mov_b32 s24, s26
ds_read_b64_tr_b16 v[212:213], v8 offset:20480
s_waitcnt lgkmcnt(7)
ds_read_b64_tr_b16 v[214:215], v8 offset:22528
v_mfma_f32_16x16x32_bf16 a[240:243], v[192:195], a[96:99], a[240:243]
ds_read_b64_tr_b16 v[216:217], v8 offset:24576
ds_read_b64_tr_b16 v[218:219], v8 offset:26624
v_mfma_f32_16x16x32_bf16 a[240:243], v[196:199], a[100:103], a[240:243]
s_add_u32 s20, s20, s21
ds_read_b64_tr_b16 v[220:221], v8 offset:28672
s_waitcnt lgkmcnt(7)
ds_read_b64_tr_b16 v[222:223], v8 offset:30720
v_mfma_f32_16x16x32_bf16 a[240:243], v[200:203], a[104:107], a[240:243]
s_add_u32 m0, s23, 2048
ds_read_b64_tr_b16 v[192:193], v9 offset:0
ds_read_b64_tr_b16 v[194:195], v9 offset:2048
buffer_load_dwordx4 v14, s[16:19], s20 offen sc1 lds
v_mfma_f32_16x16x32_bf16 a[240:243], v[204:207], a[108:111], a[240:243]
ds_read_b64_tr_b16 v[196:197], v9 offset:4096
s_waitcnt lgkmcnt(7)
ds_read_b64_tr_b16 v[198:199], v9 offset:6144
v_mfma_f32_16x16x32_bf16 a[240:243], v[208:211], a[112:115], a[240:243]
ds_read_b64_tr_b16 v[200:201], v9 offset:8192
ds_read_b64_tr_b16 v[202:203], v9 offset:10240
v_mfma_f32_16x16x32_bf16 a[240:243], v[212:215], a[116:119], a[240:243]
s_add_u32 s20, s20, s21
ds_read_b64_tr_b16 v[204:205], v9 offset:12288
s_waitcnt lgkmcnt(7)
ds_read_b64_tr_b16 v[206:207], v9 offset:14336
v_mfma_f32_16x16x32_bf16 a[240:243], v[216:219], a[120:123], a[240:243]
s_add_u32 m0, s23, 3072
ds_read_b64_tr_b16 v[208:209], v9 offset:16384
ds_read_b64_tr_b16 v[210:211], v9 offset:18432
buffer_load_dwordx4 v14, s[16:19], s20 offen sc1 lds
v_mfma_f32_16x16x32_bf16 a[240:243], v[220:223], a[124:127], a[240:243]
ds_read_b64_tr_b16 v[212:213], v9 offset:20480
s_waitcnt lgkmcnt(7)
ds_read_b64_tr_b16 v[214:215], v9 offset:22528
v_mfma_f32_16x16x32_bf16 a[244:247], v[192:195], a[96:99], a[244:247]
ds_read_b64_tr_b16 v[216:217], v9 offset:24576
ds_read_b64_tr_b16 v[218:219], v9 offset:26624
v_mfma_f32_16x16x32_bf16 a[244:247], v[196:199], a[100:103], a[244:247]
s_add_u32 s20, s20, s21
ds_read_b64_tr_b16 v[220:221], v9 offset:28672
s_waitcnt lgkmcnt(7)
ds_read_b64_tr_b16 v[222:223], v9 offset:30720
v_mfma_f32_16x16x32_bf16 a[244:247], v[200:203], a[104:107], a[244:247]
s_add_u32 m0, s23, 4096
ds_read_b64_tr_b16 v[192:193], v10 offset:0
ds_read_b64_tr_b16 v[194:195], v10 offset:2048
buffer_load_dwordx4 v14, s[16:19], s20 offen sc1 lds
v_mfma_f32_16x16x32_bf16 a[244:247], v[204:207], a[108:111], a[244:247]
ds_read_b64_tr_b16 v[196:197], v10 offset:4096
s_waitcnt lgkmcnt(7)
ds_read_b64_tr_b16 v[198:199], v10 offset:6144
v_mfma_f32_16x16x32_bf16 a[244:247], v[208:211], a[112:115], a[244:247]
ds_read_b64_tr_b16 v[200:201], v10 offset:8192
ds_read_b64_tr_b16 v[202:203], v10 offset:10240
v_mfma_f32_16x16x32_bf16 a[244:247], v[212:215], a[116:119], a[244:247]
s_add_u32 s20, s20, s21
ds_read_b64_tr_b16 v[204:205], v10 offset:12288
s_waitcnt lgkmcnt(7)
ds_read_b64_tr_b16 v[206:207], v10 offset:14336
v_mfma_f32_16x16x32_bf16 a[244:247], v[216:219], a[120:123], a[244:247]
s_add_u32 m0, s23, 5120
ds_read_b64_tr_b16 v[208:209], v10 offset:16384
ds_read_b64_tr_b16 v[210:211], v10 offset:18432
buffer_load_dwordx4 v14, s[16:19], s20 offen sc1 lds
v_mfma_f32_16x16x32_bf16 a[244:247], v[220:223], a[124:127], a[244:247]
ds_read_b64_tr_b16 v[212:213], v10 offset:20480
s_waitcnt lgkmcnt(7)
ds_read_b64_tr_b16 v[214:215], v10 offset:22528
v_mfma_f32_16x16x32_bf16 a[248:251], v[192:195], a[96:99], a[248:251]
ds_read_b64_tr_b16 v[216:217], v10 offset:24576
ds_read_b64_tr_b16 v[218:219], v10 offset:26624
v_mfma_f32_16x16x32_bf16 a[248:251], v[196:199], a[100:103], a[248:251]
s_add_u32 s20, s20, s21
ds_read_b64_tr_b16 v[220:221], v10 offset:28672
s_waitcnt lgkmcnt(7)
ds_read_b64_tr_b16 v[222:223], v10 offset:30720
v_mfma_f32_16x16x32_bf16 a[248:251], v[200:203], a[104:107], a[248:251]
s_add_u32 m0, s23, 6144
ds_read_b64_tr_b16 v[192:193], v11 offset:0
ds_read_b64_tr_b16 v[194:195], v11 offset:2048
buffer_load_dwordx4 v14, s[16:19], s20 offen sc1 lds
v_mfma_f32_16x16x32_bf16 a[248:251], v[204:207], a[108:111], a[248:251]
s_add_u32 s26, s24, 32768
ds_read_b64_tr_b16 v[196:197], v11 offset:4096
s_waitcnt lgkmcnt(7)
ds_read_b64_tr_b16 v[198:199], v11 offset:6144
v_mfma_f32_16x16x32_bf16 a[248:251], v[208:211], a[112:115], a[248:251]
s_sub_u32 s35, s26, 163840
ds_read_b64_tr_b16 v[200:201], v11 offset:8192
s_cselect_b32 s26, s26, s35
ds_read_b64_tr_b16 v[202:203], v11 offset:10240
v_mfma_f32_16x16x32_bf16 a[248:251], v[212:215], a[116:119], a[248:251]
s_add_u32 s20, s20, s21
ds_read_b64_tr_b16 v[204:205], v11 offset:12288
s_waitcnt lgkmcnt(7)
ds_read_b64_tr_b16 v[206:207], v11 offset:14336
v_mfma_f32_16x16x32_bf16 a[248:251], v[216:219], a[120:123], a[248:251]
s_add_u32 m0, s23, 7168
ds_read_b64_tr_b16 v[208:209], v11 offset:16384
ds_read_b64_tr_b16 v[210:211], v11 offset:18432
buffer_load_dwordx4 v14, s[16:19], s20 offen sc1 lds
v_mfma_f32_16x16x32_bf16 a[248:251], v[220:223], a[124:127], a[248:251]
s_add_u32 s23, s27, s33
ds_read_b64_tr_b16 v[212:213], v11 offset:20480
v_readlane_b32 s16, v16, s22
s_waitcnt lgkmcnt(7)
ds_read_b64_tr_b16 v[214:215], v11 offset:22528
v_readlane_b32 s17, v17, s22
v_mfma_f32_16x16x32_bf16 a[252:255], v[192:195], a[96:99], a[252:255]
s_waitcnt vmcnt(24)
ds_read_b64_tr_b16 v[216:217], v11 offset:24576
v_readlane_b32 s25, v18, s22
s_barrier
ds_read_b64_tr_b16 v[218:219], v11 offset:26624
v_add_u32 v8, s26, v4
v_mfma_f32_16x16x32_bf16 a[252:255], v[196:199], a[100:103], a[252:255]
s_mov_b32 s20, 0
ds_read_b64_tr_b16 v[220:221], v11 offset:28672
v_mad_u32_u24 v14, v12, s25, v13
s_waitcnt lgkmcnt(7)
ds_read_b64_tr_b16 v[222:223], v11 offset:30720
v_add_u32 v9, s26, v5
v_mfma_f32_16x16x32_bf16 a[252:255], v[200:203], a[104:107], a[252:255]
s_mov_b32 m0, s23
ds_read_b64_tr_b16 v[192:193], v8 offset:0
ds_read_b64_tr_b16 v[194:195], v8 offset:2048
v_add_u32 v10, s26, v6
buffer_load_dwordx4 v14, s[16:19], s20 offen sc1 lds
v_mfma_f32_16x16x32_bf16 a[252:255], v[204:207], a[108:111], a[252:255]
s_add_u32 s22, s22, 1
ds_read_b64_tr_b16 v[196:197], v8 offset:4096
v_add_u32 v11, s26, v7
s_waitcnt lgkmcnt(7)
ds_read_b64_tr_b16 v[198:199], v8 offset:6144
v_mfma_f32_16x16x32_bf16 a[252:255], v[208:211], a[112:115], a[252:255]
s_lshl_b32 s21, s25, 3
ds_read_b64_tr_b16 v[200:201], v8 offset:8192
s_mov_b32 s27, s24
ds_read_b64_tr_b16 v[202:203], v8 offset:10240
v_mfma_f32_16x16x32_bf16 a[252:255], v[212:215], a[116:119], a[252:255]
s_add_u32 s20, s20, s21
ds_read_b64_tr_b16 v[204:205], v8 offset:12288
s_waitcnt lgkmcnt(7)
ds_read_b64_tr_b16 v[206:207], v8 offset:14336
v_mfma_f32_16x16x32_bf16 a[252:255], v[216:219], a[120:123], a[252:255]
s_add_u32 m0, s23, 1024
ds_read_b64_tr_b16 v[208:209], v8 offset:16384
ds_read_b64_tr_b16 v[210:211], v8 offset:18432
buffer_load_dwordx4 v14, s[16:19], s20 offen sc1 lds
v_mfma_f32_16x16x32_bf16 a[252:255], v[220:223], a[124:127], a[252:255]
s_mov_b32 s24, s26
ds_read_b64_tr_b16 v[212:213], v8 offset:20480
s_waitcnt lgkmcnt(7)
ds_read_b64_tr_b16 v[214:215], v8 offset:22528
v_mfma_f32_16x16x32_bf16 a[240:243], v[192:195], a[0:3], a[240:243]
ds_read_b64_tr_b16 v[216:217], v8 offset:24576
ds_read_b64_tr_b16 v[218:219], v8 offset:26624
v_mfma_f32_16x16x32_bf16 a[240:243], v[196:199], a[4:7], a[240:243]
s_add_u32 s20, s20, s21
ds_read_b64_tr_b16 v[220:221], v8 offset:28672
s_waitcnt lgkmcnt(7)
ds_read_b64_tr_b16 v[222:223], v8 offset:30720
buffer_load_dword v15, v43, s[36:39], s58 offen sc1
v_mfma_f32_16x16x32_bf16 a[240:243], v[200:203], a[8:11], a[240:243]
s_add_u32 m0, s23, 2048
ds_read_b64_tr_b16 v[192:193], v9 offset:0
ds_read_b64_tr_b16 v[194:195], v9 offset:2048
buffer_load_dwordx4 v14, s[16:19], s20 offen sc1 lds
v_mfma_f32_16x16x32_bf16 a[240:243], v[204:207], a[12:15], a[240:243]
ds_read_b64_tr_b16 v[196:197], v9 offset:4096
s_waitcnt lgkmcnt(7)
ds_read_b64_tr_b16 v[198:199], v9 offset:6144
v_mfma_f32_16x16x32_bf16 a[240:243], v[208:211], a[16:19], a[240:243]
ds_read_b64_tr_b16 v[200:201], v9 offset:8192
ds_read_b64_tr_b16 v[202:203], v9 offset:10240
v_mfma_f32_16x16x32_bf16 a[240:243], v[212:215], a[20:23], a[240:243]
s_add_u32 s20, s20, s21
ds_read_b64_tr_b16 v[204:205], v9 offset:12288
s_waitcnt lgkmcnt(7)
ds_read_b64_tr_b16 v[206:207], v9 offset:14336
v_mfma_f32_16x16x32_bf16 a[240:243], v[216:219], a[24:27], a[240:243]
s_add_u32 m0, s23, 3072
ds_read_b64_tr_b16 v[208:209], v9 offset:16384
ds_read_b64_tr_b16 v[210:211], v9 offset:18432
buffer_load_dwordx4 v14, s[16:19], s20 offen sc1 lds
v_mfma_f32_16x16x32_bf16 a[240:243], v[220:223], a[28:31], a[240:243]
ds_read_b64_tr_b16 v[212:213], v9 offset:20480
s_waitcnt lgkmcnt(7)
ds_read_b64_tr_b16 v[214:215], v9 offset:22528
v_mfma_f32_16x16x32_bf16 a[244:247], v[192:195], a[0:3], a[244:247]
ds_read_b64_tr_b16 v[216:217], v9 offset:24576
ds_read_b64_tr_b16 v[218:219], v9 offset:26624
v_mfma_f32_16x16x32_bf16 a[244:247], v[196:199], a[4:7], a[244:247]
s_add_u32 s20, s20, s21
ds_read_b64_tr_b16 v[220:221], v9 offset:28672
s_waitcnt lgkmcnt(7)
ds_read_b64_tr_b16 v[222:223], v9 offset:30720
v_mfma_f32_16x16x32_bf16 a[244:247], v[200:203], a[8:11], a[244:247]
s_add_u32 m0, s23, 4096
ds_read_b64_tr_b16 v[192:193], v10 offset:0
ds_read_b64_tr_b16 v[194:195], v10 offset:2048
buffer_load_dwordx4 v14, s[16:19], s20 offen sc1 lds
v_mfma_f32_16x16x32_bf16 a[244:247], v[204:207], a[12:15], a[244:247]
ds_read_b64_tr_b16 v[196:197], v10 offset:4096
s_waitcnt lgkmcnt(7)
ds_read_b64_tr_b16 v[198:199], v10 offset:6144
v_mfma_f32_16x16x32_bf16 a[244:247], v[208:211], a[16:19], a[244:247]
ds_read_b64_tr_b16 v[200:201], v10 offset:8192
ds_read_b64_tr_b16 v[202:203], v10 offset:10240
v_mfma_f32_16x16x32_bf16 a[244:247], v[212:215], a[20:23], a[244:247]
s_add_u32 s20, s20, s21
ds_read_b64_tr_b16 v[204:205], v10 offset:12288
s_waitcnt lgkmcnt(7)
ds_read_b64_tr_b16 v[206:207], v10 offset:14336
v_mfma_f32_16x16x32_bf16 a[244:247], v[216:219], a[24:27], a[244:247]
s_add_u32 m0, s23, 5120
ds_read_b64_tr_b16 v[208:209], v10 offset:16384
ds_read_b64_tr_b16 v[210:211], v10 offset:18432
buffer_load_dwordx4 v14, s[16:19], s20 offen sc1 lds
v_mfma_f32_16x16x32_bf16 a[244:247], v[220:223], a[28:31], a[244:247]
ds_read_b64_tr_b16 v[212:213], v10 offset:20480
s_waitcnt lgkmcnt(7)
ds_read_b64_tr_b16 v[214:215], v10 offset:22528
v_mfma_f32_16x16x32_bf16 a[248:251], v[192:195], a[0:3], a[248:251]
ds_read_b64_tr_b16 v[216:217], v10 offset:24576
ds_read_b64_tr_b16 v[218:219], v10 offset:26624
v_mfma_f32_16x16x32_bf16 a[248:251], v[196:199], a[4:7], a[248:251]
s_add_u32 s20, s20, s21
ds_read_b64_tr_b16 v[220:221], v10 offset:28672
s_waitcnt lgkmcnt(7)
ds_read_b64_tr_b16 v[222:223], v10 offset:30720
v_mfma_f32_16x16x32_bf16 a[248:251], v[200:203], a[8:11], a[248:251]
s_add_u32 m0, s23, 6144
ds_read_b64_tr_b16 v[192:193], v11 offset:0
ds_read_b64_tr_b16 v[194:195], v11 offset:2048
buffer_load_dwordx4 v14, s[16:19], s20 offen sc1 lds
v_mfma_f32_16x16x32_bf16 a[248:251], v[204:207], a[12:15], a[248:251]
s_add_u32 s26, s24, 32768
ds_read_b64_tr_b16 v[196:197], v11 offset:4096
s_waitcnt lgkmcnt(7)
ds_read_b64_tr_b16 v[198:199], v11 offset:6144
v_mfma_f32_16x16x32_bf16 a[248:251], v[208:211], a[16:19], a[248:251]
s_sub_u32 s35, s26, 163840
ds_read_b64_tr_b16 v[200:201], v11 offset:8192
s_cselect_b32 s26, s26, s35
ds_read_b64_tr_b16 v[202:203], v11 offset:10240
v_mfma_f32_16x16x32_bf16 a[248:251], v[212:215], a[20:23], a[248:251]
s_add_u32 s20, s20, s21
ds_read_b64_tr_b16 v[204:205], v11 offset:12288
s_waitcnt lgkmcnt(7)
ds_read_b64_tr_b16 v[206:207], v11 offset:14336
v_mfma_f32_16x16x32_bf16 a[248:251], v[216:219], a[24:27], a[248:251]
s_add_u32 m0, s23, 7168
ds_read_b64_tr_b16 v[208:209], v11 offset:16384
ds_read_b64_tr_b16 v[210:211], v11 offset:18432
buffer_load_dwordx4 v14, s[16:19], s20 offen sc1 lds
v_mfma_f32_16x16x32_bf16 a[248:251], v[220:223], a[28:31], a[248:251]
s_add_u32 s23, s27, s33
ds_read_b64_tr_b16 v[212:213], v11 offset:20480
v_readlane_b32 s16, v16, s22
s_waitcnt lgkmcnt(7)
ds_read_b64_tr_b16 v[214:215], v11 offset:22528
v_readlane_b32 s17, v17, s22
v_mfma_f32_16x16x32_bf16 a[252:255], v[192:195], a[0:3], a[252:255]
s_waitcnt vmcnt(16)
ds_read_b64_tr_b16 v[216:217], v11 offset:24576
v_readlane_b32 s25, v18, s22
s_barrier
ds_read_b64_tr_b16 v[218:219], v11 offset:26624
v_add_u32 v8, s26, v4
v_mfma_f32_16x16x32_bf16 a[252:255], v[196:199], a[4:7], a[252:255]
s_mov_b32 s20, 0
ds_read_b64_tr_b16 v[220:221], v11 offset:28672
v_mad_u32_u24 v14, v12, s25, v13
s_waitcnt lgkmcnt(7)
ds_read_b64_tr_b16 v[222:223], v11 offset:30720
v_add_u32 v9, s26, v5
v_mfma_f32_16x16x32_bf16 a[252:255], v[200:203], a[8:11], a[252:255]
s_mov_b32 m0, s23
ds_read_b64_tr_b16 v[192:193], v8 offset:0
ds_read_b64_tr_b16 v[194:195], v8 offset:2048
v_add_u32 v10, s26, v6
buffer_load_dwordx4 v14, s[16:19], s20 offen sc1 lds
v_mfma_f32_16x16x32_bf16 a[252:255], v[204:207], a[12:15], a[252:255]
s_add_u32 s22, s22, 1
ds_read_b64_tr_b16 v[196:197], v8 offset:4096
v_add_u32 v11, s26, v7
s_waitcnt lgkmcnt(7)
ds_read_b64_tr_b16 v[198:199], v8 offset:6144
v_mfma_f32_16x16x32_bf16 a[252:255], v[208:211], a[16:19], a[252:255]
s_lshl_b32 s21, s25, 3
ds_read_b64_tr_b16 v[200:201], v8 offset:8192
s_mov_b32 s27, s24
ds_read_b64_tr_b16 v[202:203], v8 offset:10240
v_mfma_f32_16x16x32_bf16 a[252:255], v[212:215], a[20:23], a[252:255]
s_add_u32 s20, s20, s21
ds_read_b64_tr_b16 v[204:205], v8 offset:12288
s_waitcnt lgkmcnt(7)
ds_read_b64_tr_b16 v[206:207], v8 offset:14336
v_mfma_f32_16x16x32_bf16 a[252:255], v[216:219], a[24:27], a[252:255]
s_add_u32 m0, s23, 1024
ds_read_b64_tr_b16 v[208:209], v8 offset:16384
ds_read_b64_tr_b16 v[210:211], v8 offset:18432
buffer_load_dwordx4 v14, s[16:19], s20 offen sc1 lds
v_mfma_f32_16x16x32_bf16 a[252:255], v[220:223], a[28:31], a[252:255]
s_mov_b32 s24, s26
ds_read_b64_tr_b16 v[212:213], v8 offset:20480
s_waitcnt lgkmcnt(7)
ds_read_b64_tr_b16 v[214:215], v8 offset:22528
s_cmp_eq_u32 s40, 0
s_cbranch_scc1 FDNOQ_%=
s_waitcnt vmcnt(8)
v_readfirstlane_b32 s48, v15
s_cmp_eq_u32 s48, s98
s_cbranch_scc1 FLGOKq_%=
s_waitcnt vmcnt(0)
buffer_store_dword v42, v43, s[36:39], s56 offen sc1
buffer_store_dword v42, v43, s[36:39], s12 offen sc1
s_mov_b32 s47, 0
POLLq_%=:
buffer_load_dword v15, v43, s[36:39], s58 offen sc1
s_waitcnt vmcnt(0)
v_readfirstlane_b32 s48, v15
s_add_u32 s47, s47, 1
s_cmp_eq_u32 s48, s98
s_cbranch_scc1 FLGOKq_%=
s_sleep 1
s_cmp_lt_u32 s47, 0x40000
s_cbranch_scc1 POLLq_%=
FLGOKq_%=:
s_add_u32 s48, s59, 0
buffer_load_dwordx4 v[64:67], v2, s[36:39], s48 offen offset:0 sc1
s_add_u32 s48, s59, 0
buffer_load_dwordx4 v[68:71], v2, s[36:39], s48 offen offset:1024 sc1
s_add_u32 s48, s59, 0
buffer_load_dwordx4 v[72:75], v2, s[36:39], s48 offen offset:2048 sc1
s_add_u32 s48, s59, 0
buffer_load_dwordx4 v[76:79], v2, s[36:39], s48 offen offset:3072 sc1
s_add_u32 s48, s59, 4096
buffer_load_dwordx4 v[80:83], v2, s[36:39], s48 offen offset:0 sc1
s_add_u32 s48, s59, 4096
buffer_load_dwordx4 v[84:87], v2, s[36:39], s48 offen offset:1024 sc1
s_add_u32 s48, s59, 4096
buffer_load_dwordx4 v[88:91], v2, s[36:39], s48 offen offset:2048 sc1
s_add_u32 s48, s59, 4096
buffer_load_dwordx4 v[92:95], v2, s[36:39], s48 offen offset:3072 sc1
s_add_u32 s48, s59, 8192
buffer_load_dwordx4 v[96:99], v2, s[36:39], s48 offen offset:0 sc1
s_add_u32 s48, s59, 8192
buffer_load_dwordx4 v[100:103], v2, s[36:39], s48 offen offset:1024 sc1
s_add_u32 s48, s59, 8192
buffer_load_dwordx4 v[104:107], v2, s[36:39], s48 offen offset:2048 sc1
s_add_u32 s48, s59, 8192
buffer_load_dwordx4 v[108:111], v2, s[36:39], s48 offen offset:3072 sc1
s_add_u32 s48, s59, 12288
buffer_load_dwordx4 v[112:115], v2, s[36:39], s48 offen offset:0 sc1
s_add_u32 s48, s59, 12288
buffer_load_dwordx4 v[116:119], v2, s[36:39], s48 offen offset:1024 sc1
s_add_u32 s48, s59, 12288
buffer_load_dwordx4 v[120:123], v2, s[36:39], s48 offen offset:2048 sc1
s_add_u32 s48, s59, 12288
buffer_load_dwordx4 v[124:127], v2, s[36:39], s48 offen offset:3072 sc1
FDNOQ_%=:
v_mfma_f32_16x16x32_bf16 a[240:243], v[192:195], a[32:35], a[240:243]
ds_read_b64_tr_b16 v[216:217], v8 offset:24576
ds_read_b64_tr_b16 v[218:219], v8 offset:26624
v_mfma_f32_16x16x32_bf16 a[240:243], v[196:199], a[36:39], a[240:243]
s_add_u32 s20, s20, s21
ds_read_b64_tr_b16 v[220:221], v8 offset:28672
s_waitcnt lgkmcnt(7)
ds_read_b64_tr_b16 v[222:223], v8 offset:30720
v_mfma_f32_16x16x32_bf16 a[240:243], v[200:203], a[40:43], a[240:243]
s_add_u32 m0, s23, 2048
ds_read_b64_tr_b16 v[192:193], v9 offset:0
ds_read_b64_tr_b16 v[194:195], v9 offset:2048
buffer_load_dwordx4 v14, s[16:19], s20 offen sc1 lds
v_mfma_f32_16x16x32_bf16 a[240:243], v[204:207], a[44:47], a[240:243]
ds_read_b64_tr_b16 v[196:197], v9 offset:4096
s_waitcnt lgkmcnt(7)
ds_read_b64_tr_b16 v[198:199], v9 offset:6144
v_mfma_f32_16x16x32_bf16 a[240:243], v[208:211], a[48:51], a[240:243]
ds_read_b64_tr_b16 v[200:201], v9 offset:8192
ds_read_b64_tr_b16 v[202:203], v9 offset:10240
v_mfma_f32_16x16x32_bf16 a[240:243], v[212:215], a[52:55], a[240:243]
s_add_u32 s20, s20, s21
ds_read_b64_tr_b16 v[204:205], v9 offset:12288
s_waitcnt lgkmcnt(7)
ds_read_b64_tr_b16 v[206:207], v9 offset:14336
v_mfma_f32_16x16x32_bf16 a[240:243], v[216:219], a[56:59], a[240:243]
s_add_u32 m0, s23, 3072
ds_read_b64_tr_b16 v[208:209], v9 offset:16384
ds_read_b64_tr_b16 v[210:211], v9 offset:18432
buffer_load_dwordx4 v14, s[16:19], s20 offen sc1 lds
v_mfma_f32_16x16x32_bf16 a[240:243], v[220:223], a[60:63], a[240:243]
ds_read_b64_tr_b16 v[212:213], v9 offset:20480
s_waitcnt lgkmcnt(7)
ds_read_b64_tr_b16 v[214:215], v9 offset:22528
v_mfma_f32_16x16x32_bf16 a[244:247], v[192:195], a[32:35], a[244:247]
ds_read_b64_tr_b16 v[216:217], v9 offset:24576
ds_read_b64_tr_b16 v[218:219], v9 offset:26624
v_mfma_f32_16x16x32_bf16 a[244:247], v[196:199], a[36:39], a[244:247]
s_add_u32 s20, s20, s21
ds_read_b64_tr_b16 v[220:221], v9 offset:28672
s_waitcnt lgkmcnt(7)
ds_read_b64_tr_b16 v[222:223], v9 offset:30720
v_mfma_f32_16x16x32_bf16 a[244:247], v[200:203], a[40:43], a[244:247]
s_add_u32 m0, s23, 4096
ds_read_b64_tr_b16 v[192:193], v10 offset:0
ds_read_b64_tr_b16 v[194:195], v10 offset:2048
buffer_load_dwordx4 v14, s[16:19], s20 offen sc1 lds
v_mfma_f32_16x16x32_bf16 a[244:247], v[204:207], a[44:47], a[244:247]
ds_read_b64_tr_b16 v[196:197], v10 offset:4096
s_waitcnt lgkmcnt(7)
ds_read_b64_tr_b16 v[198:199], v10 offset:6144
v_mfma_f32_16x16x32_bf16 a[244:247], v[208:211], a[48:51], a[244:247]
ds_read_b64_tr_b16 v[200:201], v10 offset:8192
ds_read_b64_tr_b16 v[202:203], v10 offset:10240
v_mfma_f32_16x16x32_bf16 a[244:247], v[212:215], a[52:55], a[244:247]
s_add_u32 s20, s20, s21
ds_read_b64_tr_b16 v[204:205], v10 offset:12288
s_waitcnt lgkmcnt(7)
ds_read_b64_tr_b16 v[206:207], v10 offset:14336
v_mfma_f32_16x16x32_bf16 a[244:247], v[216:219], a[56:59], a[244:247]
s_add_u32 m0, s23, 5120
ds_read_b64_tr_b16 v[208:209], v10 offset:16384
ds_read_b64_tr_b16 v[210:211], v10 offset:18432
buffer_load_dwordx4 v14, s[16:19], s20 offen sc1 lds
v_mfma_f32_16x16x32_bf16 a[244:247], v[220:223], a[60:63], a[244:247]
ds_read_b64_tr_b16 v[212:213], v10 offset:20480
s_waitcnt lgkmcnt(7)
ds_read_b64_tr_b16 v[214:215], v10 offset:22528
v_mfma_f32_16x16x32_bf16 a[248:251], v[192:195], a[32:35], a[248:251]
ds_read_b64_tr_b16 v[216:217], v10 offset:24576
ds_read_b64_tr_b16 v[218:219], v10 offset:26624
v_mfma_f32_16x16x32_bf16 a[248:251], v[196:199], a[36:39], a[248:251]
s_add_u32 s20, s20, s21
ds_read_b64_tr_b16 v[220:221], v10 offset:28672
s_waitcnt lgkmcnt(7)
ds_read_b64_tr_b16 v[222:223], v10 offset:30720
v_mfma_f32_16x16x32_bf16 a[248:251], v[200:203], a[40:43], a[248:251]
s_add_u32 m0, s23, 6144
ds_read_b64_tr_b16 v[192:193], v11 offset:0
ds_read_b64_tr_b16 v[194:195], v11 offset:2048
buffer_load_dwordx4 v14, s[16:19], s20 offen sc1 lds
v_mfma_f32_16x16x32_bf16 a[248:251], v[204:207], a[44:47], a[248:251]
s_add_u32 s26, s24, 32768
ds_read_b64_tr_b16 v[196:197], v11 offset:4096
s_waitcnt lgkmcnt(7)
ds_read_b64_tr_b16 v[198:199], v11 offset:6144
v_mfma_f32_16x16x32_bf16 a[248:251], v[208:211], a[48:51], a[248:251]
s_sub_u32 s35, s26, 163840
ds_read_b64_tr_b16 v[200:201], v11 offset:8192
s_cselect_b32 s26, s26, s35
ds_read_b64_tr_b16 v[202:203], v11 offset:10240
v_mfma_f32_16x16x32_bf16 a[248:251], v[212:215], a[52:55], a[248:251]
s_add_u32 s20, s20, s21
ds_read_b64_tr_b16 v[204:205], v11 offset:12288
s_waitcnt lgkmcnt(7)
ds_read_b64_tr_b16 v[206:207], v11 offset:14336
v_mfma_f32_16x16x32_bf16 a[248:251], v[216:219], a[56:59], a[248:251]
s_add_u32 m0, s23, 7168
ds_read_b64_tr_b16 v[208:209], v11 offset:16384
ds_read_b64_tr_b16 v[210:211], v11 offset:18432
buffer_load_dwordx4 v14, s[16:19], s20 offen sc1 lds
v_mfma_f32_16x16x32_bf16 a[248:251], v[220:223], a[60:63], a[248:251]
s_add_u32 s23, s27, s33
ds_read_b64_tr_b16 v[212:213], v11 offset:20480
v_readlane_b32 s16, v16, s22
s_waitcnt lgkmcnt(7)
ds_read_b64_tr_b16 v[214:215], v11 offset:22528
v_readlane_b32 s17, v17, s22
v_mfma_f32_16x16x32_bf16 a[252:255], v[192:195], a[32:35], a[252:255]
s_waitcnt vmcnt(17)
ds_read_b64_tr_b16 v[216:217], v11 offset:24576
v_readlane_b32 s25, v18, s22
s_barrier
ds_read_b64_tr_b16 v[218:219], v11 offset:26624
v_mfma_f32_16x16x32_bf16 a[252:255], v[196:199], a[36:39], a[252:255]
s_mov_b32 s20, 0
ds_read_b64_tr_b16 v[220:221], v11 offset:28672
v_mad_u32_u24 v14, v12, s25, v13
s_waitcnt lgkmcnt(7)
ds_read_b64_tr_b16 v[222:223], v11 offset:30720
v_add_u32 v3, s26, v2
v_mfma_f32_16x16x32_bf16 a[252:255], v[200:203], a[40:43], a[252:255]
s_mov_b32 m0, s23
ds_read_b128 v[192:195], v3 offset:0
buffer_load_dwordx4 v14, s[16:19], s20 offen sc1 lds
v_mfma_f32_16x16x32_bf16 a[252:255], v[204:207], a[44:47], a[252:255]
s_add_u32 s22, s22, 1
ds_read_b128 v[196:199], v3 offset:1024
s_waitcnt lgkmcnt(6)
v_mfma_f32_16x16x32_bf16 a[252:255], v[208:211], a[48:51], a[252:255]
s_lshl_b32 s21, s25, 3
ds_read_b128 v[200:203], v3 offset:2048
s_mov_b32 s27, s24
v_mfma_f32_16x16x32_bf16 a[252:255], v[212:215], a[52:55], a[252:255]
s_add_u32 s20, s20, s21
ds_read_b128 v[204:207], v3 offset:3072
s_waitcnt lgkmcnt(4)
v_mfma_f32_16x16x32_bf16 a[252:255], v[216:219], a[56:59], a[252:255]
s_add_u32 m0, s23, 1024
ds_read_b128 v[208:211], v3 offset:4096
buffer_load_dwordx4 v14, s[16:19], s20 offen sc1 lds
v_mfma_f32_16x16x32_bf16 a[252:255], v[220:223], a[60:63], a[252:255]
s_mov_b32 s24, s26
ds_read_b128 v[212:215], v3 offset:5120
s_waitcnt lgkmcnt(4)
s_waitcnt vmcnt(8)
s_nop 7
s_setpc_b64 s[70:71]
GUEPI_%=:
s_lshl_b32 s47, s41, 3
s_lshl_b32 s48, s28, 1
s_add_u32 s47, s47, s48
s_mul_i32 s47, s47, 57344
s_lshl_b32 s48, s30, 12
s_add_u32 s47, s47, s48
s_add_u32 s47, s47, 62914560
s_mov_b32 s0, 0xbfb8aa3b
s_mov_b32 s1, 0xbfb8aa3b
s_mov_b32 s2, 1.0
s_mov_b32 s3, 1.0
v_accvgpr_read_b32 v128, a0
v_accvgpr_read_b32 v136, a64
v_accvgpr_read_b32 v129, a1
v_accvgpr_read_b32 v137, a65
v_accvgpr_read_b32 v130, a2
v_accvgpr_read_b32 v138, a66
v_accvgpr_read_b32 v131, a3
v_accvgpr_read_b32 v139, a67
v_accvgpr_read_b32 v132, a8
v_accvgpr_read_b32 v140, a72
v_accvgpr_read_b32 v133, a9
v_accvgpr_read_b32 v141, a73
v_accvgpr_read_b32 v134, a10
v_accvgpr_read_b32 v142, a74
v_accvgpr_read_b32 v135, a11
v_accvgpr_read_b32 v143, a75
v_pk_mul_f32 v[144:145], v[128:129], s[0:1]
v_pk_mul_f32 v[146:147], v[130:131], s[0:1]
v_pk_mul_f32 v[148:149], v[132:133], s[0:1]
v_pk_mul_f32 v[150:151], v[134:135], s[0:1]
v_exp_f32 v144, v144
v_exp_f32 v145, v145
v_exp_f32 v146, v146
v_exp_f32 v147, v147
v_exp_f32 v148, v148
v_exp_f32 v149, v149
v_exp_f32 v150, v150
v_exp_f32 v151, v151
v_pk_mul_f32 v[128:129], v[128:129], v[136:137]
v_pk_mul_f32 v[130:131], v[130:131], v[138:139]
v_pk_mul_f32 v[132:133], v[132:133], v[140:141]
v_pk_mul_f32 v[134:135], v[134:135], v[142:143]
v_pk_add_f32 v[144:145], v[144:145], s[2:3]
v_pk_add_f32 v[146:147], v[146:147], s[2:3]
v_pk_add_f32 v[148:149], v[148:149], s[2:3]
v_pk_add_f32 v[150:151], v[150:151], s[2:3]
v_rcp_f32 v144, v144
v_rcp_f32 v145, v145
v_rcp_f32 v146, v146
v_rcp_f32 v147, v147
v_rcp_f32 v148, v148
v_rcp_f32 v149, v149
v_rcp_f32 v150, v150
v_rcp_f32 v151, v151
s_nop 0
v_pk_mul_f32 v[128:129], v[128:129], v[144:145]
v_pk_mul_f32 v[130:131], v[130:131], v[146:147]
v_pk_mul_f32 v[132:133], v[132:133], v[148:149]
v_pk_mul_f32 v[134:135], v[134:135], v[150:151]
v_cvt_pk_bf16_f32 v152, v128, v129
v_cvt_pk_bf16_f32 v153, v130, v131
v_cvt_pk_bf16_f32 v154, v132, v133
v_cvt_pk_bf16_f32 v155, v134, v135
s_add_u32 s48, s47, 0
buffer_store_dwordx4 v[152:155], v2, s[36:39], s48 offen offset:0 sc1
v_accvgpr_read_b32 v160, a16
v_accvgpr_read_b32 v168, a80
v_accvgpr_read_b32 v161, a17
v_accvgpr_read_b32 v169, a81
v_accvgpr_read_b32 v162, a18
v_accvgpr_read_b32 v170, a82
v_accvgpr_read_b32 v163, a19
v_accvgpr_read_b32 v171, a83
v_accvgpr_read_b32 v164, a24
v_accvgpr_read_b32 v172, a88
v_accvgpr_read_b32 v165, a25
v_accvgpr_read_b32 v173, a89
v_accvgpr_read_b32 v166, a26
v_accvgpr_read_b32 v174, a90
v_accvgpr_read_b32 v167, a27
v_accvgpr_read_b32 v175, a91
v_pk_mul_f32 v[176:177], v[160:161], s[0:1]
v_pk_mul_f32 v[178:179], v[162:163], s[0:1]
v_pk_mul_f32 v[180:181], v[164:165], s[0:1]
v_pk_mul_f32 v[182:183], v[166:167], s[0:1]
v_exp_f32 v176, v176
v_exp_f32 v177, v177
v_exp_f32 v178, v178
v_exp_f32 v179, v179
v_exp_f32 v180, v180
v_exp_f32 v181, v181
v_exp_f32 v182, v182
v_exp_f32 v183, v183
v_pk_mul_f32 v[160:161], v[160:161], v[168:169]
v_pk_mul_f32 v[162:163], v[162:163], v[170:171]
v_pk_mul_f32 v[164:165], v[164:165], v[172:173]
v_pk_mul_f32 v[166:167], v[166:167], v[174:175]
v_pk_add_f32 v[176:177], v[176:177], s[2:3]
v_pk_add_f32 v[178:179], v[178:179], s[2:3]
v_pk_add_f32 v[180:181], v[180:181], s[2:3]
v_pk_add_f32 v[182:183], v[182:183], s[2:3]
v_rcp_f32 v176, v176
v_rcp_f32 v177, v177
v_rcp_f32 v178, v178
v_rcp_f32 v179, v179
v_rcp_f32 v180, v180
v_rcp_f32 v181, v181
v_rcp_f32 v182, v182
v_rcp_f32 v183, v183
s_nop 0
v_pk_mul_f32 v[160:161], v[160:161], v[176:177]
v_pk_mul_f32 v[162:163], v[162:163], v[178:179]
v_pk_mul_f32 v[164:165], v[164:165], v[180:181]
v_pk_mul_f32 v[166:167], v[166:167], v[182:183]
v_cvt_pk_bf16_f32 v184, v160, v161
v_cvt_pk_bf16_f32 v185, v162, v163
v_cvt_pk_bf16_f32 v186, v164, v165
v_cvt_pk_bf16_f32 v187, v166, v167
s_add_u32 s48, s47, 0
buffer_store_dwordx4 v[184:187], v2, s[36:39], s48 offen offset:1024 sc1
v_accvgpr_read_b32 v128, a32
v_accvgpr_read_b32 v136, a96
v_accvgpr_read_b32 v129, a33
v_accvgpr_read_b32 v137, a97
v_accvgpr_read_b32 v130, a34
v_accvgpr_read_b32 v138, a98
v_accvgpr_read_b32 v131, a35
v_accvgpr_read_b32 v139, a99
v_accvgpr_read_b32 v132, a40
v_accvgpr_read_b32 v140, a104
v_accvgpr_read_b32 v133, a41
v_accvgpr_read_b32 v141, a105
v_accvgpr_read_b32 v134, a42
v_accvgpr_read_b32 v142, a106
v_accvgpr_read_b32 v135, a43
v_accvgpr_read_b32 v143, a107
v_pk_mul_f32 v[144:145], v[128:129], s[0:1]
v_pk_mul_f32 v[146:147], v[130:131], s[0:1]
v_pk_mul_f32 v[148:149], v[132:133], s[0:1]
v_pk_mul_f32 v[150:151], v[134:135], s[0:1]
v_exp_f32 v144, v144
v_exp_f32 v145, v145
v_exp_f32 v146, v146
v_exp_f32 v147, v147
v_exp_f32 v148, v148
v_exp_f32 v149, v149
v_exp_f32 v150, v150
v_exp_f32 v151, v151
v_pk_mul_f32 v[128:129], v[128:129], v[136:137]
v_pk_mul_f32 v[130:131], v[130:131], v[138:139]
v_pk_mul_f32 v[132:133], v[132:133], v[140:141]
v_pk_mul_f32 v[134:135], v[134:135], v[142:143]
v_pk_add_f32 v[144:145], v[144:145], s[2:3]
v_pk_add_f32 v[146:147], v[146:147], s[2:3]
v_pk_add_f32 v[148:149], v[148:149], s[2:3]
v_pk_add_f32 v[150:151], v[150:151], s[2:3]
v_rcp_f32 v144, v144
v_rcp_f32 v145, v145
v_rcp_f32 v146, v146
v_rcp_f32 v147, v147
v_rcp_f32 v148, v148
v_rcp_f32 v149, v149
v_rcp_f32 v150, v150
v_rcp_f32 v151, v151
s_nop 0
v_pk_mul_f32 v[128:129], v[128:129], v[144:145]
v_pk_mul_f32 v[130:131], v[130:131], v[146:147]
v_pk_mul_f32 v[132:133], v[132:133], v[148:149]
v_pk_mul_f32 v[134:135], v[134:135], v[150:151]
v_cvt_pk_bf16_f32 v152, v128, v129
v_cvt_pk_bf16_f32 v153, v130, v131
v_cvt_pk_bf16_f32 v154, v132, v133
v_cvt_pk_bf16_f32 v155, v134, v135
s_add_u32 s48, s47, 0
buffer_store_dwordx4 v[152:155], v2, s[36:39], s48 offen offset:2048 sc1
v_accvgpr_read_b32 v160, a48
v_accvgpr_read_b32 v168, a112
v_accvgpr_read_b32 v161, a49
v_accvgpr_read_b32 v169, a113
v_accvgpr_read_b32 v162, a50
v_accvgpr_read_b32 v170, a114
v_accvgpr_read_b32 v163, a51
v_accvgpr_read_b32 v171, a115
v_accvgpr_read_b32 v164, a56
v_accvgpr_read_b32 v172, a120
v_accvgpr_read_b32 v165, a57
v_accvgpr_read_b32 v173, a121
v_accvgpr_read_b32 v166, a58
v_accvgpr_read_b32 v174, a122
v_accvgpr_read_b32 v167, a59
v_accvgpr_read_b32 v175, a123
v_pk_mul_f32 v[176:177], v[160:161], s[0:1]
v_pk_mul_f32 v[178:179], v[162:163], s[0:1]
v_pk_mul_f32 v[180:181], v[164:165], s[0:1]
v_pk_mul_f32 v[182:183], v[166:167], s[0:1]
v_exp_f32 v176, v176
v_exp_f32 v177, v177
v_exp_f32 v178, v178
v_exp_f32 v179, v179
v_exp_f32 v180, v180
v_exp_f32 v181, v181
v_exp_f32 v182, v182
v_exp_f32 v183, v183
v_pk_mul_f32 v[160:161], v[160:161], v[168:169]
v_pk_mul_f32 v[162:163], v[162:163], v[170:171]
v_pk_mul_f32 v[164:165], v[164:165], v[172:173]
v_pk_mul_f32 v[166:167], v[166:167], v[174:175]
v_pk_add_f32 v[176:177], v[176:177], s[2:3]
v_pk_add_f32 v[178:179], v[178:179], s[2:3]
v_pk_add_f32 v[180:181], v[180:181], s[2:3]
v_pk_add_f32 v[182:183], v[182:183], s[2:3]
v_rcp_f32 v176, v176
v_rcp_f32 v177, v177
v_rcp_f32 v178, v178
v_rcp_f32 v179, v179
v_rcp_f32 v180, v180
v_rcp_f32 v181, v181
v_rcp_f32 v182, v182
v_rcp_f32 v183, v183
s_nop 0
v_pk_mul_f32 v[160:161], v[160:161], v[176:177]
v_pk_mul_f32 v[162:163], v[162:163], v[178:179]
v_pk_mul_f32 v[164:165], v[164:165], v[180:181]
v_pk_mul_f32 v[166:167], v[166:167], v[182:183]
v_cvt_pk_bf16_f32 v184, v160, v161
v_cvt_pk_bf16_f32 v185, v162, v163
v_cvt_pk_bf16_f32 v186, v164, v165
v_cvt_pk_bf16_f32 v187, v166, v167
s_add_u32 s48, s47, 0
buffer_store_dwordx4 v[184:187], v2, s[36:39], s48 offen offset:3072 sc1
v_accvgpr_read_b32 v128, a4
v_accvgpr_read_b32 v136, a68
v_accvgpr_read_b32 v129, a5
v_accvgpr_read_b32 v137, a69
v_accvgpr_read_b32 v130, a6
v_accvgpr_read_b32 v138, a70
v_accvgpr_read_b32 v131, a7
v_accvgpr_read_b32 v139, a71
v_accvgpr_read_b32 v132, a12
v_accvgpr_read_b32 v140, a76
v_accvgpr_read_b32 v133, a13
v_accvgpr_read_b32 v141, a77
v_accvgpr_read_b32 v134, a14
v_accvgpr_read_b32 v142, a78
v_accvgpr_read_b32 v135, a15
v_accvgpr_read_b32 v143, a79
v_pk_mul_f32 v[144:145], v[128:129], s[0:1]
v_pk_mul_f32 v[146:147], v[130:131], s[0:1]
v_pk_mul_f32 v[148:149], v[132:133], s[0:1]
v_pk_mul_f32 v[150:151], v[134:135], s[0:1]
v_exp_f32 v144, v144
v_exp_f32 v145, v145
v_exp_f32 v146, v146
v_exp_f32 v147, v147
v_exp_f32 v148, v148
v_exp_f32 v149, v149
v_exp_f32 v150, v150
v_exp_f32 v151, v151
v_pk_mul_f32 v[128:129], v[128:129], v[136:137]
v_pk_mul_f32 v[130:131], v[130:131], v[138:139]
v_pk_mul_f32 v[132:133], v[132:133], v[140:141]
v_pk_mul_f32 v[134:135], v[134:135], v[142:143]
v_pk_add_f32 v[144:145], v[144:145], s[2:3]
v_pk_add_f32 v[146:147], v[146:147], s[2:3]
v_pk_add_f32 v[148:149], v[148:149], s[2:3]
v_pk_add_f32 v[150:151], v[150:151], s[2:3]
v_rcp_f32 v144, v144
v_rcp_f32 v145, v145
v_rcp_f32 v146, v146
v_rcp_f32 v147, v147
v_rcp_f32 v148, v148
v_rcp_f32 v149, v149
v_rcp_f32 v150, v150
v_rcp_f32 v151, v151
s_nop 0
v_pk_mul_f32 v[128:129], v[128:129], v[144:145]
v_pk_mul_f32 v[130:131], v[130:131], v[146:147]
v_pk_mul_f32 v[132:133], v[132:133], v[148:149]
v_pk_mul_f32 v[134:135], v[134:135], v[150:151]
v_cvt_pk_bf16_f32 v152, v128, v129
v_cvt_pk_bf16_f32 v153, v130, v131
v_cvt_pk_bf16_f32 v154, v132, v133
v_cvt_pk_bf16_f32 v155, v134, v135
s_add_u32 s48, s47, 57344
buffer_store_dwordx4 v[152:155], v2, s[36:39], s48 offen offset:0 sc1
v_accvgpr_read_b32 v160, a20
v_accvgpr_read_b32 v168, a84
v_accvgpr_read_b32 v161, a21
v_accvgpr_read_b32 v169, a85
v_accvgpr_read_b32 v162, a22
v_accvgpr_read_b32 v170, a86
v_accvgpr_read_b32 v163, a23
v_accvgpr_read_b32 v171, a87
v_accvgpr_read_b32 v164, a28
v_accvgpr_read_b32 v172, a92
v_accvgpr_read_b32 v165, a29
v_accvgpr_read_b32 v173, a93
v_accvgpr_read_b32 v166, a30
v_accvgpr_read_b32 v174, a94
v_accvgpr_read_b32 v167, a31
v_accvgpr_read_b32 v175, a95
v_pk_mul_f32 v[176:177], v[160:161], s[0:1]
v_pk_mul_f32 v[178:179], v[162:163], s[0:1]
v_pk_mul_f32 v[180:181], v[164:165], s[0:1]
v_pk_mul_f32 v[182:183], v[166:167], s[0:1]
v_exp_f32 v176, v176
v_exp_f32 v177, v177
v_exp_f32 v178, v178
v_exp_f32 v179, v179
v_exp_f32 v180, v180
v_exp_f32 v181, v181
v_exp_f32 v182, v182
v_exp_f32 v183, v183
v_pk_mul_f32 v[160:161], v[160:161], v[168:169]
v_pk_mul_f32 v[162:163], v[162:163], v[170:171]
v_pk_mul_f32 v[164:165], v[164:165], v[172:173]
v_pk_mul_f32 v[166:167], v[166:167], v[174:175]
v_pk_add_f32 v[176:177], v[176:177], s[2:3]
v_pk_add_f32 v[178:179], v[178:179], s[2:3]
v_pk_add_f32 v[180:181], v[180:181], s[2:3]
v_pk_add_f32 v[182:183], v[182:183], s[2:3]
v_rcp_f32 v176, v176
v_rcp_f32 v177, v177
v_rcp_f32 v178, v178
v_rcp_f32 v179, v179
v_rcp_f32 v180, v180
v_rcp_f32 v181, v181
v_rcp_f32 v182, v182
v_rcp_f32 v183, v183
s_nop 0
v_pk_mul_f32 v[160:161], v[160:161], v[176:177]
v_pk_mul_f32 v[162:163], v[162:163], v[178:179]
v_pk_mul_f32 v[164:165], v[164:165], v[180:181]
v_pk_mul_f32 v[166:167], v[166:167], v[182:183]
v_cvt_pk_bf16_f32 v184, v160, v161
v_cvt_pk_bf16_f32 v185, v162, v163
v_cvt_pk_bf16_f32 v186, v164, v165
v_cvt_pk_bf16_f32 v187, v166, v167
s_add_u32 s48, s47, 57344
buffer_store_dwordx4 v[184:187], v2, s[36:39], s48 offen offset:1024 sc1
v_accvgpr_read_b32 v128, a36
v_accvgpr_read_b32 v136, a100
v_accvgpr_read_b32 v129, a37
v_accvgpr_read_b32 v137, a101
v_accvgpr_read_b32 v130, a38
v_accvgpr_read_b32 v138, a102
v_accvgpr_read_b32 v131, a39
v_accvgpr_read_b32 v139, a103
v_accvgpr_read_b32 v132, a44
v_accvgpr_read_b32 v140, a108
v_accvgpr_read_b32 v133, a45
v_accvgpr_read_b32 v141, a109
v_accvgpr_read_b32 v134, a46
v_accvgpr_read_b32 v142, a110
v_accvgpr_read_b32 v135, a47
v_accvgpr_read_b32 v143, a111
v_pk_mul_f32 v[144:145], v[128:129], s[0:1]
v_pk_mul_f32 v[146:147], v[130:131], s[0:1]
v_pk_mul_f32 v[148:149], v[132:133], s[0:1]
v_pk_mul_f32 v[150:151], v[134:135], s[0:1]
v_exp_f32 v144, v144
v_exp_f32 v145, v145
v_exp_f32 v146, v146
v_exp_f32 v147, v147
v_exp_f32 v148, v148
v_exp_f32 v149, v149
v_exp_f32 v150, v150
v_exp_f32 v151, v151
v_pk_mul_f32 v[128:129], v[128:129], v[136:137]
v_pk_mul_f32 v[130:131], v[130:131], v[138:139]
v_pk_mul_f32 v[132:133], v[132:133], v[140:141]
v_pk_mul_f32 v[134:135], v[134:135], v[142:143]
v_pk_add_f32 v[144:145], v[144:145], s[2:3]
v_pk_add_f32 v[146:147], v[146:147], s[2:3]
v_pk_add_f32 v[148:149], v[148:149], s[2:3]
v_pk_add_f32 v[150:151], v[150:151], s[2:3]
v_rcp_f32 v144, v144
v_rcp_f32 v145, v145
v_rcp_f32 v146, v146
v_rcp_f32 v147, v147
v_rcp_f32 v148, v148
v_rcp_f32 v149, v149
v_rcp_f32 v150, v150
v_rcp_f32 v151, v151
s_nop 0
v_pk_mul_f32 v[128:129], v[128:129], v[144:145]
v_pk_mul_f32 v[130:131], v[130:131], v[146:147]
v_pk_mul_f32 v[132:133], v[132:133], v[148:149]
v_pk_mul_f32 v[134:135], v[134:135], v[150:151]
v_cvt_pk_bf16_f32 v152, v128, v129
v_cvt_pk_bf16_f32 v153, v130, v131
v_cvt_pk_bf16_f32 v154, v132, v133
v_cvt_pk_bf16_f32 v155, v134, v135
s_add_u32 s48, s47, 57344
buffer_store_dwordx4 v[152:155], v2, s[36:39], s48 offen offset:2048 sc1
v_accvgpr_read_b32 v160, a52
v_accvgpr_read_b32 v168, a116
v_accvgpr_read_b32 v161, a53
v_accvgpr_read_b32 v169, a117
v_accvgpr_read_b32 v162, a54
v_accvgpr_read_b32 v170, a118
v_accvgpr_read_b32 v163, a55
v_accvgpr_read_b32 v171, a119
v_accvgpr_read_b32 v164, a60
v_accvgpr_read_b32 v172, a124
v_accvgpr_read_b32 v165, a61
v_accvgpr_read_b32 v173, a125
v_accvgpr_read_b32 v166, a62
v_accvgpr_read_b32 v174, a126
v_accvgpr_read_b32 v167, a63
v_accvgpr_read_b32 v175, a127
v_pk_mul_f32 v[176:177], v[160:161], s[0:1]
v_pk_mul_f32 v[178:179], v[162:163], s[0:1]
v_pk_mul_f32 v[180:181], v[164:165], s[0:1]
v_pk_mul_f32 v[182:183], v[166:167], s[0:1]
v_exp_f32 v176, v176
v_exp_f32 v177, v177
v_exp_f32 v178, v178
v_exp_f32 v179, v179
v_exp_f32 v180, v180
v_exp_f32 v181, v181
v_exp_f32 v182, v182
v_exp_f32 v183, v183
v_pk_mul_f32 v[160:161], v[160:161], v[168:169]
v_pk_mul_f32 v[162:163], v[162:163], v[170:171]
v_pk_mul_f32 v[164:165], v[164:165], v[172:173]
v_pk_mul_f32 v[166:167], v[166:167], v[174:175]
v_pk_add_f32 v[176:177], v[176:177], s[2:3]
v_pk_add_f32 v[178:179], v[178:179], s[2:3]
v_pk_add_f32 v[180:181], v[180:181], s[2:3]
v_pk_add_f32 v[182:183], v[182:183], s[2:3]
v_rcp_f32 v176, v176
v_rcp_f32 v177, v177
v_rcp_f32 v178, v178
v_rcp_f32 v179, v179
v_rcp_f32 v180, v180
v_rcp_f32 v181, v181
v_rcp_f32 v182, v182
v_rcp_f32 v183, v183
s_nop 0
v_pk_mul_f32 v[160:161], v[160:161], v[176:177]
v_pk_mul_f32 v[162:163], v[162:163], v[178:179]
v_pk_mul_f32 v[164:165], v[164:165], v[180:181]
v_pk_mul_f32 v[166:167], v[166:167], v[182:183]
v_cvt_pk_bf16_f32 v184, v160, v161
v_cvt_pk_bf16_f32 v185, v162, v163
v_cvt_pk_bf16_f32 v186, v164, v165
v_cvt_pk_bf16_f32 v187, v166, v167
s_add_u32 s48, s47, 57344
buffer_store_dwordx4 v[184:187], v2, s[36:39], s48 offen offset:3072 sc1
s_setpc_b64 s[70:71]
QEPIA_%=:
s_lshl_b32 s50, s30, 4
s_add_u32 s50, s50, s41
s_lshl_b32 s50, s50, 2
s_add_u32 s50, s50, s28
s_lshl_b32 s50, s50, 14
s_add_u32 s50, s50, 35651584
s_add_u32 s51, s50, 4096
s_add_u32 s52, s50, 8192
s_add_u32 s53, s50, 12288
s_mov_b32 s54, 0x3db8aa3b
s_mov_b32 s55, 0x3db8aa3b
v_accvgpr_read_b32 v128, a0
v_accvgpr_read_b32 v129, a1
v_accvgpr_read_b32 v130, a2
v_accvgpr_read_b32 v131, a3
v_accvgpr_read_b32 v132, a8
v_accvgpr_read_b32 v133, a9
v_accvgpr_read_b32 v134, a10
v_accvgpr_read_b32 v135, a11
v_pk_mul_f32 v[128:129], v[128:129], s[54:55]
v_pk_mul_f32 v[130:131], v[130:131], s[54:55]
v_pk_mul_f32 v[132:133], v[132:133], s[54:55]
v_pk_mul_f32 v[134:135], v[134:135], s[54:55]
v_cvt_pk_bf16_f32 v64, v128, v129
v_cvt_pk_bf16_f32 v65, v130, v131
v_cvt_pk_bf16_f32 v66, v132, v133
v_cvt_pk_bf16_f32 v67, v134, v135
v_accvgpr_read_b32 v144, a4
v_accvgpr_read_b32 v145, a5
v_accvgpr_read_b32 v146, a6
v_accvgpr_read_b32 v147, a7
v_accvgpr_read_b32 v148, a12
v_accvgpr_read_b32 v149, a13
v_accvgpr_read_b32 v150, a14
v_accvgpr_read_b32 v151, a15
v_pk_mul_f32 v[144:145], v[144:145], s[54:55]
v_pk_mul_f32 v[146:147], v[146:147], s[54:55]
v_pk_mul_f32 v[148:149], v[148:149], s[54:55]
v_pk_mul_f32 v[150:151], v[150:151], s[54:55]
v_cvt_pk_bf16_f32 v68, v144, v145
v_cvt_pk_bf16_f32 v69, v146, v147
v_cvt_pk_bf16_f32 v70, v148, v149
v_cvt_pk_bf16_f32 v71, v150, v151
v_accvgpr_read_b32 v160, a16
v_accvgpr_read_b32 v161, a17
v_accvgpr_read_b32 v162, a18
v_accvgpr_read_b32 v163, a19
v_accvgpr_read_b32 v164, a24
v_accvgpr_read_b32 v165, a25
v_accvgpr_read_b32 v166, a26
v_accvgpr_read_b32 v167, a27
v_pk_mul_f32 v[160:161], v[160:161], s[54:55]
v_pk_mul_f32 v[162:163], v[162:163], s[54:55]
v_pk_mul_f32 v[164:165], v[164:165], s[54:55]
v_pk_mul_f32 v[166:167], v[166:167], s[54:55]
v_cvt_pk_bf16_f32 v72, v160, v161
v_cvt_pk_bf16_f32 v73, v162, v163
v_cvt_pk_bf16_f32 v74, v164, v165
v_cvt_pk_bf16_f32 v75, v166, v167
v_accvgpr_read_b32 v176, a20
v_accvgpr_read_b32 v177, a21
v_accvgpr_read_b32 v178, a22
v_accvgpr_read_b32 v179, a23
v_accvgpr_read_b32 v180, a28
v_accvgpr_read_b32 v181, a29
v_accvgpr_read_b32 v182, a30
v_accvgpr_read_b32 v183, a31
v_pk_mul_f32 v[176:177], v[176:177], s[54:55]
v_pk_mul_f32 v[178:179], v[178:179], s[54:55]
v_pk_mul_f32 v[180:181], v[180:181], s[54:55]
v_pk_mul_f32 v[182:183], v[182:183], s[54:55]
v_cvt_pk_bf16_f32 v76, v176, v177
v_cvt_pk_bf16_f32 v77, v178, v179
v_cvt_pk_bf16_f32 v78, v180, v181
v_cvt_pk_bf16_f32 v79, v182, v183
buffer_load_dwordx4 a[0:3], v47, s[36:39], s13 offen offset:0 sc1
buffer_load_dwordx4 a[4:7], v47, s[36:39], s13 offen offset:1024 sc1
buffer_load_dwordx4 a[8:11], v47, s[36:39], s13 offen offset:2048 sc1
buffer_load_dwordx4 a[12:15], v47, s[36:39], s13 offen offset:3072 sc1
v_add_u32 v47, 4096, v47
buffer_load_dwordx4 a[16:19], v47, s[36:39], s13 offen offset:0 sc1
buffer_load_dwordx4 a[20:23], v47, s[36:39], s13 offen offset:1024 sc1
buffer_load_dwordx4 a[24:27], v47, s[36:39], s13 offen offset:2048 sc1
buffer_load_dwordx4 a[28:31], v47, s[36:39], s13 offen offset:3072 sc1
v_add_u32 v47, 4096, v47
v_accvgpr_read_b32 v128, a32
v_accvgpr_read_b32 v129, a33
v_accvgpr_read_b32 v130, a34
v_accvgpr_read_b32 v131, a35
v_accvgpr_read_b32 v132, a40
v_accvgpr_read_b32 v133, a41
v_accvgpr_read_b32 v134, a42
v_accvgpr_read_b32 v135, a43
v_pk_mul_f32 v[128:129], v[128:129], s[54:55]
v_pk_mul_f32 v[130:131], v[130:131], s[54:55]
v_pk_mul_f32 v[132:133], v[132:133], s[54:55]
v_pk_mul_f32 v[134:135], v[134:135], s[54:55]
v_cvt_pk_bf16_f32 v80, v128, v129
v_cvt_pk_bf16_f32 v81, v130, v131
v_cvt_pk_bf16_f32 v82, v132, v133
v_cvt_pk_bf16_f32 v83, v134, v135
v_accvgpr_read_b32 v144, a36
v_accvgpr_read_b32 v145, a37
v_accvgpr_read_b32 v146, a38
v_accvgpr_read_b32 v147, a39
v_accvgpr_read_b32 v148, a44
v_accvgpr_read_b32 v149, a45
v_accvgpr_read_b32 v150, a46
v_accvgpr_read_b32 v151, a47
v_pk_mul_f32 v[144:145], v[144:145], s[54:55]
v_pk_mul_f32 v[146:147], v[146:147], s[54:55]
v_pk_mul_f32 v[148:149], v[148:149], s[54:55]
v_pk_mul_f32 v[150:151], v[150:151], s[54:55]
v_cvt_pk_bf16_f32 v84, v144, v145
v_cvt_pk_bf16_f32 v85, v146, v147
v_cvt_pk_bf16_f32 v86, v148, v149
v_cvt_pk_bf16_f32 v87, v150, v151
v_accvgpr_read_b32 v160, a48
v_accvgpr_read_b32 v161, a49
v_accvgpr_read_b32 v162, a50
v_accvgpr_read_b32 v163, a51
v_accvgpr_read_b32 v164, a56
v_accvgpr_read_b32 v165, a57
v_accvgpr_read_b32 v166, a58
v_accvgpr_read_b32 v167, a59
v_pk_mul_f32 v[160:161], v[160:161], s[54:55]
v_pk_mul_f32 v[162:163], v[162:163], s[54:55]
v_pk_mul_f32 v[164:165], v[164:165], s[54:55]
v_pk_mul_f32 v[166:167], v[166:167], s[54:55]
v_cvt_pk_bf16_f32 v88, v160, v161
v_cvt_pk_bf16_f32 v89, v162, v163
v_cvt_pk_bf16_f32 v90, v164, v165
v_cvt_pk_bf16_f32 v91, v166, v167
v_accvgpr_read_b32 v176, a52
v_accvgpr_read_b32 v177, a53
v_accvgpr_read_b32 v178, a54
v_accvgpr_read_b32 v179, a55
v_accvgpr_read_b32 v180, a60
v_accvgpr_read_b32 v181, a61
v_accvgpr_read_b32 v182, a62
v_accvgpr_read_b32 v183, a63
v_pk_mul_f32 v[176:177], v[176:177], s[54:55]
v_pk_mul_f32 v[178:179], v[178:179], s[54:55]
v_pk_mul_f32 v[180:181], v[180:181], s[54:55]
v_pk_mul_f32 v[182:183], v[182:183], s[54:55]
v_cvt_pk_bf16_f32 v92, v176, v177
v_cvt_pk_bf16_f32 v93, v178, v179
v_cvt_pk_bf16_f32 v94, v180, v181
v_cvt_pk_bf16_f32 v95, v182, v183
buffer_load_dwordx4 a[32:35], v47, s[36:39], s13 offen offset:0 sc1
buffer_load_dwordx4 a[36:39], v47, s[36:39], s13 offen offset:1024 sc1
buffer_load_dwordx4 a[40:43], v47, s[36:39], s13 offen offset:2048 sc1
buffer_load_dwordx4 a[44:47], v47, s[36:39], s13 offen offset:3072 sc1
v_add_u32 v47, 4096, v47
buffer_load_dwordx4 a[48:51], v47, s[36:39], s13 offen offset:0 sc1
buffer_load_dwordx4 a[52:55], v47, s[36:39], s13 offen offset:1024 sc1
buffer_load_dwordx4 a[56:59], v47, s[36:39], s13 offen offset:2048 sc1
buffer_load_dwordx4 a[60:63], v47, s[36:39], s13 offen offset:3072 sc1
v_add_u32 v47, 4096, v47
v_accvgpr_read_b32 v128, a64
v_accvgpr_read_b32 v129, a65
v_accvgpr_read_b32 v130, a66
v_accvgpr_read_b32 v131, a67
v_accvgpr_read_b32 v132, a72
v_accvgpr_read_b32 v133, a73
v_accvgpr_read_b32 v134, a74
v_accvgpr_read_b32 v135, a75
v_pk_mul_f32 v[128:129], v[128:129], s[54:55]
v_pk_mul_f32 v[130:131], v[130:131], s[54:55]
v_pk_mul_f32 v[132:133], v[132:133], s[54:55]
v_pk_mul_f32 v[134:135], v[134:135], s[54:55]
v_cvt_pk_bf16_f32 v96, v128, v129
v_cvt_pk_bf16_f32 v97, v130, v131
v_cvt_pk_bf16_f32 v98, v132, v133
v_cvt_pk_bf16_f32 v99, v134, v135
v_accvgpr_read_b32 v144, a68
v_accvgpr_read_b32 v145, a69
v_accvgpr_read_b32 v146, a70
v_accvgpr_read_b32 v147, a71
v_accvgpr_read_b32 v148, a76
v_accvgpr_read_b32 v149, a77
v_accvgpr_read_b32 v150, a78
v_accvgpr_read_b32 v151, a79
v_pk_mul_f32 v[144:145], v[144:145], s[54:55]
v_pk_mul_f32 v[146:147], v[146:147], s[54:55]
v_pk_mul_f32 v[148:149], v[148:149], s[54:55]
v_pk_mul_f32 v[150:151], v[150:151], s[54:55]
v_cvt_pk_bf16_f32 v100, v144, v145
v_cvt_pk_bf16_f32 v101, v146, v147
v_cvt_pk_bf16_f32 v102, v148, v149
v_cvt_pk_bf16_f32 v103, v150, v151
v_accvgpr_read_b32 v160, a80
v_accvgpr_read_b32 v161, a81
v_accvgpr_read_b32 v162, a82
v_accvgpr_read_b32 v163, a83
v_accvgpr_read_b32 v164, a88
v_accvgpr_read_b32 v165, a89
v_accvgpr_read_b32 v166, a90
v_accvgpr_read_b32 v167, a91
v_pk_mul_f32 v[160:161], v[160:161], s[54:55]
v_pk_mul_f32 v[162:163], v[162:163], s[54:55]
v_pk_mul_f32 v[164:165], v[164:165], s[54:55]
v_pk_mul_f32 v[166:167], v[166:167], s[54:55]
v_cvt_pk_bf16_f32 v104, v160, v161
v_cvt_pk_bf16_f32 v105, v162, v163
v_cvt_pk_bf16_f32 v106, v164, v165
v_cvt_pk_bf16_f32 v107, v166, v167
v_accvgpr_read_b32 v176, a84
v_accvgpr_read_b32 v177, a85
v_accvgpr_read_b32 v178, a86
v_accvgpr_read_b32 v179, a87
v_accvgpr_read_b32 v180, a92
v_accvgpr_read_b32 v181, a93
v_accvgpr_read_b32 v182, a94
v_accvgpr_read_b32 v183, a95
v_pk_mul_f32 v[176:177], v[176:177], s[54:55]
v_pk_mul_f32 v[178:179], v[178:179], s[54:55]
v_pk_mul_f32 v[180:181], v[180:181], s[54:55]
v_pk_mul_f32 v[182:183], v[182:183], s[54:55]
v_cvt_pk_bf16_f32 v108, v176, v177
v_cvt_pk_bf16_f32 v109, v178, v179
v_cvt_pk_bf16_f32 v110, v180, v181
v_cvt_pk_bf16_f32 v111, v182, v183
v_accvgpr_read_b32 v128, a96
v_accvgpr_read_b32 v129, a97
v_accvgpr_read_b32 v130, a98
v_accvgpr_read_b32 v131, a99
v_accvgpr_read_b32 v132, a104
v_accvgpr_read_b32 v133, a105
v_accvgpr_read_b32 v134, a106
v_accvgpr_read_b32 v135, a107
v_pk_mul_f32 v[128:129], v[128:129], s[54:55]
v_pk_mul_f32 v[130:131], v[130:131], s[54:55]
v_pk_mul_f32 v[132:133], v[132:133], s[54:55]
v_pk_mul_f32 v[134:135], v[134:135], s[54:55]
v_cvt_pk_bf16_f32 v112, v128, v129
v_cvt_pk_bf16_f32 v113, v130, v131
v_cvt_pk_bf16_f32 v114, v132, v133
v_cvt_pk_bf16_f32 v115, v134, v135
v_accvgpr_read_b32 v144, a100
v_accvgpr_read_b32 v145, a101
v_accvgpr_read_b32 v146, a102
v_accvgpr_read_b32 v147, a103
v_accvgpr_read_b32 v148, a108
v_accvgpr_read_b32 v149, a109
v_accvgpr_read_b32 v150, a110
v_accvgpr_read_b32 v151, a111
v_pk_mul_f32 v[144:145], v[144:145], s[54:55]
v_pk_mul_f32 v[146:147], v[146:147], s[54:55]
v_pk_mul_f32 v[148:149], v[148:149], s[54:55]
v_pk_mul_f32 v[150:151], v[150:151], s[54:55]
v_cvt_pk_bf16_f32 v116, v144, v145
v_cvt_pk_bf16_f32 v117, v146, v147
v_cvt_pk_bf16_f32 v118, v148, v149
v_cvt_pk_bf16_f32 v119, v150, v151
v_accvgpr_read_b32 v160, a112
v_accvgpr_read_b32 v161, a113
v_accvgpr_read_b32 v162, a114
v_accvgpr_read_b32 v163, a115
v_accvgpr_read_b32 v164, a120
v_accvgpr_read_b32 v165, a121
v_accvgpr_read_b32 v166, a122
v_accvgpr_read_b32 v167, a123
v_pk_mul_f32 v[160:161], v[160:161], s[54:55]
v_pk_mul_f32 v[162:163], v[162:163], s[54:55]
v_pk_mul_f32 v[164:165], v[164:165], s[54:55]
v_pk_mul_f32 v[166:167], v[166:167], s[54:55]
v_cvt_pk_bf16_f32 v120, v160, v161
v_cvt_pk_bf16_f32 v121, v162, v163
v_cvt_pk_bf16_f32 v122, v164, v165
v_cvt_pk_bf16_f32 v123, v166, v167
v_accvgpr_read_b32 v176, a116
v_accvgpr_read_b32 v177, a117
v_accvgpr_read_b32 v178, a118
v_accvgpr_read_b32 v179, a119
v_accvgpr_read_b32 v180, a124
v_accvgpr_read_b32 v181, a125
v_accvgpr_read_b32 v182, a126
v_accvgpr_read_b32 v183, a127
v_pk_mul_f32 v[176:177], v[176:177], s[54:55]
v_pk_mul_f32 v[178:179], v[178:179], s[54:55]
v_pk_mul_f32 v[180:181], v[180:181], s[54:55]
v_pk_mul_f32 v[182:183], v[182:183], s[54:55]
v_cvt_pk_bf16_f32 v124, v176, v177
v_cvt_pk_bf16_f32 v125, v178, v179
v_cvt_pk_bf16_f32 v126, v180, v181
v_cvt_pk_bf16_f32 v127, v182, v183
s_setpc_b64 s[70:71]
QEPIB_%=:
s_mov_b32 s50, 536870912
s_mov_b32 s51, 536870912
s_mov_b32 s52, 536870912
s_mov_b32 s53, 536870912
s_mov_b32 s54, 0x3db8aa3b
s_mov_b32 s55, 0x3db8aa3b
v_accvgpr_read_b32 v128, a0
v_accvgpr_read_b32 v129, a1
v_accvgpr_read_b32 v130, a2
v_accvgpr_read_b32 v131, a3
v_accvgpr_read_b32 v132, a8
v_accvgpr_read_b32 v133, a9
v_accvgpr_read_b32 v134, a10
v_accvgpr_read_b32 v135, a11
v_pk_mul_f32 v[128:129], v[128:129], s[54:55]
v_pk_mul_f32 v[130:131], v[130:131], s[54:55]
v_pk_mul_f32 v[132:133], v[132:133], s[54:55]
v_pk_mul_f32 v[134:135], v[134:135], s[54:55]
v_cvt_pk_bf16_f32 v136, v128, v129
v_cvt_pk_bf16_f32 v137, v130, v131
v_cvt_pk_bf16_f32 v138, v132, v133
v_cvt_pk_bf16_f32 v139, v134, v135
v_accvgpr_write_b32 a160, v136
v_accvgpr_write_b32 a161, v137
v_accvgpr_write_b32 a162, v138
v_accvgpr_write_b32 a163, v139
v_accvgpr_read_b32 v144, a4
v_accvgpr_read_b32 v145, a5
v_accvgpr_read_b32 v146, a6
v_accvgpr_read_b32 v147, a7
v_accvgpr_read_b32 v148, a12
v_accvgpr_read_b32 v149, a13
v_accvgpr_read_b32 v150, a14
v_accvgpr_read_b32 v151, a15
v_pk_mul_f32 v[144:145], v[144:145], s[54:55]
v_pk_mul_f32 v[146:147], v[146:147], s[54:55]
v_pk_mul_f32 v[148:149], v[148:149], s[54:55]
v_pk_mul_f32 v[150:151], v[150:151], s[54:55]
v_cvt_pk_bf16_f32 v152, v144, v145
v_cvt_pk_bf16_f32 v153, v146, v147
v_cvt_pk_bf16_f32 v154, v148, v149
v_cvt_pk_bf16_f32 v155, v150, v151
v_accvgpr_write_b32 a164, v152
v_accvgpr_write_b32 a165, v153
v_accvgpr_write_b32 a166, v154
v_accvgpr_write_b32 a167, v155
v_accvgpr_read_b32 v160, a16
v_accvgpr_read_b32 v161, a17
v_accvgpr_read_b32 v162, a18
v_accvgpr_read_b32 v163, a19
v_accvgpr_read_b32 v164, a24
v_accvgpr_read_b32 v165, a25
v_accvgpr_read_b32 v166, a26
v_accvgpr_read_b32 v167, a27
v_pk_mul_f32 v[160:161], v[160:161], s[54:55]
v_pk_mul_f32 v[162:163], v[162:163], s[54:55]
v_pk_mul_f32 v[164:165], v[164:165], s[54:55]
v_pk_mul_f32 v[166:167], v[166:167], s[54:55]
v_cvt_pk_bf16_f32 v168, v160, v161
v_cvt_pk_bf16_f32 v169, v162, v163
v_cvt_pk_bf16_f32 v170, v164, v165
v_cvt_pk_bf16_f32 v171, v166, v167
v_accvgpr_write_b32 a168, v168
v_accvgpr_write_b32 a169, v169
v_accvgpr_write_b32 a170, v170
v_accvgpr_write_b32 a171, v171
v_accvgpr_read_b32 v176, a20
v_accvgpr_read_b32 v177, a21
v_accvgpr_read_b32 v178, a22
v_accvgpr_read_b32 v179, a23
v_accvgpr_read_b32 v180, a28
v_accvgpr_read_b32 v181, a29
v_accvgpr_read_b32 v182, a30
v_accvgpr_read_b32 v183, a31
v_pk_mul_f32 v[176:177], v[176:177], s[54:55]
v_pk_mul_f32 v[178:179], v[178:179], s[54:55]
v_pk_mul_f32 v[180:181], v[180:181], s[54:55]
v_pk_mul_f32 v[182:183], v[182:183], s[54:55]
v_cvt_pk_bf16_f32 v184, v176, v177
v_cvt_pk_bf16_f32 v185, v178, v179
v_cvt_pk_bf16_f32 v186, v180, v181
v_cvt_pk_bf16_f32 v187, v182, v183
v_accvgpr_write_b32 a172, v184
v_accvgpr_write_b32 a173, v185
v_accvgpr_write_b32 a174, v186
v_accvgpr_write_b32 a175, v187
buffer_load_dwordx4 a[0:3], v47, s[36:39], s13 offen offset:0 sc1
buffer_load_dwordx4 a[4:7], v47, s[36:39], s13 offen offset:1024 sc1
buffer_load_dwordx4 a[8:11], v47, s[36:39], s13 offen offset:2048 sc1
buffer_load_dwordx4 a[12:15], v47, s[36:39], s13 offen offset:3072 sc1
v_add_u32 v47, 4096, v47
buffer_load_dwordx4 a[16:19], v47, s[36:39], s13 offen offset:0 sc1
buffer_load_dwordx4 a[20:23], v47, s[36:39], s13 offen offset:1024 sc1
buffer_load_dwordx4 a[24:27], v47, s[36:39], s13 offen offset:2048 sc1
buffer_load_dwordx4 a[28:31], v47, s[36:39], s13 offen offset:3072 sc1
v_add_u32 v47, 4096, v47
v_accvgpr_read_b32 v128, a32
v_accvgpr_read_b32 v129, a33
v_accvgpr_read_b32 v130, a34
v_accvgpr_read_b32 v131, a35
v_accvgpr_read_b32 v132, a40
v_accvgpr_read_b32 v133, a41
v_accvgpr_read_b32 v134, a42
v_accvgpr_read_b32 v135, a43
v_pk_mul_f32 v[128:129], v[128:129], s[54:55]
v_pk_mul_f32 v[130:131], v[130:131], s[54:55]
v_pk_mul_f32 v[132:133], v[132:133], s[54:55]
v_pk_mul_f32 v[134:135], v[134:135], s[54:55]
v_cvt_pk_bf16_f32 v136, v128, v129
v_cvt_pk_bf16_f32 v137, v130, v131
v_cvt_pk_bf16_f32 v138, v132, v133
v_cvt_pk_bf16_f32 v139, v134, v135
v_accvgpr_write_b32 a176, v136
v_accvgpr_write_b32 a177, v137
v_accvgpr_write_b32 a178, v138
v_accvgpr_write_b32 a179, v139
v_accvgpr_read_b32 v144, a36
v_accvgpr_read_b32 v145, a37
v_accvgpr_read_b32 v146, a38
v_accvgpr_read_b32 v147, a39
v_accvgpr_read_b32 v148, a44
v_accvgpr_read_b32 v149, a45
v_accvgpr_read_b32 v150, a46
v_accvgpr_read_b32 v151, a47
v_pk_mul_f32 v[144:145], v[144:145], s[54:55]
v_pk_mul_f32 v[146:147], v[146:147], s[54:55]
v_pk_mul_f32 v[148:149], v[148:149], s[54:55]
v_pk_mul_f32 v[150:151], v[150:151], s[54:55]
v_cvt_pk_bf16_f32 v152, v144, v145
v_cvt_pk_bf16_f32 v153, v146, v147
v_cvt_pk_bf16_f32 v154, v148, v149
v_cvt_pk_bf16_f32 v155, v150, v151
v_accvgpr_write_b32 a180, v152
v_accvgpr_write_b32 a181, v153
v_accvgpr_write_b32 a182, v154
v_accvgpr_write_b32 a183, v155
v_accvgpr_read_b32 v160, a48
v_accvgpr_read_b32 v161, a49
v_accvgpr_read_b32 v162, a50
v_accvgpr_read_b32 v163, a51
v_accvgpr_read_b32 v164, a56
v_accvgpr_read_b32 v165, a57
v_accvgpr_read_b32 v166, a58
v_accvgpr_read_b32 v167, a59
v_pk_mul_f32 v[160:161], v[160:161], s[54:55]
v_pk_mul_f32 v[162:163], v[162:163], s[54:55]
v_pk_mul_f32 v[164:165], v[164:165], s[54:55]
v_pk_mul_f32 v[166:167], v[166:167], s[54:55]
v_cvt_pk_bf16_f32 v168, v160, v161
v_cvt_pk_bf16_f32 v169, v162, v163
v_cvt_pk_bf16_f32 v170, v164, v165
v_cvt_pk_bf16_f32 v171, v166, v167
v_accvgpr_write_b32 a184, v168
v_accvgpr_write_b32 a185, v169
v_accvgpr_write_b32 a186, v170
v_accvgpr_write_b32 a187, v171
v_accvgpr_read_b32 v176, a52
v_accvgpr_read_b32 v177, a53
v_accvgpr_read_b32 v178, a54
v_accvgpr_read_b32 v179, a55
v_accvgpr_read_b32 v180, a60
v_accvgpr_read_b32 v181, a61
v_accvgpr_read_b32 v182, a62
v_accvgpr_read_b32 v183, a63
v_pk_mul_f32 v[176:177], v[176:177], s[54:55]
v_pk_mul_f32 v[178:179], v[178:179], s[54:55]
v_pk_mul_f32 v[180:181], v[180:181], s[54:55]
v_pk_mul_f32 v[182:183], v[182:183], s[54:55]
v_cvt_pk_bf16_f32 v184, v176, v177
v_cvt_pk_bf16_f32 v185, v178, v179
v_cvt_pk_bf16_f32 v186, v180, v181
v_cvt_pk_bf16_f32 v187, v182, v183
v_accvgpr_write_b32 a188, v184
v_accvgpr_write_b32 a189, v185
v_accvgpr_write_b32 a190, v186
v_accvgpr_write_b32 a191, v187
buffer_load_dwordx4 a[32:35], v47, s[36:39], s13 offen offset:0 sc1
buffer_load_dwordx4 a[36:39], v47, s[36:39], s13 offen offset:1024 sc1
buffer_load_dwordx4 a[40:43], v47, s[36:39], s13 offen offset:2048 sc1
buffer_load_dwordx4 a[44:47], v47, s[36:39], s13 offen offset:3072 sc1
v_add_u32 v47, 4096, v47
buffer_load_dwordx4 a[48:51], v47, s[36:39], s13 offen offset:0 sc1
buffer_load_dwordx4 a[52:55], v47, s[36:39], s13 offen offset:1024 sc1
buffer_load_dwordx4 a[56:59], v47, s[36:39], s13 offen offset:2048 sc1
buffer_load_dwordx4 a[60:63], v47, s[36:39], s13 offen offset:3072 sc1
v_add_u32 v47, 4096, v47
v_accvgpr_read_b32 v128, a64
v_accvgpr_read_b32 v129, a65
v_accvgpr_read_b32 v130, a66
v_accvgpr_read_b32 v131, a67
v_accvgpr_read_b32 v132, a72
v_accvgpr_read_b32 v133, a73
v_accvgpr_read_b32 v134, a74
v_accvgpr_read_b32 v135, a75
v_pk_mul_f32 v[128:129], v[128:129], s[54:55]
v_pk_mul_f32 v[130:131], v[130:131], s[54:55]
v_pk_mul_f32 v[132:133], v[132:133], s[54:55]
v_pk_mul_f32 v[134:135], v[134:135], s[54:55]
v_cvt_pk_bf16_f32 v136, v128, v129
v_cvt_pk_bf16_f32 v137, v130, v131
v_cvt_pk_bf16_f32 v138, v132, v133
v_cvt_pk_bf16_f32 v139, v134, v135
v_accvgpr_write_b32 a192, v136
v_accvgpr_write_b32 a193, v137
v_accvgpr_write_b32 a194, v138
v_accvgpr_write_b32 a195, v139
v_accvgpr_read_b32 v144, a68
v_accvgpr_read_b32 v145, a69
v_accvgpr_read_b32 v146, a70
v_accvgpr_read_b32 v147, a71
v_accvgpr_read_b32 v148, a76
v_accvgpr_read_b32 v149, a77
v_accvgpr_read_b32 v150, a78
v_accvgpr_read_b32 v151, a79
v_pk_mul_f32 v[144:145], v[144:145], s[54:55]
v_pk_mul_f32 v[146:147], v[146:147], s[54:55]
v_pk_mul_f32 v[148:149], v[148:149], s[54:55]
v_pk_mul_f32 v[150:151], v[150:151], s[54:55]
v_cvt_pk_bf16_f32 v152, v144, v145
v_cvt_pk_bf16_f32 v153, v146, v147
v_cvt_pk_bf16_f32 v154, v148, v149
v_cvt_pk_bf16_f32 v155, v150, v151
v_accvgpr_write_b32 a196, v152
v_accvgpr_write_b32 a197, v153
v_accvgpr_write_b32 a198, v154
v_accvgpr_write_b32 a199, v155
v_accvgpr_read_b32 v160, a80
v_accvgpr_read_b32 v161, a81
v_accvgpr_read_b32 v162, a82
v_accvgpr_read_b32 v163, a83
v_accvgpr_read_b32 v164, a88
v_accvgpr_read_b32 v165, a89
v_accvgpr_read_b32 v166, a90
v_accvgpr_read_b32 v167, a91
v_pk_mul_f32 v[160:161], v[160:161], s[54:55]
v_pk_mul_f32 v[162:163], v[162:163], s[54:55]
v_pk_mul_f32 v[164:165], v[164:165], s[54:55]
v_pk_mul_f32 v[166:167], v[166:167], s[54:55]
v_cvt_pk_bf16_f32 v168, v160, v161
v_cvt_pk_bf16_f32 v169, v162, v163
v_cvt_pk_bf16_f32 v170, v164, v165
v_cvt_pk_bf16_f32 v171, v166, v167
v_accvgpr_write_b32 a200, v168
v_accvgpr_write_b32 a201, v169
v_accvgpr_write_b32 a202, v170
v_accvgpr_write_b32 a203, v171
v_accvgpr_read_b32 v176, a84
v_accvgpr_read_b32 v177, a85
v_accvgpr_read_b32 v178, a86
v_accvgpr_read_b32 v179, a87
v_accvgpr_read_b32 v180, a92
v_accvgpr_read_b32 v181, a93
v_accvgpr_read_b32 v182, a94
v_accvgpr_read_b32 v183, a95
v_pk_mul_f32 v[176:177], v[176:177], s[54:55]
v_pk_mul_f32 v[178:179], v[178:179], s[54:55]
v_pk_mul_f32 v[180:181], v[180:181], s[54:55]
v_pk_mul_f32 v[182:183], v[182:183], s[54:55]
v_cvt_pk_bf16_f32 v184, v176, v177
v_cvt_pk_bf16_f32 v185, v178, v179
v_cvt_pk_bf16_f32 v186, v180, v181
v_cvt_pk_bf16_f32 v187, v182, v183
v_accvgpr_write_b32 a204, v184
v_accvgpr_write_b32 a205, v185
v_accvgpr_write_b32 a206, v186
v_accvgpr_write_b32 a207, v187
v_accvgpr_read_b32 v128, a96
v_accvgpr_read_b32 v129, a97
v_accvgpr_read_b32 v130, a98
v_accvgpr_read_b32 v131, a99
v_accvgpr_read_b32 v132, a104
v_accvgpr_read_b32 v133, a105
v_accvgpr_read_b32 v134, a106
v_accvgpr_read_b32 v135, a107
v_pk_mul_f32 v[128:129], v[128:129], s[54:55]
v_pk_mul_f32 v[130:131], v[130:131], s[54:55]
v_pk_mul_f32 v[132:133], v[132:133], s[54:55]
v_pk_mul_f32 v[134:135], v[134:135], s[54:55]
v_cvt_pk_bf16_f32 v136, v128, v129
v_cvt_pk_bf16_f32 v137, v130, v131
v_cvt_pk_bf16_f32 v138, v132, v133
v_cvt_pk_bf16_f32 v139, v134, v135
v_accvgpr_write_b32 a208, v136
v_accvgpr_write_b32 a209, v137
v_accvgpr_write_b32 a210, v138
v_accvgpr_write_b32 a211, v139
v_accvgpr_read_b32 v144, a100
v_accvgpr_read_b32 v145, a101
v_accvgpr_read_b32 v146, a102
v_accvgpr_read_b32 v147, a103
v_accvgpr_read_b32 v148, a108
v_accvgpr_read_b32 v149, a109
v_accvgpr_read_b32 v150, a110
v_accvgpr_read_b32 v151, a111
v_pk_mul_f32 v[144:145], v[144:145], s[54:55]
v_pk_mul_f32 v[146:147], v[146:147], s[54:55]
v_pk_mul_f32 v[148:149], v[148:149], s[54:55]
v_pk_mul_f32 v[150:151], v[150:151], s[54:55]
v_cvt_pk_bf16_f32 v152, v144, v145
v_cvt_pk_bf16_f32 v153, v146, v147
v_cvt_pk_bf16_f32 v154, v148, v149
v_cvt_pk_bf16_f32 v155, v150, v151
v_accvgpr_write_b32 a212, v152
v_accvgpr_write_b32 a213, v153
v_accvgpr_write_b32 a214, v154
v_accvgpr_write_b32 a215, v155
v_accvgpr_read_b32 v160, a112
v_accvgpr_read_b32 v161, a113
v_accvgpr_read_b32 v162, a114
v_accvgpr_read_b32 v163, a115
v_accvgpr_read_b32 v164, a120
v_accvgpr_read_b32 v165, a121
v_accvgpr_read_b32 v166, a122
v_accvgpr_read_b32 v167, a123
v_pk_mul_f32 v[160:161], v[160:161], s[54:55]
v_pk_mul_f32 v[162:163], v[162:163], s[54:55]
v_pk_mul_f32 v[164:165], v[164:165], s[54:55]
v_pk_mul_f32 v[166:167], v[166:167], s[54:55]
v_cvt_pk_bf16_f32 v168, v160, v161
v_cvt_pk_bf16_f32 v169, v162, v163
v_cvt_pk_bf16_f32 v170, v164, v165
v_cvt_pk_bf16_f32 v171, v166, v167
v_accvgpr_write_b32 a216, v168
v_accvgpr_write_b32 a217, v169
v_accvgpr_write_b32 a218, v170
v_accvgpr_write_b32 a219, v171
v_accvgpr_read_b32 v176, a116
v_accvgpr_read_b32 v177, a117
v_accvgpr_read_b32 v178, a118
v_accvgpr_read_b32 v179, a119
v_accvgpr_read_b32 v180, a124
v_accvgpr_read_b32 v181, a125
v_accvgpr_read_b32 v182, a126
v_accvgpr_read_b32 v183, a127
v_pk_mul_f32 v[176:177], v[176:177], s[54:55]
v_pk_mul_f32 v[178:179], v[178:179], s[54:55]
v_pk_mul_f32 v[180:181], v[180:181], s[54:55]
v_pk_mul_f32 v[182:183], v[182:183], s[54:55]
v_cvt_pk_bf16_f32 v184, v176, v177
v_cvt_pk_bf16_f32 v185, v178, v179
v_cvt_pk_bf16_f32 v186, v180, v181
v_cvt_pk_bf16_f32 v187, v182, v183
v_accvgpr_write_b32 a220, v184
v_accvgpr_write_b32 a221, v185
v_accvgpr_write_b32 a222, v186
v_accvgpr_write_b32 a223, v187
s_setpc_b64 s[70:71]
SEG_%=:
s_lshl_b32 s46, s44, 7
s_lshl_b32 s47, s28, 5
s_add_u32 s46, s46, s47
v_mfma_f32_16x16x32_bf16 v[128:131], v[192:195], v[64:67], 0
ds_read_b128 v[216:219], v3 offset:6144
v_accvgpr_write_b32 a0, 0
v_mfma_f32_16x16x32_bf16 v[132:135], v[192:195], v[68:71], 0
buffer_load_dword v15, v43, s[36:39], s58 offen sc1
v_mfma_f32_16x16x32_bf16 v[140:143], v[196:199], v[68:71], 0
s_add_u32 s20, s20, s21
ds_read_b128 v[220:223], v3 offset:7168
v_accvgpr_write_b32 a1, 0
v_mfma_f32_16x16x32_bf16 v[136:139], v[196:199], v[64:67], 0
s_waitcnt lgkmcnt(4)
v_accvgpr_write_b32 a2, 0
v_accvgpr_write_b32 a3, 0
v_mfma_f32_16x16x32_bf16 v[144:147], v[200:203], v[64:67], 0
s_add_u32 m0, s23, 2048
ds_read_b128 v[192:195], v3 offset:8192
v_accvgpr_write_b32 a4, 0
v_mfma_f32_16x16x32_bf16 v[148:151], v[200:203], v[68:71], 0
buffer_load_dwordx4 v14, s[16:19], s20 offen sc1 lds
v_mfma_f32_16x16x32_bf16 v[156:159], v[204:207], v[68:71], 0
ds_read_b128 v[196:199], v3 offset:9216
v_accvgpr_write_b32 a5, 0
v_mfma_f32_16x16x32_bf16 v[152:155], v[204:207], v[64:67], 0
s_waitcnt lgkmcnt(4)
v_accvgpr_write_b32 a6, 0
v_accvgpr_write_b32 a7, 0
v_mfma_f32_16x16x32_bf16 v[128:131], v[208:211], v[72:75], v[128:131]
ds_read_b128 v[200:203], v3 offset:10240
v_accvgpr_write_b32 a8, 0
v_mfma_f32_16x16x32_bf16 v[132:135], v[208:211], v[76:79], v[132:135]
v_accvgpr_write_b32 a9, 0
v_accvgpr_write_b32 a10, 0
v_mfma_f32_16x16x32_bf16 v[140:143], v[212:215], v[76:79], v[140:143]
s_add_u32 s20, s20, s21
ds_read_b128 v[204:207], v3 offset:11264
v_accvgpr_write_b32 a11, 0
v_mfma_f32_16x16x32_bf16 v[136:139], v[212:215], v[72:75], v[136:139]
s_waitcnt lgkmcnt(4)
v_accvgpr_write_b32 a12, 0
v_accvgpr_write_b32 a13, 0
v_mfma_f32_16x16x32_bf16 v[144:147], v[216:219], v[72:75], v[144:147]
s_add_u32 m0, s23, 3072
ds_read_b128 v[208:211], v3 offset:12288
v_accvgpr_write_b32 a14, 0
v_mfma_f32_16x16x32_bf16 v[148:151], v[216:219], v[76:79], v[148:151]
buffer_load_dwordx4 v14, s[16:19], s20 offen sc1 lds
v_mfma_f32_16x16x32_bf16 v[156:159], v[220:223], v[76:79], v[156:159]
ds_read_b128 v[212:215], v3 offset:13312
v_accvgpr_write_b32 a15, 0
v_mfma_f32_16x16x32_bf16 v[152:155], v[220:223], v[72:75], v[152:155]
s_waitcnt lgkmcnt(4)
v_accvgpr_write_b32 a16, 0
v_accvgpr_write_b32 a17, 0
v_mfma_f32_16x16x32_bf16 v[128:131], v[192:195], v[80:83], v[128:131]
ds_read_b128 v[216:219], v3 offset:14336
v_accvgpr_write_b32 a18, 0
v_mfma_f32_16x16x32_bf16 v[132:135], v[192:195], v[84:87], v[132:135]
v_accvgpr_write_b32 a19, 0
v_accvgpr_write_b32 a20, 0
v_mfma_f32_16x16x32_bf16 v[140:143], v[196:199], v[84:87], v[140:143]
s_add_u32 s20, s20, s21
ds_read_b128 v[220:223], v3 offset:15360
v_accvgpr_write_b32 a21, 0
v_mfma_f32_16x16x32_bf16 v[136:139], v[196:199], v[80:83], v[136:139]
s_waitcnt lgkmcnt(4)
v_accvgpr_write_b32 a22, 0
v_accvgpr_write_b32 a23, 0
v_mfma_f32_16x16x32_bf16 v[144:147], v[200:203], v[80:83], v[144:147]
s_add_u32 m0, s23, 4096
ds_read_b128 v[192:195], v3 offset:16384
v_accvgpr_write_b32 a24, 0
v_mfma_f32_16x16x32_bf16 v[148:151], v[200:203], v[84:87], v[148:151]
buffer_load_dwordx4 v14, s[16:19], s20 offen sc1 lds
v_mfma_f32_16x16x32_bf16 v[156:159], v[204:207], v[84:87], v[156:159]
ds_read_b128 v[196:199], v3 offset:17408
v_accvgpr_write_b32 a25, 0
v_mfma_f32_16x16x32_bf16 v[152:155], v[204:207], v[80:83], v[152:155]
s_waitcnt lgkmcnt(4)
v_accvgpr_write_b32 a26, 0
v_accvgpr_write_b32 a27, 0
v_mfma_f32_16x16x32_bf16 v[128:131], v[208:211], v[88:91], v[128:131]
ds_read_b128 v[200:203], v3 offset:18432
v_accvgpr_write_b32 a28, 0
v_mfma_f32_16x16x32_bf16 v[132:135], v[208:211], v[92:95], v[132:135]
v_accvgpr_write_b32 a29, 0
v_accvgpr_write_b32 a30, 0
v_mfma_f32_16x16x32_bf16 v[140:143], v[212:215], v[92:95], v[140:143]
s_add_u32 s20, s20, s21
ds_read_b128 v[204:207], v3 offset:19456
v_accvgpr_write_b32 a31, 0
v_mfma_f32_16x16x32_bf16 v[136:139], v[212:215], v[88:91], v[136:139]
s_waitcnt lgkmcnt(4)
v_accvgpr_write_b32 a32, 0
v_accvgpr_write_b32 a33, 0
v_mfma_f32_16x16x32_bf16 v[144:147], v[216:219], v[88:91], v[144:147]
s_add_u32 m0, s23, 5120
ds_read_b128 v[208:211], v3 offset:20480
v_accvgpr_write_b32 a34, 0
v_mfma_f32_16x16x32_bf16 v[148:151], v[216:219], v[92:95], v[148:151]
buffer_load_dwordx4 v14, s[16:19], s20 offen sc1 lds
v_mfma_f32_16x16x32_bf16 v[156:159], v[220:223], v[92:95], v[156:159]
ds_read_b128 v[212:215], v3 offset:21504
v_accvgpr_write_b32 a35, 0
v_mfma_f32_16x16x32_bf16 v[152:155], v[220:223], v[88:91], v[152:155]
s_waitcnt lgkmcnt(4)
v_accvgpr_write_b32 a36, 0
v_accvgpr_write_b32 a37, 0
v_mfma_f32_16x16x32_bf16 v[128:131], v[192:195], v[96:99], v[128:131]
ds_read_b128 v[216:219], v3 offset:22528
v_accvgpr_write_b32 a38, 0
v_mfma_f32_16x16x32_bf16 v[132:135], v[192:195], v[100:103], v[132:135]
v_accvgpr_write_b32 a39, 0
v_accvgpr_write_b32 a40, 0
v_mfma_f32_16x16x32_bf16 v[140:143], v[196:199], v[100:103], v[140:143]
s_add_u32 s20, s20, s21
ds_read_b128 v[220:223], v3 offset:23552
v_accvgpr_write_b32 a41, 0
v_mfma_f32_16x16x32_bf16 v[136:139], v[196:199], v[96:99], v[136:139]
s_waitcnt lgkmcnt(4)
v_accvgpr_write_b32 a42, 0
v_accvgpr_write_b32 a43, 0
v_mfma_f32_16x16x32_bf16 v[144:147], v[200:203], v[96:99], v[144:147]
s_add_u32 m0, s23, 6144
ds_read_b128 v[192:195], v3 offset:24576
v_accvgpr_write_b32 a44, 0
v_mfma_f32_16x16x32_bf16 v[148:151], v[200:203], v[100:103], v[148:151]
buffer_load_dwordx4 v14, s[16:19], s20 offen sc1 lds
v_mfma_f32_16x16x32_bf16 v[156:159], v[204:207], v[100:103], v[156:159]
s_add_u32 s26, s24, 32768
ds_read_b128 v[196:199], v3 offset:25600
v_accvgpr_write_b32 a45, 0
v_mfma_f32_16x16x32_bf16 v[152:155], v[204:207], v[96:99], v[152:155]
s_waitcnt lgkmcnt(4)
v_accvgpr_write_b32 a46, 0
v_accvgpr_write_b32 a47, 0
v_mfma_f32_16x16x32_bf16 v[128:131], v[208:211], v[104:107], v[128:131]
s_sub_u32 s35, s26, 163840
ds_read_b128 v[200:203], v3 offset:26624
v_accvgpr_write_b32 a48, 0
v_mfma_f32_16x16x32_bf16 v[132:135], v[208:211], v[108:111], v[132:135]
s_cselect_b32 s26, s26, s35
v_accvgpr_write_b32 a49, 0
v_accvgpr_write_b32 a50, 0
v_mfma_f32_16x16x32_bf16 v[140:143], v[212:215], v[108:111], v[140:143]
s_add_u32 s20, s20, s21
ds_read_b128 v[204:207], v3 offset:27648
v_accvgpr_write_b32 a51, 0
v_mfma_f32_16x16x32_bf16 v[136:139], v[212:215], v[104:107], v[136:139]
s_waitcnt lgkmcnt(4)
v_accvgpr_write_b32 a52, 0
v_accvgpr_write_b32 a53, 0
v_mfma_f32_16x16x32_bf16 v[144:147], v[216:219], v[104:107], v[144:147]
s_add_u32 m0, s23, 7168
ds_read_b128 v[208:211], v3 offset:28672
v_accvgpr_write_b32 a54, 0
v_mfma_f32_16x16x32_bf16 v[148:151], v[216:219], v[108:111], v[148:151]
buffer_load_dwordx4 v14, s[16:19], s20 offen sc1 lds
v_mfma_f32_16x16x32_bf16 v[156:159], v[220:223], v[108:111], v[156:159]
s_add_u32 s23, s27, s33
ds_read_b128 v[212:215], v3 offset:29696
v_readlane_b32 s16, v16, s22
v_mfma_f32_16x16x32_bf16 v[152:155], v[220:223], v[104:107], v[152:155]
s_waitcnt lgkmcnt(4)
v_readlane_b32 s17, v17, s22
v_accvgpr_write_b32 a55, 0
v_mfma_f32_16x16x32_bf16 v[128:131], v[192:195], v[112:115], v[128:131]
s_waitcnt vmcnt(16)
ds_read_b128 v[216:219], v3 offset:30720
v_readlane_b32 s25, v18, s22
v_mfma_f32_16x16x32_bf16 v[132:135], v[192:195], v[116:119], v[132:135]
s_barrier
v_accvgpr_write_b32 a56, 0
v_accvgpr_write_b32 a57, 0
v_mfma_f32_16x16x32_bf16 v[140:143], v[196:199], v[116:119], v[140:143]
s_mov_b32 s20, 0
ds_read_b128 v[220:223], v3 offset:31744
v_mad_u32_u24 v14, v12, s25, v13
v_mfma_f32_16x16x32_bf16 v[136:139], v[196:199], v[112:115], v[136:139]
s_waitcnt lgkmcnt(4)
v_add_u32 v3, s26, v2
v_accvgpr_write_b32 a58, 0
v_mfma_f32_16x16x32_bf16 v[144:147], v[200:203], v[112:115], v[144:147]
s_mov_b32 m0, s23
ds_read_b128 v[192:195], v3 offset:0
v_accvgpr_write_b32 a59, 0
v_mfma_f32_16x16x32_bf16 v[148:151], v[200:203], v[116:119], v[148:151]
buffer_load_dwordx4 v14, s[16:19], s20 offen sc1 lds
v_mfma_f32_16x16x32_bf16 v[156:159], v[204:207], v[116:119], v[156:159]
s_add_u32 s22, s22, 1
ds_read_b128 v[196:199], v3 offset:1024
v_accvgpr_write_b32 a60, 0
v_mfma_f32_16x16x32_bf16 v[152:155], v[204:207], v[112:115], v[152:155]
s_waitcnt lgkmcnt(4)
v_accvgpr_write_b32 a61, 0
v_accvgpr_write_b32 a62, 0
v_mfma_f32_16x16x32_bf16 v[128:131], v[208:211], v[120:123], v[128:131]
s_lshl_b32 s21, s25, 3
ds_read_b128 v[200:203], v3 offset:2048
v_accvgpr_write_b32 a63, 0
v_mfma_f32_16x16x32_bf16 v[132:135], v[208:211], v[124:127], v[132:135]
s_mov_b32 s27, s24
v_accvgpr_write_b32 a64, 0
v_accvgpr_write_b32 a65, 0
v_mfma_f32_16x16x32_bf16 v[140:143], v[212:215], v[124:127], v[140:143]
s_add_u32 s20, s20, s21
ds_read_b128 v[204:207], v3 offset:3072
v_accvgpr_write_b32 a66, 0
v_mfma_f32_16x16x32_bf16 v[136:139], v[212:215], v[120:123], v[136:139]
s_waitcnt lgkmcnt(4)
v_accvgpr_write_b32 a67, 0
v_accvgpr_write_b32 a68, 0
v_mfma_f32_16x16x32_bf16 v[144:147], v[216:219], v[120:123], v[144:147]
s_add_u32 m0, s23, 1024
ds_read_b128 v[208:211], v3 offset:4096
v_accvgpr_write_b32 a69, 0
v_mfma_f32_16x16x32_bf16 v[148:151], v[216:219], v[124:127], v[148:151]
buffer_load_dwordx4 v14, s[16:19], s20 offen sc1 lds
v_mfma_f32_16x16x32_bf16 v[156:159], v[220:223], v[124:127], v[156:159]
s_mov_b32 s24, s26
ds_read_b128 v[212:215], v3 offset:5120
v_accvgpr_write_b32 a70, 0
v_mfma_f32_16x16x32_bf16 v[152:155], v[220:223], v[120:123], v[152:155]
s_waitcnt lgkmcnt(4)
v_accvgpr_write_b32 a71, 0
v_accvgpr_write_b32 a72, 0
v_accvgpr_write_b32 a73, 0
v_accvgpr_write_b32 a74, 0
v_accvgpr_write_b32 a75, 0
v_accvgpr_write_b32 a76, 0
v_accvgpr_write_b32 a77, 0
v_accvgpr_write_b32 a78, 0
v_accvgpr_write_b32 a79, 0
v_accvgpr_write_b32 a80, 0
v_accvgpr_write_b32 a81, 0
v_accvgpr_write_b32 a82, 0
v_accvgpr_write_b32 a83, 0
v_accvgpr_write_b32 a84, 0
v_accvgpr_write_b32 a85, 0
v_accvgpr_write_b32 a86, 0
v_accvgpr_write_b32 a87, 0
v_accvgpr_write_b32 a88, 0
v_accvgpr_write_b32 a89, 0
v_accvgpr_write_b32 a90, 0
v_accvgpr_write_b32 a91, 0
v_accvgpr_write_b32 a92, 0
v_accvgpr_write_b32 a93, 0
v_accvgpr_write_b32 a94, 0
v_accvgpr_write_b32 a95, 0
v_accvgpr_write_b32 a96, 0
v_accvgpr_write_b32 a97, 0
v_accvgpr_write_b32 a98, 0
v_accvgpr_write_b32 a99, 0
v_accvgpr_write_b32 a100, 0
v_accvgpr_write_b32 a101, 0
v_accvgpr_write_b32 a102, 0
v_accvgpr_write_b32 a103, 0
v_accvgpr_write_b32 a104, 0
v_accvgpr_write_b32 a105, 0
v_accvgpr_write_b32 a106, 0
v_accvgpr_write_b32 a107, 0
v_accvgpr_write_b32 a108, 0
v_accvgpr_write_b32 a109, 0
v_accvgpr_write_b32 a110, 0
v_accvgpr_write_b32 a111, 0
v_accvgpr_write_b32 a112, 0
v_accvgpr_write_b32 a113, 0
v_accvgpr_write_b32 a114, 0
v_accvgpr_write_b32 a115, 0
v_accvgpr_write_b32 a116, 0
v_accvgpr_write_b32 a117, 0
v_accvgpr_write_b32 a118, 0
v_accvgpr_write_b32 a119, 0
v_accvgpr_write_b32 a120, 0
v_accvgpr_write_b32 a121, 0
v_accvgpr_write_b32 a122, 0
v_accvgpr_write_b32 a123, 0
v_accvgpr_write_b32 a124, 0
v_accvgpr_write_b32 a125, 0
v_accvgpr_write_b32 a126, 0
v_accvgpr_write_b32 a127, 0
s_nop 7
s_cmp_gt_u32 s45, s46
s_cbranch_scc0 SEG_NM_%=
s_sub_u32 s35, s46, s45
s_add_u32 s35, s35, 63
v_sub_u32 v33, s35, v19
v_mov_b32 v34, 0xff800000
v_cmp_ge_i32_e64 s[48:49], v33, 0
v_cmp_ge_i32_e64 s[50:51], v33, 1
v_cmp_ge_i32_e64 s[52:53], v33, 2
v_cmp_ge_i32_e64 s[54:55], v33, 3
v_cndmask_b32_e64 v128, v34, v128, s[48:49]
v_cndmask_b32_e64 v129, v34, v129, s[50:51]
v_cndmask_b32_e64 v130, v34, v130, s[52:53]
v_cndmask_b32_e64 v131, v34, v131, s[54:55]
v_cmp_ge_i32_e64 s[48:49], v33, -16
v_cmp_ge_i32_e64 s[50:51], v33, -15
v_cmp_ge_i32_e64 s[52:53], v33, -14
v_cmp_ge_i32_e64 s[54:55], v33, -13
v_cndmask_b32_e64 v132, v34, v132, s[48:49]
v_cndmask_b32_e64 v133, v34, v133, s[50:51]
v_cndmask_b32_e64 v134, v34, v134, s[52:53]
v_cndmask_b32_e64 v135, v34, v135, s[54:55]
v_cmp_ge_i32_e64 s[48:49], v33, 16
v_cmp_ge_i32_e64 s[50:51], v33, 17
v_cmp_ge_i32_e64 s[52:53], v33, 18
v_cmp_ge_i32_e64 s[54:55], v33, 19
v_cndmask_b32_e64 v136, v34, v136, s[48:49]
v_cndmask_b32_e64 v137, v34, v137, s[50:51]
v_cndmask_b32_e64 v138, v34, v138, s[52:53]
v_cndmask_b32_e64 v139, v34, v139, s[54:55]
v_cmp_ge_i32_e64 s[48:49], v33, 0
v_cmp_ge_i32_e64 s[50:51], v33, 1
v_cmp_ge_i32_e64 s[52:53], v33, 2
v_cmp_ge_i32_e64 s[54:55], v33, 3
v_cndmask_b32_e64 v140, v34, v140, s[48:49]
v_cndmask_b32_e64 v141, v34, v141, s[50:51]
v_cndmask_b32_e64 v142, v34, v142, s[52:53]
v_cndmask_b32_e64 v143, v34, v143, s[54:55]
v_cmp_ge_i32_e64 s[48:49], v33, 32
v_cmp_ge_i32_e64 s[50:51], v33, 33
v_cmp_ge_i32_e64 s[52:53], v33, 34
v_cmp_ge_i32_e64 s[54:55], v33, 35
v_cndmask_b32_e64 v144, v34, v144, s[48:49]
v_cndmask_b32_e64 v145, v34, v145, s[50:51]
v_cndmask_b32_e64 v146, v34, v146, s[52:53]
v_cndmask_b32_e64 v147, v34, v147, s[54:55]
v_cmp_ge_i32_e64 s[48:49], v33, 16
v_cmp_ge_i32_e64 s[50:51], v33, 17
v_cmp_ge_i32_e64 s[52:53], v33, 18
v_cmp_ge_i32_e64 s[54:55], v33, 19
v_cndmask_b32_e64 v148, v34, v148, s[48:49]
v_cndmask_b32_e64 v149, v34, v149, s[50:51]
v_cndmask_b32_e64 v150, v34, v150, s[52:53]
v_cndmask_b32_e64 v151, v34, v151, s[54:55]
v_cmp_ge_i32_e64 s[48:49], v33, 48
v_cmp_ge_i32_e64 s[50:51], v33, 49
v_cmp_ge_i32_e64 s[52:53], v33, 50
v_cmp_ge_i32_e64 s[54:55], v33, 51
v_cndmask_b32_e64 v152, v34, v152, s[48:49]
v_cndmask_b32_e64 v153, v34, v153, s[50:51]
v_cndmask_b32_e64 v154, v34, v154, s[52:53]
v_cndmask_b32_e64 v155, v34, v155, s[54:55]
v_cmp_ge_i32_e64 s[48:49], v33, 32
v_cmp_ge_i32_e64 s[50:51], v33, 33
v_cmp_ge_i32_e64 s[52:53], v33, 34
v_cmp_ge_i32_e64 s[54:55], v33, 35
v_cndmask_b32_e64 v156, v34, v156, s[48:49]
v_cndmask_b32_e64 v157, v34, v157, s[50:51]
v_cndmask_b32_e64 v158, v34, v158, s[52:53]
v_cndmask_b32_e64 v159, v34, v159, s[54:55]
SEG_NM_%=:
v_max3_f32 v35, v128, v129, v130
v_max3_f32 v35, v35, v131, v136
v_max3_f32 v35, v35, v137, v138
v_max3_f32 v35, v35, v139, v144
v_max3_f32 v35, v35, v145, v146
v_max3_f32 v35, v35, v147, v152
v_max3_f32 v35, v35, v153, v154
v_max_f32 v35, v35, v155
v_mov_b32 v36, v35
s_nop 1
v_permlane16_swap_b32 v35, v36
s_nop 1
v_max_f32 v35, v35, v36
v_mov_b32 v36, v35
s_nop 1
v_permlane32_swap_b32 v35, v36
s_nop 1
v_max_f32 v35, v35, v36
v_max_f32 v28, 0xf149f2ca, v35
v_sub_f32 v128, v128, v28
v_sub_f32 v129, v129, v28
v_sub_f32 v130, v130, v28
v_sub_f32 v131, v131, v28
v_sub_f32 v136, v136, v28
v_sub_f32 v137, v137, v28
v_sub_f32 v138, v138, v28
v_sub_f32 v139, v139, v28
v_sub_f32 v144, v144, v28
v_sub_f32 v145, v145, v28
v_sub_f32 v146, v146, v28
v_sub_f32 v147, v147, v28
v_sub_f32 v152, v152, v28
v_sub_f32 v153, v153, v28
v_sub_f32 v154, v154, v28
v_sub_f32 v155, v155, v28
v_sub_f32 v20, 0, v28
v_sub_f32 v21, 0, v28
v_sub_f32 v22, 0, v28
v_sub_f32 v23, 0, v28
v_mov_b32 v30, 0
v_max3_f32 v37, v132, v133, v134
v_max3_f32 v37, v37, v135, v140
v_max3_f32 v37, v37, v141, v142
v_max3_f32 v37, v37, v143, v148
v_max3_f32 v37, v37, v149, v150
v_max3_f32 v37, v37, v151, v156
v_max3_f32 v37, v37, v157, v158
v_max_f32 v37, v37, v159
v_mov_b32 v38, v37
s_nop 1
v_permlane16_swap_b32 v37, v38
s_nop 1
v_max_f32 v37, v37, v38
v_mov_b32 v38, v37
s_nop 1
v_permlane32_swap_b32 v37, v38
s_nop 1
v_max_f32 v37, v37, v38
v_max_f32 v29, 0xf149f2ca, v37
v_sub_f32 v132, v132, v29
v_sub_f32 v133, v133, v29
v_sub_f32 v134, v134, v29
v_sub_f32 v135, v135, v29
v_sub_f32 v140, v140, v29
v_sub_f32 v141, v141, v29
v_sub_f32 v142, v142, v29
v_sub_f32 v143, v143, v29
v_sub_f32 v148, v148, v29
v_sub_f32 v149, v149, v29
v_sub_f32 v150, v150, v29
v_sub_f32 v151, v151, v29
v_sub_f32 v156, v156, v29
v_sub_f32 v157, v157, v29
v_sub_f32 v158, v158, v29
v_sub_f32 v159, v159, v29
v_sub_f32 v24, 0, v29
v_sub_f32 v25, 0, v29
v_sub_f32 v26, 0, v29
v_sub_f32 v27, 0, v29
v_mov_b32 v31, 0
s_add_u32 s45, s45, 64
s_cmp_eq_u32 s34, 0
s_cbranch_scc1 VLAST0_%=
BODY0_%=:
v_mfma_f32_16x16x32_bf16 v[160:163], v[192:195], v[64:67], v[20:23]
s_cmp_le_u32 s34, 2
ds_read_b128 v[216:219], v3 offset:6144
v_mfma_f32_16x16x32_bf16 v[164:167], v[192:195], v[68:71], v[24:27]
s_cselect_b32 s57, s58, s69
v_exp_f32 v128, v128
v_mfma_f32_16x16x32_bf16 v[172:175], v[196:199], v[68:71], v[24:27]
s_add_u32 s20, s20, s21
ds_read_b128 v[220:223], v3 offset:7168
v_add_f32 v30, v30, v128
v_mfma_f32_16x16x32_bf16 v[168:171], v[196:199], v[64:67], v[20:23]
s_waitcnt lgkmcnt(4)
v_exp_f32 v129, v129
v_mfma_f32_16x16x32_bf16 v[176:179], v[200:203], v[64:67], v[20:23]
s_add_u32 m0, s23, 2048
ds_read_b128 v[192:195], v3 offset:8192
v_cvt_pk_bf16_f32 v48, v128, v129
v_mfma_f32_16x16x32_bf16 v[180:183], v[200:203], v[68:71], v[24:27]
buffer_load_dwordx4 v14, s[16:19], s20 offen sc1 lds
v_mfma_f32_16x16x32_bf16 v[188:191], v[204:207], v[68:71], v[24:27]
ds_read_b128 v[196:199], v3 offset:9216
v_add_f32 v30, v30, v129
v_mfma_f32_16x16x32_bf16 v[184:187], v[204:207], v[64:67], v[20:23]
s_waitcnt lgkmcnt(4)
v_exp_f32 v130, v130
v_mfma_f32_16x16x32_bf16 v[160:163], v[208:211], v[72:75], v[160:163]
ds_read_b128 v[200:203], v3 offset:10240
v_add_f32 v30, v30, v130
v_mfma_f32_16x16x32_bf16 v[164:167], v[208:211], v[76:79], v[164:167]
buffer_load_dword v15, v43, s[36:39], s57 offen sc1
v_mfma_f32_16x16x32_bf16 v[172:175], v[212:215], v[76:79], v[172:175]
s_add_u32 s20, s20, s21
ds_read_b128 v[204:207], v3 offset:11264
v_mfma_f32_16x16x32_bf16 v[168:171], v[212:215], v[72:75], v[168:171]
s_waitcnt lgkmcnt(4)
v_exp_f32 v131, v131
v_mfma_f32_16x16x32_bf16 v[176:179], v[216:219], v[72:75], v[176:179]
s_add_u32 m0, s23, 3072
ds_read_b128 v[208:211], v3 offset:12288
v_cvt_pk_bf16_f32 v49, v130, v131
v_mfma_f32_16x16x32_bf16 v[180:183], v[216:219], v[76:79], v[180:183]
buffer_load_dwordx4 v14, s[16:19], s20 offen sc1 lds
v_mfma_f32_16x16x32_bf16 v[188:191], v[220:223], v[76:79], v[188:191]
ds_read_b128 v[212:215], v3 offset:13312
v_add_f32 v30, v30, v131
v_mfma_f32_16x16x32_bf16 v[184:187], v[220:223], v[72:75], v[184:187]
s_waitcnt lgkmcnt(4)
v_exp_f32 v136, v136
v_mfma_f32_16x16x32_bf16 v[160:163], v[192:195], v[80:83], v[160:163]
ds_read_b128 v[216:219], v3 offset:14336
v_add_f32 v30, v30, v136
v_mfma_f32_16x16x32_bf16 v[164:167], v[192:195], v[84:87], v[164:167]
v_exp_f32 v137, v137
v_mfma_f32_16x16x32_bf16 v[172:175], v[196:199], v[84:87], v[172:175]
s_add_u32 s20, s20, s21
ds_read_b128 v[220:223], v3 offset:15360
v_cvt_pk_bf16_f32 v50, v136, v137
v_mfma_f32_16x16x32_bf16 v[168:171], v[196:199], v[80:83], v[168:171]
s_waitcnt lgkmcnt(4)
v_exp_f32 v138, v138
v_mfma_f32_16x16x32_bf16 v[176:179], v[200:203], v[80:83], v[176:179]
s_add_u32 m0, s23, 4096
ds_read_b128 v[192:195], v3 offset:16384
v_add_f32 v30, v30, v137
v_mfma_f32_16x16x32_bf16 v[180:183], v[200:203], v[84:87], v[180:183]
buffer_load_dwordx4 v14, s[16:19], s20 offen sc1 lds
v_mfma_f32_16x16x32_bf16 v[188:191], v[204:207], v[84:87], v[188:191]
ds_read_b128 v[196:199], v3 offset:17408
v_add_f32 v30, v30, v138
v_mfma_f32_16x16x32_bf16 v[184:187], v[204:207], v[80:83], v[184:187]
s_waitcnt lgkmcnt(4)
v_exp_f32 v139, v139
v_mfma_f32_16x16x32_bf16 v[160:163], v[208:211], v[88:91], v[160:163]
ds_read_b128 v[200:203], v3 offset:18432
v_cvt_pk_bf16_f32 v51, v138, v139
v_mfma_f32_16x16x32_bf16 v[164:167], v[208:211], v[92:95], v[164:167]
v_exp_f32 v132, v132
v_mfma_f32_16x16x32_bf16 v[172:175], v[212:215], v[92:95], v[172:175]
s_add_u32 s20, s20, s21
ds_read_b128 v[204:207], v3 offset:19456
v_add_f32 v30, v30, v139
v_mfma_f32_16x16x32_bf16 v[168:171], v[212:215], v[88:91], v[168:171]
s_waitcnt lgkmcnt(4)
v_exp_f32 v133, v133
v_mfma_f32_16x16x32_bf16 v[176:179], v[216:219], v[88:91], v[176:179]
s_add_u32 m0, s23, 5120
ds_read_b128 v[208:211], v3 offset:20480
v_cvt_pk_bf16_f32 v52, v132, v133
v_mfma_f32_16x16x32_bf16 v[180:183], v[216:219], v[92:95], v[180:183]
buffer_load_dwordx4 v14, s[16:19], s20 offen sc1 lds
v_mfma_f32_16x16x32_bf16 v[188:191], v[220:223], v[92:95], v[188:191]
ds_read_b128 v[212:215], v3 offset:21504
v_add_f32 v31, v31, v132
v_mfma_f32_16x16x32_bf16 v[184:187], v[220:223], v[88:91], v[184:187]
s_waitcnt lgkmcnt(4)
v_exp_f32 v134, v134
v_mfma_f32_16x16x32_bf16 v[160:163], v[192:195], v[96:99], v[160:163]
ds_read_b128 v[216:219], v3 offset:22528
v_add_f32 v31, v31, v133
v_mfma_f32_16x16x32_bf16 v[164:167], v[192:195], v[100:103], v[164:167]
v_exp_f32 v135, v135
v_mfma_f32_16x16x32_bf16 v[172:175], v[196:199], v[100:103], v[172:175]
s_add_u32 s20, s20, s21
ds_read_b128 v[220:223], v3 offset:23552
v_cvt_pk_bf16_f32 v53, v134, v135
v_mfma_f32_16x16x32_bf16 v[168:171], v[196:199], v[96:99], v[168:171]
s_waitcnt lgkmcnt(4)
v_exp_f32 v140, v140
v_mfma_f32_16x16x32_bf16 v[176:179], v[200:203], v[96:99], v[176:179]
s_add_u32 m0, s23, 6144
ds_read_b128 v[192:195], v3 offset:24576
v_add_f32 v31, v31, v134
v_mfma_f32_16x16x32_bf16 v[180:183], v[200:203], v[100:103], v[180:183]
buffer_load_dwordx4 v14, s[16:19], s20 offen sc1 lds
v_mfma_f32_16x16x32_bf16 v[188:191], v[204:207], v[100:103], v[188:191]
s_add_u32 s26, s24, 32768
ds_read_b128 v[196:199], v3 offset:25600
v_add_f32 v31, v31, v135
v_mfma_f32_16x16x32_bf16 v[184:187], v[204:207], v[96:99], v[184:187]
s_waitcnt lgkmcnt(4)
v_exp_f32 v141, v141
v_mfma_f32_16x16x32_bf16 v[160:163], v[208:211], v[104:107], v[160:163]
s_sub_u32 s35, s26, 163840
ds_read_b128 v[200:203], v3 offset:26624
v_cvt_pk_bf16_f32 v54, v140, v141
v_mfma_f32_16x16x32_bf16 v[164:167], v[208:211], v[108:111], v[164:167]
s_cselect_b32 s26, s26, s35
v_exp_f32 v142, v142
v_mfma_f32_16x16x32_bf16 v[172:175], v[212:215], v[108:111], v[172:175]
s_add_u32 s20, s20, s21
ds_read_b128 v[204:207], v3 offset:27648
v_add_f32 v31, v31, v140
v_mfma_f32_16x16x32_bf16 v[168:171], v[212:215], v[104:107], v[168:171]
s_waitcnt lgkmcnt(4)
v_exp_f32 v143, v143
v_mfma_f32_16x16x32_bf16 v[176:179], v[216:219], v[104:107], v[176:179]
s_add_u32 m0, s23, 7168
ds_read_b128 v[208:211], v3 offset:28672
v_cvt_pk_bf16_f32 v55, v142, v143
v_mfma_f32_16x16x32_bf16 v[180:183], v[216:219], v[108:111], v[180:183]
buffer_load_dwordx4 v14, s[16:19], s20 offen sc1 lds
v_mfma_f32_16x16x32_bf16 v[188:191], v[220:223], v[108:111], v[188:191]
s_add_u32 s23, s27, s33
ds_read_b128 v[212:215], v3 offset:29696
v_readlane_b32 s16, v16, s22
v_mfma_f32_16x16x32_bf16 v[184:187], v[220:223], v[104:107], v[184:187]
s_waitcnt lgkmcnt(4)
v_readlane_b32 s17, v17, s22
v_add_f32 v31, v31, v141
v_mfma_f32_16x16x32_bf16 v[160:163], v[192:195], v[112:115], v[160:163]
s_waitcnt vmcnt(16)
ds_read_b128 v[216:219], v3 offset:30720
v_readlane_b32 s25, v18, s22
v_mfma_f32_16x16x32_bf16 v[164:167], v[192:195], v[116:119], v[164:167]
s_barrier
v_exp_f32 v144, v144
v_mfma_f32_16x16x32_bf16 v[172:175], v[196:199], v[116:119], v[172:175]
s_mov_b32 s20, 0
ds_read_b128 v[220:223], v3 offset:31744
v_mad_u32_u24 v14, v12, s25, v13
v_mfma_f32_16x16x32_bf16 v[168:171], v[196:199], v[112:115], v[168:171]
s_waitcnt lgkmcnt(4)
v_add_u32 v3, s26, v2
v_add_f32 v31, v31, v142
v_mfma_f32_16x16x32_bf16 v[176:179], v[200:203], v[112:115], v[176:179]
s_mov_b32 m0, s23
ds_read_b128 v[192:195], v3 offset:0
v_add_f32 v31, v31, v143
v_mfma_f32_16x16x32_bf16 v[180:183], v[200:203], v[116:119], v[180:183]
buffer_load_dwordx4 v14, s[16:19], s20 offen sc1 lds
v_mfma_f32_16x16x32_bf16 v[188:191], v[204:207], v[116:119], v[188:191]
s_add_u32 s22, s22, 1
ds_read_b128 v[196:199], v3 offset:1024
v_add_f32 v30, v30, v144
v_mfma_f32_16x16x32_bf16 v[184:187], v[204:207], v[112:115], v[184:187]
s_waitcnt lgkmcnt(4)
v_exp_f32 v145, v145
v_mfma_f32_16x16x32_bf16 v[160:163], v[208:211], v[120:123], v[160:163]
s_lshl_b32 s21, s25, 3
ds_read_b128 v[200:203], v3 offset:2048
v_cvt_pk_bf16_f32 v56, v144, v145
v_mfma_f32_16x16x32_bf16 v[164:167], v[208:211], v[124:127], v[164:167]
s_mov_b32 s27, s24
v_exp_f32 v146, v146
v_mfma_f32_16x16x32_bf16 v[172:175], v[212:215], v[124:127], v[172:175]
s_add_u32 s20, s20, s21
ds_read_b128 v[204:207], v3 offset:3072
v_add_f32 v30, v30, v145
v_mfma_f32_16x16x32_bf16 v[168:171], v[212:215], v[120:123], v[168:171]
s_waitcnt lgkmcnt(4)
v_exp_f32 v147, v147
v_mfma_f32_16x16x32_bf16 v[176:179], v[216:219], v[120:123], v[176:179]
s_add_u32 m0, s23, 1024
ds_read_b128 v[208:211], v3 offset:4096
v_cvt_pk_bf16_f32 v57, v146, v147
v_mfma_f32_16x16x32_bf16 v[180:183], v[216:219], v[124:127], v[180:183]
buffer_load_dwordx4 v14, s[16:19], s20 offen sc1 lds
v_mfma_f32_16x16x32_bf16 v[188:191], v[220:223], v[124:127], v[188:191]
s_mov_b32 s24, s26
ds_read_b128 v[212:215], v3 offset:5120
v_add_f32 v30, v30, v146
v_mfma_f32_16x16x32_bf16 v[184:187], v[220:223], v[120:123], v[184:187]
s_waitcnt lgkmcnt(4)
v_exp_f32 v152, v152
v_mfma_f32_16x16x32_bf16 a[0:3], v[192:195], v[48:51], a[0:3]
ds_read_b128 v[216:219], v3 offset:6144
v_add_f32 v30, v30, v147
v_mfma_f32_16x16x32_bf16 a[4:7], v[192:195], v[52:55], a[4:7]
v_exp_f32 v153, v153
v_mfma_f32_16x16x32_bf16 a[12:15], v[196:199], v[52:55], a[12:15]
s_add_u32 s20, s20, s21
ds_read_b128 v[220:223], v3 offset:7168
v_cvt_pk_bf16_f32 v58, v152, v153
v_mfma_f32_16x16x32_bf16 a[8:11], v[196:199], v[48:51], a[8:11]
s_waitcnt lgkmcnt(4)
v_exp_f32 v154, v154
v_mfma_f32_16x16x32_bf16 a[16:19], v[200:203], v[48:51], a[16:19]
s_add_u32 m0, s23, 2048
ds_read_b128 v[192:195], v3 offset:8192
v_add_f32 v30, v30, v152
v_mfma_f32_16x16x32_bf16 a[20:23], v[200:203], v[52:55], a[20:23]
buffer_load_dwordx4 v14, s[16:19], s20 offen sc1 lds
v_mfma_f32_16x16x32_bf16 a[28:31], v[204:207], v[52:55], a[28:31]
ds_read_b128 v[196:199], v3 offset:9216
v_max3_f32 v32, v160, v161, v162
v_mfma_f32_16x16x32_bf16 a[24:27], v[204:207], v[48:51], a[24:27]
s_waitcnt lgkmcnt(4)
v_exp_f32 v155, v155
v_mfma_f32_16x16x32_bf16 a[32:35], v[208:211], v[48:51], a[32:35]
ds_read_b128 v[200:203], v3 offset:10240
v_cvt_pk_bf16_f32 v59, v154, v155
v_mfma_f32_16x16x32_bf16 a[36:39], v[208:211], v[52:55], a[36:39]
v_exp_f32 v148, v148
v_mfma_f32_16x16x32_bf16 a[44:47], v[212:215], v[52:55], a[44:47]
s_add_u32 s20, s20, s21
ds_read_b128 v[204:207], v3 offset:11264
v_max3_f32 v32, v32, v163, v164
v_mfma_f32_16x16x32_bf16 a[40:43], v[212:215], v[48:51], a[40:43]
s_waitcnt lgkmcnt(4)
v_exp_f32 v149, v149
v_mfma_f32_16x16x32_bf16 a[48:51], v[216:219], v[48:51], a[48:51]
s_add_u32 m0, s23, 3072
ds_read_b128 v[208:211], v3 offset:12288
v_cvt_pk_bf16_f32 v60, v148, v149
v_mfma_f32_16x16x32_bf16 a[52:55], v[216:219], v[52:55], a[52:55]
buffer_load_dwordx4 v14, s[16:19], s20 offen sc1 lds
v_mfma_f32_16x16x32_bf16 a[60:63], v[220:223], v[52:55], a[60:63]
ds_read_b128 v[212:215], v3 offset:13312
v_max3_f32 v32, v32, v165, v166
v_mfma_f32_16x16x32_bf16 a[56:59], v[220:223], v[48:51], a[56:59]
s_waitcnt lgkmcnt(4)
v_exp_f32 v150, v150
v_mfma_f32_16x16x32_bf16 a[64:67], v[192:195], v[48:51], a[64:67]
ds_read_b128 v[216:219], v3 offset:14336
v_max3_f32 v32, v32, v167, v168
v_mfma_f32_16x16x32_bf16 a[68:71], v[192:195], v[52:55], a[68:71]
v_exp_f32 v151, v151
v_mfma_f32_16x16x32_bf16 a[76:79], v[196:199], v[52:55], a[76:79]
s_add_u32 s20, s20, s21
ds_read_b128 v[220:223], v3 offset:15360
v_cvt_pk_bf16_f32 v61, v150, v151
v_mfma_f32_16x16x32_bf16 a[72:75], v[196:199], v[48:51], a[72:75]
s_waitcnt lgkmcnt(4)
v_exp_f32 v156, v156
v_mfma_f32_16x16x32_bf16 a[80:83], v[200:203], v[48:51], a[80:83]
s_add_u32 m0, s23, 4096
ds_read_b128 v[192:195], v3 offset:16384
v_max3_f32 v32, v32, v169, v170
v_mfma_f32_16x16x32_bf16 a[84:87], v[200:203], v[52:55], a[84:87]
buffer_load_dwordx4 v14, s[16:19], s20 offen sc1 lds
v_mfma_f32_16x16x32_bf16 a[92:95], v[204:207], v[52:55], a[92:95]
ds_read_b128 v[196:199], v3 offset:17408
v_max3_f32 v32, v32, v171, v172
v_mfma_f32_16x16x32_bf16 a[88:91], v[204:207], v[48:51], a[88:91]
s_waitcnt lgkmcnt(4)
v_exp_f32 v157, v157
v_mfma_f32_16x16x32_bf16 a[96:99], v[208:211], v[48:51], a[96:99]
ds_read_b128 v[200:203], v3 offset:18432
v_cvt_pk_bf16_f32 v62, v156, v157
v_mfma_f32_16x16x32_bf16 a[100:103], v[208:211], v[52:55], a[100:103]
v_exp_f32 v158, v158
v_mfma_f32_16x16x32_bf16 a[108:111], v[212:215], v[52:55], a[108:111]
s_add_u32 s20, s20, s21
ds_read_b128 v[204:207], v3 offset:19456
v_max3_f32 v32, v32, v173, v174
v_mfma_f32_16x16x32_bf16 a[104:107], v[212:215], v[48:51], a[104:107]
s_waitcnt lgkmcnt(4)
v_exp_f32 v159, v159
v_mfma_f32_16x16x32_bf16 a[112:115], v[216:219], v[48:51], a[112:115]
s_add_u32 m0, s23, 5120
ds_read_b128 v[208:211], v3 offset:20480
v_cvt_pk_bf16_f32 v63, v158, v159
v_mfma_f32_16x16x32_bf16 a[116:119], v[216:219], v[52:55], a[116:119]
buffer_load_dwordx4 v14, s[16:19], s20 offen sc1 lds
v_mfma_f32_16x16x32_bf16 a[124:127], v[220:223], v[52:55], a[124:127]
ds_read_b128 v[212:215], v3 offset:21504
v_max3_f32 v32, v32, v175, v176
v_mfma_f32_16x16x32_bf16 a[120:123], v[220:223], v[48:51], a[120:123]
s_waitcnt lgkmcnt(4)
v_max3_f32 v32, v32, v177, v178
v_add_f32 v30, v30, v153
v_mfma_f32_16x16x32_bf16 a[0:3], v[192:195], v[56:59], a[0:3]
ds_read_b128 v[216:219], v3 offset:22528
v_max3_f32 v32, v32, v179, v180
v_mfma_f32_16x16x32_bf16 a[4:7], v[192:195], v[60:63], a[4:7]
buffer_store_dword v42, v43, s[36:39], s56 offen sc1
v_mfma_f32_16x16x32_bf16 a[12:15], v[196:199], v[60:63], a[12:15]
s_add_u32 s20, s20, s21
ds_read_b128 v[220:223], v3 offset:23552
v_max3_f32 v32, v32, v181, v182
v_mfma_f32_16x16x32_bf16 a[8:11], v[196:199], v[56:59], a[8:11]
s_waitcnt lgkmcnt(4)
v_max3_f32 v32, v32, v183, v184
v_add_f32 v30, v30, v154
v_mfma_f32_16x16x32_bf16 a[16:19], v[200:203], v[56:59], a[16:19]
s_add_u32 m0, s23, 6144
ds_read_b128 v[192:195], v3 offset:24576
v_max3_f32 v32, v32, v185, v186
v_mfma_f32_16x16x32_bf16 a[20:23], v[200:203], v[60:63], a[20:23]
buffer_load_dwordx4 v14, s[16:19], s20 offen sc1 lds
v_mfma_f32_16x16x32_bf16 a[28:31], v[204:207], v[60:63], a[28:31]
s_add_u32 s26, s24, 32768
ds_read_b128 v[196:199], v3 offset:25600
v_max3_f32 v32, v32, v187, v188
v_mfma_f32_16x16x32_bf16 a[24:27], v[204:207], v[56:59], a[24:27]
s_waitcnt lgkmcnt(4)
v_max3_f32 v32, v32, v189, v190
v_add_f32 v30, v30, v155
v_mfma_f32_16x16x32_bf16 a[32:35], v[208:211], v[56:59], a[32:35]
s_sub_u32 s35, s26, 163840
ds_read_b128 v[200:203], v3 offset:26624
v_max3_f32 v32, v32, v191, v191
v_mfma_f32_16x16x32_bf16 a[36:39], v[208:211], v[60:63], a[36:39]
s_cselect_b32 s26, s26, s35
v_cmp_lt_f32 vcc, 0x41000000, v32
v_add_f32 v31, v31, v148
v_mfma_f32_16x16x32_bf16 a[44:47], v[212:215], v[60:63], a[44:47]
s_add_u32 s20, s20, s21
ds_read_b128 v[204:207], v3 offset:27648
v_add_f32 v31, v31, v149
v_mfma_f32_16x16x32_bf16 a[40:43], v[212:215], v[56:59], a[40:43]
s_waitcnt lgkmcnt(4)
v_add_f32 v31, v31, v150
v_add_f32 v31, v31, v151
v_mfma_f32_16x16x32_bf16 a[48:51], v[216:219], v[56:59], a[48:51]
s_add_u32 m0, s23, 7168
ds_read_b128 v[208:211], v3 offset:28672
v_add_f32 v31, v31, v156
v_mfma_f32_16x16x32_bf16 a[52:55], v[216:219], v[60:63], a[52:55]
buffer_load_dwordx4 v14, s[16:19], s20 offen sc1 lds
v_mfma_f32_16x16x32_bf16 a[60:63], v[220:223], v[60:63], a[60:63]
s_add_u32 s23, s27, s33
ds_read_b128 v[212:215], v3 offset:29696
v_readlane_b32 s16, v16, s22
v_mfma_f32_16x16x32_bf16 a[56:59], v[220:223], v[56:59], a[56:59]
s_waitcnt lgkmcnt(4)
v_readlane_b32 s17, v17, s22
v_add_f32 v31, v31, v157
v_mfma_f32_16x16x32_bf16 a[64:67], v[192:195], v[56:59], a[64:67]
s_waitcnt vmcnt(16)
ds_read_b128 v[216:219], v3 offset:30720
v_readlane_b32 s25, v18, s22
v_mfma_f32_16x16x32_bf16 a[68:71], v[192:195], v[60:63], a[68:71]
s_barrier
v_add_f32 v31, v31, v158
v_add_f32 v31, v31, v159
v_mfma_f32_16x16x32_bf16 a[76:79], v[196:199], v[60:63], a[76:79]
s_mov_b32 s20, 0
ds_read_b128 v[220:223], v3 offset:31744
v_mad_u32_u24 v14, v12, s25, v13
v_mfma_f32_16x16x32_bf16 a[72:75], v[196:199], v[56:59], a[72:75]
s_waitcnt lgkmcnt(4)
v_add_u32 v3, s26, v2
v_mfma_f32_16x16x32_bf16 a[80:83], v[200:203], v[56:59], a[80:83]
s_mov_b32 m0, s23
ds_read_b128 v[192:195], v3 offset:0
v_mfma_f32_16x16x32_bf16 a[84:87], v[200:203], v[60:63], a[84:87]
buffer_load_dwordx4 v14, s[16:19], s20 offen sc1 lds
v_mfma_f32_16x16x32_bf16 a[92:95], v[204:207], v[60:63], a[92:95]
s_add_u32 s22, s22, 1
ds_read_b128 v[196:199], v3 offset:1024
v_mfma_f32_16x16x32_bf16 a[88:91], v[204:207], v[56:59], a[88:91]
s_waitcnt lgkmcnt(4)
v_mfma_f32_16x16x32_bf16 a[96:99], v[208:211], v[56:59], a[96:99]
s_lshl_b32 s21, s25, 3
ds_read_b128 v[200:203], v3 offset:2048
v_mfma_f32_16x16x32_bf16 a[100:103], v[208:211], v[60:63], a[100:103]
s_mov_b32 s27, s24
v_mfma_f32_16x16x32_bf16 a[108:111], v[212:215], v[60:63], a[108:111]
s_add_u32 s20, s20, s21
ds_read_b128 v[204:207], v3 offset:3072
v_mfma_f32_16x16x32_bf16 a[104:107], v[212:215], v[56:59], a[104:107]
s_waitcnt lgkmcnt(4)
v_mfma_f32_16x16x32_bf16 a[112:115], v[216:219], v[56:59], a[112:115]
s_add_u32 m0, s23, 1024
ds_read_b128 v[208:211], v3 offset:4096
v_mfma_f32_16x16x32_bf16 a[116:119], v[216:219], v[60:63], a[116:119]
buffer_load_dwordx4 v14, s[16:19], s20 offen sc1 lds
v_mfma_f32_16x16x32_bf16 a[124:127], v[220:223], v[60:63], a[124:127]
s_mov_b32 s24, s26
ds_read_b128 v[212:215], v3 offset:5120
v_mfma_f32_16x16x32_bf16 a[120:123], v[220:223], v[56:59], a[120:123]
s_waitcnt lgkmcnt(4)
s_cmp_gt_u32 s45, s46
s_cbranch_scc1 MASK0_%=
MRET0_%=:
s_cbranch_vccnz RARE0_%=
RRET0_%=:
s_add_u32 s45, s45, 64
s_sub_u32 s34, s34, 1
s_cmp_eq_u32 s34, 0
s_cbranch_scc1 VLAST1_%=
BODY1_%=:
v_mfma_f32_16x16x32_bf16 v[128:131], v[192:195], v[64:67], v[20:23]
s_cmp_le_u32 s34, 2
ds_read_b128 v[216:219], v3 offset:6144
v_mfma_f32_16x16x32_bf16 v[132:135], v[192:195], v[68:71], v[24:27]
s_cselect_b32 s57, s58, s69
v_exp_f32 v160, v160
v_mfma_f32_16x16x32_bf16 v[140:143], v[196:199], v[68:71], v[24:27]
s_add_u32 s20, s20, s21
ds_read_b128 v[220:223], v3 offset:7168
v_add_f32 v30, v30, v160
v_mfma_f32_16x16x32_bf16 v[136:139], v[196:199], v[64:67], v[20:23]
s_waitcnt lgkmcnt(4)
v_exp_f32 v161, v161
v_mfma_f32_16x16x32_bf16 v[144:147], v[200:203], v[64:67], v[20:23]
s_add_u32 m0, s23, 2048
ds_read_b128 v[192:195], v3 offset:8192
v_cvt_pk_bf16_f32 v48, v160, v161
v_mfma_f32_16x16x32_bf16 v[148:151], v[200:203], v[68:71], v[24:27]
buffer_load_dwordx4 v14, s[16:19], s20 offen sc1 lds
v_mfma_f32_16x16x32_bf16 v[156:159], v[204:207], v[68:71], v[24:27]
ds_read_b128 v[196:199], v3 offset:9216
v_add_f32 v30, v30, v161
v_mfma_f32_16x16x32_bf16 v[152:155], v[204:207], v[64:67], v[20:23]
s_waitcnt lgkmcnt(4)
v_exp_f32 v162, v162
v_mfma_f32_16x16x32_bf16 v[128:131], v[208:211], v[72:75], v[128:131]
ds_read_b128 v[200:203], v3 offset:10240
v_add_f32 v30, v30, v162
v_mfma_f32_16x16x32_bf16 v[132:135], v[208:211], v[76:79], v[132:135]
buffer_load_dword v15, v43, s[36:39], s57 offen sc1
v_mfma_f32_16x16x32_bf16 v[140:143], v[212:215], v[76:79], v[140:143]
s_add_u32 s20, s20, s21
ds_read_b128 v[204:207], v3 offset:11264
v_mfma_f32_16x16x32_bf16 v[136:139], v[212:215], v[72:75], v[136:139]
s_waitcnt lgkmcnt(4)
v_exp_f32 v163, v163
v_mfma_f32_16x16x32_bf16 v[144:147], v[216:219], v[72:75], v[144:147]
s_add_u32 m0, s23, 3072
ds_read_b128 v[208:211], v3 offset:12288
v_cvt_pk_bf16_f32 v49, v162, v163
v_mfma_f32_16x16x32_bf16 v[148:151], v[216:219], v[76:79], v[148:151]
buffer_load_dwordx4 v14, s[16:19], s20 offen sc1 lds
v_mfma_f32_16x16x32_bf16 v[156:159], v[220:223], v[76:79], v[156:159]
ds_read_b128 v[212:215], v3 offset:13312
v_add_f32 v30, v30, v163
v_mfma_f32_16x16x32_bf16 v[152:155], v[220:223], v[72:75], v[152:155]
s_waitcnt lgkmcnt(4)
v_exp_f32 v168, v168
v_mfma_f32_16x16x32_bf16 v[128:131], v[192:195], v[80:83], v[128:131]
ds_read_b128 v[216:219], v3 offset:14336
v_add_f32 v30, v30, v168
v_mfma_f32_16x16x32_bf16 v[132:135], v[192:195], v[84:87], v[132:135]
v_exp_f32 v169, v169
v_mfma_f32_16x16x32_bf16 v[140:143], v[196:199], v[84:87], v[140:143]
s_add_u32 s20, s20, s21
ds_read_b128 v[220:223], v3 offset:15360
v_cvt_pk_bf16_f32 v50, v168, v169
v_mfma_f32_16x16x32_bf16 v[136:139], v[196:199], v[80:83], v[136:139]
s_waitcnt lgkmcnt(4)
v_exp_f32 v170, v170
v_mfma_f32_16x16x32_bf16 v[144:147], v[200:203], v[80:83], v[144:147]
s_add_u32 m0, s23, 4096
ds_read_b128 v[192:195], v3 offset:16384
v_add_f32 v30, v30, v169
v_mfma_f32_16x16x32_bf16 v[148:151], v[200:203], v[84:87], v[148:151]
buffer_load_dwordx4 v14, s[16:19], s20 offen sc1 lds
v_mfma_f32_16x16x32_bf16 v[156:159], v[204:207], v[84:87], v[156:159]
ds_read_b128 v[196:199], v3 offset:17408
v_add_f32 v30, v30, v170
v_mfma_f32_16x16x32_bf16 v[152:155], v[204:207], v[80:83], v[152:155]
s_waitcnt lgkmcnt(4)
v_exp_f32 v171, v171
v_mfma_f32_16x16x32_bf16 v[128:131], v[208:211], v[88:91], v[128:131]
ds_read_b128 v[200:203], v3 offset:18432
v_cvt_pk_bf16_f32 v51, v170, v171
v_mfma_f32_16x16x32_bf16 v[132:135], v[208:211], v[92:95], v[132:135]
v_exp_f32 v164, v164
v_mfma_f32_16x16x32_bf16 v[140:143], v[212:215], v[92:95], v[140:143]
s_add_u32 s20, s20, s21
ds_read_b128 v[204:207], v3 offset:19456
v_add_f32 v30, v30, v171
v_mfma_f32_16x16x32_bf16 v[136:139], v[212:215], v[88:91], v[136:139]
s_waitcnt lgkmcnt(4)
v_exp_f32 v165, v165
v_mfma_f32_16x16x32_bf16 v[144:147], v[216:219], v[88:91], v[144:147]
s_add_u32 m0, s23, 5120
ds_read_b128 v[208:211], v3 offset:20480
v_cvt_pk_bf16_f32 v52, v164, v165
v_mfma_f32_16x16x32_bf16 v[148:151], v[216:219], v[92:95], v[148:151]
buffer_load_dwordx4 v14, s[16:19], s20 offen sc1 lds
v_mfma_f32_16x16x32_bf16 v[156:159], v[220:223], v[92:95], v[156:159]
ds_read_b128 v[212:215], v3 offset:21504
v_add_f32 v31, v31, v164
v_mfma_f32_16x16x32_bf16 v[152:155], v[220:223], v[88:91], v[152:155]
s_waitcnt lgkmcnt(4)
v_exp_f32 v166, v166
v_mfma_f32_16x16x32_bf16 v[128:131], v[192:195], v[96:99], v[128:131]
ds_read_b128 v[216:219], v3 offset:22528
v_add_f32 v31, v31, v165
v_mfma_f32_16x16x32_bf16 v[132:135], v[192:195], v[100:103], v[132:135]
v_exp_f32 v167, v167
v_mfma_f32_16x16x32_bf16 v[140:143], v[196:199], v[100:103], v[140:143]
s_add_u32 s20, s20, s21
ds_read_b128 v[220:223], v3 offset:23552
v_cvt_pk_bf16_f32 v53, v166, v167
v_mfma_f32_16x16x32_bf16 v[136:139], v[196:199], v[96:99], v[136:139]
s_waitcnt lgkmcnt(4)
v_exp_f32 v172, v172
v_mfma_f32_16x16x32_bf16 v[144:147], v[200:203], v[96:99], v[144:147]
s_add_u32 m0, s23, 6144
ds_read_b128 v[192:195], v3 offset:24576
v_add_f32 v31, v31, v166
v_mfma_f32_16x16x32_bf16 v[148:151], v[200:203], v[100:103], v[148:151]
buffer_load_dwordx4 v14, s[16:19], s20 offen sc1 lds
v_mfma_f32_16x16x32_bf16 v[156:159], v[204:207], v[100:103], v[156:159]
s_add_u32 s26, s24, 32768
ds_read_b128 v[196:199], v3 offset:25600
v_add_f32 v31, v31, v167
v_mfma_f32_16x16x32_bf16 v[152:155], v[204:207], v[96:99], v[152:155]
s_waitcnt lgkmcnt(4)
v_exp_f32 v173, v173
v_mfma_f32_16x16x32_bf16 v[128:131], v[208:211], v[104:107], v[128:131]
s_sub_u32 s35, s26, 163840
ds_read_b128 v[200:203], v3 offset:26624
v_cvt_pk_bf16_f32 v54, v172, v173
v_mfma_f32_16x16x32_bf16 v[132:135], v[208:211], v[108:111], v[132:135]
s_cselect_b32 s26, s26, s35
v_exp_f32 v174, v174
v_mfma_f32_16x16x32_bf16 v[140:143], v[212:215], v[108:111], v[140:143]
s_add_u32 s20, s20, s21
ds_read_b128 v[204:207], v3 offset:27648
v_add_f32 v31, v31, v172
v_mfma_f32_16x16x32_bf16 v[136:139], v[212:215], v[104:107], v[136:139]
s_waitcnt lgkmcnt(4)
v_exp_f32 v175, v175
v_mfma_f32_16x16x32_bf16 v[144:147], v[216:219], v[104:107], v[144:147]
s_add_u32 m0, s23, 7168
ds_read_b128 v[208:211], v3 offset:28672
v_cvt_pk_bf16_f32 v55, v174, v175
v_mfma_f32_16x16x32_bf16 v[148:151], v[216:219], v[108:111], v[148:151]
buffer_load_dwordx4 v14, s[16:19], s20 offen sc1 lds
v_mfma_f32_16x16x32_bf16 v[156:159], v[220:223], v[108:111], v[156:159]
s_add_u32 s23, s27, s33
ds_read_b128 v[212:215], v3 offset:29696
v_readlane_b32 s16, v16, s22
v_mfma_f32_16x16x32_bf16 v[152:155], v[220:223], v[104:107], v[152:155]
s_waitcnt lgkmcnt(4)
v_readlane_b32 s17, v17, s22
v_add_f32 v31, v31, v173
v_mfma_f32_16x16x32_bf16 v[128:131], v[192:195], v[112:115], v[128:131]
s_waitcnt vmcnt(16)
ds_read_b128 v[216:219], v3 offset:30720
v_readlane_b32 s25, v18, s22
v_mfma_f32_16x16x32_bf16 v[132:135], v[192:195], v[116:119], v[132:135]
s_barrier
v_exp_f32 v176, v176
v_mfma_f32_16x16x32_bf16 v[140:143], v[196:199], v[116:119], v[140:143]
s_mov_b32 s20, 0
ds_read_b128 v[220:223], v3 offset:31744
v_mad_u32_u24 v14, v12, s25, v13
v_mfma_f32_16x16x32_bf16 v[136:139], v[196:199], v[112:115], v[136:139]
s_waitcnt lgkmcnt(4)
v_add_u32 v3, s26, v2
v_add_f32 v31, v31, v174
v_mfma_f32_16x16x32_bf16 v[144:147], v[200:203], v[112:115], v[144:147]
s_mov_b32 m0, s23
ds_read_b128 v[192:195], v3 offset:0
v_add_f32 v31, v31, v175
v_mfma_f32_16x16x32_bf16 v[148:151], v[200:203], v[116:119], v[148:151]
buffer_load_dwordx4 v14, s[16:19], s20 offen sc1 lds
v_mfma_f32_16x16x32_bf16 v[156:159], v[204:207], v[116:119], v[156:159]
s_add_u32 s22, s22, 1
ds_read_b128 v[196:199], v3 offset:1024
v_add_f32 v30, v30, v176
v_mfma_f32_16x16x32_bf16 v[152:155], v[204:207], v[112:115], v[152:155]
s_waitcnt lgkmcnt(4)
v_exp_f32 v177, v177
v_mfma_f32_16x16x32_bf16 v[128:131], v[208:211], v[120:123], v[128:131]
s_lshl_b32 s21, s25, 3
ds_read_b128 v[200:203], v3 offset:2048
v_cvt_pk_bf16_f32 v56, v176, v177
v_mfma_f32_16x16x32_bf16 v[132:135], v[208:211], v[124:127], v[132:135]
s_mov_b32 s27, s24
v_exp_f32 v178, v178
v_mfma_f32_16x16x32_bf16 v[140:143], v[212:215], v[124:127], v[140:143]
s_add_u32 s20, s20, s21
ds_read_b128 v[204:207], v3 offset:3072
v_add_f32 v30, v30, v177
v_mfma_f32_16x16x32_bf16 v[136:139], v[212:215], v[120:123], v[136:139]
s_waitcnt lgkmcnt(4)
v_exp_f32 v179, v179
v_mfma_f32_16x16x32_bf16 v[144:147], v[216:219], v[120:123], v[144:147]
s_add_u32 m0, s23, 1024
ds_read_b128 v[208:211], v3 offset:4096
v_cvt_pk_bf16_f32 v57, v178, v179
v_mfma_f32_16x16x32_bf16 v[148:151], v[216:219], v[124:127], v[148:151]
buffer_load_dwordx4 v14, s[16:19], s20 offen sc1 lds
v_mfma_f32_16x16x32_bf16 v[156:159], v[220:223], v[124:127], v[156:159]
s_mov_b32 s24, s26
ds_read_b128 v[212:215], v3 offset:5120
v_add_f32 v30, v30, v178
v_mfma_f32_16x16x32_bf16 v[152:155], v[220:223], v[120:123], v[152:155]
s_waitcnt lgkmcnt(4)
v_exp_f32 v184, v184
v_mfma_f32_16x16x32_bf16 a[0:3], v[192:195], v[48:51], a[0:3]
ds_read_b128 v[216:219], v3 offset:6144
v_add_f32 v30, v30, v179
v_mfma_f32_16x16x32_bf16 a[4:7], v[192:195], v[52:55], a[4:7]
v_exp_f32 v185, v185
v_mfma_f32_16x16x32_bf16 a[12:15], v[196:199], v[52:55], a[12:15]
s_add_u32 s20, s20, s21
ds_read_b128 v[220:223], v3 offset:7168
v_cvt_pk_bf16_f32 v58, v184, v185
v_mfma_f32_16x16x32_bf16 a[8:11], v[196:199], v[48:51], a[8:11]
s_waitcnt lgkmcnt(4)
v_exp_f32 v186, v186
v_mfma_f32_16x16x32_bf16 a[16:19], v[200:203], v[48:51], a[16:19]
s_add_u32 m0, s23, 2048
ds_read_b128 v[192:195], v3 offset:8192
v_add_f32 v30, v30, v184
v_mfma_f32_16x16x32_bf16 a[20:23], v[200:203], v[52:55], a[20:23]
buffer_load_dwordx4 v14, s[16:19], s20 offen sc1 lds
v_mfma_f32_16x16x32_bf16 a[28:31], v[204:207], v[52:55], a[28:31]
ds_read_b128 v[196:199], v3 offset:9216
v_max3_f32 v32, v128, v129, v130
v_mfma_f32_16x16x32_bf16 a[24:27], v[204:207], v[48:51], a[24:27]
s_waitcnt lgkmcnt(4)
v_exp_f32 v187, v187
v_mfma_f32_16x16x32_bf16 a[32:35], v[208:211], v[48:51], a[32:35]
ds_read_b128 v[200:203], v3 offset:10240
v_cvt_pk_bf16_f32 v59, v186, v187
v_mfma_f32_16x16x32_bf16 a[36:39], v[208:211], v[52:55], a[36:39]
v_exp_f32 v180, v180
v_mfma_f32_16x16x32_bf16 a[44:47], v[212:215], v[52:55], a[44:47]
s_add_u32 s20, s20, s21
ds_read_b128 v[204:207], v3 offset:11264
v_max3_f32 v32, v32, v131, v132
v_mfma_f32_16x16x32_bf16 a[40:43], v[212:215], v[48:51], a[40:43]
s_waitcnt lgkmcnt(4)
v_exp_f32 v181, v181
v_mfma_f32_16x16x32_bf16 a[48:51], v[216:219], v[48:51], a[48:51]
s_add_u32 m0, s23, 3072
ds_read_b128 v[208:211], v3 offset:12288
v_cvt_pk_bf16_f32 v60, v180, v181
v_mfma_f32_16x16x32_bf16 a[52:55], v[216:219], v[52:55], a[52:55]
buffer_load_dwordx4 v14, s[16:19], s20 offen sc1 lds
v_mfma_f32_16x16x32_bf16 a[60:63], v[220:223], v[52:55], a[60:63]
ds_read_b128 v[212:215], v3 offset:13312
v_max3_f32 v32, v32, v133, v134
v_mfma_f32_16x16x32_bf16 a[56:59], v[220:223], v[48:51], a[56:59]
s_waitcnt lgkmcnt(4)
v_exp_f32 v182, v182
v_mfma_f32_16x16x32_bf16 a[64:67], v[192:195], v[48:51], a[64:67]
ds_read_b128 v[216:219], v3 offset:14336
v_max3_f32 v32, v32, v135, v136
v_mfma_f32_16x16x32_bf16 a[68:71], v[192:195], v[52:55], a[68:71]
v_exp_f32 v183, v183
v_mfma_f32_16x16x32_bf16 a[76:79], v[196:199], v[52:55], a[76:79]
s_add_u32 s20, s20, s21
ds_read_b128 v[220:223], v3 offset:15360
v_cvt_pk_bf16_f32 v61, v182, v183
v_mfma_f32_16x16x32_bf16 a[72:75], v[196:199], v[48:51], a[72:75]
s_waitcnt lgkmcnt(4)
v_exp_f32 v188, v188
v_mfma_f32_16x16x32_bf16 a[80:83], v[200:203], v[48:51], a[80:83]
s_add_u32 m0, s23, 4096
ds_read_b128 v[192:195], v3 offset:16384
v_max3_f32 v32, v32, v137, v138
v_mfma_f32_16x16x32_bf16 a[84:87], v[200:203], v[52:55], a[84:87]
buffer_load_dwordx4 v14, s[16:19], s20 offen sc1 lds
v_mfma_f32_16x16x32_bf16 a[92:95], v[204:207], v[52:55], a[92:95]
ds_read_b128 v[196:199], v3 offset:17408
v_max3_f32 v32, v32, v139, v140
v_mfma_f32_16x16x32_bf16 a[88:91], v[204:207], v[48:51], a[88:91]
s_waitcnt lgkmcnt(4)
v_exp_f32 v189, v189
v_mfma_f32_16x16x32_bf16 a[96:99], v[208:211], v[48:51], a[96:99]
ds_read_b128 v[200:203], v3 offset:18432
v_cvt_pk_bf16_f32 v62, v188, v189
v_mfma_f32_16x16x32_bf16 a[100:103], v[208:211], v[52:55], a[100:103]
v_exp_f32 v190, v190
v_mfma_f32_16x16x32_bf16 a[108:111], v[212:215], v[52:55], a[108:111]
s_add_u32 s20, s20, s21
ds_read_b128 v[204:207], v3 offset:19456
v_max3_f32 v32, v32, v141, v142
v_mfma_f32_16x16x32_bf16 a[104:107], v[212:215], v[48:51], a[104:107]
s_waitcnt lgkmcnt(4)
v_exp_f32 v191, v191
v_mfma_f32_16x16x32_bf16 a[112:115], v[216:219], v[48:51], a[112:115]
s_add_u32 m0, s23, 5120
ds_read_b128 v[208:211], v3 offset:20480
v_cvt_pk_bf16_f32 v63, v190, v191
v_mfma_f32_16x16x32_bf16 a[116:119], v[216:219], v[52:55], a[116:119]
buffer_load_dwordx4 v14, s[16:19], s20 offen sc1 lds
v_mfma_f32_16x16x32_bf16 a[124:127], v[220:223], v[52:55], a[124:127]
ds_read_b128 v[212:215], v3 offset:21504
v_max3_f32 v32, v32, v143, v144
v_mfma_f32_16x16x32_bf16 a[120:123], v[220:223], v[48:51], a[120:123]
s_waitcnt lgkmcnt(4)
v_max3_f32 v32, v32, v145, v146
v_add_f32 v30, v30, v185
v_mfma_f32_16x16x32_bf16 a[0:3], v[192:195], v[56:59], a[0:3]
ds_read_b128 v[216:219], v3 offset:22528
v_max3_f32 v32, v32, v147, v148
v_mfma_f32_16x16x32_bf16 a[4:7], v[192:195], v[60:63], a[4:7]
buffer_store_dword v42, v43, s[36:39], s56 offen sc1
v_mfma_f32_16x16x32_bf16 a[12:15], v[196:199], v[60:63], a[12:15]
s_add_u32 s20, s20, s21
ds_read_b128 v[220:223], v3 offset:23552
v_max3_f32 v32, v32, v149, v150
v_mfma_f32_16x16x32_bf16 a[8:11], v[196:199], v[56:59], a[8:11]
s_waitcnt lgkmcnt(4)
v_max3_f32 v32, v32, v151, v152
v_add_f32 v30, v30, v186
v_mfma_f32_16x16x32_bf16 a[16:19], v[200:203], v[56:59], a[16:19]
s_add_u32 m0, s23, 6144
ds_read_b128 v[192:195], v3 offset:24576
v_max3_f32 v32, v32, v153, v154
v_mfma_f32_16x16x32_bf16 a[20:23], v[200:203], v[60:63], a[20:23]
buffer_load_dwordx4 v14, s[16:19], s20 offen sc1 lds
v_mfma_f32_16x16x32_bf16 a[28:31], v[204:207], v[60:63], a[28:31]
s_add_u32 s26, s24, 32768
ds_read_b128 v[196:199], v3 offset:25600
v_max3_f32 v32, v32, v155, v156
v_mfma_f32_16x16x32_bf16 a[24:27], v[204:207], v[56:59], a[24:27]
s_waitcnt lgkmcnt(4)
v_max3_f32 v32, v32, v157, v158
v_add_f32 v30, v30, v187
v_mfma_f32_16x16x32_bf16 a[32:35], v[208:211], v[56:59], a[32:35]
s_sub_u32 s35, s26, 163840
ds_read_b128 v[200:203], v3 offset:26624
v_max3_f32 v32, v32, v159, v159
v_mfma_f32_16x16x32_bf16 a[36:39], v[208:211], v[60:63], a[36:39]
s_cselect_b32 s26, s26, s35
v_cmp_lt_f32 vcc, 0x41000000, v32
v_add_f32 v31, v31, v180
v_mfma_f32_16x16x32_bf16 a[44:47], v[212:215], v[60:63], a[44:47]
s_add_u32 s20, s20, s21
ds_read_b128 v[204:207], v3 offset:27648
v_add_f32 v31, v31, v181
v_mfma_f32_16x16x32_bf16 a[40:43], v[212:215], v[56:59], a[40:43]
s_waitcnt lgkmcnt(4)
v_add_f32 v31, v31, v182
v_add_f32 v31, v31, v183
v_mfma_f32_16x16x32_bf16 a[48:51], v[216:219], v[56:59], a[48:51]
s_add_u32 m0, s23, 7168
ds_read_b128 v[208:211], v3 offset:28672
v_add_f32 v31, v31, v188
v_mfma_f32_16x16x32_bf16 a[52:55], v[216:219], v[60:63], a[52:55]
buffer_load_dwordx4 v14, s[16:19], s20 offen sc1 lds
v_mfma_f32_16x16x32_bf16 a[60:63], v[220:223], v[60:63], a[60:63]
s_add_u32 s23, s27, s33
ds_read_b128 v[212:215], v3 offset:29696
v_readlane_b32 s16, v16, s22
v_mfma_f32_16x16x32_bf16 a[56:59], v[220:223], v[56:59], a[56:59]
s_waitcnt lgkmcnt(4)
v_readlane_b32 s17, v17, s22
v_add_f32 v31, v31, v189
v_mfma_f32_16x16x32_bf16 a[64:67], v[192:195], v[56:59], a[64:67]
s_waitcnt vmcnt(16)
ds_read_b128 v[216:219], v3 offset:30720
v_readlane_b32 s25, v18, s22
v_mfma_f32_16x16x32_bf16 a[68:71], v[192:195], v[60:63], a[68:71]
s_barrier
v_add_f32 v31, v31, v190
v_add_f32 v31, v31, v191
v_mfma_f32_16x16x32_bf16 a[76:79], v[196:199], v[60:63], a[76:79]
s_mov_b32 s20, 0
ds_read_b128 v[220:223], v3 offset:31744
v_mad_u32_u24 v14, v12, s25, v13
v_mfma_f32_16x16x32_bf16 a[72:75], v[196:199], v[56:59], a[72:75]
s_waitcnt lgkmcnt(4)
v_add_u32 v3, s26, v2
v_mfma_f32_16x16x32_bf16 a[80:83], v[200:203], v[56:59], a[80:83]
s_mov_b32 m0, s23
ds_read_b128 v[192:195], v3 offset:0
v_mfma_f32_16x16x32_bf16 a[84:87], v[200:203], v[60:63], a[84:87]
buffer_load_dwordx4 v14, s[16:19], s20 offen sc1 lds
v_mfma_f32_16x16x32_bf16 a[92:95], v[204:207], v[60:63], a[92:95]
s_add_u32 s22, s22, 1
ds_read_b128 v[196:199], v3 offset:1024
v_mfma_f32_16x16x32_bf16 a[88:91], v[204:207], v[56:59], a[88:91]
s_waitcnt lgkmcnt(4)
v_mfma_f32_16x16x32_bf16 a[96:99], v[208:211], v[56:59], a[96:99]
s_lshl_b32 s21, s25, 3
ds_read_b128 v[200:203], v3 offset:2048
v_mfma_f32_16x16x32_bf16 a[100:103], v[208:211], v[60:63], a[100:103]
s_mov_b32 s27, s24
v_mfma_f32_16x16x32_bf16 a[108:111], v[212:215], v[60:63], a[108:111]
s_add_u32 s20, s20, s21
ds_read_b128 v[204:207], v3 offset:3072
v_mfma_f32_16x16x32_bf16 a[104:107], v[212:215], v[56:59], a[104:107]
s_waitcnt lgkmcnt(4)
v_mfma_f32_16x16x32_bf16 a[112:115], v[216:219], v[56:59], a[112:115]
s_add_u32 m0, s23, 1024
ds_read_b128 v[208:211], v3 offset:4096
v_mfma_f32_16x16x32_bf16 a[116:119], v[216:219], v[60:63], a[116:119]
buffer_load_dwordx4 v14, s[16:19], s20 offen sc1 lds
v_mfma_f32_16x16x32_bf16 a[124:127], v[220:223], v[60:63], a[124:127]
s_mov_b32 s24, s26
ds_read_b128 v[212:215], v3 offset:5120
v_mfma_f32_16x16x32_bf16 a[120:123], v[220:223], v[56:59], a[120:123]
s_waitcnt lgkmcnt(4)
s_cmp_gt_u32 s45, s46
s_cbranch_scc1 MASK1_%=
MRET1_%=:
s_cbranch_vccnz RARE1_%=
RRET1_%=:
s_add_u32 s45, s45, 64
s_sub_u32 s34, s34, 1
s_cmp_eq_u32 s34, 0
s_cbranch_scc1 VLAST0_%=
s_branch BODY0_%=
VLAST0_%=:
s_cmp_eq_u32 s60, 0
s_cbranch_scc1 VLASTR0_%=
s_waitcnt vmcnt(8)
v_readfirstlane_b32 s48, v15
s_cmp_eq_u32 s48, s98
s_cbranch_scc1 FLGOKv0_%=
s_waitcnt vmcnt(0)
buffer_store_dword v42, v43, s[36:39], s56 offen sc1
buffer_store_dword v42, v43, s[36:39], s12 offen sc1
s_mov_b32 s47, 0
POLLv0_%=:
buffer_load_dword v15, v43, s[36:39], s58 offen sc1
s_waitcnt vmcnt(0)
v_readfirstlane_b32 s48, v15
s_add_u32 s47, s47, 1
s_cmp_eq_u32 s48, s98
s_cbranch_scc1 FLGOKv0_%=
s_sleep 1
s_cmp_lt_u32 s47, 0x40000
s_cbranch_scc1 POLLv0_%=
FLGOKv0_%=:
s_add_u32 s50, s59, 0
s_add_u32 s51, s59, 4096
s_add_u32 s52, s59, 8192
s_add_u32 s53, s59, 12288
s_add_u32 s54, s59, 16384
v_exp_f32 v128, v128
v_exp_f32 v129, v129
v_exp_f32 v130, v130
v_exp_f32 v131, v131
v_exp_f32 v136, v136
v_exp_f32 v137, v137
v_exp_f32 v138, v138
v_exp_f32 v139, v139
v_exp_f32 v132, v132
v_exp_f32 v133, v133
v_exp_f32 v134, v134
v_exp_f32 v135, v135
v_exp_f32 v140, v140
v_exp_f32 v141, v141
v_exp_f32 v142, v142
v_exp_f32 v143, v143
v_exp_f32 v144, v144
v_exp_f32 v145, v145
v_exp_f32 v146, v146
v_exp_f32 v147, v147
v_exp_f32 v152, v152
v_exp_f32 v153, v153
v_exp_f32 v154, v154
v_exp_f32 v155, v155
v_exp_f32 v148, v148
v_exp_f32 v149, v149
v_exp_f32 v150, v150
v_exp_f32 v151, v151
v_exp_f32 v156, v156
v_exp_f32 v157, v157
v_exp_f32 v158, v158
v_exp_f32 v159, v159
s_nop 0
v_add_f32 v30, v30, v128
v_cvt_pk_bf16_f32 v48, v128, v129
v_add_f32 v30, v30, v129
v_cvt_pk_bf16_f32 v49, v130, v131
v_add_f32 v30, v30, v130
v_cvt_pk_bf16_f32 v50, v136, v137
v_add_f32 v30, v30, v131
v_cvt_pk_bf16_f32 v51, v138, v139
v_add_f32 v30, v30, v136
v_cvt_pk_bf16_f32 v52, v132, v133
v_add_f32 v30, v30, v137
v_cvt_pk_bf16_f32 v53, v134, v135
v_add_f32 v30, v30, v138
v_cvt_pk_bf16_f32 v54, v140, v141
v_add_f32 v30, v30, v139
v_cvt_pk_bf16_f32 v55, v142, v143
v_add_f32 v31, v31, v132
v_cvt_pk_bf16_f32 v56, v144, v145
v_add_f32 v31, v31, v133
v_cvt_pk_bf16_f32 v57, v146, v147
v_add_f32 v31, v31, v134
v_cvt_pk_bf16_f32 v58, v152, v153
v_add_f32 v31, v31, v135
v_cvt_pk_bf16_f32 v59, v154, v155
v_add_f32 v31, v31, v140
v_cvt_pk_bf16_f32 v60, v148, v149
v_add_f32 v31, v31, v141
v_cvt_pk_bf16_f32 v61, v150, v151
v_add_f32 v31, v31, v142
v_cvt_pk_bf16_f32 v62, v156, v157
v_add_f32 v31, v31, v143
s_nop 1
v_mfma_f32_16x16x32_bf16 a[0:3], v[192:195], v[48:51], a[0:3]
ds_read_b128 v[216:219], v3 offset:6144
buffer_load_dwordx4 v[64:67], v2, s[36:39], s50 offen offset:0 sc1
v_cvt_pk_bf16_f32 v63, v158, v159
v_mfma_f32_16x16x32_bf16 a[4:7], v[192:195], v[52:55], a[4:7]
v_add_f32 v30, v30, v144
v_add_f32 v30, v30, v145
v_mfma_f32_16x16x32_bf16 a[12:15], v[196:199], v[52:55], a[12:15]
s_add_u32 s20, s20, s21
ds_read_b128 v[220:223], v3 offset:7168
buffer_load_dwordx4 v[68:71], v2, s[36:39], s50 offen offset:1024 sc1
v_add_f32 v30, v30, v146
v_mfma_f32_16x16x32_bf16 a[8:11], v[196:199], v[48:51], a[8:11]
s_waitcnt lgkmcnt(4)
v_add_f32 v30, v30, v147
v_add_f32 v30, v30, v152
v_mfma_f32_16x16x32_bf16 a[16:19], v[200:203], v[48:51], a[16:19]
s_add_u32 m0, s23, 2048
ds_read_b128 v[192:195], v3 offset:8192
buffer_load_dwordx4 v[72:75], v2, s[36:39], s50 offen offset:2048 sc1
v_add_f32 v30, v30, v153
v_mfma_f32_16x16x32_bf16 a[20:23], v[200:203], v[52:55], a[20:23]
buffer_load_dwordx4 v14, s[16:19], s20 offen sc1 lds
v_mfma_f32_16x16x32_bf16 a[28:31], v[204:207], v[52:55], a[28:31]
ds_read_b128 v[196:199], v3 offset:9216
buffer_load_dwordx4 v[76:79], v2, s[36:39], s50 offen offset:3072 sc1
v_add_f32 v30, v30, v154
v_mfma_f32_16x16x32_bf16 a[24:27], v[204:207], v[48:51], a[24:27]
s_waitcnt lgkmcnt(4)
v_add_f32 v30, v30, v155
v_add_f32 v31, v31, v148
v_mfma_f32_16x16x32_bf16 a[32:35], v[208:211], v[48:51], a[32:35]
ds_read_b128 v[200:203], v3 offset:10240
buffer_load_dwordx4 v[80:83], v2, s[36:39], s51 offen offset:0 sc1
v_add_f32 v31, v31, v149
v_mfma_f32_16x16x32_bf16 a[36:39], v[208:211], v[52:55], a[36:39]
v_add_f32 v31, v31, v150
v_add_f32 v31, v31, v151
v_mfma_f32_16x16x32_bf16 a[44:47], v[212:215], v[52:55], a[44:47]
s_add_u32 s20, s20, s21
ds_read_b128 v[204:207], v3 offset:11264
buffer_load_dwordx4 v[84:87], v2, s[36:39], s51 offen offset:1024 sc1
v_add_f32 v31, v31, v156
v_mfma_f32_16x16x32_bf16 a[40:43], v[212:215], v[48:51], a[40:43]
s_waitcnt lgkmcnt(4)
v_add_f32 v31, v31, v157
v_add_f32 v31, v31, v158
v_mfma_f32_16x16x32_bf16 a[48:51], v[216:219], v[48:51], a[48:51]
s_add_u32 m0, s23, 3072
ds_read_b128 v[208:211], v3 offset:12288
buffer_load_dwordx4 v[88:91], v2, s[36:39], s51 offen offset:2048 sc1
v_add_f32 v31, v31, v159
v_mfma_f32_16x16x32_bf16 a[52:55], v[216:219], v[52:55], a[52:55]
buffer_load_dwordx4 v14, s[16:19], s20 offen sc1 lds
v_mfma_f32_16x16x32_bf16 a[60:63], v[220:223], v[52:55], a[60:63]
ds_read_b128 v[212:215], v3 offset:13312
buffer_load_dwordx4 v[92:95], v2, s[36:39], s51 offen offset:3072 sc1
v_mfma_f32_16x16x32_bf16 a[56:59], v[220:223], v[48:51], a[56:59]
s_waitcnt lgkmcnt(4)
v_mfma_f32_16x16x32_bf16 a[64:67], v[192:195], v[48:51], a[64:67]
ds_read_b128 v[216:219], v3 offset:14336
buffer_load_dwordx4 v[96:99], v2, s[36:39], s52 offen offset:0 sc1
v_mfma_f32_16x16x32_bf16 a[68:71], v[192:195], v[52:55], a[68:71]
v_mfma_f32_16x16x32_bf16 a[76:79], v[196:199], v[52:55], a[76:79]
s_add_u32 s20, s20, s21
ds_read_b128 v[220:223], v3 offset:15360
buffer_load_dwordx4 v[100:103], v2, s[36:39], s52 offen offset:1024 sc1
v_mfma_f32_16x16x32_bf16 a[72:75], v[196:199], v[48:51], a[72:75]
s_waitcnt lgkmcnt(4)
v_mfma_f32_16x16x32_bf16 a[80:83], v[200:203], v[48:51], a[80:83]
s_add_u32 m0, s23, 4096
ds_read_b128 v[192:195], v3 offset:16384
buffer_load_dwordx4 v[104:107], v2, s[36:39], s52 offen offset:2048 sc1
v_mfma_f32_16x16x32_bf16 a[84:87], v[200:203], v[52:55], a[84:87]
buffer_load_dwordx4 v14, s[16:19], s20 offen sc1 lds
v_mfma_f32_16x16x32_bf16 a[92:95], v[204:207], v[52:55], a[92:95]
ds_read_b128 v[196:199], v3 offset:17408
buffer_load_dwordx4 v[108:111], v2, s[36:39], s52 offen offset:3072 sc1
v_mfma_f32_16x16x32_bf16 a[88:91], v[204:207], v[48:51], a[88:91]
s_waitcnt lgkmcnt(4)
v_mfma_f32_16x16x32_bf16 a[96:99], v[208:211], v[48:51], a[96:99]
ds_read_b128 v[200:203], v3 offset:18432
buffer_load_dwordx4 v[112:115], v2, s[36:39], s53 offen offset:0 sc1
v_mfma_f32_16x16x32_bf16 a[100:103], v[208:211], v[52:55], a[100:103]
v_mfma_f32_16x16x32_bf16 a[108:111], v[212:215], v[52:55], a[108:111]
s_add_u32 s20, s20, s21
ds_read_b128 v[204:207], v3 offset:19456
buffer_load_dwordx4 v[116:119], v2, s[36:39], s53 offen offset:1024 sc1
v_mfma_f32_16x16x32_bf16 a[104:107], v[212:215], v[48:51], a[104:107]
s_waitcnt lgkmcnt(4)
v_mfma_f32_16x16x32_bf16 a[112:115], v[216:219], v[48:51], a[112:115]
s_add_u32 m0, s23, 5120
ds_read_b128 v[208:211], v3 offset:20480
buffer_load_dwordx4 v[120:123], v2, s[36:39], s53 offen offset:2048 sc1
v_mfma_f32_16x16x32_bf16 a[116:119], v[216:219], v[52:55], a[116:119]
buffer_load_dwordx4 v14, s[16:19], s20 offen sc1 lds
v_mfma_f32_16x16x32_bf16 a[124:127], v[220:223], v[52:55], a[124:127]
ds_read_b128 v[212:215], v3 offset:21504
buffer_load_dwordx4 v[124:127], v2, s[36:39], s53 offen offset:3072 sc1
v_mfma_f32_16x16x32_bf16 a[120:123], v[220:223], v[48:51], a[120:123]
s_waitcnt lgkmcnt(4)
v_mfma_f32_16x16x32_bf16 a[0:3], v[192:195], v[56:59], a[0:3]
ds_read_b128 v[216:219], v3 offset:22528
buffer_load_dwordx4 v[224:227], v2, s[36:39], s54 offen sc1
v_mfma_f32_16x16x32_bf16 a[4:7], v[192:195], v[60:63], a[4:7]
v_mfma_f32_16x16x32_bf16 a[12:15], v[196:199], v[60:63], a[12:15]
s_add_u32 s20, s20, s21
ds_read_b128 v[220:223], v3 offset:23552
v_mfma_f32_16x16x32_bf16 a[8:11], v[196:199], v[56:59], a[8:11]
s_waitcnt lgkmcnt(4)
v_mfma_f32_16x16x32_bf16 a[16:19], v[200:203], v[56:59], a[16:19]
s_add_u32 m0, s23, 6144
ds_read_b128 v[192:195], v3 offset:24576
v_mfma_f32_16x16x32_bf16 a[20:23], v[200:203], v[60:63], a[20:23]
buffer_load_dwordx4 v14, s[16:19], s20 offen sc1 lds
v_mfma_f32_16x16x32_bf16 a[28:31], v[204:207], v[60:63], a[28:31]
s_add_u32 s26, s24, 32768
ds_read_b128 v[196:199], v3 offset:25600
v_mfma_f32_16x16x32_bf16 a[24:27], v[204:207], v[56:59], a[24:27]
s_waitcnt lgkmcnt(4)
v_mfma_f32_16x16x32_bf16 a[32:35], v[208:211], v[56:59], a[32:35]
s_sub_u32 s35, s26, 163840
ds_read_b128 v[200:203], v3 offset:26624
v_mfma_f32_16x16x32_bf16 a[36:39], v[208:211], v[60:63], a[36:39]
s_cselect_b32 s26, s26, s35
v_mfma_f32_16x16x32_bf16 a[44:47], v[212:215], v[60:63], a[44:47]
s_add_u32 s20, s20, s21
ds_read_b128 v[204:207], v3 offset:27648
v_mfma_f32_16x16x32_bf16 a[40:43], v[212:215], v[56:59], a[40:43]
s_waitcnt lgkmcnt(4)
v_add_u32 v8, s26, v4
v_mfma_f32_16x16x32_bf16 a[48:51], v[216:219], v[56:59], a[48:51]
s_add_u32 m0, s23, 7168
ds_read_b128 v[208:211], v3 offset:28672
v_mfma_f32_16x16x32_bf16 a[52:55], v[216:219], v[60:63], a[52:55]
buffer_load_dwordx4 v14, s[16:19], s20 offen sc1 lds
v_mfma_f32_16x16x32_bf16 a[60:63], v[220:223], v[60:63], a[60:63]
s_add_u32 s23, s27, s33
ds_read_b128 v[212:215], v3 offset:29696
v_readlane_b32 s16, v16, s22
v_mfma_f32_16x16x32_bf16 a[56:59], v[220:223], v[56:59], a[56:59]
s_waitcnt lgkmcnt(4)
v_readlane_b32 s17, v17, s22
v_mfma_f32_16x16x32_bf16 a[64:67], v[192:195], v[56:59], a[64:67]
s_waitcnt vmcnt(33)
ds_read_b128 v[216:219], v3 offset:30720
v_readlane_b32 s25, v18, s22
v_mfma_f32_16x16x32_bf16 a[68:71], v[192:195], v[60:63], a[68:71]
s_barrier
v_add_u32 v9, s26, v5
v_mfma_f32_16x16x32_bf16 a[76:79], v[196:199], v[60:63], a[76:79]
s_mov_b32 s20, 0
ds_read_b128 v[220:223], v3 offset:31744
v_mad_u32_u24 v14, v12, s25, v13
v_mfma_f32_16x16x32_bf16 a[72:75], v[196:199], v[56:59], a[72:75]
s_waitcnt lgkmcnt(4)
v_add_u32 v10, s26, v6
v_mfma_f32_16x16x32_bf16 a[80:83], v[200:203], v[56:59], a[80:83]
s_mov_b32 m0, s23
ds_read_b64_tr_b16 v[192:193], v8 offset:0
ds_read_b64_tr_b16 v[194:195], v8 offset:2048
v_mfma_f32_16x16x32_bf16 a[84:87], v[200:203], v[60:63], a[84:87]
buffer_load_dwordx4 v14, s[16:19], s20 offen sc1 lds
v_mfma_f32_16x16x32_bf16 a[92:95], v[204:207], v[60:63], a[92:95]
s_add_u32 s22, s22, 1
ds_read_b64_tr_b16 v[196:197], v8 offset:4096
v_mfma_f32_16x16x32_bf16 a[88:91], v[204:207], v[56:59], a[88:91]
s_waitcnt lgkmcnt(5)
ds_read_b64_tr_b16 v[198:199], v8 offset:6144
v_add_u32 v11, s26, v7
v_mfma_f32_16x16x32_bf16 a[96:99], v[208:211], v[56:59], a[96:99]
s_lshl_b32 s21, s25, 3
ds_read_b64_tr_b16 v[200:201], v8 offset:8192
v_mfma_f32_16x16x32_bf16 a[100:103], v[208:211], v[60:63], a[100:103]
s_mov_b32 s27, s24
ds_read_b64_tr_b16 v[202:203], v8 offset:10240
v_mfma_f32_16x16x32_bf16 a[108:111], v[212:215], v[60:63], a[108:111]
s_add_u32 s20, s20, s21
ds_read_b64_tr_b16 v[204:205], v8 offset:12288
v_mfma_f32_16x16x32_bf16 a[104:107], v[212:215], v[56:59], a[104:107]
s_waitcnt lgkmcnt(7)
ds_read_b64_tr_b16 v[206:207], v8 offset:14336
v_mfma_f32_16x16x32_bf16 a[112:115], v[216:219], v[56:59], a[112:115]
s_add_u32 m0, s23, 1024
ds_read_b64_tr_b16 v[208:209], v8 offset:16384
ds_read_b64_tr_b16 v[210:211], v8 offset:18432
v_mfma_f32_16x16x32_bf16 a[116:119], v[216:219], v[60:63], a[116:119]
buffer_load_dwordx4 v14, s[16:19], s20 offen sc1 lds
v_mfma_f32_16x16x32_bf16 a[124:127], v[220:223], v[60:63], a[124:127]
s_mov_b32 s24, s26
ds_read_b64_tr_b16 v[212:213], v8 offset:20480
v_mfma_f32_16x16x32_bf16 a[120:123], v[220:223], v[56:59], a[120:123]
s_waitcnt lgkmcnt(7)
ds_read_b64_tr_b16 v[214:215], v8 offset:22528
s_waitcnt vmcnt(4)
s_nop 7
s_setpc_b64 s[70:71]
VLASTR0_%=:
v_exp_f32 v128, v128
v_exp_f32 v129, v129
v_exp_f32 v130, v130
v_exp_f32 v131, v131
v_exp_f32 v136, v136
v_exp_f32 v137, v137
v_exp_f32 v138, v138
v_exp_f32 v139, v139
v_exp_f32 v132, v132
v_exp_f32 v133, v133
v_exp_f32 v134, v134
v_exp_f32 v135, v135
v_exp_f32 v140, v140
v_exp_f32 v141, v141
v_exp_f32 v142, v142
v_exp_f32 v143, v143
v_exp_f32 v144, v144
v_exp_f32 v145, v145
v_exp_f32 v146, v146
v_exp_f32 v147, v147
v_exp_f32 v152, v152
v_exp_f32 v153, v153
v_exp_f32 v154, v154
v_exp_f32 v155, v155
v_exp_f32 v148, v148
v_exp_f32 v149, v149
v_exp_f32 v150, v150
v_exp_f32 v151, v151
v_exp_f32 v156, v156
v_exp_f32 v157, v157
v_exp_f32 v158, v158
v_exp_f32 v159, v159
s_nop 0
v_add_f32 v30, v30, v128
v_cvt_pk_bf16_f32 v48, v128, v129
v_add_f32 v30, v30, v129
v_cvt_pk_bf16_f32 v49, v130, v131
v_add_f32 v30, v30, v130
v_cvt_pk_bf16_f32 v50, v136, v137
v_add_f32 v30, v30, v131
v_cvt_pk_bf16_f32 v51, v138, v139
v_add_f32 v30, v30, v136
v_cvt_pk_bf16_f32 v52, v132, v133
v_add_f32 v30, v30, v137
v_cvt_pk_bf16_f32 v53, v134, v135
v_add_f32 v30, v30, v138
v_cvt_pk_bf16_f32 v54, v140, v141
v_add_f32 v30, v30, v139
v_cvt_pk_bf16_f32 v55, v142, v143
v_add_f32 v31, v31, v132
v_cvt_pk_bf16_f32 v56, v144, v145
v_add_f32 v31, v31, v133
v_cvt_pk_bf16_f32 v57, v146, v147
v_add_f32 v31, v31, v134
v_cvt_pk_bf16_f32 v58, v152, v153
v_add_f32 v31, v31, v135
v_cvt_pk_bf16_f32 v59, v154, v155
v_add_f32 v31, v31, v140
v_cvt_pk_bf16_f32 v60, v148, v149
v_add_f32 v31, v31, v141
v_cvt_pk_bf16_f32 v61, v150, v151
v_add_f32 v31, v31, v142
v_cvt_pk_bf16_f32 v62, v156, v157
v_add_f32 v31, v31, v143
s_nop 1
v_mfma_f32_16x16x32_bf16 a[0:3], v[192:195], v[48:51], a[0:3]
ds_read_b128 v[216:219], v3 offset:6144
v_cvt_pk_bf16_f32 v63, v158, v159
v_mfma_f32_16x16x32_bf16 a[4:7], v[192:195], v[52:55], a[4:7]
v_add_f32 v30, v30, v144
v_add_f32 v30, v30, v145
v_mfma_f32_16x16x32_bf16 a[12:15], v[196:199], v[52:55], a[12:15]
s_add_u32 s20, s20, s21
ds_read_b128 v[220:223], v3 offset:7168
v_add_f32 v30, v30, v146
v_mfma_f32_16x16x32_bf16 a[8:11], v[196:199], v[48:51], a[8:11]
s_waitcnt lgkmcnt(4)
v_add_f32 v30, v30, v147
v_add_f32 v30, v30, v152
v_mfma_f32_16x16x32_bf16 a[16:19], v[200:203], v[48:51], a[16:19]
s_add_u32 m0, s23, 2048
ds_read_b128 v[192:195], v3 offset:8192
v_add_f32 v30, v30, v153
v_mfma_f32_16x16x32_bf16 a[20:23], v[200:203], v[52:55], a[20:23]
buffer_load_dwordx4 v14, s[16:19], s20 offen sc1 lds
v_mfma_f32_16x16x32_bf16 a[28:31], v[204:207], v[52:55], a[28:31]
ds_read_b128 v[196:199], v3 offset:9216
v_add_f32 v30, v30, v154
v_mfma_f32_16x16x32_bf16 a[24:27], v[204:207], v[48:51], a[24:27]
s_waitcnt lgkmcnt(4)
v_add_f32 v30, v30, v155
v_add_f32 v31, v31, v148
v_mfma_f32_16x16x32_bf16 a[32:35], v[208:211], v[48:51], a[32:35]
ds_read_b128 v[200:203], v3 offset:10240
v_add_f32 v31, v31, v149
v_mfma_f32_16x16x32_bf16 a[36:39], v[208:211], v[52:55], a[36:39]
v_add_f32 v31, v31, v150
v_add_f32 v31, v31, v151
v_mfma_f32_16x16x32_bf16 a[44:47], v[212:215], v[52:55], a[44:47]
s_add_u32 s20, s20, s21
ds_read_b128 v[204:207], v3 offset:11264
v_add_f32 v31, v31, v156
v_mfma_f32_16x16x32_bf16 a[40:43], v[212:215], v[48:51], a[40:43]
s_waitcnt lgkmcnt(4)
v_add_f32 v31, v31, v157
v_add_f32 v31, v31, v158
v_mfma_f32_16x16x32_bf16 a[48:51], v[216:219], v[48:51], a[48:51]
s_add_u32 m0, s23, 3072
ds_read_b128 v[208:211], v3 offset:12288
v_add_f32 v31, v31, v159
v_mfma_f32_16x16x32_bf16 a[52:55], v[216:219], v[52:55], a[52:55]
buffer_load_dwordx4 v14, s[16:19], s20 offen sc1 lds
v_mfma_f32_16x16x32_bf16 a[60:63], v[220:223], v[52:55], a[60:63]
ds_read_b128 v[212:215], v3 offset:13312
v_accvgpr_read_b32 v64, a160
v_mfma_f32_16x16x32_bf16 a[56:59], v[220:223], v[48:51], a[56:59]
s_waitcnt lgkmcnt(4)
v_accvgpr_read_b32 v65, a161
v_accvgpr_read_b32 v66, a162
v_mfma_f32_16x16x32_bf16 a[64:67], v[192:195], v[48:51], a[64:67]
ds_read_b128 v[216:219], v3 offset:14336
v_accvgpr_read_b32 v67, a163
v_mfma_f32_16x16x32_bf16 a[68:71], v[192:195], v[52:55], a[68:71]
v_accvgpr_read_b32 v68, a164
v_accvgpr_read_b32 v69, a165
v_mfma_f32_16x16x32_bf16 a[76:79], v[196:199], v[52:55], a[76:79]
s_add_u32 s20, s20, s21
ds_read_b128 v[220:223], v3 offset:15360
v_accvgpr_read_b32 v70, a166
v_mfma_f32_16x16x32_bf16 a[72:75], v[196:199], v[48:51], a[72:75]
s_waitcnt lgkmcnt(4)
v_accvgpr_read_b32 v71, a167
v_accvgpr_read_b32 v72, a168
v_mfma_f32_16x16x32_bf16 a[80:83], v[200:203], v[48:51], a[80:83]
s_add_u32 m0, s23, 4096
ds_read_b128 v[192:195], v3 offset:16384
v_accvgpr_read_b32 v73, a169
v_mfma_f32_16x16x32_bf16 a[84:87], v[200:203], v[52:55], a[84:87]
buffer_load_dwordx4 v14, s[16:19], s20 offen sc1 lds
v_mfma_f32_16x16x32_bf16 a[92:95], v[204:207], v[52:55], a[92:95]
ds_read_b128 v[196:199], v3 offset:17408
v_accvgpr_read_b32 v74, a170
v_mfma_f32_16x16x32_bf16 a[88:91], v[204:207], v[48:51], a[88:91]
s_waitcnt lgkmcnt(4)
v_accvgpr_read_b32 v75, a171
v_accvgpr_read_b32 v76, a172
v_mfma_f32_16x16x32_bf16 a[96:99], v[208:211], v[48:51], a[96:99]
ds_read_b128 v[200:203], v3 offset:18432
v_accvgpr_read_b32 v77, a173
v_mfma_f32_16x16x32_bf16 a[100:103], v[208:211], v[52:55], a[100:103]
v_accvgpr_read_b32 v78, a174
v_accvgpr_read_b32 v79, a175
v_mfma_f32_16x16x32_bf16 a[108:111], v[212:215], v[52:55], a[108:111]
s_add_u32 s20, s20, s21
ds_read_b128 v[204:207], v3 offset:19456
v_accvgpr_read_b32 v80, a176
v_mfma_f32_16x16x32_bf16 a[104:107], v[212:215], v[48:51], a[104:107]
s_waitcnt lgkmcnt(4)
v_accvgpr_read_b32 v81, a177
v_accvgpr_read_b32 v82, a178
v_mfma_f32_16x16x32_bf16 a[112:115], v[216:219], v[48:51], a[112:115]
s_add_u32 m0, s23, 5120
ds_read_b128 v[208:211], v3 offset:20480
v_accvgpr_read_b32 v83, a179
v_mfma_f32_16x16x32_bf16 a[116:119], v[216:219], v[52:55], a[116:119]
buffer_load_dwordx4 v14, s[16:19], s20 offen sc1 lds
v_mfma_f32_16x16x32_bf16 a[124:127], v[220:223], v[52:55], a[124:127]
ds_read_b128 v[212:215], v3 offset:21504
v_accvgpr_read_b32 v84, a180
v_mfma_f32_16x16x32_bf16 a[120:123], v[220:223], v[48:51], a[120:123]
s_waitcnt lgkmcnt(4)
v_accvgpr_read_b32 v85, a181
v_accvgpr_read_b32 v86, a182
v_mfma_f32_16x16x32_bf16 a[0:3], v[192:195], v[56:59], a[0:3]
ds_read_b128 v[216:219], v3 offset:22528
v_accvgpr_read_b32 v87, a183
v_mfma_f32_16x16x32_bf16 a[4:7], v[192:195], v[60:63], a[4:7]
v_accvgpr_read_b32 v88, a184
v_accvgpr_read_b32 v89, a185
v_mfma_f32_16x16x32_bf16 a[12:15], v[196:199], v[60:63], a[12:15]
s_add_u32 s20, s20, s21
ds_read_b128 v[220:223], v3 offset:23552
v_accvgpr_read_b32 v90, a186
v_mfma_f32_16x16x32_bf16 a[8:11], v[196:199], v[56:59], a[8:11]
s_waitcnt lgkmcnt(4)
v_accvgpr_read_b32 v91, a187
v_accvgpr_read_b32 v92, a188
v_mfma_f32_16x16x32_bf16 a[16:19], v[200:203], v[56:59], a[16:19]
s_add_u32 m0, s23, 6144
ds_read_b128 v[192:195], v3 offset:24576
v_accvgpr_read_b32 v93, a189
v_mfma_f32_16x16x32_bf16 a[20:23], v[200:203], v[60:63], a[20:23]
buffer_load_dwordx4 v14, s[16:19], s20 offen sc1 lds
v_mfma_f32_16x16x32_bf16 a[28:31], v[204:207], v[60:63], a[28:31]
s_add_u32 s26, s24, 32768
ds_read_b128 v[196:199], v3 offset:25600
v_accvgpr_read_b32 v94, a190
v_mfma_f32_16x16x32_bf16 a[24:27], v[204:207], v[56:59], a[24:27]
s_waitcnt lgkmcnt(4)
v_accvgpr_read_b32 v95, a191
v_accvgpr_read_b32 v96, a192
v_mfma_f32_16x16x32_bf16 a[32:35], v[208:211], v[56:59], a[32:35]
s_sub_u32 s35, s26, 163840
ds_read_b128 v[200:203], v3 offset:26624
v_accvgpr_read_b32 v97, a193
v_mfma_f32_16x16x32_bf16 a[36:39], v[208:211], v[60:63], a[36:39]
s_cselect_b32 s26, s26, s35
v_accvgpr_read_b32 v98, a194
v_accvgpr_read_b32 v99, a195
v_mfma_f32_16x16x32_bf16 a[44:47], v[212:215], v[60:63], a[44:47]
s_add_u32 s20, s20, s21
ds_read_b128 v[204:207], v3 offset:27648
v_accvgpr_read_b32 v100, a196
v_mfma_f32_16x16x32_bf16 a[40:43], v[212:215], v[56:59], a[40:43]
s_waitcnt lgkmcnt(4)
v_accvgpr_read_b32 v101, a197
v_accvgpr_read_b32 v102, a198
v_mfma_f32_16x16x32_bf16 a[48:51], v[216:219], v[56:59], a[48:51]
s_add_u32 m0, s23, 7168
ds_read_b128 v[208:211], v3 offset:28672
v_accvgpr_read_b32 v103, a199
v_mfma_f32_16x16x32_bf16 a[52:55], v[216:219], v[60:63], a[52:55]
buffer_load_dwordx4 v14, s[16:19], s20 offen sc1 lds
v_mfma_f32_16x16x32_bf16 a[60:63], v[220:223], v[60:63], a[60:63]
s_add_u32 s23, s27, s33
ds_read_b128 v[212:215], v3 offset:29696
v_readlane_b32 s16, v16, s22
v_mfma_f32_16x16x32_bf16 a[56:59], v[220:223], v[56:59], a[56:59]
s_waitcnt lgkmcnt(4)
v_readlane_b32 s17, v17, s22
v_accvgpr_read_b32 v104, a200
v_mfma_f32_16x16x32_bf16 a[64:67], v[192:195], v[56:59], a[64:67]
s_waitcnt vmcnt(16)
ds_read_b128 v[216:219], v3 offset:30720
v_readlane_b32 s25, v18, s22
v_mfma_f32_16x16x32_bf16 a[68:71], v[192:195], v[60:63], a[68:71]
s_barrier
v_accvgpr_read_b32 v105, a201
v_accvgpr_read_b32 v106, a202
v_mfma_f32_16x16x32_bf16 a[76:79], v[196:199], v[60:63], a[76:79]
s_mov_b32 s20, 0
ds_read_b128 v[220:223], v3 offset:31744
v_mad_u32_u24 v14, v12, s25, v13
v_mfma_f32_16x16x32_bf16 a[72:75], v[196:199], v[56:59], a[72:75]
s_waitcnt lgkmcnt(4)
v_add_u32 v3, s26, v2
v_accvgpr_read_b32 v107, a203
v_mfma_f32_16x16x32_bf16 a[80:83], v[200:203], v[56:59], a[80:83]
s_mov_b32 m0, s23
ds_read_b128 v[192:195], v3 offset:0
v_accvgpr_read_b32 v108, a204
v_mfma_f32_16x16x32_bf16 a[84:87], v[200:203], v[60:63], a[84:87]
buffer_load_dwordx4 v14, s[16:19], s20 offen sc1 lds
v_mfma_f32_16x16x32_bf16 a[92:95], v[204:207], v[60:63], a[92:95]
s_add_u32 s22, s22, 1
ds_read_b128 v[196:199], v3 offset:1024
v_accvgpr_read_b32 v109, a205
v_mfma_f32_16x16x32_bf16 a[88:91], v[204:207], v[56:59], a[88:91]
s_waitcnt lgkmcnt(4)
v_accvgpr_read_b32 v110, a206
v_accvgpr_read_b32 v111, a207
v_mfma_f32_16x16x32_bf16 a[96:99], v[208:211], v[56:59], a[96:99]
s_lshl_b32 s21, s25, 3
ds_read_b128 v[200:203], v3 offset:2048
v_accvgpr_read_b32 v112, a208
v_mfma_f32_16x16x32_bf16 a[100:103], v[208:211], v[60:63], a[100:103]
s_mov_b32 s27, s24
v_accvgpr_read_b32 v113, a209
v_accvgpr_read_b32 v114, a210
v_mfma_f32_16x16x32_bf16 a[108:111], v[212:215], v[60:63], a[108:111]
s_add_u32 s20, s20, s21
ds_read_b128 v[204:207], v3 offset:3072
v_accvgpr_read_b32 v115, a211
v_mfma_f32_16x16x32_bf16 a[104:107], v[212:215], v[56:59], a[104:107]
s_waitcnt lgkmcnt(4)
v_accvgpr_read_b32 v116, a212
v_accvgpr_read_b32 v117, a213
v_mfma_f32_16x16x32_bf16 a[112:115], v[216:219], v[56:59], a[112:115]
s_add_u32 m0, s23, 1024
ds_read_b128 v[208:211], v3 offset:4096
v_accvgpr_read_b32 v118, a214
v_mfma_f32_16x16x32_bf16 a[116:119], v[216:219], v[60:63], a[116:119]
buffer_load_dwordx4 v14, s[16:19], s20 offen sc1 lds
v_mfma_f32_16x16x32_bf16 a[124:127], v[220:223], v[60:63], a[124:127]
s_mov_b32 s24, s26
ds_read_b128 v[212:215], v3 offset:5120
v_accvgpr_read_b32 v119, a215
v_mfma_f32_16x16x32_bf16 a[120:123], v[220:223], v[56:59], a[120:123]
s_waitcnt lgkmcnt(4)
v_accvgpr_read_b32 v120, a216
v_accvgpr_read_b32 v121, a217
v_accvgpr_read_b32 v122, a218
v_accvgpr_read_b32 v123, a219
v_accvgpr_read_b32 v124, a220
v_accvgpr_read_b32 v125, a221
v_accvgpr_read_b32 v126, a222
v_accvgpr_read_b32 v127, a223
s_nop 7
s_setpc_b64 s[70:71]
VLAST1_%=:
s_cmp_eq_u32 s60, 0
s_cbranch_scc1 VLASTR1_%=
s_waitcnt vmcnt(8)
v_readfirstlane_b32 s48, v15
s_cmp_eq_u32 s48, s98
s_cbranch_scc1 FLGOKv1_%=
s_waitcnt vmcnt(0)
buffer_store_dword v42, v43, s[36:39], s56 offen sc1
buffer_store_dword v42, v43, s[36:39], s12 offen sc1
s_mov_b32 s47, 0
POLLv1_%=:
buffer_load_dword v15, v43, s[36:39], s58 offen sc1
s_waitcnt vmcnt(0)
v_readfirstlane_b32 s48, v15
s_add_u32 s47, s47, 1
s_cmp_eq_u32 s48, s98
s_cbranch_scc1 FLGOKv1_%=
s_sleep 1
s_cmp_lt_u32 s47, 0x40000
s_cbranch_scc1 POLLv1_%=
FLGOKv1_%=:
s_add_u32 s50, s59, 0
s_add_u32 s51, s59, 4096
s_add_u32 s52, s59, 8192
s_add_u32 s53, s59, 12288
s_add_u32 s54, s59, 16384
v_exp_f32 v160, v160
v_exp_f32 v161, v161
v_exp_f32 v162, v162
v_exp_f32 v163, v163
v_exp_f32 v168, v168
v_exp_f32 v169, v169
v_exp_f32 v170, v170
v_exp_f32 v171, v171
v_exp_f32 v164, v164
v_exp_f32 v165, v165
v_exp_f32 v166, v166
v_exp_f32 v167, v167
v_exp_f32 v172, v172
v_exp_f32 v173, v173
v_exp_f32 v174, v174
v_exp_f32 v175, v175
v_exp_f32 v176, v176
v_exp_f32 v177, v177
v_exp_f32 v178, v178
v_exp_f32 v179, v179
v_exp_f32 v184, v184
v_exp_f32 v185, v185
v_exp_f32 v186, v186
v_exp_f32 v187, v187
v_exp_f32 v180, v180
v_exp_f32 v181, v181
v_exp_f32 v182, v182
v_exp_f32 v183, v183
v_exp_f32 v188, v188
v_exp_f32 v189, v189
v_exp_f32 v190, v190
v_exp_f32 v191, v191
s_nop 0
v_add_f32 v30, v30, v160
v_cvt_pk_bf16_f32 v48, v160, v161
v_add_f32 v30, v30, v161
v_cvt_pk_bf16_f32 v49, v162, v163
v_add_f32 v30, v30, v162
v_cvt_pk_bf16_f32 v50, v168, v169
v_add_f32 v30, v30, v163
v_cvt_pk_bf16_f32 v51, v170, v171
v_add_f32 v30, v30, v168
v_cvt_pk_bf16_f32 v52, v164, v165
v_add_f32 v30, v30, v169
v_cvt_pk_bf16_f32 v53, v166, v167
v_add_f32 v30, v30, v170
v_cvt_pk_bf16_f32 v54, v172, v173
v_add_f32 v30, v30, v171
v_cvt_pk_bf16_f32 v55, v174, v175
v_add_f32 v31, v31, v164
v_cvt_pk_bf16_f32 v56, v176, v177
v_add_f32 v31, v31, v165
v_cvt_pk_bf16_f32 v57, v178, v179
v_add_f32 v31, v31, v166
v_cvt_pk_bf16_f32 v58, v184, v185
v_add_f32 v31, v31, v167
v_cvt_pk_bf16_f32 v59, v186, v187
v_add_f32 v31, v31, v172
v_cvt_pk_bf16_f32 v60, v180, v181
v_add_f32 v31, v31, v173
v_cvt_pk_bf16_f32 v61, v182, v183
v_add_f32 v31, v31, v174
v_cvt_pk_bf16_f32 v62, v188, v189
v_add_f32 v31, v31, v175
s_nop 1
v_mfma_f32_16x16x32_bf16 a[0:3], v[192:195], v[48:51], a[0:3]
ds_read_b128 v[216:219], v3 offset:6144
buffer_load_dwordx4 v[64:67], v2, s[36:39], s50 offen offset:0 sc1
v_cvt_pk_bf16_f32 v63, v190, v191
v_mfma_f32_16x16x32_bf16 a[4:7], v[192:195], v[52:55], a[4:7]
v_add_f32 v30, v30, v176
v_add_f32 v30, v30, v177
v_mfma_f32_16x16x32_bf16 a[12:15], v[196:199], v[52:55], a[12:15]
s_add_u32 s20, s20, s21
ds_read_b128 v[220:223], v3 offset:7168
buffer_load_dwordx4 v[68:71], v2, s[36:39], s50 offen offset:1024 sc1
v_add_f32 v30, v30, v178
v_mfma_f32_16x16x32_bf16 a[8:11], v[196:199], v[48:51], a[8:11]
s_waitcnt lgkmcnt(4)
v_add_f32 v30, v30, v179
v_add_f32 v30, v30, v184
v_mfma_f32_16x16x32_bf16 a[16:19], v[200:203], v[48:51], a[16:19]
s_add_u32 m0, s23, 2048
ds_read_b128 v[192:195], v3 offset:8192
buffer_load_dwordx4 v[72:75], v2, s[36:39], s50 offen offset:2048 sc1
v_add_f32 v30, v30, v185
v_mfma_f32_16x16x32_bf16 a[20:23], v[200:203], v[52:55], a[20:23]
buffer_load_dwordx4 v14, s[16:19], s20 offen sc1 lds
v_mfma_f32_16x16x32_bf16 a[28:31], v[204:207], v[52:55], a[28:31]
ds_read_b128 v[196:199], v3 offset:9216
buffer_load_dwordx4 v[76:79], v2, s[36:39], s50 offen offset:3072 sc1
v_add_f32 v30, v30, v186
v_mfma_f32_16x16x32_bf16 a[24:27], v[204:207], v[48:51], a[24:27]
s_waitcnt lgkmcnt(4)
v_add_f32 v30, v30, v187
v_add_f32 v31, v31, v180
v_mfma_f32_16x16x32_bf16 a[32:35], v[208:211], v[48:51], a[32:35]
ds_read_b128 v[200:203], v3 offset:10240
buffer_load_dwordx4 v[80:83], v2, s[36:39], s51 offen offset:0 sc1
v_add_f32 v31, v31, v181
v_mfma_f32_16x16x32_bf16 a[36:39], v[208:211], v[52:55], a[36:39]
v_add_f32 v31, v31, v182
v_add_f32 v31, v31, v183
v_mfma_f32_16x16x32_bf16 a[44:47], v[212:215], v[52:55], a[44:47]
s_add_u32 s20, s20, s21
ds_read_b128 v[204:207], v3 offset:11264
buffer_load_dwordx4 v[84:87], v2, s[36:39], s51 offen offset:1024 sc1
v_add_f32 v31, v31, v188
v_mfma_f32_16x16x32_bf16 a[40:43], v[212:215], v[48:51], a[40:43]
s_waitcnt lgkmcnt(4)
v_add_f32 v31, v31, v189
v_add_f32 v31, v31, v190
v_mfma_f32_16x16x32_bf16 a[48:51], v[216:219], v[48:51], a[48:51]
s_add_u32 m0, s23, 3072
ds_read_b128 v[208:211], v3 offset:12288
buffer_load_dwordx4 v[88:91], v2, s[36:39], s51 offen offset:2048 sc1
v_add_f32 v31, v31, v191
v_mfma_f32_16x16x32_bf16 a[52:55], v[216:219], v[52:55], a[52:55]
buffer_load_dwordx4 v14, s[16:19], s20 offen sc1 lds
v_mfma_f32_16x16x32_bf16 a[60:63], v[220:223], v[52:55], a[60:63]
ds_read_b128 v[212:215], v3 offset:13312
buffer_load_dwordx4 v[92:95], v2, s[36:39], s51 offen offset:3072 sc1
v_mfma_f32_16x16x32_bf16 a[56:59], v[220:223], v[48:51], a[56:59]
s_waitcnt lgkmcnt(4)
v_mfma_f32_16x16x32_bf16 a[64:67], v[192:195], v[48:51], a[64:67]
ds_read_b128 v[216:219], v3 offset:14336
buffer_load_dwordx4 v[96:99], v2, s[36:39], s52 offen offset:0 sc1
v_mfma_f32_16x16x32_bf16 a[68:71], v[192:195], v[52:55], a[68:71]
v_mfma_f32_16x16x32_bf16 a[76:79], v[196:199], v[52:55], a[76:79]
s_add_u32 s20, s20, s21
ds_read_b128 v[220:223], v3 offset:15360
buffer_load_dwordx4 v[100:103], v2, s[36:39], s52 offen offset:1024 sc1
v_mfma_f32_16x16x32_bf16 a[72:75], v[196:199], v[48:51], a[72:75]
s_waitcnt lgkmcnt(4)
v_mfma_f32_16x16x32_bf16 a[80:83], v[200:203], v[48:51], a[80:83]
s_add_u32 m0, s23, 4096
ds_read_b128 v[192:195], v3 offset:16384
buffer_load_dwordx4 v[104:107], v2, s[36:39], s52 offen offset:2048 sc1
v_mfma_f32_16x16x32_bf16 a[84:87], v[200:203], v[52:55], a[84:87]
buffer_load_dwordx4 v14, s[16:19], s20 offen sc1 lds
v_mfma_f32_16x16x32_bf16 a[92:95], v[204:207], v[52:55], a[92:95]
ds_read_b128 v[196:199], v3 offset:17408
buffer_load_dwordx4 v[108:111], v2, s[36:39], s52 offen offset:3072 sc1
v_mfma_f32_16x16x32_bf16 a[88:91], v[204:207], v[48:51], a[88:91]
s_waitcnt lgkmcnt(4)
v_mfma_f32_16x16x32_bf16 a[96:99], v[208:211], v[48:51], a[96:99]
ds_read_b128 v[200:203], v3 offset:18432
buffer_load_dwordx4 v[112:115], v2, s[36:39], s53 offen offset:0 sc1
v_mfma_f32_16x16x32_bf16 a[100:103], v[208:211], v[52:55], a[100:103]
v_mfma_f32_16x16x32_bf16 a[108:111], v[212:215], v[52:55], a[108:111]
s_add_u32 s20, s20, s21
ds_read_b128 v[204:207], v3 offset:19456
buffer_load_dwordx4 v[116:119], v2, s[36:39], s53 offen offset:1024 sc1
v_mfma_f32_16x16x32_bf16 a[104:107], v[212:215], v[48:51], a[104:107]
s_waitcnt lgkmcnt(4)
v_mfma_f32_16x16x32_bf16 a[112:115], v[216:219], v[48:51], a[112:115]
s_add_u32 m0, s23, 5120
ds_read_b128 v[208:211], v3 offset:20480
buffer_load_dwordx4 v[120:123], v2, s[36:39], s53 offen offset:2048 sc1
v_mfma_f32_16x16x32_bf16 a[116:119], v[216:219], v[52:55], a[116:119]
buffer_load_dwordx4 v14, s[16:19], s20 offen sc1 lds
v_mfma_f32_16x16x32_bf16 a[124:127], v[220:223], v[52:55], a[124:127]
ds_read_b128 v[212:215], v3 offset:21504
buffer_load_dwordx4 v[124:127], v2, s[36:39], s53 offen offset:3072 sc1
v_mfma_f32_16x16x32_bf16 a[120:123], v[220:223], v[48:51], a[120:123]
s_waitcnt lgkmcnt(4)
v_mfma_f32_16x16x32_bf16 a[0:3], v[192:195], v[56:59], a[0:3]
ds_read_b128 v[216:219], v3 offset:22528
buffer_load_dwordx4 v[224:227], v2, s[36:39], s54 offen sc1
v_mfma_f32_16x16x32_bf16 a[4:7], v[192:195], v[60:63], a[4:7]
v_mfma_f32_16x16x32_bf16 a[12:15], v[196:199], v[60:63], a[12:15]
s_add_u32 s20, s20, s21
ds_read_b128 v[220:223], v3 offset:23552
v_mfma_f32_16x16x32_bf16 a[8:11], v[196:199], v[56:59], a[8:11]
s_waitcnt lgkmcnt(4)
v_mfma_f32_16x16x32_bf16 a[16:19], v[200:203], v[56:59], a[16:19]
s_add_u32 m0, s23, 6144
ds_read_b128 v[192:195], v3 offset:24576
v_mfma_f32_16x16x32_bf16 a[20:23], v[200:203], v[60:63], a[20:23]
buffer_load_dwordx4 v14, s[16:19], s20 offen sc1 lds
v_mfma_f32_16x16x32_bf16 a[28:31], v[204:207], v[60:63], a[28:31]
s_add_u32 s26, s24, 32768
ds_read_b128 v[196:199], v3 offset:25600
v_mfma_f32_16x16x32_bf16 a[24:27], v[204:207], v[56:59], a[24:27]
s_waitcnt lgkmcnt(4)
v_mfma_f32_16x16x32_bf16 a[32:35], v[208:211], v[56:59], a[32:35]
s_sub_u32 s35, s26, 163840
ds_read_b128 v[200:203], v3 offset:26624
v_mfma_f32_16x16x32_bf16 a[36:39], v[208:211], v[60:63], a[36:39]
s_cselect_b32 s26, s26, s35
v_mfma_f32_16x16x32_bf16 a[44:47], v[212:215], v[60:63], a[44:47]
s_add_u32 s20, s20, s21
ds_read_b128 v[204:207], v3 offset:27648
v_mfma_f32_16x16x32_bf16 a[40:43], v[212:215], v[56:59], a[40:43]
s_waitcnt lgkmcnt(4)
v_add_u32 v8, s26, v4
v_mfma_f32_16x16x32_bf16 a[48:51], v[216:219], v[56:59], a[48:51]
s_add_u32 m0, s23, 7168
ds_read_b128 v[208:211], v3 offset:28672
v_mfma_f32_16x16x32_bf16 a[52:55], v[216:219], v[60:63], a[52:55]
buffer_load_dwordx4 v14, s[16:19], s20 offen sc1 lds
v_mfma_f32_16x16x32_bf16 a[60:63], v[220:223], v[60:63], a[60:63]
s_add_u32 s23, s27, s33
ds_read_b128 v[212:215], v3 offset:29696
v_readlane_b32 s16, v16, s22
v_mfma_f32_16x16x32_bf16 a[56:59], v[220:223], v[56:59], a[56:59]
s_waitcnt lgkmcnt(4)
v_readlane_b32 s17, v17, s22
v_mfma_f32_16x16x32_bf16 a[64:67], v[192:195], v[56:59], a[64:67]
s_waitcnt vmcnt(33)
ds_read_b128 v[216:219], v3 offset:30720
v_readlane_b32 s25, v18, s22
v_mfma_f32_16x16x32_bf16 a[68:71], v[192:195], v[60:63], a[68:71]
s_barrier
v_add_u32 v9, s26, v5
v_mfma_f32_16x16x32_bf16 a[76:79], v[196:199], v[60:63], a[76:79]
s_mov_b32 s20, 0
ds_read_b128 v[220:223], v3 offset:31744
v_mad_u32_u24 v14, v12, s25, v13
v_mfma_f32_16x16x32_bf16 a[72:75], v[196:199], v[56:59], a[72:75]
s_waitcnt lgkmcnt(4)
v_add_u32 v10, s26, v6
v_mfma_f32_16x16x32_bf16 a[80:83], v[200:203], v[56:59], a[80:83]
s_mov_b32 m0, s23
ds_read_b64_tr_b16 v[192:193], v8 offset:0
ds_read_b64_tr_b16 v[194:195], v8 offset:2048
v_mfma_f32_16x16x32_bf16 a[84:87], v[200:203], v[60:63], a[84:87]
buffer_load_dwordx4 v14, s[16:19], s20 offen sc1 lds
v_mfma_f32_16x16x32_bf16 a[92:95], v[204:207], v[60:63], a[92:95]
s_add_u32 s22, s22, 1
ds_read_b64_tr_b16 v[196:197], v8 offset:4096
v_mfma_f32_16x16x32_bf16 a[88:91], v[204:207], v[56:59], a[88:91]
s_waitcnt lgkmcnt(5)
ds_read_b64_tr_b16 v[198:199], v8 offset:6144
v_add_u32 v11, s26, v7
v_mfma_f32_16x16x32_bf16 a[96:99], v[208:211], v[56:59], a[96:99]
s_lshl_b32 s21, s25, 3
ds_read_b64_tr_b16 v[200:201], v8 offset:8192
v_mfma_f32_16x16x32_bf16 a[100:103], v[208:211], v[60:63], a[100:103]
s_mov_b32 s27, s24
ds_read_b64_tr_b16 v[202:203], v8 offset:10240
v_mfma_f32_16x16x32_bf16 a[108:111], v[212:215], v[60:63], a[108:111]
s_add_u32 s20, s20, s21
ds_read_b64_tr_b16 v[204:205], v8 offset:12288
v_mfma_f32_16x16x32_bf16 a[104:107], v[212:215], v[56:59], a[104:107]
s_waitcnt lgkmcnt(7)
ds_read_b64_tr_b16 v[206:207], v8 offset:14336
v_mfma_f32_16x16x32_bf16 a[112:115], v[216:219], v[56:59], a[112:115]
s_add_u32 m0, s23, 1024
ds_read_b64_tr_b16 v[208:209], v8 offset:16384
ds_read_b64_tr_b16 v[210:211], v8 offset:18432
v_mfma_f32_16x16x32_bf16 a[116:119], v[216:219], v[60:63], a[116:119]
buffer_load_dwordx4 v14, s[16:19], s20 offen sc1 lds
v_mfma_f32_16x16x32_bf16 a[124:127], v[220:223], v[60:63], a[124:127]
s_mov_b32 s24, s26
ds_read_b64_tr_b16 v[212:213], v8 offset:20480
v_mfma_f32_16x16x32_bf16 a[120:123], v[220:223], v[56:59], a[120:123]
s_waitcnt lgkmcnt(7)
ds_read_b64_tr_b16 v[214:215], v8 offset:22528
s_waitcnt vmcnt(4)
s_nop 7
s_setpc_b64 s[70:71]
VLASTR1_%=:
v_exp_f32 v160, v160
v_exp_f32 v161, v161
v_exp_f32 v162, v162
v_exp_f32 v163, v163
v_exp_f32 v168, v168
v_exp_f32 v169, v169
v_exp_f32 v170, v170
v_exp_f32 v171, v171
v_exp_f32 v164, v164
v_exp_f32 v165, v165
v_exp_f32 v166, v166
v_exp_f32 v167, v167
v_exp_f32 v172, v172
v_exp_f32 v173, v173
v_exp_f32 v174, v174
v_exp_f32 v175, v175
v_exp_f32 v176, v176
v_exp_f32 v177, v177
v_exp_f32 v178, v178
v_exp_f32 v179, v179
v_exp_f32 v184, v184
v_exp_f32 v185, v185
v_exp_f32 v186, v186
v_exp_f32 v187, v187
v_exp_f32 v180, v180
v_exp_f32 v181, v181
v_exp_f32 v182, v182
v_exp_f32 v183, v183
v_exp_f32 v188, v188
v_exp_f32 v189, v189
v_exp_f32 v190, v190
v_exp_f32 v191, v191
s_nop 0
v_add_f32 v30, v30, v160
v_cvt_pk_bf16_f32 v48, v160, v161
v_add_f32 v30, v30, v161
v_cvt_pk_bf16_f32 v49, v162, v163
v_add_f32 v30, v30, v162
v_cvt_pk_bf16_f32 v50, v168, v169
v_add_f32 v30, v30, v163
v_cvt_pk_bf16_f32 v51, v170, v171
v_add_f32 v30, v30, v168
v_cvt_pk_bf16_f32 v52, v164, v165
v_add_f32 v30, v30, v169
v_cvt_pk_bf16_f32 v53, v166, v167
v_add_f32 v30, v30, v170
v_cvt_pk_bf16_f32 v54, v172, v173
v_add_f32 v30, v30, v171
v_cvt_pk_bf16_f32 v55, v174, v175
v_add_f32 v31, v31, v164
v_cvt_pk_bf16_f32 v56, v176, v177
v_add_f32 v31, v31, v165
v_cvt_pk_bf16_f32 v57, v178, v179
v_add_f32 v31, v31, v166
v_cvt_pk_bf16_f32 v58, v184, v185
v_add_f32 v31, v31, v167
v_cvt_pk_bf16_f32 v59, v186, v187
v_add_f32 v31, v31, v172
v_cvt_pk_bf16_f32 v60, v180, v181
v_add_f32 v31, v31, v173
v_cvt_pk_bf16_f32 v61, v182, v183
v_add_f32 v31, v31, v174
v_cvt_pk_bf16_f32 v62, v188, v189
v_add_f32 v31, v31, v175
s_nop 1
v_mfma_f32_16x16x32_bf16 a[0:3], v[192:195], v[48:51], a[0:3]
ds_read_b128 v[216:219], v3 offset:6144
v_cvt_pk_bf16_f32 v63, v190, v191
v_mfma_f32_16x16x32_bf16 a[4:7], v[192:195], v[52:55], a[4:7]
v_add_f32 v30, v30, v176
v_add_f32 v30, v30, v177
v_mfma_f32_16x16x32_bf16 a[12:15], v[196:199], v[52:55], a[12:15]
s_add_u32 s20, s20, s21
ds_read_b128 v[220:223], v3 offset:7168
v_add_f32 v30, v30, v178
v_mfma_f32_16x16x32_bf16 a[8:11], v[196:199], v[48:51], a[8:11]
s_waitcnt lgkmcnt(4)
v_add_f32 v30, v30, v179
v_add_f32 v30, v30, v184
v_mfma_f32_16x16x32_bf16 a[16:19], v[200:203], v[48:51], a[16:19]
s_add_u32 m0, s23, 2048
ds_read_b128 v[192:195], v3 offset:8192
v_add_f32 v30, v30, v185
v_mfma_f32_16x16x32_bf16 a[20:23], v[200:203], v[52:55], a[20:23]
buffer_load_dwordx4 v14, s[16:19], s20 offen sc1 lds
v_mfma_f32_16x16x32_bf16 a[28:31], v[204:207], v[52:55], a[28:31]
ds_read_b128 v[196:199], v3 offset:9216
v_add_f32 v30, v30, v186
v_mfma_f32_16x16x32_bf16 a[24:27], v[204:207], v[48:51], a[24:27]
s_waitcnt lgkmcnt(4)
v_add_f32 v30, v30, v187
v_add_f32 v31, v31, v180
v_mfma_f32_16x16x32_bf16 a[32:35], v[208:211], v[48:51], a[32:35]
ds_read_b128 v[200:203], v3 offset:10240
v_add_f32 v31, v31, v181
v_mfma_f32_16x16x32_bf16 a[36:39], v[208:211], v[52:55], a[36:39]
v_add_f32 v31, v31, v182
v_add_f32 v31, v31, v183
v_mfma_f32_16x16x32_bf16 a[44:47], v[212:215], v[52:55], a[44:47]
s_add_u32 s20, s20, s21
ds_read_b128 v[204:207], v3 offset:11264
v_add_f32 v31, v31, v188
v_mfma_f32_16x16x32_bf16 a[40:43], v[212:215], v[48:51], a[40:43]
s_waitcnt lgkmcnt(4)
v_add_f32 v31, v31, v189
v_add_f32 v31, v31, v190
v_mfma_f32_16x16x32_bf16 a[48:51], v[216:219], v[48:51], a[48:51]
s_add_u32 m0, s23, 3072
ds_read_b128 v[208:211], v3 offset:12288
v_add_f32 v31, v31, v191
v_mfma_f32_16x16x32_bf16 a[52:55], v[216:219], v[52:55], a[52:55]
buffer_load_dwordx4 v14, s[16:19], s20 offen sc1 lds
v_mfma_f32_16x16x32_bf16 a[60:63], v[220:223], v[52:55], a[60:63]
ds_read_b128 v[212:215], v3 offset:13312
v_accvgpr_read_b32 v64, a160
v_mfma_f32_16x16x32_bf16 a[56:59], v[220:223], v[48:51], a[56:59]
s_waitcnt lgkmcnt(4)
v_accvgpr_read_b32 v65, a161
v_accvgpr_read_b32 v66, a162
v_mfma_f32_16x16x32_bf16 a[64:67], v[192:195], v[48:51], a[64:67]
ds_read_b128 v[216:219], v3 offset:14336
v_accvgpr_read_b32 v67, a163
v_mfma_f32_16x16x32_bf16 a[68:71], v[192:195], v[52:55], a[68:71]
v_accvgpr_read_b32 v68, a164
v_accvgpr_read_b32 v69, a165
v_mfma_f32_16x16x32_bf16 a[76:79], v[196:199], v[52:55], a[76:79]
s_add_u32 s20, s20, s21
ds_read_b128 v[220:223], v3 offset:15360
v_accvgpr_read_b32 v70, a166
v_mfma_f32_16x16x32_bf16 a[72:75], v[196:199], v[48:51], a[72:75]
s_waitcnt lgkmcnt(4)
v_accvgpr_read_b32 v71, a167
v_accvgpr_read_b32 v72, a168
v_mfma_f32_16x16x32_bf16 a[80:83], v[200:203], v[48:51], a[80:83]
s_add_u32 m0, s23, 4096
ds_read_b128 v[192:195], v3 offset:16384
v_accvgpr_read_b32 v73, a169
v_mfma_f32_16x16x32_bf16 a[84:87], v[200:203], v[52:55], a[84:87]
buffer_load_dwordx4 v14, s[16:19], s20 offen sc1 lds
v_mfma_f32_16x16x32_bf16 a[92:95], v[204:207], v[52:55], a[92:95]
ds_read_b128 v[196:199], v3 offset:17408
v_accvgpr_read_b32 v74, a170
v_mfma_f32_16x16x32_bf16 a[88:91], v[204:207], v[48:51], a[88:91]
s_waitcnt lgkmcnt(4)
v_accvgpr_read_b32 v75, a171
v_accvgpr_read_b32 v76, a172
v_mfma_f32_16x16x32_bf16 a[96:99], v[208:211], v[48:51], a[96:99]
ds_read_b128 v[200:203], v3 offset:18432
v_accvgpr_read_b32 v77, a173
v_mfma_f32_16x16x32_bf16 a[100:103], v[208:211], v[52:55], a[100:103]
v_accvgpr_read_b32 v78, a174
v_accvgpr_read_b32 v79, a175
v_mfma_f32_16x16x32_bf16 a[108:111], v[212:215], v[52:55], a[108:111]
s_add_u32 s20, s20, s21
ds_read_b128 v[204:207], v3 offset:19456
v_accvgpr_read_b32 v80, a176
v_mfma_f32_16x16x32_bf16 a[104:107], v[212:215], v[48:51], a[104:107]
s_waitcnt lgkmcnt(4)
v_accvgpr_read_b32 v81, a177
v_accvgpr_read_b32 v82, a178
v_mfma_f32_16x16x32_bf16 a[112:115], v[216:219], v[48:51], a[112:115]
s_add_u32 m0, s23, 5120
ds_read_b128 v[208:211], v3 offset:20480
v_accvgpr_read_b32 v83, a179
v_mfma_f32_16x16x32_bf16 a[116:119], v[216:219], v[52:55], a[116:119]
buffer_load_dwordx4 v14, s[16:19], s20 offen sc1 lds
v_mfma_f32_16x16x32_bf16 a[124:127], v[220:223], v[52:55], a[124:127]
ds_read_b128 v[212:215], v3 offset:21504
v_accvgpr_read_b32 v84, a180
v_mfma_f32_16x16x32_bf16 a[120:123], v[220:223], v[48:51], a[120:123]
s_waitcnt lgkmcnt(4)
v_accvgpr_read_b32 v85, a181
v_accvgpr_read_b32 v86, a182
v_mfma_f32_16x16x32_bf16 a[0:3], v[192:195], v[56:59], a[0:3]
ds_read_b128 v[216:219], v3 offset:22528
v_accvgpr_read_b32 v87, a183
v_mfma_f32_16x16x32_bf16 a[4:7], v[192:195], v[60:63], a[4:7]
v_accvgpr_read_b32 v88, a184
v_accvgpr_read_b32 v89, a185
v_mfma_f32_16x16x32_bf16 a[12:15], v[196:199], v[60:63], a[12:15]
s_add_u32 s20, s20, s21
ds_read_b128 v[220:223], v3 offset:23552
v_accvgpr_read_b32 v90, a186
v_mfma_f32_16x16x32_bf16 a[8:11], v[196:199], v[56:59], a[8:11]
s_waitcnt lgkmcnt(4)
v_accvgpr_read_b32 v91, a187
v_accvgpr_read_b32 v92, a188
v_mfma_f32_16x16x32_bf16 a[16:19], v[200:203], v[56:59], a[16:19]
s_add_u32 m0, s23, 6144
ds_read_b128 v[192:195], v3 offset:24576
v_accvgpr_read_b32 v93, a189
v_mfma_f32_16x16x32_bf16 a[20:23], v[200:203], v[60:63], a[20:23]
buffer_load_dwordx4 v14, s[16:19], s20 offen sc1 lds
v_mfma_f32_16x16x32_bf16 a[28:31], v[204:207], v[60:63], a[28:31]
s_add_u32 s26, s24, 32768
ds_read_b128 v[196:199], v3 offset:25600
v_accvgpr_read_b32 v94, a190
v_mfma_f32_16x16x32_bf16 a[24:27], v[204:207], v[56:59], a[24:27]
s_waitcnt lgkmcnt(4)
v_accvgpr_read_b32 v95, a191
v_accvgpr_read_b32 v96, a192
v_mfma_f32_16x16x32_bf16 a[32:35], v[208:211], v[56:59], a[32:35]
s_sub_u32 s35, s26, 163840
ds_read_b128 v[200:203], v3 offset:26624
v_accvgpr_read_b32 v97, a193
v_mfma_f32_16x16x32_bf16 a[36:39], v[208:211], v[60:63], a[36:39]
s_cselect_b32 s26, s26, s35
v_accvgpr_read_b32 v98, a194
v_accvgpr_read_b32 v99, a195
v_mfma_f32_16x16x32_bf16 a[44:47], v[212:215], v[60:63], a[44:47]
s_add_u32 s20, s20, s21
ds_read_b128 v[204:207], v3 offset:27648
v_accvgpr_read_b32 v100, a196
v_mfma_f32_16x16x32_bf16 a[40:43], v[212:215], v[56:59], a[40:43]
s_waitcnt lgkmcnt(4)
v_accvgpr_read_b32 v101, a197
v_accvgpr_read_b32 v102, a198
v_mfma_f32_16x16x32_bf16 a[48:51], v[216:219], v[56:59], a[48:51]
s_add_u32 m0, s23, 7168
ds_read_b128 v[208:211], v3 offset:28672
v_accvgpr_read_b32 v103, a199
v_mfma_f32_16x16x32_bf16 a[52:55], v[216:219], v[60:63], a[52:55]
buffer_load_dwordx4 v14, s[16:19], s20 offen sc1 lds
v_mfma_f32_16x16x32_bf16 a[60:63], v[220:223], v[60:63], a[60:63]
s_add_u32 s23, s27, s33
ds_read_b128 v[212:215], v3 offset:29696
v_readlane_b32 s16, v16, s22
v_mfma_f32_16x16x32_bf16 a[56:59], v[220:223], v[56:59], a[56:59]
s_waitcnt lgkmcnt(4)
v_readlane_b32 s17, v17, s22
v_accvgpr_read_b32 v104, a200
v_mfma_f32_16x16x32_bf16 a[64:67], v[192:195], v[56:59], a[64:67]
s_waitcnt vmcnt(16)
ds_read_b128 v[216:219], v3 offset:30720
v_readlane_b32 s25, v18, s22
v_mfma_f32_16x16x32_bf16 a[68:71], v[192:195], v[60:63], a[68:71]
s_barrier
v_accvgpr_read_b32 v105, a201
v_accvgpr_read_b32 v106, a202
v_mfma_f32_16x16x32_bf16 a[76:79], v[196:199], v[60:63], a[76:79]
s_mov_b32 s20, 0
ds_read_b128 v[220:223], v3 offset:31744
v_mad_u32_u24 v14, v12, s25, v13
v_mfma_f32_16x16x32_bf16 a[72:75], v[196:199], v[56:59], a[72:75]
s_waitcnt lgkmcnt(4)
v_add_u32 v3, s26, v2
v_accvgpr_read_b32 v107, a203
v_mfma_f32_16x16x32_bf16 a[80:83], v[200:203], v[56:59], a[80:83]
s_mov_b32 m0, s23
ds_read_b128 v[192:195], v3 offset:0
v_accvgpr_read_b32 v108, a204
v_mfma_f32_16x16x32_bf16 a[84:87], v[200:203], v[60:63], a[84:87]
buffer_load_dwordx4 v14, s[16:19], s20 offen sc1 lds
v_mfma_f32_16x16x32_bf16 a[92:95], v[204:207], v[60:63], a[92:95]
s_add_u32 s22, s22, 1
ds_read_b128 v[196:199], v3 offset:1024
v_accvgpr_read_b32 v109, a205
v_mfma_f32_16x16x32_bf16 a[88:91], v[204:207], v[56:59], a[88:91]
s_waitcnt lgkmcnt(4)
v_accvgpr_read_b32 v110, a206
v_accvgpr_read_b32 v111, a207
v_mfma_f32_16x16x32_bf16 a[96:99], v[208:211], v[56:59], a[96:99]
s_lshl_b32 s21, s25, 3
ds_read_b128 v[200:203], v3 offset:2048
v_accvgpr_read_b32 v112, a208
v_mfma_f32_16x16x32_bf16 a[100:103], v[208:211], v[60:63], a[100:103]
s_mov_b32 s27, s24
v_accvgpr_read_b32 v113, a209
v_accvgpr_read_b32 v114, a210
v_mfma_f32_16x16x32_bf16 a[108:111], v[212:215], v[60:63], a[108:111]
s_add_u32 s20, s20, s21
ds_read_b128 v[204:207], v3 offset:3072
v_accvgpr_read_b32 v115, a211
v_mfma_f32_16x16x32_bf16 a[104:107], v[212:215], v[56:59], a[104:107]
s_waitcnt lgkmcnt(4)
v_accvgpr_read_b32 v116, a212
v_accvgpr_read_b32 v117, a213
v_mfma_f32_16x16x32_bf16 a[112:115], v[216:219], v[56:59], a[112:115]
s_add_u32 m0, s23, 1024
ds_read_b128 v[208:211], v3 offset:4096
v_accvgpr_read_b32 v118, a214
v_mfma_f32_16x16x32_bf16 a[116:119], v[216:219], v[60:63], a[116:119]
buffer_load_dwordx4 v14, s[16:19], s20 offen sc1 lds
v_mfma_f32_16x16x32_bf16 a[124:127], v[220:223], v[60:63], a[124:127]
s_mov_b32 s24, s26
ds_read_b128 v[212:215], v3 offset:5120
v_accvgpr_read_b32 v119, a215
v_mfma_f32_16x16x32_bf16 a[120:123], v[220:223], v[56:59], a[120:123]
s_waitcnt lgkmcnt(4)
v_accvgpr_read_b32 v120, a216
v_accvgpr_read_b32 v121, a217
v_accvgpr_read_b32 v122, a218
v_accvgpr_read_b32 v123, a219
v_accvgpr_read_b32 v124, a220
v_accvgpr_read_b32 v125, a221
v_accvgpr_read_b32 v126, a222
v_accvgpr_read_b32 v127, a223
s_nop 7
s_setpc_b64 s[70:71]
MASK0_%=:
s_nop 3
s_sub_u32 s35, s46, s45
s_add_u32 s35, s35, 63
v_sub_u32 v33, s35, v19
v_mov_b32 v34, 0xff800000
v_cmp_ge_i32_e64 s[48:49], v33, 0
v_cmp_ge_i32_e64 s[50:51], v33, 1
v_cmp_ge_i32_e64 s[52:53], v33, 2
v_cmp_ge_i32_e64 s[54:55], v33, 3
v_cndmask_b32_e64 v160, v34, v160, s[48:49]
v_cndmask_b32_e64 v161, v34, v161, s[50:51]
v_cndmask_b32_e64 v162, v34, v162, s[52:53]
v_cndmask_b32_e64 v163, v34, v163, s[54:55]
v_cmp_ge_i32_e64 s[48:49], v33, -16
v_cmp_ge_i32_e64 s[50:51], v33, -15
v_cmp_ge_i32_e64 s[52:53], v33, -14
v_cmp_ge_i32_e64 s[54:55], v33, -13
v_cndmask_b32_e64 v164, v34, v164, s[48:49]
v_cndmask_b32_e64 v165, v34, v165, s[50:51]
v_cndmask_b32_e64 v166, v34, v166, s[52:53]
v_cndmask_b32_e64 v167, v34, v167, s[54:55]
v_cmp_ge_i32_e64 s[48:49], v33, 16
v_cmp_ge_i32_e64 s[50:51], v33, 17
v_cmp_ge_i32_e64 s[52:53], v33, 18
v_cmp_ge_i32_e64 s[54:55], v33, 19
v_cndmask_b32_e64 v168, v34, v168, s[48:49]
v_cndmask_b32_e64 v169, v34, v169, s[50:51]
v_cndmask_b32_e64 v170, v34, v170, s[52:53]
v_cndmask_b32_e64 v171, v34, v171, s[54:55]
v_cmp_ge_i32_e64 s[48:49], v33, 0
v_cmp_ge_i32_e64 s[50:51], v33, 1
v_cmp_ge_i32_e64 s[52:53], v33, 2
v_cmp_ge_i32_e64 s[54:55], v33, 3
v_cndmask_b32_e64 v172, v34, v172, s[48:49]
v_cndmask_b32_e64 v173, v34, v173, s[50:51]
v_cndmask_b32_e64 v174, v34, v174, s[52:53]
v_cndmask_b32_e64 v175, v34, v175, s[54:55]
v_cmp_ge_i32_e64 s[48:49], v33, 32
v_cmp_ge_i32_e64 s[50:51], v33, 33
v_cmp_ge_i32_e64 s[52:53], v33, 34
v_cmp_ge_i32_e64 s[54:55], v33, 35
v_cndmask_b32_e64 v176, v34, v176, s[48:49]
v_cndmask_b32_e64 v177, v34, v177, s[50:51]
v_cndmask_b32_e64 v178, v34, v178, s[52:53]
v_cndmask_b32_e64 v179, v34, v179, s[54:55]
v_cmp_ge_i32_e64 s[48:49], v33, 16
v_cmp_ge_i32_e64 s[50:51], v33, 17
v_cmp_ge_i32_e64 s[52:53], v33, 18
v_cmp_ge_i32_e64 s[54:55], v33, 19
v_cndmask_b32_e64 v180, v34, v180, s[48:49]
v_cndmask_b32_e64 v181, v34, v181, s[50:51]
v_cndmask_b32_e64 v182, v34, v182, s[52:53]
v_cndmask_b32_e64 v183, v34, v183, s[54:55]
v_cmp_ge_i32_e64 s[48:49], v33, 48
v_cmp_ge_i32_e64 s[50:51], v33, 49
v_cmp_ge_i32_e64 s[52:53], v33, 50
v_cmp_ge_i32_e64 s[54:55], v33, 51
v_cndmask_b32_e64 v184, v34, v184, s[48:49]
v_cndmask_b32_e64 v185, v34, v185, s[50:51]
v_cndmask_b32_e64 v186, v34, v186, s[52:53]
v_cndmask_b32_e64 v187, v34, v187, s[54:55]
v_cmp_ge_i32_e64 s[48:49], v33, 32
v_cmp_ge_i32_e64 s[50:51], v33, 33
v_cmp_ge_i32_e64 s[52:53], v33, 34
v_cmp_ge_i32_e64 s[54:55], v33, 35
v_cndmask_b32_e64 v188, v34, v188, s[48:49]
v_cndmask_b32_e64 v189, v34, v189, s[50:51]
v_cndmask_b32_e64 v190, v34, v190, s[52:53]
v_cndmask_b32_e64 v191, v34, v191, s[54:55]
v_max3_f32 v32, v160, v161, v162
v_max3_f32 v32, v32, v163, v164
v_max3_f32 v32, v32, v165, v166
v_max3_f32 v32, v32, v167, v168
v_max3_f32 v32, v32, v169, v170
v_max3_f32 v32, v32, v171, v172
v_max3_f32 v32, v32, v173, v174
v_max3_f32 v32, v32, v175, v176
v_max3_f32 v32, v32, v177, v178
v_max3_f32 v32, v32, v179, v180
v_max3_f32 v32, v32, v181, v182
v_max3_f32 v32, v32, v183, v184
v_max3_f32 v32, v32, v185, v186
v_max3_f32 v32, v32, v187, v188
v_max3_f32 v32, v32, v189, v190
v_max_f32 v32, v32, v191
v_cmp_lt_f32 vcc, 0x41000000, v32
s_nop 4
s_branch MRET0_%=
RARE0_%=:
s_nop 7
s_nop 7
v_max3_f32 v35, v160, v161, v162
v_max3_f32 v35, v35, v163, v168
v_max3_f32 v35, v35, v169, v170
v_max3_f32 v35, v35, v171, v176
v_max3_f32 v35, v35, v177, v178
v_max3_f32 v35, v35, v179, v184
v_max3_f32 v35, v35, v185, v186
v_max_f32 v35, v35, v187
v_mov_b32 v36, v35
s_nop 1
v_permlane16_swap_b32 v35, v36
s_nop 1
v_max_f32 v35, v35, v36
v_mov_b32 v36, v35
s_nop 1
v_permlane32_swap_b32 v35, v36
s_nop 1
v_max_f32 v35, v35, v36
v_max_f32 v35, 0, v35
v_add_f32 v28, v28, v35
v_sub_f32 v160, v160, v35
v_sub_f32 v161, v161, v35
v_sub_f32 v162, v162, v35
v_sub_f32 v163, v163, v35
v_sub_f32 v168, v168, v35
v_sub_f32 v169, v169, v35
v_sub_f32 v170, v170, v35
v_sub_f32 v171, v171, v35
v_sub_f32 v176, v176, v35
v_sub_f32 v177, v177, v35
v_sub_f32 v178, v178, v35
v_sub_f32 v179, v179, v35
v_sub_f32 v184, v184, v35
v_sub_f32 v185, v185, v35
v_sub_f32 v186, v186, v35
v_sub_f32 v187, v187, v35
v_sub_f32 v20, 0, v28
v_sub_f32 v21, 0, v28
v_sub_f32 v22, 0, v28
v_sub_f32 v23, 0, v28
v_sub_f32 v36, 0, v35
v_exp_f32 v36, v36
s_nop 0
v_mul_f32 v30, v30, v36
v_accvgpr_read_b32 v224, a0
v_accvgpr_read_b32 v225, a1
v_accvgpr_read_b32 v226, a2
v_accvgpr_read_b32 v227, a3
v_mul_f32 v224, v224, v36
v_mul_f32 v225, v225, v36
v_mul_f32 v226, v226, v36
v_mul_f32 v227, v227, v36
v_accvgpr_write_b32 a0, v224
v_accvgpr_write_b32 a1, v225
v_accvgpr_write_b32 a2, v226
v_accvgpr_write_b32 a3, v227
v_accvgpr_read_b32 v224, a8
v_accvgpr_read_b32 v225, a9
v_accvgpr_read_b32 v226, a10
v_accvgpr_read_b32 v227, a11
v_mul_f32 v224, v224, v36
v_mul_f32 v225, v225, v36
v_mul_f32 v226, v226, v36
v_mul_f32 v227, v227, v36
v_accvgpr_write_b32 a8, v224
v_accvgpr_write_b32 a9, v225
v_accvgpr_write_b32 a10, v226
v_accvgpr_write_b32 a11, v227
v_accvgpr_read_b32 v224, a16
v_accvgpr_read_b32 v225, a17
v_accvgpr_read_b32 v226, a18
v_accvgpr_read_b32 v227, a19
v_mul_f32 v224, v224, v36
v_mul_f32 v225, v225, v36
v_mul_f32 v226, v226, v36
v_mul_f32 v227, v227, v36
v_accvgpr_write_b32 a16, v224
v_accvgpr_write_b32 a17, v225
v_accvgpr_write_b32 a18, v226
v_accvgpr_write_b32 a19, v227
v_accvgpr_read_b32 v224, a24
v_accvgpr_read_b32 v225, a25
v_accvgpr_read_b32 v226, a26
v_accvgpr_read_b32 v227, a27
v_mul_f32 v224, v224, v36
v_mul_f32 v225, v225, v36
v_mul_f32 v226, v226, v36
v_mul_f32 v227, v227, v36
v_accvgpr_write_b32 a24, v224
v_accvgpr_write_b32 a25, v225
v_accvgpr_write_b32 a26, v226
v_accvgpr_write_b32 a27, v227
v_accvgpr_read_b32 v224, a32
v_accvgpr_read_b32 v225, a33
v_accvgpr_read_b32 v226, a34
v_accvgpr_read_b32 v227, a35
v_mul_f32 v224, v224, v36
v_mul_f32 v225, v225, v36
v_mul_f32 v226, v226, v36
v_mul_f32 v227, v227, v36
v_accvgpr_write_b32 a32, v224
v_accvgpr_write_b32 a33, v225
v_accvgpr_write_b32 a34, v226
v_accvgpr_write_b32 a35, v227
v_accvgpr_read_b32 v224, a40
v_accvgpr_read_b32 v225, a41
v_accvgpr_read_b32 v226, a42
v_accvgpr_read_b32 v227, a43
v_mul_f32 v224, v224, v36
v_mul_f32 v225, v225, v36
v_mul_f32 v226, v226, v36
v_mul_f32 v227, v227, v36
v_accvgpr_write_b32 a40, v224
v_accvgpr_write_b32 a41, v225
v_accvgpr_write_b32 a42, v226
v_accvgpr_write_b32 a43, v227
v_accvgpr_read_b32 v224, a48
v_accvgpr_read_b32 v225, a49
v_accvgpr_read_b32 v226, a50
v_accvgpr_read_b32 v227, a51
v_mul_f32 v224, v224, v36
v_mul_f32 v225, v225, v36
v_mul_f32 v226, v226, v36
v_mul_f32 v227, v227, v36
v_accvgpr_write_b32 a48, v224
v_accvgpr_write_b32 a49, v225
v_accvgpr_write_b32 a50, v226
v_accvgpr_write_b32 a51, v227
v_accvgpr_read_b32 v224, a56
v_accvgpr_read_b32 v225, a57
v_accvgpr_read_b32 v226, a58
v_accvgpr_read_b32 v227, a59
v_mul_f32 v224, v224, v36
v_mul_f32 v225, v225, v36
v_mul_f32 v226, v226, v36
v_mul_f32 v227, v227, v36
v_accvgpr_write_b32 a56, v224
v_accvgpr_write_b32 a57, v225
v_accvgpr_write_b32 a58, v226
v_accvgpr_write_b32 a59, v227
v_accvgpr_read_b32 v224, a64
v_accvgpr_read_b32 v225, a65
v_accvgpr_read_b32 v226, a66
v_accvgpr_read_b32 v227, a67
v_mul_f32 v224, v224, v36
v_mul_f32 v225, v225, v36
v_mul_f32 v226, v226, v36
v_mul_f32 v227, v227, v36
v_accvgpr_write_b32 a64, v224
v_accvgpr_write_b32 a65, v225
v_accvgpr_write_b32 a66, v226
v_accvgpr_write_b32 a67, v227
v_accvgpr_read_b32 v224, a72
v_accvgpr_read_b32 v225, a73
v_accvgpr_read_b32 v226, a74
v_accvgpr_read_b32 v227, a75
v_mul_f32 v224, v224, v36
v_mul_f32 v225, v225, v36
v_mul_f32 v226, v226, v36
v_mul_f32 v227, v227, v36
v_accvgpr_write_b32 a72, v224
v_accvgpr_write_b32 a73, v225
v_accvgpr_write_b32 a74, v226
v_accvgpr_write_b32 a75, v227
v_accvgpr_read_b32 v224, a80
v_accvgpr_read_b32 v225, a81
v_accvgpr_read_b32 v226, a82
v_accvgpr_read_b32 v227, a83
v_mul_f32 v224, v224, v36
v_mul_f32 v225, v225, v36
v_mul_f32 v226, v226, v36
v_mul_f32 v227, v227, v36
v_accvgpr_write_b32 a80, v224
v_accvgpr_write_b32 a81, v225
v_accvgpr_write_b32 a82, v226
v_accvgpr_write_b32 a83, v227
v_accvgpr_read_b32 v224, a88
v_accvgpr_read_b32 v225, a89
v_accvgpr_read_b32 v226, a90
v_accvgpr_read_b32 v227, a91
v_mul_f32 v224, v224, v36
v_mul_f32 v225, v225, v36
v_mul_f32 v226, v226, v36
v_mul_f32 v227, v227, v36
v_accvgpr_write_b32 a88, v224
v_accvgpr_write_b32 a89, v225
v_accvgpr_write_b32 a90, v226
v_accvgpr_write_b32 a91, v227
v_accvgpr_read_b32 v224, a96
v_accvgpr_read_b32 v225, a97
v_accvgpr_read_b32 v226, a98
v_accvgpr_read_b32 v227, a99
v_mul_f32 v224, v224, v36
v_mul_f32 v225, v225, v36
v_mul_f32 v226, v226, v36
v_mul_f32 v227, v227, v36
v_accvgpr_write_b32 a96, v224
v_accvgpr_write_b32 a97, v225
v_accvgpr_write_b32 a98, v226
v_accvgpr_write_b32 a99, v227
v_accvgpr_read_b32 v224, a104
v_accvgpr_read_b32 v225, a105
v_accvgpr_read_b32 v226, a106
v_accvgpr_read_b32 v227, a107
v_mul_f32 v224, v224, v36
v_mul_f32 v225, v225, v36
v_mul_f32 v226, v226, v36
v_mul_f32 v227, v227, v36
v_accvgpr_write_b32 a104, v224
v_accvgpr_write_b32 a105, v225
v_accvgpr_write_b32 a106, v226
v_accvgpr_write_b32 a107, v227
v_accvgpr_read_b32 v224, a112
v_accvgpr_read_b32 v225, a113
v_accvgpr_read_b32 v226, a114
v_accvgpr_read_b32 v227, a115
v_mul_f32 v224, v224, v36
v_mul_f32 v225, v225, v36
v_mul_f32 v226, v226, v36
v_mul_f32 v227, v227, v36
v_accvgpr_write_b32 a112, v224
v_accvgpr_write_b32 a113, v225
v_accvgpr_write_b32 a114, v226
v_accvgpr_write_b32 a115, v227
v_accvgpr_read_b32 v224, a120
v_accvgpr_read_b32 v225, a121
v_accvgpr_read_b32 v226, a122
v_accvgpr_read_b32 v227, a123
v_mul_f32 v224, v224, v36
v_mul_f32 v225, v225, v36
v_mul_f32 v226, v226, v36
v_mul_f32 v227, v227, v36
v_accvgpr_write_b32 a120, v224
v_accvgpr_write_b32 a121, v225
v_accvgpr_write_b32 a122, v226
v_accvgpr_write_b32 a123, v227
v_max3_f32 v39, v164, v165, v166
v_max3_f32 v39, v39, v167, v172
v_max3_f32 v39, v39, v173, v174
v_max3_f32 v39, v39, v175, v180
v_max3_f32 v39, v39, v181, v182
v_max3_f32 v39, v39, v183, v188
v_max3_f32 v39, v39, v189, v190
v_max_f32 v39, v39, v191
v_mov_b32 v40, v39
s_nop 1
v_permlane16_swap_b32 v39, v40
s_nop 1
v_max_f32 v39, v39, v40
v_mov_b32 v40, v39
s_nop 1
v_permlane32_swap_b32 v39, v40
s_nop 1
v_max_f32 v39, v39, v40
v_max_f32 v39, 0, v39
v_add_f32 v29, v29, v39
v_sub_f32 v164, v164, v39
v_sub_f32 v165, v165, v39
v_sub_f32 v166, v166, v39
v_sub_f32 v167, v167, v39
v_sub_f32 v172, v172, v39
v_sub_f32 v173, v173, v39
v_sub_f32 v174, v174, v39
v_sub_f32 v175, v175, v39
v_sub_f32 v180, v180, v39
v_sub_f32 v181, v181, v39
v_sub_f32 v182, v182, v39
v_sub_f32 v183, v183, v39
v_sub_f32 v188, v188, v39
v_sub_f32 v189, v189, v39
v_sub_f32 v190, v190, v39
v_sub_f32 v191, v191, v39
v_sub_f32 v24, 0, v29
v_sub_f32 v25, 0, v29
v_sub_f32 v26, 0, v29
v_sub_f32 v27, 0, v29
v_sub_f32 v40, 0, v39
v_exp_f32 v40, v40
s_nop 0
v_mul_f32 v31, v31, v40
v_accvgpr_read_b32 v224, a4
v_accvgpr_read_b32 v225, a5
v_accvgpr_read_b32 v226, a6
v_accvgpr_read_b32 v227, a7
v_mul_f32 v224, v224, v40
v_mul_f32 v225, v225, v40
v_mul_f32 v226, v226, v40
v_mul_f32 v227, v227, v40
v_accvgpr_write_b32 a4, v224
v_accvgpr_write_b32 a5, v225
v_accvgpr_write_b32 a6, v226
v_accvgpr_write_b32 a7, v227
v_accvgpr_read_b32 v224, a12
v_accvgpr_read_b32 v225, a13
v_accvgpr_read_b32 v226, a14
v_accvgpr_read_b32 v227, a15
v_mul_f32 v224, v224, v40
v_mul_f32 v225, v225, v40
v_mul_f32 v226, v226, v40
v_mul_f32 v227, v227, v40
v_accvgpr_write_b32 a12, v224
v_accvgpr_write_b32 a13, v225
v_accvgpr_write_b32 a14, v226
v_accvgpr_write_b32 a15, v227
v_accvgpr_read_b32 v224, a20
v_accvgpr_read_b32 v225, a21
v_accvgpr_read_b32 v226, a22
v_accvgpr_read_b32 v227, a23
v_mul_f32 v224, v224, v40
v_mul_f32 v225, v225, v40
v_mul_f32 v226, v226, v40
v_mul_f32 v227, v227, v40
v_accvgpr_write_b32 a20, v224
v_accvgpr_write_b32 a21, v225
v_accvgpr_write_b32 a22, v226
v_accvgpr_write_b32 a23, v227
v_accvgpr_read_b32 v224, a28
v_accvgpr_read_b32 v225, a29
v_accvgpr_read_b32 v226, a30
v_accvgpr_read_b32 v227, a31
v_mul_f32 v224, v224, v40
v_mul_f32 v225, v225, v40
v_mul_f32 v226, v226, v40
v_mul_f32 v227, v227, v40
v_accvgpr_write_b32 a28, v224
v_accvgpr_write_b32 a29, v225
v_accvgpr_write_b32 a30, v226
v_accvgpr_write_b32 a31, v227
v_accvgpr_read_b32 v224, a36
v_accvgpr_read_b32 v225, a37
v_accvgpr_read_b32 v226, a38
v_accvgpr_read_b32 v227, a39
v_mul_f32 v224, v224, v40
v_mul_f32 v225, v225, v40
v_mul_f32 v226, v226, v40
v_mul_f32 v227, v227, v40
v_accvgpr_write_b32 a36, v224
v_accvgpr_write_b32 a37, v225
v_accvgpr_write_b32 a38, v226
v_accvgpr_write_b32 a39, v227
v_accvgpr_read_b32 v224, a44
v_accvgpr_read_b32 v225, a45
v_accvgpr_read_b32 v226, a46
v_accvgpr_read_b32 v227, a47
v_mul_f32 v224, v224, v40
v_mul_f32 v225, v225, v40
v_mul_f32 v226, v226, v40
v_mul_f32 v227, v227, v40
v_accvgpr_write_b32 a44, v224
v_accvgpr_write_b32 a45, v225
v_accvgpr_write_b32 a46, v226
v_accvgpr_write_b32 a47, v227
v_accvgpr_read_b32 v224, a52
v_accvgpr_read_b32 v225, a53
v_accvgpr_read_b32 v226, a54
v_accvgpr_read_b32 v227, a55
v_mul_f32 v224, v224, v40
v_mul_f32 v225, v225, v40
v_mul_f32 v226, v226, v40
v_mul_f32 v227, v227, v40
v_accvgpr_write_b32 a52, v224
v_accvgpr_write_b32 a53, v225
v_accvgpr_write_b32 a54, v226
v_accvgpr_write_b32 a55, v227
v_accvgpr_read_b32 v224, a60
v_accvgpr_read_b32 v225, a61
v_accvgpr_read_b32 v226, a62
v_accvgpr_read_b32 v227, a63
v_mul_f32 v224, v224, v40
v_mul_f32 v225, v225, v40
v_mul_f32 v226, v226, v40
v_mul_f32 v227, v227, v40
v_accvgpr_write_b32 a60, v224
v_accvgpr_write_b32 a61, v225
v_accvgpr_write_b32 a62, v226
v_accvgpr_write_b32 a63, v227
v_accvgpr_read_b32 v224, a68
v_accvgpr_read_b32 v225, a69
v_accvgpr_read_b32 v226, a70
v_accvgpr_read_b32 v227, a71
v_mul_f32 v224, v224, v40
v_mul_f32 v225, v225, v40
v_mul_f32 v226, v226, v40
v_mul_f32 v227, v227, v40
v_accvgpr_write_b32 a68, v224
v_accvgpr_write_b32 a69, v225
v_accvgpr_write_b32 a70, v226
v_accvgpr_write_b32 a71, v227
v_accvgpr_read_b32 v224, a76
v_accvgpr_read_b32 v225, a77
v_accvgpr_read_b32 v226, a78
v_accvgpr_read_b32 v227, a79
v_mul_f32 v224, v224, v40
v_mul_f32 v225, v225, v40
v_mul_f32 v226, v226, v40
v_mul_f32 v227, v227, v40
v_accvgpr_write_b32 a76, v224
v_accvgpr_write_b32 a77, v225
v_accvgpr_write_b32 a78, v226
v_accvgpr_write_b32 a79, v227
v_accvgpr_read_b32 v224, a84
v_accvgpr_read_b32 v225, a85
v_accvgpr_read_b32 v226, a86
v_accvgpr_read_b32 v227, a87
v_mul_f32 v224, v224, v40
v_mul_f32 v225, v225, v40
v_mul_f32 v226, v226, v40
v_mul_f32 v227, v227, v40
v_accvgpr_write_b32 a84, v224
v_accvgpr_write_b32 a85, v225
v_accvgpr_write_b32 a86, v226
v_accvgpr_write_b32 a87, v227
v_accvgpr_read_b32 v224, a92
v_accvgpr_read_b32 v225, a93
v_accvgpr_read_b32 v226, a94
v_accvgpr_read_b32 v227, a95
v_mul_f32 v224, v224, v40
v_mul_f32 v225, v225, v40
v_mul_f32 v226, v226, v40
v_mul_f32 v227, v227, v40
v_accvgpr_write_b32 a92, v224
v_accvgpr_write_b32 a93, v225
v_accvgpr_write_b32 a94, v226
v_accvgpr_write_b32 a95, v227
v_accvgpr_read_b32 v224, a100
v_accvgpr_read_b32 v225, a101
v_accvgpr_read_b32 v226, a102
v_accvgpr_read_b32 v227, a103
v_mul_f32 v224, v224, v40
v_mul_f32 v225, v225, v40
v_mul_f32 v226, v226, v40
v_mul_f32 v227, v227, v40
v_accvgpr_write_b32 a100, v224
v_accvgpr_write_b32 a101, v225
v_accvgpr_write_b32 a102, v226
v_accvgpr_write_b32 a103, v227
v_accvgpr_read_b32 v224, a108
v_accvgpr_read_b32 v225, a109
v_accvgpr_read_b32 v226, a110
v_accvgpr_read_b32 v227, a111
v_mul_f32 v224, v224, v40
v_mul_f32 v225, v225, v40
v_mul_f32 v226, v226, v40
v_mul_f32 v227, v227, v40
v_accvgpr_write_b32 a108, v224
v_accvgpr_write_b32 a109, v225
v_accvgpr_write_b32 a110, v226
v_accvgpr_write_b32 a111, v227
v_accvgpr_read_b32 v224, a116
v_accvgpr_read_b32 v225, a117
v_accvgpr_read_b32 v226, a118
v_accvgpr_read_b32 v227, a119
v_mul_f32 v224, v224, v40
v_mul_f32 v225, v225, v40
v_mul_f32 v226, v226, v40
v_mul_f32 v227, v227, v40
v_accvgpr_write_b32 a116, v224
v_accvgpr_write_b32 a117, v225
v_accvgpr_write_b32 a118, v226
v_accvgpr_write_b32 a119, v227
v_accvgpr_read_b32 v224, a124
v_accvgpr_read_b32 v225, a125
v_accvgpr_read_b32 v226, a126
v_accvgpr_read_b32 v227, a127
v_mul_f32 v224, v224, v40
v_mul_f32 v225, v225, v40
v_mul_f32 v226, v226, v40
v_mul_f32 v227, v227, v40
v_accvgpr_write_b32 a124, v224
v_accvgpr_write_b32 a125, v225
v_accvgpr_write_b32 a126, v226
v_accvgpr_write_b32 a127, v227
s_nop 1
s_branch RRET0_%=
MASK1_%=:
s_nop 3
s_sub_u32 s35, s46, s45
s_add_u32 s35, s35, 63
v_sub_u32 v33, s35, v19
v_mov_b32 v34, 0xff800000
v_cmp_ge_i32_e64 s[48:49], v33, 0
v_cmp_ge_i32_e64 s[50:51], v33, 1
v_cmp_ge_i32_e64 s[52:53], v33, 2
v_cmp_ge_i32_e64 s[54:55], v33, 3
v_cndmask_b32_e64 v128, v34, v128, s[48:49]
v_cndmask_b32_e64 v129, v34, v129, s[50:51]
v_cndmask_b32_e64 v130, v34, v130, s[52:53]
v_cndmask_b32_e64 v131, v34, v131, s[54:55]
v_cmp_ge_i32_e64 s[48:49], v33, -16
v_cmp_ge_i32_e64 s[50:51], v33, -15
v_cmp_ge_i32_e64 s[52:53], v33, -14
v_cmp_ge_i32_e64 s[54:55], v33, -13
v_cndmask_b32_e64 v132, v34, v132, s[48:49]
v_cndmask_b32_e64 v133, v34, v133, s[50:51]
v_cndmask_b32_e64 v134, v34, v134, s[52:53]
v_cndmask_b32_e64 v135, v34, v135, s[54:55]
v_cmp_ge_i32_e64 s[48:49], v33, 16
v_cmp_ge_i32_e64 s[50:51], v33, 17
v_cmp_ge_i32_e64 s[52:53], v33, 18
v_cmp_ge_i32_e64 s[54:55], v33, 19
v_cndmask_b32_e64 v136, v34, v136, s[48:49]
v_cndmask_b32_e64 v137, v34, v137, s[50:51]
v_cndmask_b32_e64 v138, v34, v138, s[52:53]
v_cndmask_b32_e64 v139, v34, v139, s[54:55]
v_cmp_ge_i32_e64 s[48:49], v33, 0
v_cmp_ge_i32_e64 s[50:51], v33, 1
v_cmp_ge_i32_e64 s[52:53], v33, 2
v_cmp_ge_i32_e64 s[54:55], v33, 3
v_cndmask_b32_e64 v140, v34, v140, s[48:49]
v_cndmask_b32_e64 v141, v34, v141, s[50:51]
v_cndmask_b32_e64 v142, v34, v142, s[52:53]
v_cndmask_b32_e64 v143, v34, v143, s[54:55]
v_cmp_ge_i32_e64 s[48:49], v33, 32
v_cmp_ge_i32_e64 s[50:51], v33, 33
v_cmp_ge_i32_e64 s[52:53], v33, 34
v_cmp_ge_i32_e64 s[54:55], v33, 35
v_cndmask_b32_e64 v144, v34, v144, s[48:49]
v_cndmask_b32_e64 v145, v34, v145, s[50:51]
v_cndmask_b32_e64 v146, v34, v146, s[52:53]
v_cndmask_b32_e64 v147, v34, v147, s[54:55]
v_cmp_ge_i32_e64 s[48:49], v33, 16
v_cmp_ge_i32_e64 s[50:51], v33, 17
v_cmp_ge_i32_e64 s[52:53], v33, 18
v_cmp_ge_i32_e64 s[54:55], v33, 19
v_cndmask_b32_e64 v148, v34, v148, s[48:49]
v_cndmask_b32_e64 v149, v34, v149, s[50:51]
v_cndmask_b32_e64 v150, v34, v150, s[52:53]
v_cndmask_b32_e64 v151, v34, v151, s[54:55]
v_cmp_ge_i32_e64 s[48:49], v33, 48
v_cmp_ge_i32_e64 s[50:51], v33, 49
v_cmp_ge_i32_e64 s[52:53], v33, 50
v_cmp_ge_i32_e64 s[54:55], v33, 51
v_cndmask_b32_e64 v152, v34, v152, s[48:49]
v_cndmask_b32_e64 v153, v34, v153, s[50:51]
v_cndmask_b32_e64 v154, v34, v154, s[52:53]
v_cndmask_b32_e64 v155, v34, v155, s[54:55]
v_cmp_ge_i32_e64 s[48:49], v33, 32
v_cmp_ge_i32_e64 s[50:51], v33, 33
v_cmp_ge_i32_e64 s[52:53], v33, 34
v_cmp_ge_i32_e64 s[54:55], v33, 35
v_cndmask_b32_e64 v156, v34, v156, s[48:49]
v_cndmask_b32_e64 v157, v34, v157, s[50:51]
v_cndmask_b32_e64 v158, v34, v158, s[52:53]
v_cndmask_b32_e64 v159, v34, v159, s[54:55]
v_max3_f32 v32, v128, v129, v130
v_max3_f32 v32, v32, v131, v132
v_max3_f32 v32, v32, v133, v134
v_max3_f32 v32, v32, v135, v136
v_max3_f32 v32, v32, v137, v138
v_max3_f32 v32, v32, v139, v140
v_max3_f32 v32, v32, v141, v142
v_max3_f32 v32, v32, v143, v144
v_max3_f32 v32, v32, v145, v146
v_max3_f32 v32, v32, v147, v148
v_max3_f32 v32, v32, v149, v150
v_max3_f32 v32, v32, v151, v152
v_max3_f32 v32, v32, v153, v154
v_max3_f32 v32, v32, v155, v156
v_max3_f32 v32, v32, v157, v158
v_max_f32 v32, v32, v159
v_cmp_lt_f32 vcc, 0x41000000, v32
s_nop 4
s_branch MRET1_%=
RARE1_%=:
s_nop 7
s_nop 7
v_max3_f32 v35, v128, v129, v130
v_max3_f32 v35, v35, v131, v136
v_max3_f32 v35, v35, v137, v138
v_max3_f32 v35, v35, v139, v144
v_max3_f32 v35, v35, v145, v146
v_max3_f32 v35, v35, v147, v152
v_max3_f32 v35, v35, v153, v154
v_max_f32 v35, v35, v155
v_mov_b32 v36, v35
s_nop 1
v_permlane16_swap_b32 v35, v36
s_nop 1
v_max_f32 v35, v35, v36
v_mov_b32 v36, v35
s_nop 1
v_permlane32_swap_b32 v35, v36
s_nop 1
v_max_f32 v35, v35, v36
v_max_f32 v35, 0, v35
v_add_f32 v28, v28, v35
v_sub_f32 v128, v128, v35
v_sub_f32 v129, v129, v35
v_sub_f32 v130, v130, v35
v_sub_f32 v131, v131, v35
v_sub_f32 v136, v136, v35
v_sub_f32 v137, v137, v35
v_sub_f32 v138, v138, v35
v_sub_f32 v139, v139, v35
v_sub_f32 v144, v144, v35
v_sub_f32 v145, v145, v35
v_sub_f32 v146, v146, v35
v_sub_f32 v147, v147, v35
v_sub_f32 v152, v152, v35
v_sub_f32 v153, v153, v35
v_sub_f32 v154, v154, v35
v_sub_f32 v155, v155, v35
v_sub_f32 v20, 0, v28
v_sub_f32 v21, 0, v28
v_sub_f32 v22, 0, v28
v_sub_f32 v23, 0, v28
v_sub_f32 v36, 0, v35
v_exp_f32 v36, v36
s_nop 0
v_mul_f32 v30, v30, v36
v_accvgpr_read_b32 v224, a0
v_accvgpr_read_b32 v225, a1
v_accvgpr_read_b32 v226, a2
v_accvgpr_read_b32 v227, a3
v_mul_f32 v224, v224, v36
v_mul_f32 v225, v225, v36
v_mul_f32 v226, v226, v36
v_mul_f32 v227, v227, v36
v_accvgpr_write_b32 a0, v224
v_accvgpr_write_b32 a1, v225
v_accvgpr_write_b32 a2, v226
v_accvgpr_write_b32 a3, v227
v_accvgpr_read_b32 v224, a8
v_accvgpr_read_b32 v225, a9
v_accvgpr_read_b32 v226, a10
v_accvgpr_read_b32 v227, a11
v_mul_f32 v224, v224, v36
v_mul_f32 v225, v225, v36
v_mul_f32 v226, v226, v36
v_mul_f32 v227, v227, v36
v_accvgpr_write_b32 a8, v224
v_accvgpr_write_b32 a9, v225
v_accvgpr_write_b32 a10, v226
v_accvgpr_write_b32 a11, v227
v_accvgpr_read_b32 v224, a16
v_accvgpr_read_b32 v225, a17
v_accvgpr_read_b32 v226, a18
v_accvgpr_read_b32 v227, a19
v_mul_f32 v224, v224, v36
v_mul_f32 v225, v225, v36
v_mul_f32 v226, v226, v36
v_mul_f32 v227, v227, v36
v_accvgpr_write_b32 a16, v224
v_accvgpr_write_b32 a17, v225
v_accvgpr_write_b32 a18, v226
v_accvgpr_write_b32 a19, v227
v_accvgpr_read_b32 v224, a24
v_accvgpr_read_b32 v225, a25
v_accvgpr_read_b32 v226, a26
v_accvgpr_read_b32 v227, a27
v_mul_f32 v224, v224, v36
v_mul_f32 v225, v225, v36
v_mul_f32 v226, v226, v36
v_mul_f32 v227, v227, v36
v_accvgpr_write_b32 a24, v224
v_accvgpr_write_b32 a25, v225
v_accvgpr_write_b32 a26, v226
v_accvgpr_write_b32 a27, v227
v_accvgpr_read_b32 v224, a32
v_accvgpr_read_b32 v225, a33
v_accvgpr_read_b32 v226, a34
v_accvgpr_read_b32 v227, a35
v_mul_f32 v224, v224, v36
v_mul_f32 v225, v225, v36
v_mul_f32 v226, v226, v36
v_mul_f32 v227, v227, v36
v_accvgpr_write_b32 a32, v224
v_accvgpr_write_b32 a33, v225
v_accvgpr_write_b32 a34, v226
v_accvgpr_write_b32 a35, v227
v_accvgpr_read_b32 v224, a40
v_accvgpr_read_b32 v225, a41
v_accvgpr_read_b32 v226, a42
v_accvgpr_read_b32 v227, a43
v_mul_f32 v224, v224, v36
v_mul_f32 v225, v225, v36
v_mul_f32 v226, v226, v36
v_mul_f32 v227, v227, v36
v_accvgpr_write_b32 a40, v224
v_accvgpr_write_b32 a41, v225
v_accvgpr_write_b32 a42, v226
v_accvgpr_write_b32 a43, v227
v_accvgpr_read_b32 v224, a48
v_accvgpr_read_b32 v225, a49
v_accvgpr_read_b32 v226, a50
v_accvgpr_read_b32 v227, a51
v_mul_f32 v224, v224, v36
v_mul_f32 v225, v225, v36
v_mul_f32 v226, v226, v36
v_mul_f32 v227, v227, v36
v_accvgpr_write_b32 a48, v224
v_accvgpr_write_b32 a49, v225
v_accvgpr_write_b32 a50, v226
v_accvgpr_write_b32 a51, v227
v_accvgpr_read_b32 v224, a56
v_accvgpr_read_b32 v225, a57
v_accvgpr_read_b32 v226, a58
v_accvgpr_read_b32 v227, a59
v_mul_f32 v224, v224, v36
v_mul_f32 v225, v225, v36
v_mul_f32 v226, v226, v36
v_mul_f32 v227, v227, v36
v_accvgpr_write_b32 a56, v224
v_accvgpr_write_b32 a57, v225
v_accvgpr_write_b32 a58, v226
v_accvgpr_write_b32 a59, v227
v_accvgpr_read_b32 v224, a64
v_accvgpr_read_b32 v225, a65
v_accvgpr_read_b32 v226, a66
v_accvgpr_read_b32 v227, a67
v_mul_f32 v224, v224, v36
v_mul_f32 v225, v225, v36
v_mul_f32 v226, v226, v36
v_mul_f32 v227, v227, v36
v_accvgpr_write_b32 a64, v224
v_accvgpr_write_b32 a65, v225
v_accvgpr_write_b32 a66, v226
v_accvgpr_write_b32 a67, v227
v_accvgpr_read_b32 v224, a72
v_accvgpr_read_b32 v225, a73
v_accvgpr_read_b32 v226, a74
v_accvgpr_read_b32 v227, a75
v_mul_f32 v224, v224, v36
v_mul_f32 v225, v225, v36
v_mul_f32 v226, v226, v36
v_mul_f32 v227, v227, v36
v_accvgpr_write_b32 a72, v224
v_accvgpr_write_b32 a73, v225
v_accvgpr_write_b32 a74, v226
v_accvgpr_write_b32 a75, v227
v_accvgpr_read_b32 v224, a80
v_accvgpr_read_b32 v225, a81
v_accvgpr_read_b32 v226, a82
v_accvgpr_read_b32 v227, a83
v_mul_f32 v224, v224, v36
v_mul_f32 v225, v225, v36
v_mul_f32 v226, v226, v36
v_mul_f32 v227, v227, v36
v_accvgpr_write_b32 a80, v224
v_accvgpr_write_b32 a81, v225
v_accvgpr_write_b32 a82, v226
v_accvgpr_write_b32 a83, v227
v_accvgpr_read_b32 v224, a88
v_accvgpr_read_b32 v225, a89
v_accvgpr_read_b32 v226, a90
v_accvgpr_read_b32 v227, a91
v_mul_f32 v224, v224, v36
v_mul_f32 v225, v225, v36
v_mul_f32 v226, v226, v36
v_mul_f32 v227, v227, v36
v_accvgpr_write_b32 a88, v224
v_accvgpr_write_b32 a89, v225
v_accvgpr_write_b32 a90, v226
v_accvgpr_write_b32 a91, v227
v_accvgpr_read_b32 v224, a96
v_accvgpr_read_b32 v225, a97
v_accvgpr_read_b32 v226, a98
v_accvgpr_read_b32 v227, a99
v_mul_f32 v224, v224, v36
v_mul_f32 v225, v225, v36
v_mul_f32 v226, v226, v36
v_mul_f32 v227, v227, v36
v_accvgpr_write_b32 a96, v224
v_accvgpr_write_b32 a97, v225
v_accvgpr_write_b32 a98, v226
v_accvgpr_write_b32 a99, v227
v_accvgpr_read_b32 v224, a104
v_accvgpr_read_b32 v225, a105
v_accvgpr_read_b32 v226, a106
v_accvgpr_read_b32 v227, a107
v_mul_f32 v224, v224, v36
v_mul_f32 v225, v225, v36
v_mul_f32 v226, v226, v36
v_mul_f32 v227, v227, v36
v_accvgpr_write_b32 a104, v224
v_accvgpr_write_b32 a105, v225
v_accvgpr_write_b32 a106, v226
v_accvgpr_write_b32 a107, v227
v_accvgpr_read_b32 v224, a112
v_accvgpr_read_b32 v225, a113
v_accvgpr_read_b32 v226, a114
v_accvgpr_read_b32 v227, a115
v_mul_f32 v224, v224, v36
v_mul_f32 v225, v225, v36
v_mul_f32 v226, v226, v36
v_mul_f32 v227, v227, v36
v_accvgpr_write_b32 a112, v224
v_accvgpr_write_b32 a113, v225
v_accvgpr_write_b32 a114, v226
v_accvgpr_write_b32 a115, v227
v_accvgpr_read_b32 v224, a120
v_accvgpr_read_b32 v225, a121
v_accvgpr_read_b32 v226, a122
v_accvgpr_read_b32 v227, a123
v_mul_f32 v224, v224, v36
v_mul_f32 v225, v225, v36
v_mul_f32 v226, v226, v36
v_mul_f32 v227, v227, v36
v_accvgpr_write_b32 a120, v224
v_accvgpr_write_b32 a121, v225
v_accvgpr_write_b32 a122, v226
v_accvgpr_write_b32 a123, v227
v_max3_f32 v39, v132, v133, v134
v_max3_f32 v39, v39, v135, v140
v_max3_f32 v39, v39, v141, v142
v_max3_f32 v39, v39, v143, v148
v_max3_f32 v39, v39, v149, v150
v_max3_f32 v39, v39, v151, v156
v_max3_f32 v39, v39, v157, v158
v_max_f32 v39, v39, v159
v_mov_b32 v40, v39
s_nop 1
v_permlane16_swap_b32 v39, v40
s_nop 1
v_max_f32 v39, v39, v40
v_mov_b32 v40, v39
s_nop 1
v_permlane32_swap_b32 v39, v40
s_nop 1
v_max_f32 v39, v39, v40
v_max_f32 v39, 0, v39
v_add_f32 v29, v29, v39
v_sub_f32 v132, v132, v39
v_sub_f32 v133, v133, v39
v_sub_f32 v134, v134, v39
v_sub_f32 v135, v135, v39
v_sub_f32 v140, v140, v39
v_sub_f32 v141, v141, v39
v_sub_f32 v142, v142, v39
v_sub_f32 v143, v143, v39
v_sub_f32 v148, v148, v39
v_sub_f32 v149, v149, v39
v_sub_f32 v150, v150, v39
v_sub_f32 v151, v151, v39
v_sub_f32 v156, v156, v39
v_sub_f32 v157, v157, v39
v_sub_f32 v158, v158, v39
v_sub_f32 v159, v159, v39
v_sub_f32 v24, 0, v29
v_sub_f32 v25, 0, v29
v_sub_f32 v26, 0, v29
v_sub_f32 v27, 0, v29
v_sub_f32 v40, 0, v39
v_exp_f32 v40, v40
s_nop 0
v_mul_f32 v31, v31, v40
v_accvgpr_read_b32 v224, a4
v_accvgpr_read_b32 v225, a5
v_accvgpr_read_b32 v226, a6
v_accvgpr_read_b32 v227, a7
v_mul_f32 v224, v224, v40
v_mul_f32 v225, v225, v40
v_mul_f32 v226, v226, v40
v_mul_f32 v227, v227, v40
v_accvgpr_write_b32 a4, v224
v_accvgpr_write_b32 a5, v225
v_accvgpr_write_b32 a6, v226
v_accvgpr_write_b32 a7, v227
v_accvgpr_read_b32 v224, a12
v_accvgpr_read_b32 v225, a13
v_accvgpr_read_b32 v226, a14
v_accvgpr_read_b32 v227, a15
v_mul_f32 v224, v224, v40
v_mul_f32 v225, v225, v40
v_mul_f32 v226, v226, v40
v_mul_f32 v227, v227, v40
v_accvgpr_write_b32 a12, v224
v_accvgpr_write_b32 a13, v225
v_accvgpr_write_b32 a14, v226
v_accvgpr_write_b32 a15, v227
v_accvgpr_read_b32 v224, a20
v_accvgpr_read_b32 v225, a21
v_accvgpr_read_b32 v226, a22
v_accvgpr_read_b32 v227, a23
v_mul_f32 v224, v224, v40
v_mul_f32 v225, v225, v40
v_mul_f32 v226, v226, v40
v_mul_f32 v227, v227, v40
v_accvgpr_write_b32 a20, v224
v_accvgpr_write_b32 a21, v225
v_accvgpr_write_b32 a22, v226
v_accvgpr_write_b32 a23, v227
v_accvgpr_read_b32 v224, a28
v_accvgpr_read_b32 v225, a29
v_accvgpr_read_b32 v226, a30
v_accvgpr_read_b32 v227, a31
v_mul_f32 v224, v224, v40
v_mul_f32 v225, v225, v40
v_mul_f32 v226, v226, v40
v_mul_f32 v227, v227, v40
v_accvgpr_write_b32 a28, v224
v_accvgpr_write_b32 a29, v225
v_accvgpr_write_b32 a30, v226
v_accvgpr_write_b32 a31, v227
v_accvgpr_read_b32 v224, a36
v_accvgpr_read_b32 v225, a37
v_accvgpr_read_b32 v226, a38
v_accvgpr_read_b32 v227, a39
v_mul_f32 v224, v224, v40
v_mul_f32 v225, v225, v40
v_mul_f32 v226, v226, v40
v_mul_f32 v227, v227, v40
v_accvgpr_write_b32 a36, v224
v_accvgpr_write_b32 a37, v225
v_accvgpr_write_b32 a38, v226
v_accvgpr_write_b32 a39, v227
v_accvgpr_read_b32 v224, a44
v_accvgpr_read_b32 v225, a45
v_accvgpr_read_b32 v226, a46
v_accvgpr_read_b32 v227, a47
v_mul_f32 v224, v224, v40
v_mul_f32 v225, v225, v40
v_mul_f32 v226, v226, v40
v_mul_f32 v227, v227, v40
v_accvgpr_write_b32 a44, v224
v_accvgpr_write_b32 a45, v225
v_accvgpr_write_b32 a46, v226
v_accvgpr_write_b32 a47, v227
v_accvgpr_read_b32 v224, a52
v_accvgpr_read_b32 v225, a53
v_accvgpr_read_b32 v226, a54
v_accvgpr_read_b32 v227, a55
v_mul_f32 v224, v224, v40
v_mul_f32 v225, v225, v40
v_mul_f32 v226, v226, v40
v_mul_f32 v227, v227, v40
v_accvgpr_write_b32 a52, v224
v_accvgpr_write_b32 a53, v225
v_accvgpr_write_b32 a54, v226
v_accvgpr_write_b32 a55, v227
v_accvgpr_read_b32 v224, a60
v_accvgpr_read_b32 v225, a61
v_accvgpr_read_b32 v226, a62
v_accvgpr_read_b32 v227, a63
v_mul_f32 v224, v224, v40
v_mul_f32 v225, v225, v40
v_mul_f32 v226, v226, v40
v_mul_f32 v227, v227, v40
v_accvgpr_write_b32 a60, v224
v_accvgpr_write_b32 a61, v225
v_accvgpr_write_b32 a62, v226
v_accvgpr_write_b32 a63, v227
v_accvgpr_read_b32 v224, a68
v_accvgpr_read_b32 v225, a69
v_accvgpr_read_b32 v226, a70
v_accvgpr_read_b32 v227, a71
v_mul_f32 v224, v224, v40
v_mul_f32 v225, v225, v40
v_mul_f32 v226, v226, v40
v_mul_f32 v227, v227, v40
v_accvgpr_write_b32 a68, v224
v_accvgpr_write_b32 a69, v225
v_accvgpr_write_b32 a70, v226
v_accvgpr_write_b32 a71, v227
v_accvgpr_read_b32 v224, a76
v_accvgpr_read_b32 v225, a77
v_accvgpr_read_b32 v226, a78
v_accvgpr_read_b32 v227, a79
v_mul_f32 v224, v224, v40
v_mul_f32 v225, v225, v40
v_mul_f32 v226, v226, v40
v_mul_f32 v227, v227, v40
v_accvgpr_write_b32 a76, v224
v_accvgpr_write_b32 a77, v225
v_accvgpr_write_b32 a78, v226
v_accvgpr_write_b32 a79, v227
v_accvgpr_read_b32 v224, a84
v_accvgpr_read_b32 v225, a85
v_accvgpr_read_b32 v226, a86
v_accvgpr_read_b32 v227, a87
v_mul_f32 v224, v224, v40
v_mul_f32 v225, v225, v40
v_mul_f32 v226, v226, v40
v_mul_f32 v227, v227, v40
v_accvgpr_write_b32 a84, v224
v_accvgpr_write_b32 a85, v225
v_accvgpr_write_b32 a86, v226
v_accvgpr_write_b32 a87, v227
v_accvgpr_read_b32 v224, a92
v_accvgpr_read_b32 v225, a93
v_accvgpr_read_b32 v226, a94
v_accvgpr_read_b32 v227, a95
v_mul_f32 v224, v224, v40
v_mul_f32 v225, v225, v40
v_mul_f32 v226, v226, v40
v_mul_f32 v227, v227, v40
v_accvgpr_write_b32 a92, v224
v_accvgpr_write_b32 a93, v225
v_accvgpr_write_b32 a94, v226
v_accvgpr_write_b32 a95, v227
v_accvgpr_read_b32 v224, a100
v_accvgpr_read_b32 v225, a101
v_accvgpr_read_b32 v226, a102
v_accvgpr_read_b32 v227, a103
v_mul_f32 v224, v224, v40
v_mul_f32 v225, v225, v40
v_mul_f32 v226, v226, v40
v_mul_f32 v227, v227, v40
v_accvgpr_write_b32 a100, v224
v_accvgpr_write_b32 a101, v225
v_accvgpr_write_b32 a102, v226
v_accvgpr_write_b32 a103, v227
v_accvgpr_read_b32 v224, a108
v_accvgpr_read_b32 v225, a109
v_accvgpr_read_b32 v226, a110
v_accvgpr_read_b32 v227, a111
v_mul_f32 v224, v224, v40
v_mul_f32 v225, v225, v40
v_mul_f32 v226, v226, v40
v_mul_f32 v227, v227, v40
v_accvgpr_write_b32 a108, v224
v_accvgpr_write_b32 a109, v225
v_accvgpr_write_b32 a110, v226
v_accvgpr_write_b32 a111, v227
v_accvgpr_read_b32 v224, a116
v_accvgpr_read_b32 v225, a117
v_accvgpr_read_b32 v226, a118
v_accvgpr_read_b32 v227, a119
v_mul_f32 v224, v224, v40
v_mul_f32 v225, v225, v40
v_mul_f32 v226, v226, v40
v_mul_f32 v227, v227, v40
v_accvgpr_write_b32 a116, v224
v_accvgpr_write_b32 a117, v225
v_accvgpr_write_b32 a118, v226
v_accvgpr_write_b32 a119, v227
v_accvgpr_read_b32 v224, a124
v_accvgpr_read_b32 v225, a125
v_accvgpr_read_b32 v226, a126
v_accvgpr_read_b32 v227, a127
v_mul_f32 v224, v224, v40
v_mul_f32 v225, v225, v40
v_mul_f32 v226, v226, v40
v_mul_f32 v227, v227, v40
v_accvgpr_write_b32 a124, v224
v_accvgpr_write_b32 a125, v225
v_accvgpr_write_b32 a126, v226
v_accvgpr_write_b32 a127, v227
s_nop 1
s_branch RRET1_%=
MEGA_END_%=:
)ASM"
    :
    : "s"(__builtin_amdgcn_kernarg_segment_ptr()), "v"(threadIdx.x), "s"(blockIdx.x)
    : "v1","v2","v3","v4","v5","v6","v7","v8","v9","v10","v11","v12","v13","v14","v15","v16","v17","v18","v19","v20","v21","v22","v23","v24","v25","v26",
      "v27","v28","v29","v30","v31","v32","v33","v34","v35","v36","v37","v38","v39","v40","v41","v42","v43","v44","v45","v46","v47","v48","v49","v50","v51",
      "v52","v53","v54","v55","v56","v57","v58","v59","v60","v61","v62","v63","v64","v65","v66","v67","v68","v69","v70","v71","v72","v73","v74","v75","v76",
      "v77","v78","v79","v80","v81","v82","v83","v84","v85","v86","v87","v88","v89","v90","v91","v92","v93","v94","v95","v96","v97","v98","v99","v100",
      "v101","v102","v103","v104","v105","v106","v107","v108","v109","v110","v111","v112","v113","v114","v115","v116","v117","v118","v119","v120","v121",
      "v122","v123","v124","v125","v126","v127","v128","v129","v130","v131","v132","v133","v134","v135","v136","v137","v138","v139","v140","v141","v142",
      "v143","v144","v145","v146","v147","v148","v149","v150","v151","v152","v153","v154","v155","v156","v157","v158","v159","v160","v161","v162","v163",
      "v164","v165","v166","v167","v168","v169","v170","v171","v172","v173","v174","v175","v176","v177","v178","v179","v180","v181","v182","v183","v184",
      "v185","v186","v187","v188","v189","v190","v191","v192","v193","v194","v195","v196","v197","v198","v199","v200","v201","v202","v203","v204","v205",
      "v206","v207","v208","v209","v210","v211","v212","v213","v214","v215","v216","v217","v218","v219","v220","v221","v222","v223","v224","v225","v226",
      "v227","v228","v229","v230","v231","v232","v233","v234","v235","v236","v237","v238","v239","v240","v241","v242","v243","v244","v245","v246","v247",
      "v248","v249","v250","v251","v252","v253","v254","v255","a0","a1","a2","a3","a4","a5","a6","a7","a8","a9","a10","a11","a12","a13","a14","a15","a16",
      "a17","a18","a19","a20","a21","a22","a23","a24","a25","a26","a27","a28","a29","a30","a31","a32","a33","a34","a35","a36","a37","a38","a39","a40","a41",
      "a42","a43","a44","a45","a46","a47","a48","a49","a50","a51","a52","a53","a54","a55","a56","a57","a58","a59","a60","a61","a62","a63","a64","a65","a66",
      "a67","a68","a69","a70","a71","a72","a73","a74","a75","a76","a77","a78","a79","a80","a81","a82","a83","a84","a85","a86","a87","a88","a89","a90","a91",
      "a92","a93","a94","a95","a96","a97","a98","a99","a100","a101","a102","a103","a104","a105","a106","a107","a108","a109","a110","a111","a112","a113",
      "a114","a115","a116","a117","a118","a119","a120","a121","a122","a123","a124","a125","a126","a127","a128","a129","a130","a131","a132","a133","a134",
      "a135","a136","a137","a138","a139","a140","a141","a142","a143","a144","a145","a146","a147","a148","a149","a150","a151","a152","a153","a154","a155",
      "a156","a157","a158","a159","a160","a161","a162","a163","a164","a165","a166","a167","a168","a169","a170","a171","a172","a173","a174","a175","a176",
      "a177","a178","a179","a180","a181","a182","a183","a184","a185","a186","a187","a188","a189","a190","a191","a192","a193","a194","a195","a196","a197",
      "a198","a199","a200","a201","a202","a203","a204","a205","a206","a207","a208","a209","a210","a211","a212","a213","a214","a215","a216","a217","a218",
      "a219","a220","a221","a222","a223","a224","a225","a226","a227","a228","a229","a230","a231","a232","a233","a234","a235","a236","a237","a238","a239",
      "a240","a241","a242","a243","a244","a245","a246","a247","a248","a249","a250","a251","a252","a253","a254","a255","s16","s17","s18","s19","s20","s21",
      "s22","s23","s24","s25","s26","s27","s28","s29","s30","s31","s33","s34","s35","s36","s37","s38","s39","s40","s41","s42","s43","s44","s45","s46","s47",
      "s48","s49","s50","s51","s52","s53","s54","s55","s56","s57","s58","s59","s60","s61","s62","s63","s64","s65","s66","s67","s68","s69","s70","s71","s72",
      "s73","s74","s75","s76","s77","s78","s79","s80","s81","s82","s83","s84","s85","s86","s87","s88","s89","s90","s91","s92","s93","s94","s95","s96","s97",
      "s98","s99","vcc","scc","memory");
}

extern "C" void launch_stage(int stage, void* const* d_in, void* d_out, void* d_ws, hipStream_t stream) {
  if (stage == 1) {
    (void)hipFuncSetAttribute((const void*)mla_mega, hipFuncAttributeMaxDynamicSharedMemorySize, 163840);
    mla_mega<<<256, 256, 163840, stream>>>(d_in[0], d_in[1], d_in[2], d_in[3], d_in[4], d_in[5], d_in[7], d_in[8], d_in[9], d_ws, d_out);
  }
}
extern "C" void kernel_launch(void* const* d_in, const int* in_sizes, int n_in, void* d_out, int out_size, void* d_ws, size_t ws_size, hipStream_t stream) {
  launch_stage(1, d_in, d_out, d_ws, stream);
}
```
